```python
import torch

def _softmax(x, axis=-1):
    return torch.softmax(x, dim=axis)
def _gelu(x):
    return torch.nn.functional.gelu(x, approximate="tanh")
def _silu(x):
    return torch.nn.functional.silu(x)
def _sigmoid(x):
    return torch.sigmoid(x)
def _layernorm(x, axis=-1, eps=1e-5):
    m = x.mean(dim=axis, keepdim=True)
    v = x.var(dim=axis, keepdim=True, unbiased=False)
    return (x - m) / torch.sqrt(v + eps)
def _scatter_add(tgt, idx, src, axis=0):
    return tgt.clone().index_add_(axis, idx, src)
def _segment_sum(src, seg, n):
    out = torch.zeros((n,) + src.shape[1:], dtype=src.dtype)
    return out.index_add_(0, seg, src)
def _pad(x, pw):
    flat = tuple(v for pair in reversed(pw) for v in pair)
    return torch.nn.functional.pad(x, flat)

kernel_name = 'gated_mix__spectral_gfnet__ssm_conv_s5__prompt'

def setup_inputs():

    _w = (lambda a: (lambda t: t.to(torch.bfloat16) if t.is_floating_point() else t)(torch.as_tensor(a)))
    def _A_setup():
        B, S, D = 1, 2048, 1024
        Df = D // 2 + 1
        return {
            "x": _w((lambda *s: torch.randn(*s, dtype=torch.bfloat16) * 0.05)(B, S, D)),
            "w_r": _w((lambda *s: torch.randn(*s, dtype=torch.bfloat16) * 0.8)(S, Df) * 0.02),
            "w_i": _w((lambda *s: torch.randn(*s, dtype=torch.bfloat16) * 0.8)(S, Df) * 0.02),
        }
    _A_in = _A_setup()

    def _B_setup():
        B, S, D = 1, 2048, 1024
        P = D
        Lambda_re = -0.5 * (lambda _x: torch.exp(torch.as_tensor(_x)))((lambda *s: torch.randn(*s, dtype=torch.bfloat16) * 0.2)(P))
        Lambda_im = (lambda *s: torch.randn(*s, dtype=torch.bfloat16) * 0.4)(P) * 1.0
        B_re = (lambda *s: torch.randn(*s, dtype=torch.bfloat16) * 0.2)(P, D) * (D ** -0.5)
        B_im = (lambda *s: torch.randn(*s, dtype=torch.bfloat16) * 0.2)(P, D) * (D ** -0.5)
        C_re = (lambda *s: torch.randn(*s, dtype=torch.bfloat16) * 0.2)(D, P) * (P ** -0.5)
        C_im = (lambda *s: torch.randn(*s, dtype=torch.bfloat16) * 0.2)(D, P) * (P ** -0.5)
        log_dt = (lambda *s: torch.randn(*s, dtype=torch.bfloat16) * 0.2)(P) * 0.3 - 2.0
        Dskip = (lambda *s: torch.randn(*s, dtype=torch.bfloat16) * 0.05)(D)
        return {
            "x": _w((lambda *s: torch.randn(*s, dtype=torch.bfloat16) * 0.05)(B, S, D)),
            "Lambda_re": _w(Lambda_re),
            "Lambda_im": _w(Lambda_im),
            "B_re": _w(B_re),
            "B_im": _w(B_im),
            "C_re": _w(C_re),
            "C_im": _w(C_im),
            "log_dt": _w(log_dt),
            "Dskip": _w(Dskip),
        }
    _B_in = _B_setup()

    return {
        "A_x": _A_in["x"],
        "A_w_r": _A_in["w_r"],
        "A_w_i": _A_in["w_i"],
        "B_x": _B_in["x"],
        "B_Lambda_re": _B_in["Lambda_re"],
        "B_Lambda_im": _B_in["Lambda_im"],
        "B_B_re": _B_in["B_re"],
        "B_B_im": _B_in["B_im"],
        "B_C_re": _B_in["C_re"],
        "B_C_im": _B_in["C_im"],
        "B_log_dt": _B_in["log_dt"],
        "B_Dskip": _B_in["Dskip"],
        "gate_w": _w((lambda *s: torch.randn(*s, dtype=torch.bfloat16) * 0.05)(1024) * 16.0),
    }

def reference(A_x, A_w_r, A_w_i, B_x, B_Lambda_re, B_Lambda_im, B_B_re, B_B_im, B_C_re, B_C_im, B_log_dt, B_Dskip, gate_w):
    def _A_ref(x, w_r, w_i):
        B, S, D = x.shape
        X = (lambda x, axes=(-2,-1): torch.fft.rfft2(x.float(), dim=axes))(x, axes=(-2, -1))
        W = w_r + 1j * w_i
        X = X * W
        y = (lambda x, s, axes=(-2,-1): torch.fft.irfft2(x, s=s, dim=axes).to(torch.bfloat16))(X, (S, D), axes=(-2, -1))
        return y

    def _B_ref(x, Lambda_re, Lambda_im, B_re, B_im, C_re, C_im, log_dt, Dskip):
        Bsz, L, H = x.shape
        Lam = Lambda_re + 1j * Lambda_im
        Bmat = B_re + 1j * B_im
        Cmat = C_re + 1j * C_im
        dt = torch.exp(log_dt)
        Lam_bar = torch.exp(dt * Lam)
        B_bar = ((Lam_bar - 1.0) / Lam)[:, None] * Bmat
        Bu = torch.einsum("bld,pd->blp", x + 0j, B_bar)
        h = torch.zeros_like(Bu[:, 0, :])
        hs = []
        for t in range(L):
            h = Lam_bar[None, :] * h + Bu[:, t, :]
            hs.append(h)
        hseq = torch.stack(hs, 1)
        y = 2.0 * torch.einsum("blp,dp->bld", hseq, Cmat).real + x * Dskip[None, None, :]
        return y

    x = A_x
    g = _sigmoid((x * gate_w).sum(-1, keepdims=True))
    a = _A_ref(x, A_w_r, A_w_i)
    b = _B_ref(x, B_Lambda_re, B_Lambda_im, B_B_re, B_B_im, B_C_re, B_C_im, B_log_dt, B_Dskip)
    return g * a + (1 - g) * b
```

```cpp
#include <hip/hip_runtime.h>
#include <hip/hip_bf16.h>
#include <stdint.h>

typedef __attribute__((ext_vector_type(8))) short bf16x8;
typedef __attribute__((ext_vector_type(4))) float f32x4;
typedef unsigned short u16;
typedef unsigned long long u64;
#define GAS __attribute__((address_space(1)))
#define NOPK __attribute__((target("no-packed-fp32-ops")))
#define RLX_AGENT __ATOMIC_RELAXED, __HIP_MEMORY_SCOPE_AGENT

constexpr int S_ = 2048, D_ = 1024, P_ = 1024, KF = 513;

constexpr double kPi = 3.14159265358979323846264338327950288;
constexpr double c_sin(double x) {
    double t = x, s = x;
    for (int i = 1; i < 14; ++i) { t *= -x * x / ((2.0 * i) * (2.0 * i + 1.0)); s += t; }
    return s;
}
constexpr double c_cos(double x) {
    double t = 1.0, s = 1.0;
    for (int i = 1; i < 14; ++i) { t *= -x * x / ((2.0 * i - 1.0) * (2.0 * i)); s += t; }
    return s;
}
struct TwTab {
    float v[4096];
    constexpr TwTab() : v{} {
        for (int j = 0; j < 2048; ++j) {
            int q = j >> 9, r = j & 511;
            double c = 0, s = 0;
            if (r <= 256) { double th = 2.0 * kPi * r / 2048.0; c = c_cos(th); s = c_sin(th); }
            else { double th = 2.0 * kPi * (512 - r) / 2048.0; c = c_sin(th); s = c_cos(th); }
            double cc = 0, ss = 0;
            if (q == 0) { cc = c; ss = s; } else if (q == 1) { cc = -s; ss = c; }
            else if (q == 2) { cc = -c; ss = -s; } else { cc = s; ss = -c; }
            v[2 * j] = (float)cc; v[2 * j + 1] = (float)(-ss);
        }
    }
};
__device__ const TwTab TWT = TwTab();
__device__ __forceinline__ float2 tw(int j) { return *reinterpret_cast<const float2*>(&TWT.v[2 * j]); }

__device__ __forceinline__ void st16(void* p, uint4 v) { *reinterpret_cast<uint4*>(p) = v; }
__device__ __forceinline__ void st16f(void* p, float4 v) { st16(p, make_uint4(__float_as_uint(v.x), __float_as_uint(v.y), __float_as_uint(v.z), __float_as_uint(v.w))); }
__device__ __forceinline__ float bf2f(u16 h) { return __uint_as_float((unsigned)h << 16); }
__device__ __forceinline__ u16 f2bf(float f) { __hip_bfloat16 b = __float2bfloat16(f); return *reinterpret_cast<u16*>(&b); }
__device__ __forceinline__ float bfr(float f) { return bf2f(f2bf(f)); }
__device__ __forceinline__ float2 cadd(float2 a, float2 b) { return make_float2(a.x + b.x, a.y + b.y); }
__device__ __forceinline__ float2 csub(float2 a, float2 b) { return make_float2(a.x - b.x, a.y - b.y); }
__device__ __forceinline__ float2 cmul(float2 a, float2 b) { return make_float2(a.x * b.x - a.y * b.y, a.x * b.y + a.y * b.x); }
__device__ __forceinline__ float2 cmulc(float2 a, float2 b) { return make_float2(a.x * b.x + a.y * b.y, a.y * b.x - a.x * b.y); }
template <int DIR> __device__ __forceinline__ float2 cmuld(float2 a, float2 w) { return DIR < 0 ? cmul(a, w) : cmulc(a, w); }
template <int DIR> __device__ __forceinline__ float2 rot90(float2 a) { return DIR < 0 ? make_float2(a.y, -a.x) : make_float2(-a.y, a.x); }
#define PACE() do { __builtin_amdgcn_s_barrier(); asm volatile("" ::: "memory"); } while (0)
#define FENCE16(v) do { _Pragma("unroll") for (int i_ = 0; i_ < 16; ++i_) asm volatile("" : "+v"(v[i_].x), "+v"(v[i_].y)); } while (0)
#define LAS __attribute__((address_space(3)))
__device__ __forceinline__ void grp_barrier(unsigned* c, unsigned target) {
    const unsigned a = (unsigned)(uintptr_t)(LAS unsigned*)c;
    asm volatile("s_waitcnt lgkmcnt(0)" ::: "memory");
    if ((threadIdx.x & 63) == 0) asm volatile("ds_add_u32 %0, %1" :: "v"(a), "v"(1u) : "memory");
    for (int spin = 0; spin < (1 << 24); ++spin) {
        unsigned v;
        asm volatile("ds_read_b32 %0, %1\n\ts_waitcnt lgkmcnt(0)" : "=v"(v) : "v"(a) : "memory");
        if ((unsigned)__builtin_amdgcn_readfirstlane(v) >= target) break;
        __builtin_amdgcn_s_sleep(1);
    }
    asm volatile("" ::: "memory");
}
template <int DIR, bool PRE, bool MIDPACE = false> __device__ __forceinline__ void twiddle16(float2 (&v)[16], float2 w, float2 pre) {
    asm volatile("" : "+v"(w.x), "+v"(w.y));
    const float2 w2 = cmul(w, w), w3 = cmul(w2, w), w4 = cmul(w2, w2), w8 = cmul(w4, w4);
#pragma unroll
    for (int q = 0; q < 4; ++q) {
        float2 b = q == 0 ? make_float2(1.f, 0.f) : q == 1 ? w4 : q == 2 ? w8 : cmul(w8, w4);
        if (PRE) b = q == 0 ? pre : cmul(b, pre);
        if (PRE || q > 0) v[4 * q] = cmuld<DIR>(v[4 * q], b);
        v[4 * q + 1] = cmuld<DIR>(v[4 * q + 1], (PRE || q > 0) ? cmul(b, w) : w);
        v[4 * q + 2] = cmuld<DIR>(v[4 * q + 2], (PRE || q > 0) ? cmul(b, w2) : w2);
        v[4 * q + 3] = cmuld<DIR>(v[4 * q + 3], (PRE || q > 0) ? cmul(b, w3) : w3);
        if (MIDPACE && q == 1) { FENCE16(v); PACE(); }
    }
}
#define LDS_WAIT() asm volatile("s_waitcnt lgkmcnt(0)" ::: "memory")
#define WAVE_SYNC() do { LDS_WAIT(); __builtin_amdgcn_wave_barrier(); } while (0)
#define WG_BARRIER() do { LDS_WAIT(); __builtin_amdgcn_s_barrier(); asm volatile("" ::: "memory"); } while (0)

template <int DIR> __device__ __forceinline__ void dft4(float2& a0, float2& a1, float2& a2, float2& a3) {
    float2 t0 = cadd(a0, a2), t1 = csub(a0, a2), t2 = cadd(a1, a3), t3 = rot90<DIR>(csub(a1, a3));
    a0 = cadd(t0, t2); a1 = cadd(t1, t3); a2 = csub(t0, t2); a3 = csub(t1, t3);
}
template <int DIR, bool MIDPACE = false> __device__ __forceinline__ void dft16(float2 (&v)[16]) {
    constexpr float C1 = 0.92387953251128675613f, S1 = 0.38268343236508977173f, R = 0.70710678118654752440f;
#pragma unroll
    for (int n2 = 0; n2 < 4; ++n2) dft4<DIR>(v[n2], v[4 + n2], v[8 + n2], v[12 + n2]);
    if (MIDPACE) { FENCE16(v); PACE(); }
    const float2 w1 = make_float2(C1, -S1), w2 = make_float2(R, -R), w3 = make_float2(S1, -C1);
    const float2 w6 = make_float2(-R, -R), w9 = make_float2(-C1, S1);
    v[4 * 1 + 1] = cmuld<DIR>(v[4 * 1 + 1], w1);
    v[4 * 1 + 2] = cmuld<DIR>(v[4 * 1 + 2], w2);
    v[4 * 1 + 3] = cmuld<DIR>(v[4 * 1 + 3], w3);
    v[4 * 2 + 1] = cmuld<DIR>(v[4 * 2 + 1], w2);
    v[4 * 2 + 2] = rot90<DIR>(v[4 * 2 + 2]);
    v[4 * 2 + 3] = cmuld<DIR>(v[4 * 2 + 3], w6);
    v[4 * 3 + 1] = cmuld<DIR>(v[4 * 3 + 1], w3);
    v[4 * 3 + 2] = cmuld<DIR>(v[4 * 3 + 2], w6);
    v[4 * 3 + 3] = cmuld<DIR>(v[4 * 3 + 3], w9);
    if (MIDPACE) {
#pragma unroll
        for (int i = 0; i < 16; ++i) asm volatile("" : "+v"(v[i].x), "+v"(v[i].y));
        PACE();
    }
#pragma unroll
    for (int k1 = 0; k1 < 4; ++k1) dft4<DIR>(v[4 * k1], v[4 * k1 + 1], v[4 * k1 + 2], v[4 * k1 + 3]);
    float2 t[16];
#pragma unroll
    for (int k1 = 0; k1 < 4; ++k1)
#pragma unroll
        for (int k2 = 0; k2 < 4; ++k2) t[k1 + 4 * k2] = v[4 * k1 + k2];
#pragma unroll
    for (int i = 0; i < 16; ++i) v[i] = t[i];
}
template <int DIR> __device__ __forceinline__ void dft8(float2 (&v)[8]) {
    constexpr float R = 0.70710678118654752440f;
    dft4<DIR>(v[0], v[2], v[4], v[6]);
    dft4<DIR>(v[1], v[3], v[5], v[7]);
    v[3] = cmuld<DIR>(v[3], make_float2(R, -R));
    v[5] = rot90<DIR>(v[5]);
    v[7] = cmuld<DIR>(v[7], make_float2(-R, -R));
    float2 t[8];
#pragma unroll
    for (int k1 = 0; k1 < 4; ++k1) { t[k1] = cadd(v[2 * k1], v[2 * k1 + 1]); t[k1 + 4] = csub(v[2 * k1], v[2 * k1 + 1]); }
#pragma unroll
    for (int i = 0; i < 8; ++i) v[i] = t[i];
}

constexpr int KP = 514;
constexpr size_t OFF_X1T = 0;
constexpr size_t OFF_WT = OFF_X1T + (size_t)KP * S_ * 8;
constexpr size_t OFF_G = OFF_WT + (size_t)KP * S_ * 4;
constexpr size_t OFF_GRAN = OFF_G + 2 * S_ * 4;
constexpr size_t OFF_FLAG = OFF_GRAN + (size_t)16 * P_ * 2 * 8;
constexpr size_t OFF_A2 = OFF_FLAG + 4096;
constexpr size_t OFF_GA = OFF_A2 + (size_t)S_ * 4096 * 2;
constexpr size_t WS_END = OFF_GA + (size_t)S_ * D_ * 2;
#define TS(slot, i)
#define TSN(slot, i)

constexpr int RW = 1128;
__device__ __forceinline__ void k1_body(const u16* __restrict__ x, const u16* __restrict__ gate_w,
                                        const u16* __restrict__ w_r, const u16* __restrict__ w_i,
                                        const u16* __restrict__ Bre, const u16* __restrict__ Bim,
                                        const u16* __restrict__ Cre, const u16* __restrict__ Cim,
                                        char* __restrict__ ws) {
    __shared__ __attribute__((aligned(16))) float2 lds[4 * RW];
    __shared__ __attribute__((aligned(16))) uint4 wst[2][516];
    __shared__ __attribute__((aligned(16))) float4 ost[KF * 4 + KF / 8 + 4];
    __shared__ unsigned auxcnt;
    const int tid = threadIdx.x, bid = blockIdx.x;
    if (tid == 0) auxcnt = 0u;
    const int w = __builtin_amdgcn_readfirstlane(tid >> 6), t = tid & 63;
    const int r0 = bid + 512 * (w & 3);
    if (w == 0) TSN(0, 0);
    uint2 xa[4], xb[4];
#pragma unroll
    for (int a = 0; a < 4; ++a) {
        xa[a] = *reinterpret_cast<const uint2*>(x + (size_t)r0 * D_ + 256 * a + 4 * t);
        xb[a] = *reinterpret_cast<const uint2*>(x + (size_t)(r0 + 256) * D_ + 256 * a + 4 * t);
    }
    float2 twr[8];
#pragma unroll
    for (int j = 1; j < 8; ++j) twr[j] = tw(bid * j);
    __syncthreads();
    if (w >= 4) {
        const int at = tid - 256;
        uint2 gw[4];
#pragma unroll
        for (int a = 0; a < 4; ++a) gw[a] = *reinterpret_cast<const uint2*>(gate_w + 256 * a + 4 * t);
        const uint4* wr4 = reinterpret_cast<const uint4*>(w_r + (size_t)bid * 8 * KF);
        const uint4* wi4 = reinterpret_cast<const uint4*>(w_i + (size_t)bid * 8 * KF);
        uint4 wq[4];
        wq[0] = wr4[at]; wq[1] = wr4[at + 256]; wq[2] = wi4[at]; wq[3] = wi4[at + 256];
        uint4 wq4 = make_uint4(0, 0, 0, 0);
        if (at < 2) wq4 = (at == 0 ? wr4 : wi4)[512];
        uint4 pf[8];
#pragma unroll
        for (int i = 0; i < 2; ++i) {
            const size_t o = ((size_t)bid * 512 + i * 256 + at) * 8;
            pf[4 * i + 0] = *reinterpret_cast<const uint4*>(Bre + o);
            pf[4 * i + 1] = *reinterpret_cast<const uint4*>(Bim + o);
            pf[4 * i + 2] = *reinterpret_cast<const uint4*>(Cre + o);
            pf[4 * i + 3] = *reinterpret_cast<const uint4*>(Cim + o);
        }
        if (at < 128) reinterpret_cast<u64*>(ws + OFF_GRAN)[bid * 128 + at] = 0ull;
        else if (at < 132) reinterpret_cast<unsigned*>(ws + OFF_FLAG)[bid * 4 + at - 128] = 0u;
        float s0 = 0.f, s1 = 0.f;
#pragma unroll
        for (int a = 0; a < 4; ++a) {
            const unsigned ua[2] = {xa[a].x, xa[a].y}, ub[2] = {xb[a].x, xb[a].y}, ug[2] = {gw[a].x, gw[a].y};
#pragma unroll
            for (int e = 0; e < 2; ++e) {
                const float g0 = __uint_as_float(ug[e] << 16), g1 = __uint_as_float(ug[e] & 0xffff0000u);
                s0 += bfr(__uint_as_float(ua[e] << 16) * g0); s0 += bfr(__uint_as_float(ua[e] & 0xffff0000u) * g1);
                s1 += bfr(__uint_as_float(ub[e] << 16) * g0); s1 += bfr(__uint_as_float(ub[e] & 0xffff0000u) * g1);
            }
        }
#pragma unroll
        for (int m = 32; m >= 1; m >>= 1) { s0 += __shfl_xor(s0, m); s1 += __shfl_xor(s1, m); }
        if (t < 2) {
            float sv = bfr(t == 0 ? s0 : s1);
            float g = bfr(1.0f / (1.0f + expf(-sv)));
            float* G = reinterpret_cast<float*>(ws + OFF_G);
            G[r0 + 256 * t] = g;
            G[S_ + r0 + 256 * t] = bfr(1.0f - g);
        }
        wst[0][at] = wq[0]; wst[0][at + 256] = wq[1]; wst[1][at] = wq[2]; wst[1][at + 256] = wq[3];
        if (at < 2) wst[at][512] = wq4;
        grp_barrier(&auxcnt, 4u);
        {
            unsigned* WT = reinterpret_cast<unsigned*>(ws + OFF_WT);
            const u16* sr = reinterpret_cast<const u16*>(&wst[0][0]);
            const u16* si = reinterpret_cast<const u16*>(&wst[1][0]);
#pragma unroll
            for (int it = 0; it < 3; ++it) {
                const int k = at + it * 256;
                if (k < KF) {
                    unsigned q[8];
#pragma unroll
                    for (int r = 0; r < 8; ++r) q[r] = (unsigned)sr[r * KF + k] | ((unsigned)si[r * KF + k] << 16);
                    uint4* dst = reinterpret_cast<uint4*>(WT + (size_t)k * S_ + bid * 8);
                    st16(dst, make_uint4(q[0], q[1], q[2], q[3]));
                    st16(dst + 1, make_uint4(q[4], q[5], q[6], q[7]));
                }
            }
        }
#pragma unroll
        for (int i = 0; i < 8; ++i) asm volatile("" :: "v"(pf[i].x));
    } else {
        float2* buf = lds + w * RW;
        float2 twa[4];
#pragma unroll
        for (int e = 0; e < 4; ++e) twa[e] = tw(2 * (4 * t + e));
        const float2 twb = tw(8 * (t & 15));
        float2 v[16];
#pragma unroll
        for (int a = 0; a < 4; ++a) {
            v[4 * a + 0] = make_float2(__uint_as_float(xa[a].x << 16), __uint_as_float(xb[a].x << 16));
            v[4 * a + 1] = make_float2(__uint_as_float(xa[a].x & 0xffff0000u), __uint_as_float(xb[a].x & 0xffff0000u));
            v[4 * a + 2] = make_float2(__uint_as_float(xa[a].y << 16), __uint_as_float(xb[a].y << 16));
            v[4 * a + 3] = make_float2(__uint_as_float(xa[a].y & 0xffff0000u), __uint_as_float(xb[a].y & 0xffff0000u));
        }
        if (w == 0) TS(0, 1);
#pragma unroll
        for (int e = 0; e < 4; ++e) {
            dft4<-1>(v[e], v[4 + e], v[8 + e], v[12 + e]);
            float2 w1 = twa[e], w2 = cmul(w1, w1), w3 = cmul(w2, w1);
            v[4 + e] = cmul(v[4 + e], w1);
            v[8 + e] = cmul(v[8 + e], w2);
            v[12 + e] = cmul(v[12 + e], w3);
        }
#pragma unroll
        for (int ka = 0; ka < 4; ++ka)
#pragma unroll
            for (int e = 0; e < 4; ++e) buf[ka * 272 + 4 * t + e] = v[4 * ka + e];
        WAVE_SYNC();
        {
            const int ka = t >> 4, d = t & 15;
#pragma unroll
            for (int c = 0; c < 16; ++c) v[c] = buf[ka * 272 + 16 * c + d];
            dft16<-1>(v);
            twiddle16<-1, false>(v, twb, twb);
            WAVE_SYNC();
#pragma unroll
            for (int kc = 0; kc < 16; ++kc) buf[ka * 280 + kc * 17 + d] = v[kc];
        }
        WAVE_SYNC();
        {
            const int ka = t & 3, kc = t >> 2;
#pragma unroll
            for (int d = 0; d < 16; ++d) v[d] = buf[ka * 280 + kc * 17 + d];
            dft16<-1>(v);
            WAVE_SYNC();
#pragma unroll
            for (int kd = 0; kd < 16; ++kd) buf[t + 64 * kd] = v[kd];
        }
        if (w == 0) TS(0, 2);
    }
    __syncthreads();
    float2* X1T = reinterpret_cast<float2*>(ws + OFF_X1T);
    {
        const int k = tid;
        float2 u[8];
#pragma unroll
        for (int ww = 0; ww < 4; ++ww) {
            const float2 zk = lds[ww * RW + k], zn = lds[ww * RW + (k == 0 ? 512 : 1024 - k)];
            u[2 * ww] = k == 0 ? make_float2(zk.x, zn.x) : make_float2(0.5f * (zk.x + zn.x), 0.5f * (zk.y - zn.y));
            u[2 * ww + 1] = k == 0 ? make_float2(zk.y, zn.y) : make_float2(0.5f * (zk.y + zn.y), 0.5f * (zn.x - zk.x));
        }
        dft8<-1>(u);
#pragma unroll
        for (int j = 1; j < 8; ++j) u[j] = cmul(u[j], twr[j]);
#pragma unroll
        for (int j = 0; j < 4; ++j) ost[k * 4 + j + (k >> 3)] = make_float4(u[2 * j].x, u[2 * j].y, u[2 * j + 1].x, u[2 * j + 1].y);
    }
    __syncthreads();
    {
        float4* dst = reinterpret_cast<float4*>(X1T + (size_t)bid * KP * 8);
#pragma unroll
        for (int it = 0; it < 4; ++it) {
            const int i = tid + it * 512;
            st16f(dst + i, ost[i + (i >> 5)]);
        }
    }
    if (w == 0) TS(0, 3);
    if (w == 0) TS(0, 4);
}
NOPK __global__ __launch_bounds__(512) void k1_kernel(const u16* __restrict__ x, const u16* __restrict__ gate_w,
                                                      const u16* __restrict__ w_r, const u16* __restrict__ w_i,
                                                      const u16* __restrict__ Bre, const u16* __restrict__ Bim,
                                                      const u16* __restrict__ Cre, const u16* __restrict__ Cim,
                                                      char* __restrict__ ws) {
    k1_body(x, gate_w, w_r, w_i, Bre, Bim, Cre, Cim, ws);
}

constexpr int CPITCH = 273;
constexpr int CLDS = 16 * CPITCH * 8;
struct FSt { int nb; unsigned ep; unsigned* cnt; };
#define FPACE() do { if (fs.nb < 16) { PACE(); ++fs.nb; } } while (0)
#define FSYNC() do { if (fs.nb < 16) { WG_BARRIER(); ++fs.nb; } else { fs.ep += 4; grp_barrier(fs.cnt, fs.ep); } } while (0)
template <int DIR> __device__ __forceinline__ void dft16f(float2 (&v)[16], FSt& fs) {
    constexpr float C1 = 0.92387953251128675613f, S1 = 0.38268343236508977173f, R = 0.70710678118654752440f;
#pragma unroll
    for (int n2 = 0; n2 < 4; ++n2) dft4<DIR>(v[n2], v[4 + n2], v[8 + n2], v[12 + n2]);
    FENCE16(v); FPACE();
    const float2 w1 = make_float2(C1, -S1), w2 = make_float2(R, -R), w3 = make_float2(S1, -C1);
    const float2 w6 = make_float2(-R, -R), w9 = make_float2(-C1, S1);
    v[5] = cmuld<DIR>(v[5], w1); v[6] = cmuld<DIR>(v[6], w2); v[7] = cmuld<DIR>(v[7], w3);
    v[9] = cmuld<DIR>(v[9], w2); v[10] = rot90<DIR>(v[10]); v[11] = cmuld<DIR>(v[11], w6);
    v[13] = cmuld<DIR>(v[13], w3); v[14] = cmuld<DIR>(v[14], w6); v[15] = cmuld<DIR>(v[15], w9);
    FENCE16(v); FPACE();
#pragma unroll
    for (int k1 = 0; k1 < 4; ++k1) dft4<DIR>(v[4 * k1], v[4 * k1 + 1], v[4 * k1 + 2], v[4 * k1 + 3]);
    float2 t[16];
#pragma unroll
    for (int k1 = 0; k1 < 4; ++k1)
#pragma unroll
        for (int k2 = 0; k2 < 4; ++k2) t[k1 + 4 * k2] = v[4 * k1 + k2];
#pragma unroll
    for (int i = 0; i < 16; ++i) v[i] = t[i];
}
template <int DIR> __device__ __forceinline__ void twiddle16f(float2 (&v)[16], float2 w, FSt& fs) {
    asm volatile("" : "+v"(w.x), "+v"(w.y));
    const float2 w2 = cmul(w, w), w3 = cmul(w2, w), w4 = cmul(w2, w2), w8 = cmul(w4, w4);
#pragma unroll
    for (int q = 0; q < 4; ++q) {
        const float2 b = q == 0 ? make_float2(1.f, 0.f) : q == 1 ? w4 : q == 2 ? w8 : cmul(w8, w4);
        if (q > 0) v[4 * q] = cmuld<DIR>(v[4 * q], b);
        v[4 * q + 1] = cmuld<DIR>(v[4 * q + 1], q > 0 ? cmul(b, w) : w);
        v[4 * q + 2] = cmuld<DIR>(v[4 * q + 2], q > 0 ? cmul(b, w2) : w2);
        v[4 * q + 3] = cmuld<DIR>(v[4 * q + 3], q > 0 ? cmul(b, w3) : w3);
        if (q == 1) { FENCE16(v); FPACE(); }
    }
}
__device__ __forceinline__ void colfft_cols(const bool PACKED, char* __restrict__ ws, float2* gbuf, float2* ex, int k0, int b, int hj, float2 twb, int npre, FSt& fs) {
    float2* col = reinterpret_cast<float2*>(ws + OFF_X1T) + (size_t)k0 * 8 + hj;
    const unsigned* wt = reinterpret_cast<const unsigned*>(ws + OFF_WT) + (size_t)(k0 + (hj >> 3)) * S_ + (hj & 7) + 8 * b;
    FPACE();
    float2 v[16];
#pragma unroll
    for (int a = 0; a < 16; ++a) v[a] = col[(size_t)(b + 16 * a) * (KP * 8)];
    unsigned wv[16];
#pragma unroll
    for (int kd = 0; kd < 16; ++kd) wv[kd] = wt[128 * kd];
    for (int i = 0; i < npre; ++i) FPACE();
    dft16f<-1>(v, fs);
    FENCE16(v); FPACE();
    twiddle16f<-1>(v, twb, fs);
    FENCE16(v); FPACE();
#pragma unroll
    for (int kc = 0; kc < 16; ++kc) gbuf[kc * 17 + b] = v[kc];
    FSYNC();
#pragma unroll
    for (int d = 0; d < 16; ++d) v[d] = gbuf[b * 17 + d];
    dft16f<-1>(v, fs);
    FENCE16(v);
    FPACE();
    if (PACKED) {
        FSYNC();
        if (hj < 8) {
#pragma unroll
            for (int kd = 0; kd < 16; ++kd) ex[hj + 8 * b + 128 * kd] = v[kd];
        }
        FSYNC();
        const unsigned* w0 = reinterpret_cast<const unsigned*>(ws + OFF_WT);
        const unsigned* w512 = w0 + (size_t)512 * S_;
#pragma unroll
        for (int g4 = 0; g4 < 4; ++g4) {
            if (hj < 8) {
                unsigned a1[4], b0[4], b1[4];
#pragma unroll
                for (int q = 0; q < 4; ++q) {
                    const int ks = hj + 8 * b + 128 * (4 * g4 + q), kn = (S_ - ks) & (S_ - 1);
                    a1[q] = w0[kn]; b0[q] = w512[ks]; b1[q] = w512[kn];
                }
#pragma unroll
                for (int q = 0; q < 4; ++q) {
                    const int kd = 4 * g4 + q;
                    const int ks = hj + 8 * b + 128 * kd, kn = (S_ - ks) & (S_ - 1);
                    const float2 c = v[kd], n = ex[kn];
                    const float2 f0 = make_float2(0.5f * (c.x + n.x), 0.5f * (c.y - n.y));
                    const float2 f1 = make_float2(0.5f * (c.y + n.y), 0.5f * (n.x - c.x));
                    const unsigned a0 = wv[kd];
                    const float2 h0 = make_float2(0.5f * (__uint_as_float(a0 << 16) + __uint_as_float(a1[q] << 16)),
                                                  0.5f * (__uint_as_float(a0 & 0xffff0000u) - __uint_as_float(a1[q] & 0xffff0000u)));
                    const float2 h1 = make_float2(0.5f * (__uint_as_float(b0[q] << 16) + __uint_as_float(b1[q] << 16)),
                                                  0.5f * (__uint_as_float(b0[q] & 0xffff0000u) - __uint_as_float(b1[q] & 0xffff0000u)));
                    const float2 z0 = cmul(f0, h0), z1 = cmul(f1, h1);
                    v[kd] = make_float2(z0.x - z1.y, z0.y + z1.x);
                }
            } else {
#pragma unroll
                for (int q = 0; q < 4; ++q) {
                    const int kd = 4 * g4 + q;
                    v[kd] = cmul(v[kd], make_float2(__uint_as_float(wv[kd] << 16), __uint_as_float(wv[kd] & 0xffff0000u)));
                }
            }
            FENCE16(v); FPACE();
        }
        FSYNC();
    } else {
#pragma unroll
        for (int kd = 0; kd < 16; ++kd) v[kd] = cmul(v[kd], make_float2(__uint_as_float(wv[kd] << 16), __uint_as_float(wv[kd] & 0xffff0000u)));
    }
    FENCE16(v); FPACE();
    dft16f<1>(v, fs);
    FENCE16(v); FPACE();
    twiddle16f<1>(v, twb, fs);
    FENCE16(v); FPACE();
#pragma unroll
    for (int d = 0; d < 16; ++d) gbuf[b * 17 + d] = v[d];
    FSYNC();
#pragma unroll
    for (int kc = 0; kc < 16; ++kc) v[kc] = gbuf[kc * 17 + b];
    dft16f<1>(v, fs);
    FENCE16(v); FPACE();
    const float sc = 1.0f / (float)(S_ * D_);
#pragma unroll
    for (int a = 0; a < 16; ++a) col[(size_t)(b + 16 * a) * (KP * 8)] = make_float2(v[a].x * sc, v[a].y * sc);
}
__device__ __forceinline__ void colfft_role(char* __restrict__ ws, char* smem, unsigned* fcnt, int gb, int ftid) {
    const int b = ftid >> 4, hj = ftid & 15;
    float2* gbuf = reinterpret_cast<float2*>(smem) + hj * CPITCH;
    const float2 twb = tw(8 * b);
    if (ftid < 64) TSN(2, 0);
    float2* ex = reinterpret_cast<float2*>(smem);
    FSt fs; fs.nb = 0; fs.ep = 0; fs.cnt = fcnt;
    colfft_cols(gb == 255, ws, gbuf, ex, ((gb + 1) & 255) * 2, b, hj, twb, gb == 255 ? 6 : 2, fs);
    if (ftid < 64) TS(2, 1);
}

#define WAIT_VM(n) asm volatile("s_waitcnt vmcnt(" #n ")" ::: "memory")
#define SCHED() __builtin_amdgcn_sched_barrier(0)
#define BAR() do { __builtin_amdgcn_s_barrier(); asm volatile("" ::: "memory"); } while (0)
#define MF(a, b, c) c = __builtin_amdgcn_mfma_f32_16x16x32_bf16(a, b, c, 0, 0, 0)
__device__ __forceinline__ void glds16(const u16* g, char* l) {
    __builtin_amdgcn_global_load_lds((const GAS unsigned*)g, (__attribute__((address_space(3))) unsigned*)l, 16, 0, 0);
}
__device__ __forceinline__ bf16x8 ldfrag(const char* tile, int row, int chunk) {
    return *reinterpret_cast<const bf16x8*>(tile + row * 128 + ((chunk ^ (row & 7)) << 4));
}
#define HILO 0
constexpr int G_NST = 3;
constexpr int G_STAGE = 32768;
constexpr int G_LDS = G_NST * G_STAGE;
constexpr int G2_NST = HILO ? 3 : 4;
constexpr int G2_BOFF = HILO ? 16384 : 8192;
constexpr int G2_STAGE = HILO ? 32768 : 24576;

__device__ __forceinline__ void gemm1_tile(int gb, int& lt, int& pt) {
    const int xcd = gb & 7, loc = gb >> 3;
    lt = (xcd >> 1) * 4 + (loc >> 3);
    pt = (xcd & 1) * 8 + (loc & 7);
}
constexpr int SPITCH = 132;
__device__ __forceinline__ void ssm_params(const u16* __restrict__ lam_re, const u16* __restrict__ lam_im,
                                           const u16* __restrict__ log_dt, int p, float2& lam, float2& coef) {
    float lr = bf2f(lam_re[p]), li = bf2f(lam_im[p]);
    float dt = bfr(expf(bf2f(log_dt[p])));
    float er = expf(dt * lr), sn, cs;
    sincosf(dt * li, &sn, &cs);
    lam = make_float2(er * cs, er * sn);
    float nr = lam.x - 1.0f, ni = lam.y, den = lr * lr + li * li;
    coef = make_float2((nr * lr + ni * li) / den, (ni * lr - nr * li) / den);
}
__device__ __forceinline__ void scan_epilogue(char* __restrict__ ws, char* smem, int lt, int pt, int gt, float2 lam, float2 coef,
                                              unsigned* gcnt, unsigned gep) {
    const int l0 = lt * 128, p0 = pt * 64;
    const int ch = gt & 63, seg = gt >> 6;
    const float* ST = reinterpret_cast<const float*>(smem);
    float2* segagg = reinterpret_cast<float2*>(smem + 128 * SPITCH * 4);
    float2* part = segagg + 512;
    float2 lam16 = lam;
#pragma unroll
    for (int i = 0; i < 4; ++i) lam16 = cmul(lam16, lam16);
    float hr[16], hi_[16];
    {
        const float* pr = ST + ch * SPITCH + seg * 16;
        const float* pi = pr + 64 * SPITCH;
        float2 h = make_float2(0.f, 0.f);
#pragma unroll
        for (int i4 = 0; i4 < 4; ++i4) {
            const f32x4 a = *reinterpret_cast<const f32x4*>(pr + 4 * i4), b = *reinterpret_cast<const f32x4*>(pi + 4 * i4);
#pragma unroll
            for (int j = 0; j < 4; ++j) {
                float2 bu = cmul(coef, make_float2(a[j], b[j]));
                h = cadd(cmul(lam, h), bu);
                hr[4 * i4 + j] = h.x; hi_[4 * i4 + j] = h.y;
            }
        }
        segagg[seg * 64 + ch] = h;
    }
    gep += 8; grp_barrier(gcnt, gep);
    if (gt < 64) TSN(1, 2);
    GAS u64* gran = (GAS u64*)(ws + OFF_GRAN);
    if (seg == 0) {
        float2 ag = segagg[ch];
#pragma unroll
        for (int s = 1; s < 8; ++s) ag = cadd(cmul(lam16, ag), segagg[s * 64 + ch]);
        GAS u64* gp = gran + ((size_t)lt * P_ + p0 + ch) * 2;
        __hip_atomic_store(gp, (1ull << 32) | (u64)__float_as_uint(ag.x), RLX_AGENT);
        __hip_atomic_store(gp + 1, (1ull << 32) | (u64)__float_as_uint(ag.y), RLX_AGENT);
    }
    float pwr[16], pwi[16];
    {
        float2 c = make_float2(0.f, 0.f), pw = make_float2(1.f, 0.f);
        for (int s = 0; s < seg; ++s) { c = cadd(cmul(lam16, c), segagg[s * 64 + ch]); pw = cmul(pw, lam16); }
#pragma unroll
        for (int i = 0; i < 16; ++i) {
            c = cmul(lam, c); pw = cmul(lam, pw);
            hr[i] += c.x; hi_[i] += c.y; pwr[i] = pw.x; pwi[i] = pw.y;
        }
    }
    {
        float2 lam128 = cmul(lam16, lam16); lam128 = cmul(lam128, lam128); lam128 = cmul(lam128, lam128);
        float2 lam1024 = cmul(lam128, lam128); lam1024 = cmul(lam1024, lam1024); lam1024 = cmul(lam1024, lam1024);
        u64 ga[2], gb_[2];
        for (int spin = 0; spin < (1 << 22); ++spin) {
            bool ok = true;
#pragma unroll
            for (int q = 0; q < 2; ++q) {
                const int cc = lt - 1 - seg - 8 * q;
                ga[q] = 1ull << 32; gb_[q] = 1ull << 32;
                if (cc >= 0) {
                    GAS u64* gq = gran + ((size_t)cc * P_ + p0 + ch) * 2;
                    ga[q] = __hip_atomic_load(gq, RLX_AGENT);
                    gb_[q] = __hip_atomic_load(gq + 1, RLX_AGENT);
                }
            }
#pragma unroll
            for (int q = 0; q < 2; ++q) ok = ok && (ga[q] >> 32) == 1ull && (gb_[q] >> 32) == 1ull;
            if (__all(ok)) break;
            __builtin_amdgcn_s_sleep(1);
        }
        float2 c = make_float2(__uint_as_float((unsigned)ga[1]), __uint_as_float((unsigned)gb_[1]));
        c = cadd(cmul(lam1024, c), make_float2(__uint_as_float((unsigned)ga[0]), __uint_as_float((unsigned)gb_[0])));
        for (int s = 0; s < seg; ++s) c = cmul(c, lam128);
        part[seg * 64 + ch] = c;
    }
    gep += 8; grp_barrier(gcnt, gep);
    if (gt < 64) TSN(1, 3);
    unsigned* O32 = reinterpret_cast<unsigned*>(smem);
    {
        float2 c = make_float2(0.f, 0.f);
#pragma unroll
        for (int s = 0; s < 8; ++s) c = cadd(c, part[s * 64 + ch]);
        if (HILO) {
            unsigned* op = O32 + (seg * 16) * SPITCH + ch;
#pragma unroll
            for (int i = 0; i < 16; ++i) {
                const float2 d = cmul(make_float2(pwr[i], pwi[i]), c);
                const float a = hr[i] + d.x, b = -(hi_[i] + d.y);
                const u16 ah = f2bf(a), bh = f2bf(b);
                const u16 al = f2bf(a - bf2f(ah)), bl = f2bf(b - bf2f(bh));
                op[i * SPITCH] = (unsigned)ah | ((unsigned)al << 16);
                op[i * SPITCH + 64] = (unsigned)bh | ((unsigned)bl << 16);
            }
        } else {
            u16* op = reinterpret_cast<u16*>(smem) + (seg * 16) * 136 + ch;
#pragma unroll
            for (int i = 0; i < 16; ++i) {
                const float2 d = cmul(make_float2(pwr[i], pwi[i]), c);
                op[i * 136] = f2bf(hr[i] + d.x);
                op[i * 136 + 64] = f2bf(-(hi_[i] + d.y));
            }
        }
    }
    gep += 8; grp_barrier(gcnt, gep);
    if (gt < 64) TSN(1, 4);
    u16* A2 = reinterpret_cast<u16*>(ws + OFF_A2);
#pragma unroll
    for (int it = 0; it < 4; ++it) {
        const int idx = it * 512 + gt;
        const int row = idx >> 4, g = idx & 15;
        u16* dst = A2 + (size_t)(l0 + row) * 4096 + (g < 8 ? 0 : 1024) + p0 + (g & 7) * 8;
        if (HILO) {
            const unsigned* sp = O32 + row * SPITCH + g * 8;
            const uint4 f0 = *reinterpret_cast<const uint4*>(sp), f1 = *reinterpret_cast<const uint4*>(sp + 4);
            uint4 hi, lo;
            hi.x = __builtin_amdgcn_perm(f0.y, f0.x, 0x05040100u); lo.x = __builtin_amdgcn_perm(f0.y, f0.x, 0x07060302u);
            hi.y = __builtin_amdgcn_perm(f0.w, f0.z, 0x05040100u); lo.y = __builtin_amdgcn_perm(f0.w, f0.z, 0x07060302u);
            hi.z = __builtin_amdgcn_perm(f1.y, f1.x, 0x05040100u); lo.z = __builtin_amdgcn_perm(f1.y, f1.x, 0x07060302u);
            hi.w = __builtin_amdgcn_perm(f1.w, f1.z, 0x05040100u); lo.w = __builtin_amdgcn_perm(f1.w, f1.z, 0x07060302u);
            st16(dst, hi);
            st16(dst + 2048, lo);
        } else {
            st16(dst, *reinterpret_cast<const uint4*>(reinterpret_cast<const u16*>(smem) + row * 136 + g * 8));
        }
    }
    if (gt < 64) TS(1, 5);
}
__device__ __forceinline__ void gemm1_loader(const u16* __restrict__ x, const u16* __restrict__ Bre, const u16* __restrict__ Bim,
                                             const u16* __restrict__ lam_re, const u16* __restrict__ lam_im, const u16* __restrict__ log_dt,
                                             char* __restrict__ ws, char* smem, unsigned* gcnt, int gb, int lw, int lane) {
    unsigned gep = 0;
    int lt, pt; gemm1_tile(gb, lt, pt);
    const int l0 = lt * 128, p0 = pt * 64;
    const int srow = lane >> 3, schunk = (lane & 7) ^ srow;
    const u16* gA = x + (size_t)(l0 + 32 * lw + srow) * D_ + schunk * 8;
    const u16* gB = (lw < 2 ? Bre : Bim) + (size_t)(p0 + (32 * lw & 63) + srow) * D_ + schunk * 8;
    auto stage = [&](int kt, int sb) {
        char* sA = smem + sb * G_STAGE + (32 * lw) * 128 + lane * 16;
#pragma unroll
        for (int q = 0; q < 4; ++q) glds16(gA + (size_t)(8 * q) * D_ + kt * 64, sA + q * 1024);
#pragma unroll
        for (int q = 0; q < 4; ++q) glds16(gB + (size_t)(8 * q) * D_ + kt * 64, sA + 16384 + q * 1024);
    };
    constexpr int NT = D_ / 64;
    stage(0, 0); stage(1, 1);
    int sb = 0;
    for (int kt = 0; kt < NT; ++kt) {
        if (kt + 1 < NT) WAIT_VM(8); else WAIT_VM(0);
        BAR();
        int nb = sb + 2; if (nb >= G_NST) nb -= G_NST;
        if (kt + 2 < NT) stage(kt + 2, nb);
        sb = sb + 1 == G_NST ? 0 : sb + 1;
    }
    float2 lam, coef;
    ssm_params(lam_re, lam_im, log_dt, p0 + lane, lam, coef);
    gep += 8; grp_barrier(gcnt, gep);
    gep += 8; grp_barrier(gcnt, gep);
    scan_epilogue(ws, smem, lt, pt, 256 + lw * 64 + lane, lam, coef, gcnt, gep);
}
__device__ __forceinline__ void gemm1_role(const u16* __restrict__ lam_re, const u16* __restrict__ lam_im,
                                           const u16* __restrict__ log_dt, char* __restrict__ ws, char* smem, unsigned* gcnt, int gb, int tid) {
    unsigned gep = 0;
    const int wid = tid >> 6, lane = tid & 63;
    int lt, pt; gemm1_tile(gb, lt, pt);
    const int wr = wid >> 1, wc = wid & 1, fr = lane & 15, fq = lane >> 4;
    if (wid == 0) TSN(1, 0);
    float2 lam, coef;
    ssm_params(lam_re, lam_im, log_dt, pt * 64 + lane, lam, coef);
    f32x4 acc[4][4];
#pragma unroll
    for (int m = 0; m < 4; ++m)
#pragma unroll
        for (int n = 0; n < 4; ++n) acc[m][n] = (f32x4){0.f, 0.f, 0.f, 0.f};
    constexpr int NT = D_ / 64;
    bf16x8 af[2][4], bf_[2][4];
#pragma unroll
    for (int m = 0; m < 4; ++m) { af[1][m] = (bf16x8){0, 0, 0, 0, 0, 0, 0, 0}; bf_[1][m] = af[1][m]; }
#pragma unroll 8
    for (int kt = 0; kt < NT; ++kt) {
        BAR();
        if (wid == 0) TSN(5, kt);
        const char* tA = smem + (kt % G_NST) * G_STAGE;
        const char* tB = tA + 16384;
#pragma unroll
        for (int m = 0; m < 4; ++m) af[0][m] = ldfrag(tA, wr * 64 + m * 16 + fr, fq);
#pragma unroll
        for (int n = 0; n < 4; ++n) bf_[0][n] = ldfrag(tB, wc * 64 + n * 16 + fr, fq);
        SCHED();
#pragma unroll
        for (int m = 0; m < 4; ++m)
#pragma unroll
            for (int n = 0; n < 4; ++n) MF(af[1][m], bf_[1][n], acc[m][n]);
        SCHED();
#pragma unroll
        for (int m = 0; m < 2; ++m)
#pragma unroll
            for (int n = 0; n < 4; ++n) MF(af[0][m], bf_[0][n], acc[m][n]);
        SCHED();
#pragma unroll
        for (int m = 0; m < 4; ++m) af[1][m] = ldfrag(tA, wr * 64 + m * 16 + fr, 4 + fq);
#pragma unroll
        for (int n = 0; n < 4; ++n) bf_[1][n] = ldfrag(tB, wc * 64 + n * 16 + fr, 4 + fq);
        SCHED();
#pragma unroll
        for (int m = 2; m < 4; ++m)
#pragma unroll
            for (int n = 0; n < 4; ++n) MF(af[0][m], bf_[0][n], acc[m][n]);
        SCHED();
        LDS_WAIT();
    }
#pragma unroll
    for (int m = 0; m < 4; ++m)
#pragma unroll
        for (int n = 0; n < 4; ++n) MF(af[1][m], bf_[1][n], acc[m][n]);
    if (wid == 0) TSN(1, 1);
    gep += 8; grp_barrier(gcnt, gep);
    float* ST = reinterpret_cast<float*>(smem);
#pragma unroll
    for (int m = 0; m < 4; ++m)
#pragma unroll
        for (int n = 0; n < 4; ++n)
            *reinterpret_cast<f32x4*>(ST + (wc * 64 + n * 16 + fr) * SPITCH + wr * 64 + m * 16 + fq * 4) = acc[m][n];
    gep += 8; grp_barrier(gcnt, gep);
    scan_epilogue(ws, smem, lt, pt, tid, lam, coef, gcnt, gep);
}

constexpr int K2_SMEM = G_LDS + CLDS + 64;
NOPK __global__ __launch_bounds__(768, 1) void k2_kernel(const u16* __restrict__ x, const u16* __restrict__ Bre, const u16* __restrict__ Bim,
                                                    const u16* __restrict__ lam_re, const u16* __restrict__ lam_im,
                                                    const u16* __restrict__ log_dt, char* __restrict__ ws) {
    extern __shared__ __attribute__((aligned(16))) char smem[];
    const int role = __builtin_amdgcn_readfirstlane(threadIdx.x >> 8);
    unsigned* cnts = reinterpret_cast<unsigned*>(smem + G_LDS + CLDS);
    if (threadIdx.x == 0) { cnts[0] = 0u; cnts[8] = 0u; LDS_WAIT(); }
    if (role == 0) gemm1_role(lam_re, lam_im, log_dt, ws, smem, cnts, blockIdx.x, threadIdx.x);
    else if (role == 1) gemm1_loader(x, Bre, Bim, lam_re, lam_im, log_dt, ws, smem, cnts, blockIdx.x, __builtin_amdgcn_readfirstlane((threadIdx.x >> 6) & 3), threadIdx.x & 63);
    else {
        colfft_role(ws, smem + G_LDS, cnts + 8, blockIdx.x, threadIdx.x - 512);
    }
}

__device__ __forceinline__ void rowifft_role(char* __restrict__ ws, char* smem, int bid, int tid) {
    float2* lds = reinterpret_cast<float2*>(smem);
    const float2* Z1T = reinterpret_cast<const float2*>(ws + OFF_X1T);
    const int w = tid >> 6, t = tid & 63;
    if (w == 0) TSN(4, 0);
    PACE();
    {
        const u16* src = reinterpret_cast<const u16*>(Z1T + (size_t)bid * KP * 8);
#pragma unroll
        for (int i = 0; i < 8; ++i) {
            const int piece = w + 4 * i;
            glds16(src + (size_t)piece * 512 + t * 8, smem + piece * 1024 + t * 16);
        }
    }
    float2 twr[8];
#pragma unroll
    for (int j = 1; j < 8; ++j) twr[j] = tw(bid * j);
    const float2 twc = tw(8 * (t >> 2));
    const float2 twb = tw(32 * (t >> 4));
    const float2 twb0 = tw(2 * (t & 15) * (t >> 4));
    const int r0 = bid + 512 * w;
    const float* G = reinterpret_cast<const float*>(ws + OFF_G);
    const float g0 = G[r0], g1 = G[r0 + 256];
    PACE(); PACE();
    asm volatile("s_waitcnt vmcnt(0)" ::: "memory");
    WG_BARRIER();
    float4 q[2][4];
#pragma unroll
    for (int it = 0; it < 2; ++it) {
        const float4* src = reinterpret_cast<const float4*>(smem) + (tid + it * 256) * 4;
#pragma unroll
        for (int j = 0; j < 4; ++j) q[it][j] = src[j];
    }
    WG_BARRIER();
#pragma unroll
    for (int it = 0; it < 2; ++it) {
        const int k = tid + it * 256;
        float2 u[8];
#pragma unroll
        for (int j = 0; j < 4; ++j) { u[2 * j] = make_float2(q[it][j].x, q[it][j].y); u[2 * j + 1] = make_float2(q[it][j].z, q[it][j].w); }
#pragma unroll
        for (int j = 1; j < 8; ++j) u[j] = cmulc(u[j], twr[j]);
        dft8<1>(u);
#pragma unroll
        for (int j = 0; j < 8; ++j) asm volatile("" : "+v"(u[j].x), "+v"(u[j].y));
        PACE();
#pragma unroll
        for (int ww = 0; ww < 4; ++ww) {
            const float2 A = u[2 * ww], B = u[2 * ww + 1];
            if (k == 0) {
                lds[ww * RW] = make_float2(A.x, B.x);
                lds[ww * RW + 512] = make_float2(A.y, B.y);
            } else {
                lds[ww * RW + k] = make_float2(A.x - B.y, A.y + B.x);
                lds[ww * RW + 1024 - k] = make_float2(A.x + B.y, B.x - A.y);
            }
        }
        if (it < 1) PACE();
    }
    WG_BARRIER();
    float2* buf = lds + w * RW;
    float2 v[16];
    {
        const int ka = t & 3, kc = t >> 2;
#pragma unroll
        for (int kd = 0; kd < 16; ++kd) v[kd] = buf[t + 64 * kd];
        dft16<1, true>(v);
        WG_BARRIER();
        twiddle16<1, false, true>(v, twc, twc);
        FENCE16(v); PACE();
#pragma unroll
        for (int d = 0; d < 8; ++d) buf[ka * 280 + kc * 17 + d] = v[d];
        PACE();
#pragma unroll
        for (int d = 8; d < 16; ++d) buf[ka * 280 + kc * 17 + d] = v[d];
    }
    WG_BARRIER();
    {
        const int ka = t >> 4, d = t & 15;
#pragma unroll
        for (int kc = 0; kc < 16; ++kc) v[kc] = buf[ka * 280 + kc * 17 + d];
        dft16<1, true>(v);
        WG_BARRIER();
        twiddle16<1, true, true>(v, twb, twb0);
        FENCE16(v); PACE();
#pragma unroll
        for (int c = 0; c < 8; ++c) buf[ka * 272 + 16 * c + d] = v[c];
        PACE();
#pragma unroll
        for (int c = 8; c < 16; ++c) buf[ka * 272 + 16 * c + d] = v[c];
    }
    WG_BARRIER();
#pragma unroll
    for (int ka = 0; ka < 4; ++ka)
#pragma unroll
        for (int e = 0; e < 4; ++e) v[4 * ka + e] = buf[ka * 272 + 4 * t + e];
#pragma unroll
    for (int e = 0; e < 4; ++e) dft4<1>(v[e], v[4 + e], v[8 + e], v[12 + e]);
#pragma unroll
    for (int i = 0; i < 16; ++i) asm volatile("" : "+v"(v[i].x), "+v"(v[i].y));
    PACE();
    GAS u64* GA = (GAS u64*)(ws + OFF_GA);
#pragma unroll
    for (int a = 0; a < 4; ++a) {
        u16 o0[4], o1[4];
#pragma unroll
        for (int e = 0; e < 4; ++e) { o0[e] = f2bf(g0 * bfr(v[4 * a + e].x)); o1[e] = f2bf(g1 * bfr(v[4 * a + e].y)); }
        u64 p0 = (u64)o0[0] | ((u64)o0[1] << 16) | ((u64)o0[2] << 32) | ((u64)o0[3] << 48);
        u64 p1 = (u64)o1[0] | ((u64)o1[1] << 16) | ((u64)o1[2] << 32) | ((u64)o1[3] << 48);
        __hip_atomic_store(GA + ((size_t)r0 * D_ + 256 * a + 4 * t) / 4, p0, RLX_AGENT);
        __hip_atomic_store(GA + ((size_t)(r0 + 256) * D_ + 256 * a + 4 * t) / 4, p1, RLX_AGENT);
    }
    PACE(); PACE(); PACE(); PACE(); PACE();
    asm volatile("s_waitcnt vmcnt(0)" ::: "memory");
    if (t == 0) __hip_atomic_store((GAS unsigned*)(ws + OFF_FLAG) + bid * 4 + w, 1u, RLX_AGENT);
    if (w == 0) TS(4, 1);
}

__device__ __forceinline__ void gemm2_tile(int gb, int& lt, int& dt) {
    const int xcd = gb & 7, loc = gb >> 3;
    lt = xcd * 4 + (loc >> 3);
    dt = loc & 7;
}
__device__ __forceinline__ void gemm2_loader(const u16* __restrict__ Cre, const u16* __restrict__ Cim, char* __restrict__ ws,
                                             char* smem, int gb, int lw, int lane) {
    int lt, dt; gemm2_tile(gb, lt, dt);
    const int l0 = lt * 64, d0 = dt * 128;
    const u16* A2 = reinterpret_cast<const u16*>(ws + OFF_A2);
    const int srow = lane >> 3, schunk = (lane & 7) ^ srow;
    const u16* gA = A2 + (size_t)(l0 + 16 * lw + srow) * 4096 + schunk * 8;
    const size_t offB = (size_t)(d0 + 32 * lw + srow) * P_ + schunk * 8;
    auto stage = [&](int kt, int sb) {
        char* sA = smem + sb * G2_STAGE + (16 * lw) * 128 + lane * 16;
        glds16(gA + kt * 64, sA);
        glds16(gA + (size_t)8 * 4096 + kt * 64, sA + 1024);
        if (HILO) {
            glds16(gA + 2048 + kt * 64, sA + 8192);
            glds16(gA + (size_t)8 * 4096 + 2048 + kt * 64, sA + 8192 + 1024);
        }
        char* sB = smem + sb * G2_STAGE + G2_BOFF + (32 * lw) * 128 + lane * 16;
        const u16* gB = (kt < 16 ? Cre : Cim) + offB + (kt & 15) * 64;
#pragma unroll
        for (int q = 0; q < 4; ++q) glds16(gB + (size_t)(8 * q) * P_, sB + q * 1024);
    };
    constexpr int NT = 32;
    stage(0, 0); stage(1, 1);
    if (!HILO) stage(2, 2);
    int sb = 0;
    for (int kt = 0; kt < NT; ++kt) {
        if (HILO) { if (kt + 1 < NT) WAIT_VM(8); else WAIT_VM(0); }
        else { if (kt + 2 < NT) WAIT_VM(12); else if (kt + 1 < NT) WAIT_VM(6); else WAIT_VM(0); }
        BAR();
        int nb = sb + G2_NST - 1; if (nb >= G2_NST) nb -= G2_NST;
        if (kt + G2_NST - 1 < NT) stage(kt + G2_NST - 1, nb);
        sb = sb + 1 == G2_NST ? 0 : sb + 1;
    }
}
constexpr int OPITCH = 132;
__device__ __forceinline__ void gemm2_role(const u16* __restrict__ x, const u16* __restrict__ Dskip, char* __restrict__ ws,
                                           float* __restrict__ out, char* smem, int gb, int tid) {
    const int wid = tid >> 6, lane = tid & 63;
    int lt, dt; gemm2_tile(gb, lt, dt);
    const int l0 = lt * 64, d0 = dt * 128;
    const int wr = wid >> 1, wc = wid & 1, fr = lane & 15, fq = lane >> 4;
    if (wid == 0) TSN(3, 0);
    const float* G = reinterpret_cast<const float*>(ws + OFF_G);
    const int g8 = tid & 15;
    const uint4 dsk = *reinterpret_cast<const uint4*>(Dskip + d0 + g8 * 8);
    uint4 xv[4]; float omg[4];
#pragma unroll
    for (int it = 0; it < 4; ++it) {
        const int l = l0 + it * 16 + (tid >> 4);
        xv[it] = *reinterpret_cast<const uint4*>(x + (size_t)l * D_ + d0 + g8 * 8);
        omg[it] = G[S_ + l];
    }
    f32x4 acc[2][4];
#pragma unroll
    for (int m = 0; m < 2; ++m)
#pragma unroll
        for (int n = 0; n < 4; ++n) acc[m][n] = (f32x4){0.f, 0.f, 0.f, 0.f};
    constexpr int NT = 32;
    bf16x8 ah[2][2], al[2][2], bf_[2][4];
#pragma unroll
    for (int m = 0; m < 2; ++m) { ah[1][m] = (bf16x8){0, 0, 0, 0, 0, 0, 0, 0}; al[1][m] = ah[1][m]; }
#pragma unroll
    for (int n = 0; n < 4; ++n) bf_[1][n] = ah[1][0];
#pragma unroll 8
    for (int kt = 0; kt < NT; ++kt) {
        BAR();
        if (wid == 0) TSN(6 + (kt >> 4), kt & 15);
        const char* tA = smem + (kt % G2_NST) * G2_STAGE;
        const char* tB = tA + G2_BOFF;
#pragma unroll
        for (int m = 0; m < 2; ++m) { ah[0][m] = ldfrag(tA, wr * 32 + m * 16 + fr, fq); if (HILO) al[0][m] = ldfrag(tA + 8192, wr * 32 + m * 16 + fr, fq); }
#pragma unroll
        for (int n = 0; n < 4; ++n) bf_[0][n] = ldfrag(tB, wc * 64 + n * 16 + fr, fq);
        SCHED();
        if (HILO) {
#pragma unroll
            for (int m = 0; m < 2; ++m)
#pragma unroll
                for (int n = 0; n < 4; ++n) MF(al[1][m], bf_[1][n], acc[m][n]);
        }
#pragma unroll
        for (int m = 0; m < 2; ++m)
#pragma unroll
            for (int n = 0; n < 4; ++n) MF(ah[1][m], bf_[1][n], acc[m][n]);
        SCHED();
#pragma unroll
        for (int m = 0; m < 2; ++m)
#pragma unroll
            for (int n = 0; n < 4; ++n) MF(ah[0][m], bf_[0][n], acc[m][n]);
        SCHED();
#pragma unroll
        for (int m = 0; m < 2; ++m) { ah[1][m] = ldfrag(tA, wr * 32 + m * 16 + fr, 4 + fq); if (HILO) al[1][m] = ldfrag(tA + 8192, wr * 32 + m * 16 + fr, 4 + fq); }
#pragma unroll
        for (int n = 0; n < 4; ++n) bf_[1][n] = ldfrag(tB, wc * 64 + n * 16 + fr, 4 + fq);
        SCHED();
        if (HILO) {
#pragma unroll
            for (int m = 0; m < 2; ++m)
#pragma unroll
                for (int n = 0; n < 4; ++n) MF(al[0][m], bf_[0][n], acc[m][n]);
        }
        SCHED();
        LDS_WAIT();
    }
#pragma unroll
    for (int m = 0; m < 2; ++m)
#pragma unroll
        for (int n = 0; n < 4; ++n) { if (HILO) MF(al[1][m], bf_[1][n], acc[m][n]); MF(ah[1][m], bf_[1][n], acc[m][n]); }
    if (wid == 0) TSN(3, 1);
    WG_BARRIER();
    float* Ox = reinterpret_cast<float*>(smem);
#pragma unroll
    for (int m = 0; m < 2; ++m)
#pragma unroll
        for (int n = 0; n < 4; ++n)
#pragma unroll
            for (int j = 0; j < 4; ++j) Ox[(wr * 32 + m * 16 + fq * 4 + j) * OPITCH + wc * 64 + n * 16 + fr] = acc[m][n][j];
    if (wid == 0) {
        const int l = l0 + lane;
        GAS unsigned* fl = (GAS unsigned*)(ws + OFF_FLAG) + (l & 255) * 4 + (l >> 9);
        for (int spin = 0; spin < (1 << 22); ++spin) {
            if (__hip_atomic_load(fl, RLX_AGENT) == 1u) break;
            __builtin_amdgcn_s_sleep(1);
        }
    }
    WG_BARRIER();
    if (wid == 0) TSN(3, 2);
    GAS u64* GA = (GAS u64*)(ws + OFF_GA);
    const unsigned dq[4] = {dsk.x, dsk.y, dsk.z, dsk.w};
    u64 ga0[4], ga1[4];
#pragma unroll
    for (int it = 0; it < 4; ++it) {
        const size_t o = (size_t)(l0 + it * 16 + (tid >> 4)) * D_ + d0 + g8 * 8;
        ga0[it] = __hip_atomic_load(GA + o / 4, RLX_AGENT); ga1[it] = __hip_atomic_load(GA + o / 4 + 1, RLX_AGENT);
    }
#pragma unroll
    for (int it = 0; it < 4; ++it) {
        const int row = it * 16 + (tid >> 4);
        const size_t o = (size_t)(l0 + row) * D_ + d0 + g8 * 8;
        const float* sp = Ox + row * OPITCH + g8 * 8;
        float4 f0 = *reinterpret_cast<const float4*>(sp), f1 = *reinterpret_cast<const float4*>(sp + 4);
        const float f[8] = {f0.x, f0.y, f0.z, f0.w, f1.x, f1.y, f1.z, f1.w};
        const unsigned xq[4] = {xv[it].x, xv[it].y, xv[it].z, xv[it].w};
        const unsigned gq[4] = {(unsigned)ga0[it], (unsigned)(ga0[it] >> 32), (unsigned)ga1[it], (unsigned)(ga1[it] >> 32)};
        float r[8];
#pragma unroll
        for (int j = 0; j < 4; ++j) {
            float y0 = 2.0f * f[2 * j] + bfr(__uint_as_float(xq[j] << 16) * __uint_as_float(dq[j] << 16));
            float y1 = 2.0f * f[2 * j + 1] + bfr(__uint_as_float(xq[j] & 0xffff0000u) * __uint_as_float(dq[j] & 0xffff0000u));
            r[2 * j] = __uint_as_float(gq[j] << 16) + omg[it] * y0;
            r[2 * j + 1] = __uint_as_float(gq[j] & 0xffff0000u) + omg[it] * y1;
        }
        st16f(out + o, make_float4(r[0], r[1], r[2], r[3]));
        st16f(out + o + 4, make_float4(r[4], r[5], r[6], r[7]));
    }
    if (wid == 0) TS(3, 3);
}

constexpr int K3_SMEM = G_LDS + 4 * RW * 8;
NOPK __global__ __launch_bounds__(768, 1) void k3_kernel(const u16* __restrict__ x, const u16* __restrict__ Cre, const u16* __restrict__ Cim,
                                                    const u16* __restrict__ Dskip, char* __restrict__ ws, float* __restrict__ out) {
    extern __shared__ __attribute__((aligned(16))) char smem[];
    const int role = __builtin_amdgcn_readfirstlane(threadIdx.x >> 8);
    if (role == 0) gemm2_role(x, Dskip, ws, out, smem, blockIdx.x, threadIdx.x);
    else if (role == 1) gemm2_loader(Cre, Cim, ws, smem, blockIdx.x, __builtin_amdgcn_readfirstlane((threadIdx.x >> 6) & 3), threadIdx.x & 63);
    else {
        rowifft_role(ws, smem + G_LDS, blockIdx.x, threadIdx.x - 512);
    }
}

typedef __attribute__((ext_vector_type(4))) unsigned pu32x4;
__global__ __launch_bounds__(256) void probe_chain(const char* __restrict__ A, unsigned* sink) {
    if (threadIdx.x != 0) return;
    unsigned off = 0, acc = 0;
    const char* p = A + (size_t)blockIdx.x * 16384;
    for (int i = 0; i < 32; ++i) {
        unsigned v = *(const volatile unsigned*)(p + i * 512 + off);
        acc ^= v;
        asm volatile("v_and_b32 %0, 0, %1" : "=v"(off) : "v"(v));
    }
    sink[blockIdx.x] = acc;
}
template<int NTL> __global__ __launch_bounds__(256) void probe_stream(const char* __restrict__ A, unsigned* sink) {
    pu32x4 v[32];
#pragma unroll
    for (int i = 0; i < 32; ++i) {
        const pu32x4* p = (const pu32x4*)(A + ((size_t)blockIdx.x * 32 + i) * 4096 + threadIdx.x * 16);
        v[i] = NTL ? __builtin_nontemporal_load(p) : *p;
    }
    unsigned acc = 0;
#pragma unroll
    for (int i = 0; i < 32; ++i) acc ^= v[i].x ^ v[i].w;
    if (acc == 0x1234567) sink[blockIdx.x] = acc;
}
__global__ void probe_null(unsigned* sink) { if (threadIdx.x == 0) sink[blockIdx.x] = 0; }
extern "C" void kernel_launch(void* const* d_in, const int* in_sizes, int n_in,
                              void* d_out, int out_size, void* d_ws, size_t ws_size,
                              hipStream_t stream) {
    const u16* x = (const u16*)d_in[0];
    const u16* w_r = (const u16*)d_in[1];
    const u16* w_i = (const u16*)d_in[2];
    const u16* lam_re = (const u16*)d_in[4];
    const u16* lam_im = (const u16*)d_in[5];
    const u16* Bre = (const u16*)d_in[6];
    const u16* Bim = (const u16*)d_in[7];
    const u16* Cre = (const u16*)d_in[8];
    const u16* Cim = (const u16*)d_in[9];
    const u16* log_dt = (const u16*)d_in[10];
    const u16* Dskip = (const u16*)d_in[11];
    const u16* gate_w = (const u16*)d_in[12];
    char* ws = (char*)d_ws;
    float* out = (float*)d_out;
    (void)hipFuncSetAttribute((const void*)k2_kernel, hipFuncAttributeMaxDynamicSharedMemorySize, K2_SMEM);
    (void)hipFuncSetAttribute((const void*)k3_kernel, hipFuncAttributeMaxDynamicSharedMemorySize, K3_SMEM);
    probe_null<<<256, 256, 0, stream>>>((unsigned*)(ws + (100u << 20)));
    k1_kernel<<<256, 512, 0, stream>>>(x, gate_w, w_r, w_i, Bre, Bim, Cre, Cim, ws);
    k2_kernel<<<256, 768, K2_SMEM, stream>>>(x, Bre, Bim, lam_re, lam_im, log_dt, ws);
    k3_kernel<<<256, 768, K3_SMEM, stream>>>(x, Cre, Cim, Dskip, ws, out);
}
```

```cpp
#define CODE2 256
#include <hip/hip_runtime.h>
#include <hip/hip_bf16.h>
#include <stdint.h>

typedef __attribute__((ext_vector_type(8))) short bf16x8;
typedef __attribute__((ext_vector_type(4))) float f32x4;
typedef unsigned short u16;
typedef unsigned long long u64;
#define GAS __attribute__((address_space(1)))
#define NOPK __attribute__((target("no-packed-fp32-ops")))
#define RLX_AGENT __ATOMIC_RELAXED, __HIP_MEMORY_SCOPE_AGENT

constexpr int S_ = 2048, D_ = 1024, P_ = 1024, KF = 513;

constexpr double kPi = 3.14159265358979323846264338327950288;
constexpr double c_sin(double x) {
    double t = x, s = x;
    for (int i = 1; i < 14; ++i) { t *= -x * x / ((2.0 * i) * (2.0 * i + 1.0)); s += t; }
    return s;
}
constexpr double c_cos(double x) {
    double t = 1.0, s = 1.0;
    for (int i = 1; i < 14; ++i) { t *= -x * x / ((2.0 * i - 1.0) * (2.0 * i)); s += t; }
    return s;
}
struct TwTab {
    float v[4096];
    constexpr TwTab() : v{} {
        for (int j = 0; j < 2048; ++j) {
            int q = j >> 9, r = j & 511;
            double c = 0, s = 0;
            if (r <= 256) { double th = 2.0 * kPi * r / 2048.0; c = c_cos(th); s = c_sin(th); }
            else { double th = 2.0 * kPi * (512 - r) / 2048.0; c = c_sin(th); s = c_cos(th); }
            double cc = 0, ss = 0;
            if (q == 0) { cc = c; ss = s; } else if (q == 1) { cc = -s; ss = c; }
            else if (q == 2) { cc = -c; ss = -s; } else { cc = s; ss = -c; }
            v[2 * j] = (float)cc; v[2 * j + 1] = (float)(-ss);
        }
    }
};
__device__ const TwTab TWT = TwTab();
__device__ __forceinline__ float2 tw(int j) { return *reinterpret_cast<const float2*>(&TWT.v[2 * j]); }

__device__ __forceinline__ void st16(void* p, uint4 v) { *reinterpret_cast<uint4*>(p) = v; }
__device__ __forceinline__ void st16f(void* p, float4 v) { st16(p, make_uint4(__float_as_uint(v.x), __float_as_uint(v.y), __float_as_uint(v.z), __float_as_uint(v.w))); }
__device__ __forceinline__ float bf2f(u16 h) { return __uint_as_float((unsigned)h << 16); }
__device__ __forceinline__ u16 f2bf(float f) { __hip_bfloat16 b = __float2bfloat16(f); return *reinterpret_cast<u16*>(&b); }
__device__ __forceinline__ float bfr(float f) { return bf2f(f2bf(f)); }
__device__ __forceinline__ float2 cadd(float2 a, float2 b) { return make_float2(a.x + b.x, a.y + b.y); }
__device__ __forceinline__ float2 csub(float2 a, float2 b) { return make_float2(a.x - b.x, a.y - b.y); }
__device__ __forceinline__ float2 cmul(float2 a, float2 b) { return make_float2(a.x * b.x - a.y * b.y, a.x * b.y + a.y * b.x); }
__device__ __forceinline__ float2 cmulc(float2 a, float2 b) { return make_float2(a.x * b.x + a.y * b.y, a.y * b.x - a.x * b.y); }
template <int DIR> __device__ __forceinline__ float2 cmuld(float2 a, float2 w) { return DIR < 0 ? cmul(a, w) : cmulc(a, w); }
template <int DIR> __device__ __forceinline__ float2 rot90(float2 a) { return DIR < 0 ? make_float2(a.y, -a.x) : make_float2(-a.y, a.x); }
#define PACE() do { __builtin_amdgcn_s_barrier(); asm volatile("" ::: "memory"); } while (0)
#define FENCE16(v) do { _Pragma("unroll") for (int i_ = 0; i_ < 16; ++i_) asm volatile("" : "+v"(v[i_].x), "+v"(v[i_].y)); } while (0)
#define LAS __attribute__((address_space(3)))
__device__ __forceinline__ void grp_barrier(unsigned* c, unsigned target) {
    const unsigned a = (unsigned)(uintptr_t)(LAS unsigned*)c;
    asm volatile("s_waitcnt lgkmcnt(0)" ::: "memory");
    if ((threadIdx.x & 63) == 0) asm volatile("ds_add_u32 %0, %1" :: "v"(a), "v"(1u) : "memory");
    for (int spin = 0; spin < (1 << 24); ++spin) {
        unsigned v;
        asm volatile("ds_read_b32 %0, %1\n\ts_waitcnt lgkmcnt(0)" : "=v"(v) : "v"(a) : "memory");
        if ((unsigned)__builtin_amdgcn_readfirstlane(v) >= target) break;
        __builtin_amdgcn_s_sleep(1);
    }
    asm volatile("" ::: "memory");
}
template <int DIR, bool PRE, bool MIDPACE = false> __device__ __forceinline__ void twiddle16(float2 (&v)[16], float2 w, float2 pre) {
    asm volatile("" : "+v"(w.x), "+v"(w.y));
    const float2 w2 = cmul(w, w), w3 = cmul(w2, w), w4 = cmul(w2, w2), w8 = cmul(w4, w4);
#pragma unroll
    for (int q = 0; q < 4; ++q) {
        float2 b = q == 0 ? make_float2(1.f, 0.f) : q == 1 ? w4 : q == 2 ? w8 : cmul(w8, w4);
        if (PRE) b = q == 0 ? pre : cmul(b, pre);
        if (PRE || q > 0) v[4 * q] = cmuld<DIR>(v[4 * q], b);
        v[4 * q + 1] = cmuld<DIR>(v[4 * q + 1], (PRE || q > 0) ? cmul(b, w) : w);
        v[4 * q + 2] = cmuld<DIR>(v[4 * q + 2], (PRE || q > 0) ? cmul(b, w2) : w2);
        v[4 * q + 3] = cmuld<DIR>(v[4 * q + 3], (PRE || q > 0) ? cmul(b, w3) : w3);
        if (MIDPACE && q == 1) { FENCE16(v); PACE(); }
    }
}
#define LDS_WAIT() asm volatile("s_waitcnt lgkmcnt(0)" ::: "memory")
#define WAVE_SYNC() do { LDS_WAIT(); __builtin_amdgcn_wave_barrier(); } while (0)
#define WG_BARRIER() do { LDS_WAIT(); __builtin_amdgcn_s_barrier(); asm volatile("" ::: "memory"); } while (0)

template <int DIR> __device__ __forceinline__ void dft4(float2& a0, float2& a1, float2& a2, float2& a3) {
    float2 t0 = cadd(a0, a2), t1 = csub(a0, a2), t2 = cadd(a1, a3), t3 = rot90<DIR>(csub(a1, a3));
    a0 = cadd(t0, t2); a1 = cadd(t1, t3); a2 = csub(t0, t2); a3 = csub(t1, t3);
}
template <int DIR, bool MIDPACE = false> __device__ __forceinline__ void dft16(float2 (&v)[16]) {
    constexpr float C1 = 0.92387953251128675613f, S1 = 0.38268343236508977173f, R = 0.70710678118654752440f;
#pragma unroll
    for (int n2 = 0; n2 < 4; ++n2) dft4<DIR>(v[n2], v[4 + n2], v[8 + n2], v[12 + n2]);
    if (MIDPACE) { FENCE16(v); PACE(); }
    const float2 w1 = make_float2(C1, -S1), w2 = make_float2(R, -R), w3 = make_float2(S1, -C1);
    const float2 w6 = make_float2(-R, -R), w9 = make_float2(-C1, S1);
    v[4 * 1 + 1] = cmuld<DIR>(v[4 * 1 + 1], w1);
    v[4 * 1 + 2] = cmuld<DIR>(v[4 * 1 + 2], w2);
    v[4 * 1 + 3] = cmuld<DIR>(v[4 * 1 + 3], w3);
    v[4 * 2 + 1] = cmuld<DIR>(v[4 * 2 + 1], w2);
    v[4 * 2 + 2] = rot90<DIR>(v[4 * 2 + 2]);
    v[4 * 2 + 3] = cmuld<DIR>(v[4 * 2 + 3], w6);
    v[4 * 3 + 1] = cmuld<DIR>(v[4 * 3 + 1], w3);
    v[4 * 3 + 2] = cmuld<DIR>(v[4 * 3 + 2], w6);
    v[4 * 3 + 3] = cmuld<DIR>(v[4 * 3 + 3], w9);
    if (MIDPACE) {
#pragma unroll
        for (int i = 0; i < 16; ++i) asm volatile("" : "+v"(v[i].x), "+v"(v[i].y));
        PACE();
    }
#pragma unroll
    for (int k1 = 0; k1 < 4; ++k1) dft4<DIR>(v[4 * k1], v[4 * k1 + 1], v[4 * k1 + 2], v[4 * k1 + 3]);
    float2 t[16];
#pragma unroll
    for (int k1 = 0; k1 < 4; ++k1)
#pragma unroll
        for (int k2 = 0; k2 < 4; ++k2) t[k1 + 4 * k2] = v[4 * k1 + k2];
#pragma unroll
    for (int i = 0; i < 16; ++i) v[i] = t[i];
}
template <int DIR> __device__ __forceinline__ void dft8(float2 (&v)[8]) {
    constexpr float R = 0.70710678118654752440f;
    dft4<DIR>(v[0], v[2], v[4], v[6]);
    dft4<DIR>(v[1], v[3], v[5], v[7]);
    v[3] = cmuld<DIR>(v[3], make_float2(R, -R));
    v[5] = rot90<DIR>(v[5]);
    v[7] = cmuld<DIR>(v[7], make_float2(-R, -R));
    float2 t[8];
#pragma unroll
    for (int k1 = 0; k1 < 4; ++k1) { t[k1] = cadd(v[2 * k1], v[2 * k1 + 1]); t[k1 + 4] = csub(v[2 * k1], v[2 * k1 + 1]); }
#pragma unroll
    for (int i = 0; i < 8; ++i) v[i] = t[i];
}

constexpr int KP = 514;
constexpr size_t OFF_X1T = 0;
constexpr size_t OFF_WT = OFF_X1T + (size_t)KP * S_ * 8;
constexpr size_t OFF_G = OFF_WT + (size_t)KP * S_ * 4;
constexpr size_t OFF_GRAN = OFF_G + 2 * S_ * 4;
constexpr size_t OFF_FLAG = OFF_GRAN + (size_t)16 * P_ * 2 * 8;
constexpr size_t OFF_A2 = OFF_FLAG + 4096;
constexpr size_t OFF_GA = OFF_A2 + (size_t)S_ * 4096 * 2;
constexpr size_t WS_END = OFF_GA + (size_t)S_ * D_ * 2;
#define TS(slot, i)
#define TSN(slot, i)

constexpr int RW = 1128;
__device__ __forceinline__ void k1_body(const u16* __restrict__ x, const u16* __restrict__ gate_w,
                                        const u16* __restrict__ w_r, const u16* __restrict__ w_i,
                                        const u16* __restrict__ Bre, const u16* __restrict__ Bim,
                                        const u16* __restrict__ Cre, const u16* __restrict__ Cim,
                                        char* __restrict__ ws) {
    __shared__ __attribute__((aligned(16))) float2 lds[4 * RW];
    __shared__ __attribute__((aligned(16))) uint4 wst[2][516];
    __shared__ __attribute__((aligned(16))) float4 ost[KF * 4 + KF / 8 + 4];
    __shared__ unsigned auxcnt;
    const int tid = threadIdx.x, bid = blockIdx.x;
    if (tid == 0) auxcnt = 0u;
    const int w = __builtin_amdgcn_readfirstlane(tid >> 6), t = tid & 63;
    const int r0 = bid + 512 * (w & 3);
    if (w == 0) TSN(0, 0);
    uint2 xa[4], xb[4];
#pragma unroll
    for (int a = 0; a < 4; ++a) {
        xa[a] = *reinterpret_cast<const uint2*>(x + (size_t)r0 * D_ + 256 * a + 4 * t);
        xb[a] = *reinterpret_cast<const uint2*>(x + (size_t)(r0 + 256) * D_ + 256 * a + 4 * t);
    }
    float2 twr[8];
#pragma unroll
    for (int j = 1; j < 8; ++j) twr[j] = tw(bid * j);
    __syncthreads();
    if (w >= 4) {
        const int at = tid - 256;

        unsigned cw0 = 0, cw1 = 0;
        {
            unsigned lo, hi;
            asm volatile("s_getpc_b64 s[40:41]\n\ts_add_u32 s40, s40, _Z9k1_kernelPKtS0_S0_S0_S0_S0_S0_S0_Pc@rel32@lo+4\n\ts_addc_u32 s41, s41, _Z9k1_kernelPKtS0_S0_S0_S0_S0_S0_S0_Pc@rel32@hi+12\n\ts_mov_b32 %0, s40\n\ts_mov_b32 %1, s41" : "=s"(lo), "=s"(hi) :: "s40", "s41", "scc");
            const char* cb = (const char*)(((unsigned long long)hi << 32) | lo);
            if (bid < 64) { cw0 = *(const unsigned*)(cb + at * 128); if (at < CODE2) cw1 = *(const unsigned*)(cb + 32768 + at * 128); }
        }
        uint2 gw[4];
#pragma unroll
        for (int a = 0; a < 4; ++a) gw[a] = *reinterpret_cast<const uint2*>(gate_w + 256 * a + 4 * t);
        const uint4* wr4 = reinterpret_cast<const uint4*>(w_r + (size_t)bid * 8 * KF);
        const uint4* wi4 = reinterpret_cast<const uint4*>(w_i + (size_t)bid * 8 * KF);
        uint4 wq[4];
        wq[0] = wr4[at]; wq[1] = wr4[at + 256]; wq[2] = wi4[at]; wq[3] = wi4[at + 256];
        uint4 wq4 = make_uint4(0, 0, 0, 0);
        if (at < 2) wq4 = (at == 0 ? wr4 : wi4)[512];
        uint4 pf[8];
#pragma unroll
        for (int i = 0; i < 2; ++i) {
            const size_t o = ((size_t)bid * 512 + i * 256 + at) * 8;
            pf[4 * i + 0] = *reinterpret_cast<const uint4*>(Bre + o);
            pf[4 * i + 1] = *reinterpret_cast<const uint4*>(Bim + o);
            pf[4 * i + 2] = *reinterpret_cast<const uint4*>(Cre + o);
            pf[4 * i + 3] = *reinterpret_cast<const uint4*>(Cim + o);
        }
        if (at < 128) reinterpret_cast<u64*>(ws + OFF_GRAN)[bid * 128 + at] = 0ull;
        else if (at < 132) reinterpret_cast<unsigned*>(ws + OFF_FLAG)[bid * 4 + at - 128] = 0u;
        float s0 = 0.f, s1 = 0.f;
#pragma unroll
        for (int a = 0; a < 4; ++a) {
            const unsigned ua[2] = {xa[a].x, xa[a].y}, ub[2] = {xb[a].x, xb[a].y}, ug[2] = {gw[a].x, gw[a].y};
#pragma unroll
            for (int e = 0; e < 2; ++e) {
                const float g0 = __uint_as_float(ug[e] << 16), g1 = __uint_as_float(ug[e] & 0xffff0000u);
                s0 += bfr(__uint_as_float(ua[e] << 16) * g0); s0 += bfr(__uint_as_float(ua[e] & 0xffff0000u) * g1);
                s1 += bfr(__uint_as_float(ub[e] << 16) * g0); s1 += bfr(__uint_as_float(ub[e] & 0xffff0000u) * g1);
            }
        }
#pragma unroll
        for (int m = 32; m >= 1; m >>= 1) { s0 += __shfl_xor(s0, m); s1 += __shfl_xor(s1, m); }
        if (t < 2) {
            float sv = bfr(t == 0 ? s0 : s1);
            float g = bfr(1.0f / (1.0f + expf(-sv)));
            float* G = reinterpret_cast<float*>(ws + OFF_G);
            G[r0 + 256 * t] = g;
            G[S_ + r0 + 256 * t] = bfr(1.0f - g);
        }
        wst[0][at] = wq[0]; wst[0][at + 256] = wq[1]; wst[1][at] = wq[2]; wst[1][at + 256] = wq[3];
        if (at < 2) wst[at][512] = wq4;
        grp_barrier(&auxcnt, 4u);
        {
            unsigned* WT = reinterpret_cast<unsigned*>(ws + OFF_WT);
            const u16* sr = reinterpret_cast<const u16*>(&wst[0][0]);
            const u16* si = reinterpret_cast<const u16*>(&wst[1][0]);
#pragma unroll
            for (int it = 0; it < 3; ++it) {
                const int k = at + it * 256;
                if (k < KF) {
                    unsigned q[8];
#pragma unroll
                    for (int r = 0; r < 8; ++r) q[r] = (unsigned)sr[r * KF + k] | ((unsigned)si[r * KF + k] << 16);
                    uint4* dst = reinterpret_cast<uint4*>(WT + (size_t)k * S_ + bid * 8);
                    st16(dst, make_uint4(q[0], q[1], q[2], q[3]));
                    st16(dst + 1, make_uint4(q[4], q[5], q[6], q[7]));
                }
            }
        }
#pragma unroll
        for (int i = 0; i < 8; ++i) asm volatile("" :: "v"(pf[i].x));
        asm volatile("" :: "v"(cw0), "v"(cw1));
    } else {
        float2* buf = lds + w * RW;
        float2 twa[4];
#pragma unroll
        for (int e = 0; e < 4; ++e) twa[e] = tw(2 * (4 * t + e));
        const float2 twb = tw(8 * (t & 15));
        float2 v[16];
#pragma unroll
        for (int a = 0; a < 4; ++a) {
            v[4 * a + 0] = make_float2(__uint_as_float(xa[a].x << 16), __uint_as_float(xb[a].x << 16));
            v[4 * a + 1] = make_float2(__uint_as_float(xa[a].x & 0xffff0000u), __uint_as_float(xb[a].x & 0xffff0000u));
            v[4 * a + 2] = make_float2(__uint_as_float(xa[a].y << 16), __uint_as_float(xb[a].y << 16));
            v[4 * a + 3] = make_float2(__uint_as_float(xa[a].y & 0xffff0000u), __uint_as_float(xb[a].y & 0xffff0000u));
        }
        if (w == 0) TS(0, 1);
#pragma unroll
        for (int e = 0; e < 4; ++e) {
            dft4<-1>(v[e], v[4 + e], v[8 + e], v[12 + e]);
            float2 w1 = twa[e], w2 = cmul(w1, w1), w3 = cmul(w2, w1);
            v[4 + e] = cmul(v[4 + e], w1);
            v[8 + e] = cmul(v[8 + e], w2);
            v[12 + e] = cmul(v[12 + e], w3);
        }
#pragma unroll
        for (int ka = 0; ka < 4; ++ka)
#pragma unroll
            for (int e = 0; e < 4; ++e) buf[ka * 272 + 4 * t + e] = v[4 * ka + e];
        WAVE_SYNC();
        {
            const int ka = t >> 4, d = t & 15;
#pragma unroll
            for (int c = 0; c < 16; ++c) v[c] = buf[ka * 272 + 16 * c + d];
            dft16<-1>(v);
            twiddle16<-1, false>(v, twb, twb);
            WAVE_SYNC();
#pragma unroll
            for (int kc = 0; kc < 16; ++kc) buf[ka * 280 + kc * 17 + d] = v[kc];
        }
        WAVE_SYNC();
        {
            const int ka = t & 3, kc = t >> 2;
#pragma unroll
            for (int d = 0; d < 16; ++d) v[d] = buf[ka * 280 + kc * 17 + d];
            dft16<-1>(v);
            WAVE_SYNC();
#pragma unroll
            for (int kd = 0; kd < 16; ++kd) buf[t + 64 * kd] = v[kd];
        }
        if (w == 0) TS(0, 2);
    }
    __syncthreads();
    float2* X1T = reinterpret_cast<float2*>(ws + OFF_X1T);
    {
        const int k = tid;
        float2 u[8];
#pragma unroll
        for (int ww = 0; ww < 4; ++ww) {
            const float2 zk = lds[ww * RW + k], zn = lds[ww * RW + (k == 0 ? 512 : 1024 - k)];
            u[2 * ww] = k == 0 ? make_float2(zk.x, zn.x) : make_float2(0.5f * (zk.x + zn.x), 0.5f * (zk.y - zn.y));
            u[2 * ww + 1] = k == 0 ? make_float2(zk.y, zn.y) : make_float2(0.5f * (zk.y + zn.y), 0.5f * (zn.x - zk.x));
        }
        dft8<-1>(u);
#pragma unroll
        for (int j = 1; j < 8; ++j) u[j] = cmul(u[j], twr[j]);
#pragma unroll
        for (int j = 0; j < 4; ++j) ost[k * 4 + j + (k >> 3)] = make_float4(u[2 * j].x, u[2 * j].y, u[2 * j + 1].x, u[2 * j + 1].y);
    }
    __syncthreads();
    {
        float4* dst = reinterpret_cast<float4*>(X1T + (size_t)bid * KP * 8);
#pragma unroll
        for (int it = 0; it < 4; ++it) {
            const int i = tid + it * 512;
            st16f(dst + i, ost[i + (i >> 5)]);
        }
    }
    if (w == 0) TS(0, 3);
    if (w == 0) TS(0, 4);
}
NOPK __global__ __launch_bounds__(512) void k1_kernel(const u16* __restrict__ x, const u16* __restrict__ gate_w,
                                                      const u16* __restrict__ w_r, const u16* __restrict__ w_i,
                                                      const u16* __restrict__ Bre, const u16* __restrict__ Bim,
                                                      const u16* __restrict__ Cre, const u16* __restrict__ Cim,
                                                      char* __restrict__ ws) {
    k1_body(x, gate_w, w_r, w_i, Bre, Bim, Cre, Cim, ws);
}

constexpr int CPITCH = 273;
constexpr int CLDS = 16 * CPITCH * 8;
struct FSt { int nb; unsigned ep; unsigned* cnt; };
#define FPACE() do { if (fs.nb < 16) { PACE(); ++fs.nb; } } while (0)
#define FSYNC() do { if (fs.nb < 16) { WG_BARRIER(); ++fs.nb; } else { fs.ep += 4; grp_barrier(fs.cnt, fs.ep); } } while (0)
template <int DIR> __device__ __forceinline__ void dft16f(float2 (&v)[16], FSt& fs) {
    constexpr float C1 = 0.92387953251128675613f, S1 = 0.38268343236508977173f, R = 0.70710678118654752440f;
#pragma unroll
    for (int n2 = 0; n2 < 4; ++n2) dft4<DIR>(v[n2], v[4 + n2], v[8 + n2], v[12 + n2]);
    FENCE16(v); FPACE();
    const float2 w1 = make_float2(C1, -S1), w2 = make_float2(R, -R), w3 = make_float2(S1, -C1);
    const float2 w6 = make_float2(-R, -R), w9 = make_float2(-C1, S1);
    v[5] = cmuld<DIR>(v[5], w1); v[6] = cmuld<DIR>(v[6], w2); v[7] = cmuld<DIR>(v[7], w3);
    v[9] = cmuld<DIR>(v[9], w2); v[10] = rot90<DIR>(v[10]); v[11] = cmuld<DIR>(v[11], w6);
    v[13] = cmuld<DIR>(v[13], w3); v[14] = cmuld<DIR>(v[14], w6); v[15] = cmuld<DIR>(v[15], w9);
    FENCE16(v); FPACE();
#pragma unroll
    for (int k1 = 0; k1 < 4; ++k1) dft4<DIR>(v[4 * k1], v[4 * k1 + 1], v[4 * k1 + 2], v[4 * k1 + 3]);
    float2 t[16];
#pragma unroll
    for (int k1 = 0; k1 < 4; ++k1)
#pragma unroll
        for (int k2 = 0; k2 < 4; ++k2) t[k1 + 4 * k2] = v[4 * k1 + k2];
#pragma unroll
    for (int i = 0; i < 16; ++i) v[i] = t[i];
}
template <int DIR> __device__ __forceinline__ void twiddle16f(float2 (&v)[16], float2 w, FSt& fs) {
    asm volatile("" : "+v"(w.x), "+v"(w.y));
    const float2 w2 = cmul(w, w), w3 = cmul(w2, w), w4 = cmul(w2, w2), w8 = cmul(w4, w4);
#pragma unroll
    for (int q = 0; q < 4; ++q) {
        const float2 b = q == 0 ? make_float2(1.f, 0.f) : q == 1 ? w4 : q == 2 ? w8 : cmul(w8, w4);
        if (q > 0) v[4 * q] = cmuld<DIR>(v[4 * q], b);
        v[4 * q + 1] = cmuld<DIR>(v[4 * q + 1], q > 0 ? cmul(b, w) : w);
        v[4 * q + 2] = cmuld<DIR>(v[4 * q + 2], q > 0 ? cmul(b, w2) : w2);
        v[4 * q + 3] = cmuld<DIR>(v[4 * q + 3], q > 0 ? cmul(b, w3) : w3);
        if (q == 1) { FENCE16(v); FPACE(); }
    }
}
__device__ __forceinline__ void colfft_cols(const bool PACKED, char* __restrict__ ws, float2* gbuf, float2* ex, int k0, int b, int hj, float2 twb, int npre, FSt& fs) {
    float2* col = reinterpret_cast<float2*>(ws + OFF_X1T) + (size_t)k0 * 8 + hj;
    const unsigned* wt = reinterpret_cast<const unsigned*>(ws + OFF_WT) + (size_t)(k0 + (hj >> 3)) * S_ + (hj & 7) + 8 * b;
    FPACE();
    float2 v[16];
#pragma unroll
    for (int a = 0; a < 16; ++a) v[a] = col[(size_t)(b + 16 * a) * (KP * 8)];
    unsigned wv[16];
#pragma unroll
    for (int kd = 0; kd < 16; ++kd) wv[kd] = wt[128 * kd];
    for (int i = 0; i < npre; ++i) FPACE();
    dft16f<-1>(v, fs);
    FENCE16(v); FPACE();
    twiddle16f<-1>(v, twb, fs);
    FENCE16(v); FPACE();
#pragma unroll
    for (int kc = 0; kc < 16; ++kc) gbuf[kc * 17 + b] = v[kc];
    FSYNC();
#pragma unroll
    for (int d = 0; d < 16; ++d) v[d] = gbuf[b * 17 + d];
    dft16f<-1>(v, fs);
    FENCE16(v);
    FPACE();
    if (PACKED) {
        FSYNC();
        if (hj < 8) {
#pragma unroll
            for (int kd = 0; kd < 16; ++kd) ex[hj + 8 * b + 128 * kd] = v[kd];
        }
        FSYNC();
        const unsigned* w0 = reinterpret_cast<const unsigned*>(ws + OFF_WT);
        const unsigned* w512 = w0 + (size_t)512 * S_;
#pragma unroll
        for (int g4 = 0; g4 < 4; ++g4) {
            if (hj < 8) {
                unsigned a1[4], b0[4], b1[4];
#pragma unroll
                for (int q = 0; q < 4; ++q) {
                    const int ks = hj + 8 * b + 128 * (4 * g4 + q), kn = (S_ - ks) & (S_ - 1);
                    a1[q] = w0[kn]; b0[q] = w512[ks]; b1[q] = w512[kn];
                }
#pragma unroll
                for (int q = 0; q < 4; ++q) {
                    const int kd = 4 * g4 + q;
                    const int ks = hj + 8 * b + 128 * kd, kn = (S_ - ks) & (S_ - 1);
                    const float2 c = v[kd], n = ex[kn];
                    const float2 f0 = make_float2(0.5f * (c.x + n.x), 0.5f * (c.y - n.y));
                    const float2 f1 = make_float2(0.5f * (c.y + n.y), 0.5f * (n.x - c.x));
                    const unsigned a0 = wv[kd];
                    const float2 h0 = make_float2(0.5f * (__uint_as_float(a0 << 16) + __uint_as_float(a1[q] << 16)),
                                                  0.5f * (__uint_as_float(a0 & 0xffff0000u) - __uint_as_float(a1[q] & 0xffff0000u)));
                    const float2 h1 = make_float2(0.5f * (__uint_as_float(b0[q] << 16) + __uint_as_float(b1[q] << 16)),
                                                  0.5f * (__uint_as_float(b0[q] & 0xffff0000u) - __uint_as_float(b1[q] & 0xffff0000u)));
                    const float2 z0 = cmul(f0, h0), z1 = cmul(f1, h1);
                    v[kd] = make_float2(z0.x - z1.y, z0.y + z1.x);
                }
            } else {
#pragma unroll
                for (int q = 0; q < 4; ++q) {
                    const int kd = 4 * g4 + q;
                    v[kd] = cmul(v[kd], make_float2(__uint_as_float(wv[kd] << 16), __uint_as_float(wv[kd] & 0xffff0000u)));
                }
            }
            FENCE16(v); FPACE();
        }
        FSYNC();
    } else {
#pragma unroll
        for (int kd = 0; kd < 16; ++kd) v[kd] = cmul(v[kd], make_float2(__uint_as_float(wv[kd] << 16), __uint_as_float(wv[kd] & 0xffff0000u)));
    }
    FENCE16(v); FPACE();
    dft16f<1>(v, fs);
    FENCE16(v); FPACE();
    twiddle16f<1>(v, twb, fs);
    FENCE16(v); FPACE();
#pragma unroll
    for (int d = 0; d < 16; ++d) gbuf[b * 17 + d] = v[d];
    FSYNC();
#pragma unroll
    for (int kc = 0; kc < 16; ++kc) v[kc] = gbuf[kc * 17 + b];
    dft16f<1>(v, fs);
    FENCE16(v); FPACE();
    const float sc = 1.0f / (float)(S_ * D_);
#pragma unroll
    for (int a = 0; a < 16; ++a) col[(size_t)(b + 16 * a) * (KP * 8)] = make_float2(v[a].x * sc, v[a].y * sc);
}
__device__ __forceinline__ void colfft_role(char* __restrict__ ws, char* smem, unsigned* fcnt, int gb, int ftid) {
    const int b = ftid >> 4, hj = ftid & 15;
    float2* gbuf = reinterpret_cast<float2*>(smem) + hj * CPITCH;
    const float2 twb = tw(8 * b);
    if (ftid < 64) TSN(2, 0);
    float2* ex = reinterpret_cast<float2*>(smem);
    FSt fs; fs.nb = 0; fs.ep = 0; fs.cnt = fcnt;
    colfft_cols(gb == 255, ws, gbuf, ex, ((gb + 1) & 255) * 2, b, hj, twb, gb == 255 ? 6 : 2, fs);
    if (ftid < 64) TS(2, 1);
}

#define WAIT_VM(n) asm volatile("s_waitcnt vmcnt(" #n ")" ::: "memory")
#define SCHED() __builtin_amdgcn_sched_barrier(0)
#define BAR() do { __builtin_amdgcn_s_barrier(); asm volatile("" ::: "memory"); } while (0)
#define MF(a, b, c) c = __builtin_amdgcn_mfma_f32_16x16x32_bf16(a, b, c, 0, 0, 0)
__device__ __forceinline__ void glds16(const u16* g, char* l) {
    __builtin_amdgcn_global_load_lds((const GAS unsigned*)g, (__attribute__((address_space(3))) unsigned*)l, 16, 0, 0);
}
__device__ __forceinline__ bf16x8 ldfrag(const char* tile, int row, int chunk) {
    return *reinterpret_cast<const bf16x8*>(tile + row * 128 + ((chunk ^ (row & 7)) << 4));
}
#define HILO 0
constexpr int G_NST = 3;
constexpr int G_STAGE = 32768;
constexpr int G_LDS = G_NST * G_STAGE;
constexpr int G2_NST = HILO ? 3 : 4;
constexpr int G2_BOFF = HILO ? 16384 : 8192;
constexpr int G2_STAGE = HILO ? 32768 : 24576;

__device__ __forceinline__ void gemm1_tile(int gb, int& lt, int& pt) {
    const int xcd = gb & 7, loc = gb >> 3;
    lt = (xcd >> 1) * 4 + (loc >> 3);
    pt = (xcd & 1) * 8 + (loc & 7);
}
constexpr int SPITCH = 132;
__device__ __forceinline__ void ssm_params(const u16* __restrict__ lam_re, const u16* __restrict__ lam_im,
                                           const u16* __restrict__ log_dt, int p, float2& lam, float2& coef) {
    float lr = bf2f(lam_re[p]), li = bf2f(lam_im[p]);
    float dt = bfr(expf(bf2f(log_dt[p])));
    float er = expf(dt * lr), sn, cs;
    sincosf(dt * li, &sn, &cs);
    lam = make_float2(er * cs, er * sn);
    float nr = lam.x - 1.0f, ni = lam.y, den = lr * lr + li * li;
    coef = make_float2((nr * lr + ni * li) / den, (ni * lr - nr * li) / den);
}
__device__ __forceinline__ void scan_epilogue(char* __restrict__ ws, char* smem, int lt, int pt, int gt, float2 lam, float2 coef,
                                              unsigned* gcnt, unsigned gep) {
    const int l0 = lt * 128, p0 = pt * 64;
    const int ch = gt & 63, seg = gt >> 6;
    const float* ST = reinterpret_cast<const float*>(smem);
    float2* segagg = reinterpret_cast<float2*>(smem + 128 * SPITCH * 4);
    float2* part = segagg + 512;
    float2 lam16 = lam;
#pragma unroll
    for (int i = 0; i < 4; ++i) lam16 = cmul(lam16, lam16);
    float hr[16], hi_[16];
    {
        const float* pr = ST + ch * SPITCH + seg * 16;
        const float* pi = pr + 64 * SPITCH;
        float2 h = make_float2(0.f, 0.f);
#pragma unroll
        for (int i4 = 0; i4 < 4; ++i4) {
            const f32x4 a = *reinterpret_cast<const f32x4*>(pr + 4 * i4), b = *reinterpret_cast<const f32x4*>(pi + 4 * i4);
#pragma unroll
            for (int j = 0; j < 4; ++j) {
                float2 bu = cmul(coef, make_float2(a[j], b[j]));
                h = cadd(cmul(lam, h), bu);
                hr[4 * i4 + j] = h.x; hi_[4 * i4 + j] = h.y;
            }
        }
        segagg[seg * 64 + ch] = h;
    }
    gep += 8; grp_barrier(gcnt, gep);
    if (gt < 64) TSN(1, 2);
    GAS u64* gran = (GAS u64*)(ws + OFF_GRAN);
    if (seg == 0) {
        float2 ag = segagg[ch];
#pragma unroll
        for (int s = 1; s < 8; ++s) ag = cadd(cmul(lam16, ag), segagg[s * 64 + ch]);
        GAS u64* gp = gran + ((size_t)lt * P_ + p0 + ch) * 2;
        __hip_atomic_store(gp, (1ull << 32) | (u64)__float_as_uint(ag.x), RLX_AGENT);
        __hip_atomic_store(gp + 1, (1ull << 32) | (u64)__float_as_uint(ag.y), RLX_AGENT);
    }
    float pwr[16], pwi[16];
    {
        float2 c = make_float2(0.f, 0.f), pw = make_float2(1.f, 0.f);
        for (int s = 0; s < seg; ++s) { c = cadd(cmul(lam16, c), segagg[s * 64 + ch]); pw = cmul(pw, lam16); }
#pragma unroll
        for (int i = 0; i < 16; ++i) {
            c = cmul(lam, c); pw = cmul(lam, pw);
            hr[i] += c.x; hi_[i] += c.y; pwr[i] = pw.x; pwi[i] = pw.y;
        }
    }
    {
        float2 lam128 = cmul(lam16, lam16); lam128 = cmul(lam128, lam128); lam128 = cmul(lam128, lam128);
        float2 lam1024 = cmul(lam128, lam128); lam1024 = cmul(lam1024, lam1024); lam1024 = cmul(lam1024, lam1024);
        u64 ga[2], gb_[2];
        for (int spin = 0; spin < (1 << 22); ++spin) {
            bool ok = true;
#pragma unroll
            for (int q = 0; q < 2; ++q) {
                const int cc = lt - 1 - seg - 8 * q;
                ga[q] = 1ull << 32; gb_[q] = 1ull << 32;
                if (cc >= 0) {
                    GAS u64* gq = gran + ((size_t)cc * P_ + p0 + ch) * 2;
                    ga[q] = __hip_atomic_load(gq, RLX_AGENT);
                    gb_[q] = __hip_atomic_load(gq + 1, RLX_AGENT);
                }
            }
#pragma unroll
            for (int q = 0; q < 2; ++q) ok = ok && (ga[q] >> 32) == 1ull && (gb_[q] >> 32) == 1ull;
            if (__all(ok)) break;
            __builtin_amdgcn_s_sleep(1);
        }
        float2 c = make_float2(__uint_as_float((unsigned)ga[1]), __uint_as_float((unsigned)gb_[1]));
        c = cadd(cmul(lam1024, c), make_float2(__uint_as_float((unsigned)ga[0]), __uint_as_float((unsigned)gb_[0])));
        for (int s = 0; s < seg; ++s) c = cmul(c, lam128);
        part[seg * 64 + ch] = c;
    }
    gep += 8; grp_barrier(gcnt, gep);
    if (gt < 64) TSN(1, 3);
    unsigned* O32 = reinterpret_cast<unsigned*>(smem);
    {
        float2 c = make_float2(0.f, 0.f);
#pragma unroll
        for (int s = 0; s < 8; ++s) c = cadd(c, part[s * 64 + ch]);
        if (HILO) {
            unsigned* op = O32 + (seg * 16) * SPITCH + ch;
#pragma unroll
            for (int i = 0; i < 16; ++i) {
                const float2 d = cmul(make_float2(pwr[i], pwi[i]), c);
                const float a = hr[i] + d.x, b = -(hi_[i] + d.y);
                const u16 ah = f2bf(a), bh = f2bf(b);
                const u16 al = f2bf(a - bf2f(ah)), bl = f2bf(b - bf2f(bh));
                op[i * SPITCH] = (unsigned)ah | ((unsigned)al << 16);
                op[i * SPITCH + 64] = (unsigned)bh | ((unsigned)bl << 16);
            }
        } else {
            u16* op = reinterpret_cast<u16*>(smem) + (seg * 16) * 136 + ch;
#pragma unroll
            for (int i = 0; i < 16; ++i) {
                const float2 d = cmul(make_float2(pwr[i], pwi[i]), c);
                op[i * 136] = f2bf(hr[i] + d.x);
                op[i * 136 + 64] = f2bf(-(hi_[i] + d.y));
            }
        }
    }
    gep += 8; grp_barrier(gcnt, gep);
    if (gt < 64) TSN(1, 4);
    u16* A2 = reinterpret_cast<u16*>(ws + OFF_A2);
#pragma unroll
    for (int it = 0; it < 4; ++it) {
        const int idx = it * 512 + gt;
        const int row = idx >> 4, g = idx & 15;
        u16* dst = A2 + (size_t)(l0 + row) * 4096 + (g < 8 ? 0 : 1024) + p0 + (g & 7) * 8;
        if (HILO) {
            const unsigned* sp = O32 + row * SPITCH + g * 8;
            const uint4 f0 = *reinterpret_cast<const uint4*>(sp), f1 = *reinterpret_cast<const uint4*>(sp + 4);
            uint4 hi, lo;
            hi.x = __builtin_amdgcn_perm(f0.y, f0.x, 0x05040100u); lo.x = __builtin_amdgcn_perm(f0.y, f0.x, 0x07060302u);
            hi.y = __builtin_amdgcn_perm(f0.w, f0.z, 0x05040100u); lo.y = __builtin_amdgcn_perm(f0.w, f0.z, 0x07060302u);
            hi.z = __builtin_amdgcn_perm(f1.y, f1.x, 0x05040100u); lo.z = __builtin_amdgcn_perm(f1.y, f1.x, 0x07060302u);
            hi.w = __builtin_amdgcn_perm(f1.w, f1.z, 0x05040100u); lo.w = __builtin_amdgcn_perm(f1.w, f1.z, 0x07060302u);
            st16(dst, hi);
            st16(dst + 2048, lo);
        } else {
            st16(dst, *reinterpret_cast<const uint4*>(reinterpret_cast<const u16*>(smem) + row * 136 + g * 8));
        }
    }
    if (gt < 64) TS(1, 5);
}
__device__ __forceinline__ void gemm1_loader(const u16* __restrict__ x, const u16* __restrict__ Bre, const u16* __restrict__ Bim,
                                             const u16* __restrict__ lam_re, const u16* __restrict__ lam_im, const u16* __restrict__ log_dt,
                                             char* __restrict__ ws, char* smem, unsigned* gcnt, int gb, int lw, int lane) {
    unsigned gep = 0;
    int lt, pt; gemm1_tile(gb, lt, pt);
    const int l0 = lt * 128, p0 = pt * 64;
    const int srow = lane >> 3, schunk = (lane & 7) ^ srow;
    const u16* gA = x + (size_t)(l0 + 32 * lw + srow) * D_ + schunk * 8;
    const u16* gB = (lw < 2 ? Bre : Bim) + (size_t)(p0 + (32 * lw & 63) + srow) * D_ + schunk * 8;
    auto stage = [&](int kt, int sb) {
        char* sA = smem + sb * G_STAGE + (32 * lw) * 128 + lane * 16;
#pragma unroll
        for (int q = 0; q < 4; ++q) glds16(gA + (size_t)(8 * q) * D_ + kt * 64, sA + q * 1024);
#pragma unroll
        for (int q = 0; q < 4; ++q) glds16(gB + (size_t)(8 * q) * D_ + kt * 64, sA + 16384 + q * 1024);
    };
    constexpr int NT = D_ / 64;
    stage(0, 0); stage(1, 1);
    int sb = 0;
    for (int kt = 0; kt < NT; ++kt) {
        if (kt + 1 < NT) WAIT_VM(8); else WAIT_VM(0);
        BAR();
        int nb = sb + 2; if (nb >= G_NST) nb -= G_NST;
        if (kt + 2 < NT) stage(kt + 2, nb);
        sb = sb + 1 == G_NST ? 0 : sb + 1;
    }
    float2 lam, coef;
    ssm_params(lam_re, lam_im, log_dt, p0 + lane, lam, coef);
    gep += 8; grp_barrier(gcnt, gep);
    gep += 8; grp_barrier(gcnt, gep);
    scan_epilogue(ws, smem, lt, pt, 256 + lw * 64 + lane, lam, coef, gcnt, gep);
}
__device__ __forceinline__ void gemm1_role(const u16* __restrict__ lam_re, const u16* __restrict__ lam_im,
                                           const u16* __restrict__ log_dt, char* __restrict__ ws, char* smem, unsigned* gcnt, int gb, int tid) {
    unsigned gep = 0;
    const int wid = tid >> 6, lane = tid & 63;
    int lt, pt; gemm1_tile(gb, lt, pt);
    const int wr = wid >> 1, wc = wid & 1, fr = lane & 15, fq = lane >> 4;
    if (wid == 0) TSN(1, 0);
    float2 lam, coef;
    ssm_params(lam_re, lam_im, log_dt, pt * 64 + lane, lam, coef);
    f32x4 acc[4][4];
#pragma unroll
    for (int m = 0; m < 4; ++m)
#pragma unroll
        for (int n = 0; n < 4; ++n) acc[m][n] = (f32x4){0.f, 0.f, 0.f, 0.f};
    constexpr int NT = D_ / 64;
    bf16x8 af[2][4], bf_[2][4];
#pragma unroll
    for (int m = 0; m < 4; ++m) { af[1][m] = (bf16x8){0, 0, 0, 0, 0, 0, 0, 0}; bf_[1][m] = af[1][m]; }
#pragma unroll 8
    for (int kt = 0; kt < NT; ++kt) {
        BAR();
        if (wid == 0) TSN(5, kt);
        const char* tA = smem + (kt % G_NST) * G_STAGE;
        const char* tB = tA + 16384;
#pragma unroll
        for (int m = 0; m < 4; ++m) af[0][m] = ldfrag(tA, wr * 64 + m * 16 + fr, fq);
#pragma unroll
        for (int n = 0; n < 4; ++n) bf_[0][n] = ldfrag(tB, wc * 64 + n * 16 + fr, fq);
        SCHED();
#pragma unroll
        for (int m = 0; m < 4; ++m)
#pragma unroll
            for (int n = 0; n < 4; ++n) MF(af[1][m], bf_[1][n], acc[m][n]);
        SCHED();
#pragma unroll
        for (int m = 0; m < 2; ++m)
#pragma unroll
            for (int n = 0; n < 4; ++n) MF(af[0][m], bf_[0][n], acc[m][n]);
        SCHED();
#pragma unroll
        for (int m = 0; m < 4; ++m) af[1][m] = ldfrag(tA, wr * 64 + m * 16 + fr, 4 + fq);
#pragma unroll
        for (int n = 0; n < 4; ++n) bf_[1][n] = ldfrag(tB, wc * 64 + n * 16 + fr, 4 + fq);
        SCHED();
#pragma unroll
        for (int m = 2; m < 4; ++m)
#pragma unroll
            for (int n = 0; n < 4; ++n) MF(af[0][m], bf_[0][n], acc[m][n]);
        SCHED();
        LDS_WAIT();
    }
#pragma unroll
    for (int m = 0; m < 4; ++m)
#pragma unroll
        for (int n = 0; n < 4; ++n) MF(af[1][m], bf_[1][n], acc[m][n]);
    if (wid == 0) TSN(1, 1);
    gep += 8; grp_barrier(gcnt, gep);
    float* ST = reinterpret_cast<float*>(smem);
#pragma unroll
    for (int m = 0; m < 4; ++m)
#pragma unroll
        for (int n = 0; n < 4; ++n)
            *reinterpret_cast<f32x4*>(ST + (wc * 64 + n * 16 + fr) * SPITCH + wr * 64 + m * 16 + fq * 4) = acc[m][n];
    gep += 8; grp_barrier(gcnt, gep);
    scan_epilogue(ws, smem, lt, pt, tid, lam, coef, gcnt, gep);
}

constexpr int K2_SMEM = G_LDS + CLDS + 64;
NOPK __global__ __launch_bounds__(768, 1) void k2_kernel(const u16* __restrict__ x, const u16* __restrict__ Bre, const u16* __restrict__ Bim,
                                                    const u16* __restrict__ lam_re, const u16* __restrict__ lam_im,
                                                    const u16* __restrict__ log_dt, char* __restrict__ ws) {
    extern __shared__ __attribute__((aligned(16))) char smem[];
    const int role = __builtin_amdgcn_readfirstlane(threadIdx.x >> 8);
    unsigned* cnts = reinterpret_cast<unsigned*>(smem + G_LDS + CLDS);
    if (threadIdx.x == 0) { cnts[0] = 0u; cnts[8] = 0u; LDS_WAIT(); }
    if (role == 0) gemm1_role(lam_re, lam_im, log_dt, ws, smem, cnts, blockIdx.x, threadIdx.x);
    else if (role == 1) gemm1_loader(x, Bre, Bim, lam_re, lam_im, log_dt, ws, smem, cnts, blockIdx.x, __builtin_amdgcn_readfirstlane((threadIdx.x >> 6) & 3), threadIdx.x & 63);
    else {
        colfft_role(ws, smem + G_LDS, cnts + 8, blockIdx.x, threadIdx.x - 512);
    }
}

__device__ __forceinline__ void rowifft_role(char* __restrict__ ws, char* smem, int bid, int tid) {
    float2* lds = reinterpret_cast<float2*>(smem);
    const float2* Z1T = reinterpret_cast<const float2*>(ws + OFF_X1T);
    const int w = tid >> 6, t = tid & 63;
    if (w == 0) TSN(4, 0);
    PACE();
    {
        const u16* src = reinterpret_cast<const u16*>(Z1T + (size_t)bid * KP * 8);
#pragma unroll
        for (int i = 0; i < 8; ++i) {
            const int piece = w + 4 * i;
            glds16(src + (size_t)piece * 512 + t * 8, smem + piece * 1024 + t * 16);
        }
    }
    float2 twr[8];
#pragma unroll
    for (int j = 1; j < 8; ++j) twr[j] = tw(bid * j);
    const float2 twc = tw(8 * (t >> 2));
    const float2 twb = tw(32 * (t >> 4));
    const float2 twb0 = tw(2 * (t & 15) * (t >> 4));
    const int r0 = bid + 512 * w;
    const float* G = reinterpret_cast<const float*>(ws + OFF_G);
    const float g0 = G[r0], g1 = G[r0 + 256];
    PACE(); PACE();
    asm volatile("s_waitcnt vmcnt(0)" ::: "memory");
    WG_BARRIER();
    float4 q[2][4];
#pragma unroll
    for (int it = 0; it < 2; ++it) {
        const float4* src = reinterpret_cast<const float4*>(smem) + (tid + it * 256) * 4;
#pragma unroll
        for (int j = 0; j < 4; ++j) q[it][j] = src[j];
    }
    WG_BARRIER();
#pragma unroll
    for (int it = 0; it < 2; ++it) {
        const int k = tid + it * 256;
        float2 u[8];
#pragma unroll
        for (int j = 0; j < 4; ++j) { u[2 * j] = make_float2(q[it][j].x, q[it][j].y); u[2 * j + 1] = make_float2(q[it][j].z, q[it][j].w); }
#pragma unroll
        for (int j = 1; j < 8; ++j) u[j] = cmulc(u[j], twr[j]);
        dft8<1>(u);
#pragma unroll
        for (int j = 0; j < 8; ++j) asm volatile("" : "+v"(u[j].x), "+v"(u[j].y));
        PACE();
#pragma unroll
        for (int ww = 0; ww < 4; ++ww) {
            const float2 A = u[2 * ww], B = u[2 * ww + 1];
            if (k == 0) {
                lds[ww * RW] = make_float2(A.x, B.x);
                lds[ww * RW + 512] = make_float2(A.y, B.y);
            } else {
                lds[ww * RW + k] = make_float2(A.x - B.y, A.y + B.x);
                lds[ww * RW + 1024 - k] = make_float2(A.x + B.y, B.x - A.y);
            }
        }
        if (it < 1) PACE();
    }
    WG_BARRIER();
    float2* buf = lds + w * RW;
    float2 v[16];
    {
        const int ka = t & 3, kc = t >> 2;
#pragma unroll
        for (int kd = 0; kd < 16; ++kd) v[kd] = buf[t + 64 * kd];
        dft16<1, true>(v);
        WG_BARRIER();
        twiddle16<1, false, true>(v, twc, twc);
        FENCE16(v); PACE();
#pragma unroll
        for (int d = 0; d < 8; ++d) buf[ka * 280 + kc * 17 + d] = v[d];
        PACE();
#pragma unroll
        for (int d = 8; d < 16; ++d) buf[ka * 280 + kc * 17 + d] = v[d];
    }
    WG_BARRIER();
    {
        const int ka = t >> 4, d = t & 15;
#pragma unroll
        for (int kc = 0; kc < 16; ++kc) v[kc] = buf[ka * 280 + kc * 17 + d];
        dft16<1, true>(v);
        WG_BARRIER();
        twiddle16<1, true, true>(v, twb, twb0);
        FENCE16(v); PACE();
#pragma unroll
        for (int c = 0; c < 8; ++c) buf[ka * 272 + 16 * c + d] = v[c];
        PACE();
#pragma unroll
        for (int c = 8; c < 16; ++c) buf[ka * 272 + 16 * c + d] = v[c];
    }
    WG_BARRIER();
#pragma unroll
    for (int ka = 0; ka < 4; ++ka)
#pragma unroll
        for (int e = 0; e < 4; ++e) v[4 * ka + e] = buf[ka * 272 + 4 * t + e];
#pragma unroll
    for (int e = 0; e < 4; ++e) dft4<1>(v[e], v[4 + e], v[8 + e], v[12 + e]);
#pragma unroll
    for (int i = 0; i < 16; ++i) asm volatile("" : "+v"(v[i].x), "+v"(v[i].y));
    PACE();
    GAS u64* GA = (GAS u64*)(ws + OFF_GA);
#pragma unroll
    for (int a = 0; a < 4; ++a) {
        u16 o0[4], o1[4];
#pragma unroll
        for (int e = 0; e < 4; ++e) { o0[e] = f2bf(g0 * bfr(v[4 * a + e].x)); o1[e] = f2bf(g1 * bfr(v[4 * a + e].y)); }
        u64 p0 = (u64)o0[0] | ((u64)o0[1] << 16) | ((u64)o0[2] << 32) | ((u64)o0[3] << 48);
        u64 p1 = (u64)o1[0] | ((u64)o1[1] << 16) | ((u64)o1[2] << 32) | ((u64)o1[3] << 48);
        __hip_atomic_store(GA + ((size_t)r0 * D_ + 256 * a + 4 * t) / 4, p0, RLX_AGENT);
        __hip_atomic_store(GA + ((size_t)(r0 + 256) * D_ + 256 * a + 4 * t) / 4, p1, RLX_AGENT);
    }
    PACE(); PACE(); PACE(); PACE(); PACE();
    asm volatile("s_waitcnt vmcnt(0)" ::: "memory");
    if (t == 0) __hip_atomic_store((GAS unsigned*)(ws + OFF_FLAG) + bid * 4 + w, 1u, RLX_AGENT);
    if (w == 0) TS(4, 1);
}

__device__ __forceinline__ void gemm2_tile(int gb, int& lt, int& dt) {
    const int xcd = gb & 7, loc = gb >> 3;
    lt = xcd * 4 + (loc >> 3);
    dt = loc & 7;
}
__device__ __forceinline__ void gemm2_loader(const u16* __restrict__ Cre, const u16* __restrict__ Cim, char* __restrict__ ws,
                                             char* smem, int gb, int lw, int lane) {
    int lt, dt; gemm2_tile(gb, lt, dt);
    const int l0 = lt * 64, d0 = dt * 128;
    const u16* A2 = reinterpret_cast<const u16*>(ws + OFF_A2);
    const int srow = lane >> 3, schunk = (lane & 7) ^ srow;
    const u16* gA = A2 + (size_t)(l0 + 16 * lw + srow) * 4096 + schunk * 8;
    const size_t offB = (size_t)(d0 + 32 * lw + srow) * P_ + schunk * 8;
    auto stage = [&](int kt, int sb) {
        char* sA = smem + sb * G2_STAGE + (16 * lw) * 128 + lane * 16;
        glds16(gA + kt * 64, sA);
        glds16(gA + (size_t)8 * 4096 + kt * 64, sA + 1024);
        if (HILO) {
            glds16(gA + 2048 + kt * 64, sA + 8192);
            glds16(gA + (size_t)8 * 4096 + 2048 + kt * 64, sA + 8192 + 1024);
        }
        char* sB = smem + sb * G2_STAGE + G2_BOFF + (32 * lw) * 128 + lane * 16;
        const u16* gB = (kt < 16 ? Cre : Cim) + offB + (kt & 15) * 64;
#pragma unroll
        for (int q = 0; q < 4; ++q) glds16(gB + (size_t)(8 * q) * P_, sB + q * 1024);
    };
    constexpr int NT = 32;
    stage(0, 0); stage(1, 1);
    if (!HILO) stage(2, 2);
    int sb = 0;
    for (int kt = 0; kt < NT; ++kt) {
        if (HILO) { if (kt + 1 < NT) WAIT_VM(8); else WAIT_VM(0); }
        else { if (kt + 2 < NT) WAIT_VM(12); else if (kt + 1 < NT) WAIT_VM(6); else WAIT_VM(0); }
        BAR();
        int nb = sb + G2_NST - 1; if (nb >= G2_NST) nb -= G2_NST;
        if (kt + G2_NST - 1 < NT) stage(kt + G2_NST - 1, nb);
        sb = sb + 1 == G2_NST ? 0 : sb + 1;
    }
}
constexpr int OPITCH = 132;
__device__ __forceinline__ void gemm2_role(const u16* __restrict__ x, const u16* __restrict__ Dskip, char* __restrict__ ws,
                                           float* __restrict__ out, char* smem, int gb, int tid) {
    const int wid = tid >> 6, lane = tid & 63;
    int lt, dt; gemm2_tile(gb, lt, dt);
    const int l0 = lt * 64, d0 = dt * 128;
    const int wr = wid >> 1, wc = wid & 1, fr = lane & 15, fq = lane >> 4;
    if (wid == 0) TSN(3, 0);
    const float* G = reinterpret_cast<const float*>(ws + OFF_G);
    const int g8 = tid & 15;
    const uint4 dsk = *reinterpret_cast<const uint4*>(Dskip + d0 + g8 * 8);
    uint4 xv[4]; float omg[4];
#pragma unroll
    for (int it = 0; it < 4; ++it) {
        const int l = l0 + it * 16 + (tid >> 4);
        xv[it] = *reinterpret_cast<const uint4*>(x + (size_t)l * D_ + d0 + g8 * 8);
        omg[it] = G[S_ + l];
    }
    f32x4 acc[2][4];
#pragma unroll
    for (int m = 0; m < 2; ++m)
#pragma unroll
        for (int n = 0; n < 4; ++n) acc[m][n] = (f32x4){0.f, 0.f, 0.f, 0.f};
    constexpr int NT = 32;
    bf16x8 ah[2][2], al[2][2], bf_[2][4];
#pragma unroll
    for (int m = 0; m < 2; ++m) { ah[1][m] = (bf16x8){0, 0, 0, 0, 0, 0, 0, 0}; al[1][m] = ah[1][m]; }
#pragma unroll
    for (int n = 0; n < 4; ++n) bf_[1][n] = ah[1][0];
#pragma unroll 8
    for (int kt = 0; kt < NT; ++kt) {
        BAR();
        if (wid == 0) TSN(6 + (kt >> 4), kt & 15);
        const char* tA = smem + (kt % G2_NST) * G2_STAGE;
        const char* tB = tA + G2_BOFF;
#pragma unroll
        for (int m = 0; m < 2; ++m) { ah[0][m] = ldfrag(tA, wr * 32 + m * 16 + fr, fq); if (HILO) al[0][m] = ldfrag(tA + 8192, wr * 32 + m * 16 + fr, fq); }
#pragma unroll
        for (int n = 0; n < 4; ++n) bf_[0][n] = ldfrag(tB, wc * 64 + n * 16 + fr, fq);
        SCHED();
        if (HILO) {
#pragma unroll
            for (int m = 0; m < 2; ++m)
#pragma unroll
                for (int n = 0; n < 4; ++n) MF(al[1][m], bf_[1][n], acc[m][n]);
        }
#pragma unroll
        for (int m = 0; m < 2; ++m)
#pragma unroll
            for (int n = 0; n < 4; ++n) MF(ah[1][m], bf_[1][n], acc[m][n]);
        SCHED();
#pragma unroll
        for (int m = 0; m < 2; ++m)
#pragma unroll
            for (int n = 0; n < 4; ++n) MF(ah[0][m], bf_[0][n], acc[m][n]);
        SCHED();
#pragma unroll
        for (int m = 0; m < 2; ++m) { ah[1][m] = ldfrag(tA, wr * 32 + m * 16 + fr, 4 + fq); if (HILO) al[1][m] = ldfrag(tA + 8192, wr * 32 + m * 16 + fr, 4 + fq); }
#pragma unroll
        for (int n = 0; n < 4; ++n) bf_[1][n] = ldfrag(tB, wc * 64 + n * 16 + fr, 4 + fq);
        SCHED();
        if (HILO) {
#pragma unroll
            for (int m = 0; m < 2; ++m)
#pragma unroll
                for (int n = 0; n < 4; ++n) MF(al[0][m], bf_[0][n], acc[m][n]);
        }
        SCHED();
        LDS_WAIT();
    }
#pragma unroll
    for (int m = 0; m < 2; ++m)
#pragma unroll
        for (int n = 0; n < 4; ++n) { if (HILO) MF(al[1][m], bf_[1][n], acc[m][n]); MF(ah[1][m], bf_[1][n], acc[m][n]); }
    if (wid == 0) TSN(3, 1);
    WG_BARRIER();
    float* Ox = reinterpret_cast<float*>(smem);
#pragma unroll
    for (int m = 0; m < 2; ++m)
#pragma unroll
        for (int n = 0; n < 4; ++n)
#pragma unroll
            for (int j = 0; j < 4; ++j) Ox[(wr * 32 + m * 16 + fq * 4 + j) * OPITCH + wc * 64 + n * 16 + fr] = acc[m][n][j];
    if (wid == 0) {
        const int l = l0 + lane;
        GAS unsigned* fl = (GAS unsigned*)(ws + OFF_FLAG) + (l & 255) * 4 + (l >> 9);
        for (int spin = 0; spin < (1 << 22); ++spin) {
            if (__hip_atomic_load(fl, RLX_AGENT) == 1u) break;
            __builtin_amdgcn_s_sleep(1);
        }
    }
    WG_BARRIER();
    if (wid == 0) TSN(3, 2);
    GAS u64* GA = (GAS u64*)(ws + OFF_GA);
    const unsigned dq[4] = {dsk.x, dsk.y, dsk.z, dsk.w};
    u64 ga0[4], ga1[4];
#pragma unroll
    for (int it = 0; it < 4; ++it) {
        const size_t o = (size_t)(l0 + it * 16 + (tid >> 4)) * D_ + d0 + g8 * 8;
        ga0[it] = __hip_atomic_load(GA + o / 4, RLX_AGENT); ga1[it] = __hip_atomic_load(GA + o / 4 + 1, RLX_AGENT);
    }
#pragma unroll
    for (int it = 0; it < 4; ++it) {
        const int row = it * 16 + (tid >> 4);
        const size_t o = (size_t)(l0 + row) * D_ + d0 + g8 * 8;
        const float* sp = Ox + row * OPITCH + g8 * 8;
        float4 f0 = *reinterpret_cast<const float4*>(sp), f1 = *reinterpret_cast<const float4*>(sp + 4);
        const float f[8] = {f0.x, f0.y, f0.z, f0.w, f1.x, f1.y, f1.z, f1.w};
        const unsigned xq[4] = {xv[it].x, xv[it].y, xv[it].z, xv[it].w};
        const unsigned gq[4] = {(unsigned)ga0[it], (unsigned)(ga0[it] >> 32), (unsigned)ga1[it], (unsigned)(ga1[it] >> 32)};
        float r[8];
#pragma unroll
        for (int j = 0; j < 4; ++j) {
            float y0 = 2.0f * f[2 * j] + bfr(__uint_as_float(xq[j] << 16) * __uint_as_float(dq[j] << 16));
            float y1 = 2.0f * f[2 * j + 1] + bfr(__uint_as_float(xq[j] & 0xffff0000u) * __uint_as_float(dq[j] & 0xffff0000u));
            r[2 * j] = __uint_as_float(gq[j] << 16) + omg[it] * y0;
            r[2 * j + 1] = __uint_as_float(gq[j] & 0xffff0000u) + omg[it] * y1;
        }
        st16f(out + o, make_float4(r[0], r[1], r[2], r[3]));
        st16f(out + o + 4, make_float4(r[4], r[5], r[6], r[7]));
    }
    if (wid == 0) TS(3, 3);
}

constexpr int K3_SMEM = G_LDS + 4 * RW * 8;
NOPK __global__ __launch_bounds__(768, 1) void k3_kernel(const u16* __restrict__ x, const u16* __restrict__ Cre, const u16* __restrict__ Cim,
                                                    const u16* __restrict__ Dskip, char* __restrict__ ws, float* __restrict__ out) {
    extern __shared__ __attribute__((aligned(16))) char smem[];
    const int role = __builtin_amdgcn_readfirstlane(threadIdx.x >> 8);
    if (role == 0) gemm2_role(x, Dskip, ws, out, smem, blockIdx.x, threadIdx.x);
    else if (role == 1) gemm2_loader(Cre, Cim, ws, smem, blockIdx.x, __builtin_amdgcn_readfirstlane((threadIdx.x >> 6) & 3), threadIdx.x & 63);
    else {
        rowifft_role(ws, smem + G_LDS, blockIdx.x, threadIdx.x - 512);
    }
}

extern "C" void kernel_launch(void* const* d_in, const int* in_sizes, int n_in,
                              void* d_out, int out_size, void* d_ws, size_t ws_size,
                              hipStream_t stream) {
    const u16* x = (const u16*)d_in[0];
    const u16* w_r = (const u16*)d_in[1];
    const u16* w_i = (const u16*)d_in[2];
    const u16* lam_re = (const u16*)d_in[4];
    const u16* lam_im = (const u16*)d_in[5];
    const u16* Bre = (const u16*)d_in[6];
    const u16* Bim = (const u16*)d_in[7];
    const u16* Cre = (const u16*)d_in[8];
    const u16* Cim = (const u16*)d_in[9];
    const u16* log_dt = (const u16*)d_in[10];
    const u16* Dskip = (const u16*)d_in[11];
    const u16* gate_w = (const u16*)d_in[12];
    char* ws = (char*)d_ws;
    float* out = (float*)d_out;
    (void)hipFuncSetAttribute((const void*)k2_kernel, hipFuncAttributeMaxDynamicSharedMemorySize, K2_SMEM);
    (void)hipFuncSetAttribute((const void*)k3_kernel, hipFuncAttributeMaxDynamicSharedMemorySize, K3_SMEM);
    k1_kernel<<<256, 512, 0, stream>>>(x, gate_w, w_r, w_i, Bre, Bim, Cre, Cim, ws);
    k2_kernel<<<256, 768, K2_SMEM, stream>>>(x, Bre, Bim, lam_re, lam_im, log_dt, ws);
    k3_kernel<<<256, 768, K3_SMEM, stream>>>(x, Cre, Cim, Dskip, ws, out);
}
```

```cpp
#include <hip/hip_runtime.h>
#include <stdint.h>

typedef __attribute__((ext_vector_type(8))) __bf16 bf16x8;
typedef __attribute__((ext_vector_type(2))) __bf16 bf16x2;
typedef __attribute__((ext_vector_type(8))) _Float16 f16x8;
typedef __attribute__((ext_vector_type(2))) _Float16 f16x2;
typedef __attribute__((ext_vector_type(4))) float f32x4;
typedef __attribute__((ext_vector_type(2))) float f32x2;
typedef __attribute__((ext_vector_type(4))) unsigned u32x4;
typedef __attribute__((ext_vector_type(2))) unsigned u32x2;
typedef __attribute__((ext_vector_type(4))) short s16x4;
typedef __attribute__((ext_vector_type(2))) unsigned short u16x2;
typedef unsigned short u16;
typedef unsigned long long u64;
#define GAS __attribute__((address_space(1)))
#define LAS __attribute__((address_space(3)))
#define NOPK __attribute__((target("no-packed-fp32-ops")))
#define RLX_AGENT __ATOMIC_RELAXED, __HIP_MEMORY_SCOPE_AGENT
#define DEV __device__ __forceinline__

constexpr int S_ = 2048, D_ = 1024, KF = 513;

constexpr unsigned OFF_H = 0;
constexpr unsigned OFF_U = OFF_H + (8u << 20);
constexpr unsigned OFF_T = OFF_U + (4u << 20);
constexpr unsigned OFF_W = OFF_T + (4u << 20);
constexpr unsigned OFF_G = OFF_W + 65u * 65536u;
constexpr unsigned OFF_MX = OFF_G + 16384u;
constexpr unsigned OFF_GRAN = OFF_MX + 4096u;
constexpr unsigned OFF_E = OFF_GRAN + 16u * 1024u * 16u;
constexpr unsigned WS_END = OFF_E + 16u * 128u;

#define TSN(slot, i)

DEV float bf2f(u16 h) { return __uint_as_float((unsigned)h << 16); }
DEV unsigned pkb(float a, float b) { f32x2 v = {a, b}; return __builtin_bit_cast(unsigned, __builtin_convertvector(v, bf16x2)); }
DEV unsigned pkh(float a, float b) { f32x2 v = {a, b}; return __builtin_bit_cast(unsigned, __builtin_convertvector(v, f16x2)); }
DEV float lo2f(unsigned p) { return __uint_as_float(p << 16); }
DEV float hi2f(unsigned p) { return __uint_as_float(p & 0xffff0000u); }
DEV float bfr(float f) { return lo2f(pkb(f, f)); }
DEV float2 cmul(float2 a, float2 b) { return make_float2(a.x * b.x - a.y * b.y, a.x * b.y + a.y * b.x); }
DEV float2 cfma(float2 a, float2 b, float2 c) { return make_float2(fmaf(a.x, b.x, fmaf(-a.y, b.y, c.x)), fmaf(a.x, b.y, fmaf(a.y, b.x, c.y))); }
DEV f32x4 mfb(u32x4 a, u32x4 b, f32x4 c) { return __builtin_amdgcn_mfma_f32_16x16x32_bf16(__builtin_bit_cast(bf16x8, a), __builtin_bit_cast(bf16x8, b), c, 0, 0, 0); }
DEV f32x4 mfh(u32x4 a, u32x4 b, f32x4 c) { return __builtin_amdgcn_mfma_f32_16x16x32_f16(__builtin_bit_cast(f16x8, a), __builtin_bit_cast(f16x8, b), c, 0, 0, 0); }
DEV unsigned pkmax(unsigned a, unsigned b) { return __builtin_bit_cast(unsigned, __builtin_elementwise_max(__builtin_bit_cast(u16x2, a), __builtin_bit_cast(u16x2, b))); }
DEV float vcos(float r) { return __builtin_amdgcn_cosf(__builtin_amdgcn_fractf(r)); }
DEV float vsin(float r) { return __builtin_amdgcn_sinf(__builtin_amdgcn_fractf(r)); }
DEV unsigned ldsa(const void* p) { return (unsigned)(uintptr_t)(LAS const char*)p; }
DEV u32x2 trread(const char* p) { return __builtin_bit_cast(u32x2, __builtin_amdgcn_ds_read_tr16_b64_v4i16((LAS s16x4*)p)); }
DEV u32x4 trfrag(const char* p, int second) { const u32x2 a = trread(p), b = trread(p + second); return (u32x4){a.x, a.y, b.x, b.y}; }
DEV void glds16(const void* g, char* l) { __builtin_amdgcn_global_load_lds((const GAS unsigned*)g, (LAS unsigned*)l, 16, 0, 0); }
DEV u32x4 ldfrag(const char* tile, int row, int chunk) { return *reinterpret_cast<const u32x4*>(tile + row * 128 + ((chunk ^ (row & 7)) << 4)); }
template <int N> DEV void wait_vm() { asm volatile("s_waitcnt vmcnt(%0)" :: "i"(N) : "memory"); }
#define LDS_WAIT() asm volatile("s_waitcnt lgkmcnt(0)" ::: "memory")
#define SCHED() __builtin_amdgcn_sched_barrier(0)
#define BAR() do { __builtin_amdgcn_s_barrier(); asm volatile("" ::: "memory"); } while (0)
#define PIN(x) asm volatile("" : "+v"(x))
typedef __amdgpu_buffer_rsrc_t rsrc_t;
DEV rsrc_t mkrsrc(void* p, unsigned n) { return __builtin_amdgcn_make_buffer_rsrc(p, 0, n, 0x00020000); }
constexpr int SC1 = 16;
#define WT 16
#define WTO 16
template <int AUX> DEV void st128(u32x4 v, rsrc_t rs, unsigned off) { __builtin_amdgcn_raw_buffer_store_b128(v, rs, off, 0, AUX); }
DEV void lds_add(unsigned a) { asm volatile("ds_add_u32 %0, %1" :: "v"(a), "v"(1u) : "memory"); }
DEV unsigned lds_peek(unsigned a) {
    unsigned v;
    asm volatile("ds_read_b32 %0, %1\n\ts_waitcnt lgkmcnt(0)" : "=v"(v) : "v"(a) : "memory");
    return __builtin_amdgcn_readfirstlane(v);
}
DEV void tile_of(int gb, int& ta, int& tb) {
    const int xcd = gb & 7, loc = gb >> 3;
    ta = (xcd >> 1) * 4 + (loc >> 3);
    tb = (xcd & 1) * 8 + (loc & 7);
}
DEV void rows4(float a, float& a0, float& a1, float& a2, float& a3) {
    const unsigned u = __float_as_uint(a);
    const auto r = __builtin_amdgcn_permlane32_swap(u, u, false, false);
    const auto p = __builtin_amdgcn_permlane16_swap(r[0], r[0], false, false);
    const auto s = __builtin_amdgcn_permlane16_swap(r[1], r[1], false, false);
    a0 = __uint_as_float(p[0]); a1 = __uint_as_float(p[1]); a2 = __uint_as_float(s[0]); a3 = __uint_as_float(s[1]);
}
DEV float rowsum4(float a) {
    const unsigned u = __float_as_uint(a);
    const auto r = __builtin_amdgcn_permlane32_swap(u, u, false, false);
    const unsigned t = __float_as_uint(__uint_as_float(r[0]) + __uint_as_float(r[1]));
    const auto p = __builtin_amdgcn_permlane16_swap(t, t, false, false);
    return __uint_as_float(p[0]) + __uint_as_float(p[1]);
}
DEV float2 sel4(int q, float2 a, float2 b, float2 c, float2 d) {
    PIN(d.x); PIN(d.y);
    const float2 e = (q & 1) ? b : a, f = (q & 1) ? d : c;
    return (q & 2) ? f : e;
}
DEV unsigned wave_pkmax(unsigned m) {
#pragma unroll
    for (int s = 32; s >= 1; s >>= 1) m = pkmax(m, (unsigned)__shfl_xor((int)m, s));
    return m;
}
DEV u32x4 to16(unsigned p0, unsigned p1, unsigned n0, unsigned n1) {
    const auto a = __builtin_amdgcn_permlane16_swap(p0, n0, false, false);
    const auto b = __builtin_amdgcn_permlane16_swap(p1, n1, false, false);
    return (u32x4){a[0], b[0], a[1], b[1]};
}

constexpr int A_STAGE = 32768, A_XT = 4 * A_STAGE, A_PRM = A_XT + 16384, A_AGG = A_PRM + 2048, A_FLG = A_AGG + 512, A_GW = A_FLG + 64,
              A_RAW = A_GW + 2048, KA_SMEM = A_RAW + 1024;

DEV void ka_mma(const u16* __restrict__ lam_re, const u16* __restrict__ lam_im, const u16* __restrict__ log_dt,
                const u16* __restrict__ gate_w, char* ws, char* smem, int gb, int wid, int lane) {
    int lt, pt; tile_of(gb, lt, pt);
    const int wr = wid >> 1, wc = wid & 1, c = lane & 15, q = lane >> 4;
    const rsrc_t rs = mkrsrc(ws, WS_END);
    if (wid == 0) TSN(0, 0);
    GAS u64* Ep = (GAS u64*)(ws + OFF_E + pt * 128);
    u64 tag = __hip_atomic_load(Ep, RLX_AGENT) + 0x9E3779B97F4A7C15ull;
    const u32x2 gwv = *reinterpret_cast<const u32x2*>(gate_w + wid * 256 + lane * 4);
    const unsigned praw = (wid == 0 ? lam_re : wid == 1 ? lam_im : log_dt)[pt * 64 + lane];
#pragma unroll
    for (int i = 0; i < 4; ++i)
        st128<WT>((u32x4){~0u, ~0u, ~0u, ~0u}, rs, wid * 1024 + lane * 16 + OFF_T + gb * 16384 + i * 4096);
    if (wid == 0 && lane < 8) reinterpret_cast<unsigned*>(smem + A_FLG)[lane] = 0u;
    *reinterpret_cast<u32x2*>(smem + A_GW + wid * 512 + lane * 8) = gwv;
    reinterpret_cast<unsigned*>(smem + A_RAW)[lane * 4 + wid] = praw;
    PIN(tag);
    LDS_WAIT();
    f32x4 acc[4][4];
#pragma unroll
    for (int m = 0; m < 4; ++m)
#pragma unroll
        for (int n = 0; n < 4; ++n) acc[m][n] = (f32x4){0.f, 0.f, 0.f, 0.f};
    u32x4 af[2][4], bf_[2][4];
#pragma unroll
    for (int m = 0; m < 4; ++m) { af[1][m] = (u32x4){0, 0, 0, 0}; bf_[1][m] = af[1][m]; }
#pragma unroll 4
    for (int kt = 0; kt < 16; ++kt) {
        BAR();
        if (wid == 0) TSN(0, 8 + kt);
        const char* tA = smem + (kt & 3) * A_STAGE;
        const char* tB = tA + 16384;
#pragma unroll
        for (int m = 0; m < 4; ++m) af[0][m] = ldfrag(tA, wr * 64 + m * 16 + c, q);
#pragma unroll
        for (int n = 0; n < 4; ++n) bf_[0][n] = ldfrag(tB, wc * 64 + n * 16 + c, q);
        SCHED();
#pragma unroll
        for (int m = 0; m < 4; ++m)
#pragma unroll
            for (int n = 0; n < 4; ++n) acc[m][n] = mfb(af[1][m], bf_[1][n], acc[m][n]);
        SCHED();
#pragma unroll
        for (int m = 0; m < 2; ++m)
#pragma unroll
            for (int n = 0; n < 4; ++n) acc[m][n] = mfb(af[0][m], bf_[0][n], acc[m][n]);
        SCHED();
#pragma unroll
        for (int m = 0; m < 4; ++m) af[1][m] = ldfrag(tA, wr * 64 + m * 16 + c, 4 + q);
#pragma unroll
        for (int n = 0; n < 4; ++n) bf_[1][n] = ldfrag(tB, wc * 64 + n * 16 + c, 4 + q);
        SCHED();
#pragma unroll
        for (int m = 2; m < 4; ++m)
#pragma unroll
            for (int n = 0; n < 4; ++n) acc[m][n] = mfb(af[0][m], bf_[0][n], acc[m][n]);
        SCHED();
        LDS_WAIT();
    }
    float2 lam[2], coef[2], l16[2], l128[2];
#pragma unroll
    for (int n = 0; n < 2; ++n) {
        const f32x4* pp = reinterpret_cast<const f32x4*>(smem + A_PRM + (wc * 32 + 2 * c + n) * 32);
        const f32x4 a = pp[0], b = pp[1];
        lam[n] = make_float2(a[0], a[1]); coef[n] = make_float2(a[2], a[3]); l16[n] = make_float2(b[0], b[1]); l128[n] = make_float2(b[2], b[3]);
    }
#pragma unroll
    for (int m = 0; m < 4; ++m)
#pragma unroll
        for (int n = 0; n < 4; ++n) acc[m][n] = mfb(af[1][m], bf_[1][n], acc[m][n]);
    if (wid == 0) TSN(0, 1);
    float2 h[2][16];
#pragma unroll
    for (int n = 0; n < 2; ++n) h[n][0] = make_float2(acc[0][n][0], acc[0][2 + n][0]);
#pragma unroll
    for (int t = 1; t < 16; ++t)
#pragma unroll
        for (int n = 0; n < 2; ++n) h[n][t] = cfma(lam[n], h[n][t - 1], make_float2(acc[t >> 2][n][t & 3], acc[t >> 2][2 + n][t & 3]));
    float2 loc[2], wag[2], wq[2];
#pragma unroll
    for (int n = 0; n < 2; ++n) {
        float2 a0, a1, a2, a3;
        rows4(h[n][15].x, a0.x, a1.x, a2.x, a3.x);
        rows4(h[n][15].y, a0.y, a1.y, a2.y, a3.y);
        const float2 s2 = cfma(l16[n], a0, a1), s3 = cfma(l16[n], s2, a2);
        wag[n] = cfma(l16[n], s3, a3);
        loc[n] = sel4(q, make_float2(0.f, 0.f), a0, s2, s3);
    }
    f32x4* aggp = reinterpret_cast<f32x4*>(smem + A_AGG) + wc * 16 + c;
    const unsigned flg = ldsa(smem + A_FLG) + wc * 4;
    if (wr == 0) {
        if (q == 0) *aggp = (f32x4){wag[0].x, wag[0].y, wag[1].x, wag[1].y};
        LDS_WAIT();
        if (lane == 0) lds_add(flg);
    }
#pragma unroll
    for (int n = 0; n < 2; ++n) {
        const float2 l32 = cmul(l16[n], l16[n]);
        wq[n] = sel4(q, make_float2(1.f, 0.f), l16[n], l32, cmul(l32, l16[n]));
        if (wr == 1) l16[n] = cmul(l32, l32);
    }
    if (wr == 1) {
        for (int spin = 0; spin < (1 << 20); ++spin) { if (lds_peek(flg)) break; }
        const f32x4 w0 = *aggp;
        const float2 W0[2] = {make_float2(w0[0], w0[1]), make_float2(w0[2], w0[3])};
        const float2 g0 = cfma(l16[0], W0[0], wag[0]), g1 = cfma(l16[1], W0[1], wag[1]);
        if (lt < 15 && q < 2) {
            const float2 gg = q == 0 ? g0 : g1;
            st128<SC1>((u32x4){__float_as_uint(gg.x), (unsigned)tag, __float_as_uint(gg.y), (unsigned)(tag >> 32)},
                       rs, (2 * c + q) * 16 + OFF_GRAN + (lt * 1024 + pt * 64 + wc * 32) * 16);
        }
#pragma unroll
        for (int n = 0; n < 2; ++n) { loc[n] = cfma(wq[n], W0[n], loc[n]); wq[n] = cmul(wq[n], l16[n]); }
    }
    if (wid == 0) TSN(0, 2);
    float2 pw[2][16];
#pragma unroll
    for (int n = 0; n < 2; ++n) pw[n][0] = cmul(coef[n], lam[n]);
#pragma unroll
    for (int t = 1; t < 16; ++t)
#pragma unroll
        for (int n = 0; n < 2; ++n) pw[n][t] = cmul(pw[n][t - 1], lam[n]);
#pragma unroll
    for (int t = 0; t < 16; ++t)
#pragma unroll
        for (int n = 0; n < 2; ++n) h[n][t] = cmul(coef[n], h[n][t]);
    float2 lq[2], l512[2];
#pragma unroll
    for (int n = 0; n < 2; ++n) {
        const float2 l256 = cmul(l128[n], l128[n]);
        lq[n] = sel4(q, make_float2(1.f, 0.f), l128[n], l256, cmul(l256, l128[n]));
        l512[n] = cmul(l256, l256);
    }
#pragma unroll
    for (int t = 0; t < 16; ++t) { PIN(h[0][t].x); PIN(h[0][t].y); PIN(h[1][t].x); PIN(h[1][t].y); PIN(pw[0][t].x); PIN(pw[0][t].y); PIN(pw[1][t].x); PIN(pw[1][t].y); }
    if (wid == 0) TSN(0, 3);
    if (lt > 0) {
        u32x4 gr[4][2];
#pragma unroll 1
        for (int spin = 0; spin < (1 << 20); ++spin) {
            bool ok = true;
#pragma unroll
            for (int j = 0; j < 4; ++j) {
                const int cc = lt - 1 - (4 * j + q);
                unsigned off = cc >= 0 ? (unsigned)(OFF_GRAN + (cc * 1024 + pt * 64 + wc * 32 + 2 * c) * 16) : 0x80000000u;
                PIN(off);
                gr[j][0] = __builtin_amdgcn_raw_buffer_load_b128(rs, off, 0, SC1);
                gr[j][1] = __builtin_amdgcn_raw_buffer_load_b128(rs, off + 16, 0, SC1);
            }
#pragma unroll
            for (int j = 0; j < 4; ++j) {
                const bool v = lt - 1 - (4 * j + q) >= 0;
#pragma unroll
                for (int n = 0; n < 2; ++n) ok = ok && (!v || (gr[j][n].y == (unsigned)tag && gr[j][n].w == (unsigned)(tag >> 32)));
            }
            if (__all(ok)) break;
            asm volatile("" ::: "memory");
        }
        if (lt == 15 && wid == 0 && lane == 0) __hip_atomic_store(Ep, tag, RLX_AGENT);
#pragma unroll
        for (int n = 0; n < 2; ++n) {
            float2 cs = make_float2(__uint_as_float(gr[3][n].x), __uint_as_float(gr[3][n].z));
#pragma unroll
            for (int j = 2; j >= 0; --j) cs = cfma(l512[n], cs, make_float2(__uint_as_float(gr[j][n].x), __uint_as_float(gr[j][n].z)));
            cs = cmul(cs, lq[n]);
            cs.x = rowsum4(cs.x); cs.y = rowsum4(cs.y);
            loc[n] = cfma(wq[n], cs, loc[n]);
        }
    }
    if (wid == 0) TSN(0, 4);
    const unsigned hoff = (unsigned)((lt * 8 + wr * 4 + q) * 128 + wc * 64 + c * 4);
#pragma unroll
    for (int t = 0; t < 16; ++t) {
        const float2 v0 = cfma(pw[0][t], loc[0], h[0][t]), v1 = cfma(pw[1][t], loc[1], h[1][t]);
        __builtin_amdgcn_raw_buffer_store_b32(pkb(v0.x, v1.x), rs, hoff, OFF_H + (t * 32 + pt) * 16384, 0);
        __builtin_amdgcn_raw_buffer_store_b32(pkb(-v0.y, -v1.y), rs, hoff, OFF_H + (t * 32 + 16 + pt) * 16384, 0);
    }
    if (wid == 0) TSN(0, 5);
}

struct __attribute__((packed, aligned(2))) U64U { u32x2 v; };
DEV void w_load(const u16* __restrict__ w_r, const u16* __restrict__ w_i, int k1, int qw, int lw, int lane, u32x2 (&R)[4], u32x2 (&I)[4], unsigned& sp) {
    const int kd0 = 4 * (lane & 15) + 64 * (4 * lw + (lane >> 4));
    const int col = lw < 2 ? kd0 : 1021 - kd0;
#pragma unroll
    for (int r = 0; r < 4; ++r) {
        const int ks = k1 + 128 * (4 * qw + r), row = lw < 2 ? ks : (S_ - ks) & (S_ - 1);
        R[r] = reinterpret_cast<const U64U*>(w_r + row * KF + col)->v;
        I[r] = reinterpret_cast<const U64U*>(w_i + row * KF + col)->v;
    }
    const int ks = k1 + 128 * (4 * qw + (lane & 3)), row = lw < 2 ? (S_ - ks) & (S_ - 1) : ks;
    sp = ((lane & 4) ? w_i : w_r)[row * KF + (lw < 2 ? 0 : 512)];
}
DEV unsigned w_store(rsrc_t rs, int k1, int qw, int lw, int lane, u32x2 (&R)[4], u32x2 (&I)[4], unsigned sp) {
    unsigned o[4][4], mx = 0;
    const unsigned cj = lw < 2 ? 0u : 0x80008000u;
#pragma unroll
    for (int r = 0; r < 4; ++r) {
        mx = pkmax(mx, pkmax(pkmax(R[r].x & 0x7fff7fffu, R[r].y & 0x7fff7fffu), pkmax(I[r].x & 0x7fff7fffu, I[r].y & 0x7fff7fffu)));
        const unsigned i0 = I[r].x ^ cj, i1 = I[r].y ^ cj;
        o[0][r] = __builtin_amdgcn_perm(i0, R[r].x, 0x05040100u); o[1][r] = __builtin_amdgcn_perm(i0, R[r].x, 0x07060302u);
        o[2][r] = __builtin_amdgcn_perm(i1, R[r].y, 0x05040100u); o[3][r] = __builtin_amdgcn_perm(i1, R[r].y, 0x07060302u);
    }
    mx = pkmax(mx, sp & 0x7fffu);
    const bool f0 = lane == 0 && lw == 0, f3 = lane == 0 && lw == 2;
#pragma unroll
    for (int r = 0; r < 4; ++r) {
        const float pr = lo2f(__builtin_amdgcn_readlane(sp, r)), pi = lo2f(__builtin_amdgcn_readlane(sp, 4 + r) ^ (lw < 2 ? 0x8000u : 0u));
        const unsigned t = lw < 2 ? o[0][r] : o[3][r];
        unsigned v = pkb(0.5f * (lo2f(t) + pr), 0.5f * (hi2f(t) + pi));
        PIN(v);
        o[0][r] = f0 ? v : o[0][r]; o[3][r] = f3 ? v : o[3][r];
    }
#pragma unroll
    for (int e = 0; e < 4; ++e)
        st128<WT>((u32x4){o[e][0], o[e][1], o[e][2], o[e][3]}, rs, (lane & 15) * 1024 + (lane >> 4) * 16 +
                  OFF_W + (k1 * 4 + (lw < 2 ? e : 3 - e)) * 16384 + qw * 256 + lw * 64);
    return mx;
}

DEV void ka_load(const u16* __restrict__ x, const u16* __restrict__ w_r, const u16* __restrict__ w_i, const u16* __restrict__ Bre,
                 const u16* __restrict__ Bim, const u16* __restrict__ Cre, const u16* __restrict__ Cim, char* ws, char* smem, int gb, int lw, int lane) {
    int lt, pt; tile_of(gb, lt, pt);
    const int c = lane & 15, q = lane >> 4;
    const int srow = lane >> 3, schunk = (lane & 7) ^ srow;
    const int rot = (lt + pt) & 15;
    const rsrc_t rs = mkrsrc(ws, WS_END);
    const char* gA = (const char*)x + (size_t)(lt * 128 + 64 * (lw >> 1) + 8 * (lw & 1) + 16 * (srow >> 2) + (srow & 3)) * 2048 + schunk * 16;
    const char* gB = (const char*)((lw & 1) ? Bim : Bre) + (size_t)(pt * 64 + 32 * (lw >> 1) + 2 * srow) * 2048 + schunk * 16;
    auto stage = [&](int kk) {
        const int ko = ((kk + rot) & 15) * 128;
        char* l = smem + (kk & 3) * A_STAGE + lw * 4096 + lane * 16;
#pragma unroll
        for (int i = 0; i < 4; ++i) glds16(gA + (32 * (i & 1) + 4 * (i >> 1)) * 2048 + ko, l + i * 1024);
#pragma unroll
        for (int i = 0; i < 4; ++i) glds16(gB + (16 * (i & 1) + (i >> 1)) * 2048 + ko, l + 16384 + i * 1024);
    };
    stage(0); stage(1); stage(2);
    SCHED();
    const int gi = 8 * pt + 2 * lw + (lane >> 5);
    const unsigned gaddr = ldsa(smem) + gi * 128 + ((((lane & 31) >> 2) ^ (gi & 7)) << 4) + (lane & 3) * 4;
    const unsigned gwaddr = ldsa(smem + A_GW) + (lane & 31) * 4;
    float gsum = 0.f;
    u32x4 fch[4], fcl[4], fsh[4], fsl[4];
    const float mrev = (float)(16 * lw + c) * (1.0f / 128.0f);
    const bool m0 = 16 * lw + c == 0;
    u32x2 R[4], I[4]; unsigned sp;
    constexpr int NAUX = 13;
#pragma unroll
    for (int kt = 0; kt < 16; ++kt) {
        if (kt <= 12) wait_vm<16>(); else if (kt == 13) wait_vm<16 + NAUX>(); else if (kt == 14) wait_vm<8 + NAUX>(); else wait_vm<NAUX>();
        BAR();
        if (kt + 3 < 16) stage(kt + 3);
        if (kt == 12) {
            SCHED();
            const char* gx = (const char*)x + (size_t)(16 * (lane >> 1) + lt) * 2048 + lw * 512 + pt * 32 + (lane & 1) * 16;
#pragma unroll
            for (int i = 0; i < 4; ++i) glds16(gx + (size_t)i * 512 * 2048, smem + A_XT + lw * 4096 + i * 1024 + lane * 16);
            w_load(w_r, w_i, gb >> 2, gb & 3, lw, lane, R, I, sp);
        }
        SCHED();
        unsigned xv, gw;
        asm volatile("ds_read_b32 %0, %2\n\tds_read_b32 %1, %3\n\ts_waitcnt lgkmcnt(0)" : "=&v"(xv), "=&v"(gw)
                     : "v"(gaddr + (kt & 3) * A_STAGE), "v"(gwaddr + ((kt + rot) & 15) * 128) : "memory");
        const unsigned pr = pkb(lo2f(xv) * lo2f(gw), hi2f(xv) * hi2f(gw));
        gsum += lo2f(pr); gsum += hi2f(pr); PIN(gsum);
        {
            float th = mrev; PIN(th);
            const float t0 = th * (float)(32 * (kt >> 2) + 16 * ((kt >> 1) & 1) + 2 * (kt & 1) + 4 * q), t1 = t0 + th;
            const float c0 = vcos(t0), c1 = vcos(t1), s0 = m0 ? 1.f : -vsin(t0), s1 = m0 ? -1.f : -vsin(t1);
            const unsigned ch = pkb(c0, c1), sh = pkb(s0, s1);
            fch[kt >> 2][kt & 3] = ch; fcl[kt >> 2][kt & 3] = pkb(c0 - lo2f(ch), c1 - hi2f(ch));
            fsh[kt >> 2][kt & 3] = sh; fsl[kt >> 2][kt & 3] = pkb(s0 - lo2f(sh), s1 - hi2f(sh));
            PIN(fch[kt >> 2][kt & 3]); PIN(fcl[kt >> 2][kt & 3]); PIN(fsh[kt >> 2][kt & 3]); PIN(fsl[kt >> 2][kt & 3]);
        }
        if (kt == 2 && lw == 1) {
            u32x4 raw;
            asm volatile("ds_read_b128 %0, %1\n\ts_waitcnt lgkmcnt(0)" : "=v"(raw) : "v"(ldsa(smem + A_RAW) + lane * 16) : "memory");
            const float lr = lo2f(raw.x), li = lo2f(raw.y);
            const float dt = bfr(expf(lo2f(raw.z)));
            const float er = expf(dt * lr);
            float sn, cs; sincosf(dt * li, &sn, &cs);
            const float2 lam = make_float2(er * cs, er * sn);
            const float nr = lam.x - 1.0f, ni = lam.y, den = lr * lr + li * li;
            const float2 coef = make_float2((nr * lr + ni * li) / den, (ni * lr - nr * li) / den);
            float2 l16 = lam;
#pragma unroll
            for (int i = 0; i < 4; ++i) l16 = cmul(l16, l16);
            float2 l128 = l16;
#pragma unroll
            for (int i = 0; i < 3; ++i) l128 = cmul(l128, l128);
            const unsigned pa = ldsa(smem + A_PRM) + lane * 32;
            asm volatile("ds_write_b128 %0, %1\n\tds_write_b128 %0, %2 offset:16\n\ts_waitcnt lgkmcnt(0)" ::
                         "v"(pa), "v"((f32x4){lam.x, lam.y, coef.x, coef.y}), "v"((f32x4){l16.x, l16.y, l128.x, l128.y}) : "memory");
        }
        SCHED();
    }
    if (lw == 0) TSN(1, 0);
    PIN(sp);
    const unsigned cnt = ldsa(smem + A_FLG) + 8;
    if (lane == 0) lds_add(cnt);
#pragma unroll
    for (int s = 16; s >= 1; s >>= 1) gsum += __shfl_xor(gsum, s);
    {
        const float g = bfr(1.0f / (1.0f + expf(-bfr(gsum))));
        const int tau = (gi & 64) | ((gi & 12) << 2) | ((gi & 48) >> 2) | (gi & 3);
        if ((lane & 31) == 0)
            __builtin_amdgcn_raw_buffer_store_b64((u32x2){__float_as_uint(g), __float_as_uint(bfr(1.0f - g))}, rs, (lt * 128 + tau) * 8, OFF_G, 0);
    }
    if (lw == 0) TSN(1, 1);
    unsigned mx = w_store(rs, gb >> 2, gb & 3, lw, lane, R, I, sp);
    if (gb < 4) w_load(w_r, w_i, 64, gb & 3, lw, lane, R, I, sp);
    const unsigned ctouch = *reinterpret_cast<const unsigned*>((const char*)((lw & 1) ? Cim : Cre) + (size_t)gb * 8192 + (lane & 31) * 128 + (lw >> 1) * 4096);
    for (int spin = 0; spin < (1 << 20); ++spin) { if (lds_peek(cnt) >= 4u) break; }
    if (lw == 0) TSN(1, 2);
    f32x4 vr[4], vi[4];
#pragma unroll
    for (int d1 = 0; d1 < 4; ++d1) { vr[d1] = (f32x4){0.f, 0.f, 0.f, 0.f}; vi[d1] = vr[d1]; }
#pragma unroll
    for (int ks = 0; ks < 4; ++ks) {
        u32x4 xa[4];
#pragma unroll
        for (int d1 = 0; d1 < 4; ++d1) xa[d1] = trfrag(smem + A_XT + d1 * 4096 + ks * 1024 + lane * 8, 512);
#pragma unroll
        for (int d1 = 0; d1 < 4; ++d1) {
            vr[d1] = mfb(xa[d1], fcl[ks], vr[d1]); vi[d1] = mfb(xa[d1], fsl[ks], vi[d1]);
            vr[d1] = mfb(xa[d1], fch[ks], vr[d1]); vi[d1] = mfb(xa[d1], fsh[ks], vi[d1]);
        }
    }
    f32x4 ur[4], ui[4];
    {
        const f32x4 t0r = vr[0] + vr[2], t1r = vr[0] - vr[2], t2r = vr[1] + vr[3], t3r = vr[1] - vr[3];
        const f32x4 t0i = vi[0] + vi[2], t1i = vi[0] - vi[2], t2i = vi[1] + vi[3], t3i = vi[1] - vi[3];
        ur[0] = t0r + t2r; ui[0] = t0i + t2i; ur[2] = t0r - t2r; ui[2] = t0i - t2i;
        ur[1] = t1r + t3i; ui[1] = t1i - t3r; ur[3] = t1r - t3i; ui[3] = t1i + t3r;
        if (m0) { ur[1] = t1r; ui[1] = -t3r; ur[3] = t1i; ui[3] = t3i; }
    }
    unsigned um = 0;
#pragma unroll
    for (int j1 = 0; j1 < 4; ++j1) {
        const unsigned p0 = pkb(ur[j1][0], ur[j1][1]), p1 = pkb(ur[j1][2], ur[j1][3]), n0 = pkb(ui[j1][0], ui[j1][1]), n1 = pkb(ui[j1][2], ui[j1][3]);
        st128<WT>(to16(p0, p1, n0, n1), rs, c * 65536 + (q & 1) * 32 + (q >> 1) * 16 + OFF_U + (lw * 64 + j1) * 16384 + lt * 1024 + pt * 64);
        um = pkmax(um, pkmax(pkmax(p0 & 0x7fff7fffu, p1 & 0x7fff7fffu), pkmax(n0 & 0x7fff7fffu, n1 & 0x7fff7fffu)));
    }
    if (gb < 4) mx = pkmax(mx, w_store(rs, 64, gb & 3, lw, lane, R, I, sp));
    um = pkmax(um, um << 16) & 0xffff0000u;
    mx = pkmax(mx, mx >> 16) & 0xffffu;
    um = wave_pkmax(um | mx);
    if (lane == 0) __builtin_amdgcn_raw_buffer_store_b32(um, rs, 0, OFF_MX + (gb * 4 + lw) * 4, 0);
    asm volatile("" :: "v"(ctouch));
    if (lw == 0) TSN(1, 3);
}

NOPK __global__ __launch_bounds__(512, 1) void ka_kernel(const u16* __restrict__ x, const u16* __restrict__ w_r, const u16* __restrict__ w_i,
                                                         const u16* __restrict__ Bre, const u16* __restrict__ Bim, const u16* __restrict__ Cre,
                                                         const u16* __restrict__ Cim, const u16* __restrict__ lam_re,
                                                         const u16* __restrict__ lam_im, const u16* __restrict__ log_dt,
                                                         const u16* __restrict__ gate_w, char* ws) {
    extern __shared__ __attribute__((aligned(16))) char smem[];
    const int wid = __builtin_amdgcn_readfirstlane(threadIdx.x >> 6), lane = threadIdx.x & 63;
    if (wid < 4) ka_mma(lam_re, lam_im, log_dt, gate_w, ws, smem, blockIdx.x, wid, lane);
    else ka_load(x, w_r, w_i, Bre, Bim, Cre, Cim, ws, smem, blockIdx.x, wid - 4, lane);
}

constexpr int B_STAGE = 24576, B_UT = 4 * B_STAGE, B_E1 = B_UT + 16384, E1P = 1040, B_E2 = B_E1 + 18 * E1P + 32, B_EX = B_E2 + 16384,
              B_MX = B_EX + 2048, B_CNT = B_MX + 16, KB_SMEM = B_CNT + 16;
constexpr int B_GA = B_E1;

DEV void kb_load(const u16* __restrict__ Cre, const u16* __restrict__ Cim, char* ws, char* smem, int gb, int lw, int lane) {
    int s2, da; tile_of(gb, s2, da);
    const int srow = lane >> 3, schunk = (lane & 7) ^ srow;
    const size_t offA = (size_t)(256 * lw + 16 * da + srow) * 2048 + schunk * 16;
    const char* gB = ws + OFF_H + (size_t)s2 * 32 * 16384 + (32 * lw + srow) * 128 + schunk * 16;
    auto stage = [&](int kt) {
        char* l = smem + (kt & 3) * B_STAGE + lane * 16;
        const char* gA = (const char*)(kt < 16 ? Cre : Cim) + offA + (kt & 15) * 128;
        glds16(gA, l + lw * 2048);
        glds16(gA + 8 * 2048, l + lw * 2048 + 1024);
#pragma unroll
        for (int i = 0; i < 4; ++i) glds16(gB + kt * 16384 + i * 1024, l + 8192 + lw * 4096 + i * 1024);
    };
    stage(0); stage(1); stage(2);
#pragma unroll 1
    for (int kt = 0; kt < 29; ++kt) { wait_vm<12>(); BAR(); stage(kt + 3); }
    wait_vm<12>(); BAR();
    wait_vm<6>(); BAR();
    wait_vm<0>(); BAR();
}

DEV void kb_mma(const u16* __restrict__ x, const u16* __restrict__ Dskip, char* ws, float* __restrict__ out, char* smem,
                int gb, int wid, int lane) {
    int s2, da; tile_of(gb, s2, da);
    const int c = lane & 15, q = lane >> 4;
    if (wid == 0) TSN(2, 0);
    f32x4 acc[4][2];
#pragma unroll
    for (int m = 0; m < 4; ++m) { acc[m][0] = (f32x4){0.f, 0.f, 0.f, 0.f}; acc[m][1] = acc[m][0]; }
    u32x4 af[2][4], bf_[2][2];
#pragma unroll
    for (int m = 0; m < 4; ++m) af[1][m] = (u32x4){0, 0, 0, 0};
    bf_[1][0] = af[1][0]; bf_[1][1] = af[1][0];
    u32x2 xv[4][2], dk[4];
    float omg[2];
    auto step = [&](int kt) {
        BAR();
        if (wid == 0) TSN(2, 8 + kt);
        const char* tA = smem + (kt & 3) * B_STAGE;
        const char* tB = tA + 8192;
#pragma unroll
        for (int m = 0; m < 4; ++m) af[0][m] = ldfrag(tA, m * 16 + c, q);
#pragma unroll
        for (int n = 0; n < 2; ++n) bf_[0][n] = ldfrag(tB, wid * 32 + n * 16 + c, q);
        SCHED();
#pragma unroll
        for (int m = 0; m < 4; ++m)
#pragma unroll
            for (int n = 0; n < 2; ++n) acc[m][n] = mfb(af[1][m], bf_[1][n], acc[m][n]);
        SCHED();
#pragma unroll
        for (int m = 0; m < 2; ++m)
#pragma unroll
            for (int n = 0; n < 2; ++n) acc[m][n] = mfb(af[0][m], bf_[0][n], acc[m][n]);
        SCHED();
#pragma unroll
        for (int m = 0; m < 4; ++m) af[1][m] = ldfrag(tA, m * 16 + c, 4 + q);
#pragma unroll
        for (int n = 0; n < 2; ++n) bf_[1][n] = ldfrag(tB, wid * 32 + n * 16 + c, 4 + q);
        SCHED();
#pragma unroll
        for (int m = 2; m < 4; ++m)
#pragma unroll
            for (int n = 0; n < 2; ++n) acc[m][n] = mfb(af[0][m], bf_[0][n], acc[m][n]);
        SCHED();
        LDS_WAIT();
    };
#pragma unroll 4
    for (int kt = 0; kt < 24; ++kt) step(kt);
    const int col = 16 * da + 4 * q;
#pragma unroll
    for (int n = 0; n < 2; ++n) {
        const int row = 16 * (32 * wid + 16 * n + c) + s2;
#pragma unroll
        for (int m = 0; m < 4; ++m) xv[m][n] = *reinterpret_cast<const u32x2*>(x + (size_t)row * D_ + 256 * m + col);
        omg[n] = reinterpret_cast<const float*>(ws + OFF_G)[2 * row + 1];
    }
#pragma unroll
    for (int m = 0; m < 4; ++m) dk[m] = *reinterpret_cast<const u32x2*>(Dskip + 256 * m + col);
    SCHED();
#pragma unroll 4
    for (int kt = 24; kt < 32; ++kt) step(kt);
#pragma unroll
    for (int m = 0; m < 4; ++m)
#pragma unroll
        for (int n = 0; n < 2; ++n) acc[m][n] = mfb(af[1][m], bf_[1][n], acc[m][n]);
#pragma unroll
    for (int m = 0; m < 4; ++m)
#pragma unroll
        for (int n = 0; n < 2; ++n) {
            const unsigned p0 = pkb(lo2f(xv[m][n].x) * lo2f(dk[m].x), hi2f(xv[m][n].x) * hi2f(dk[m].x));
            const unsigned p1 = pkb(lo2f(xv[m][n].y) * lo2f(dk[m].y), hi2f(xv[m][n].y) * hi2f(dk[m].y));
            acc[m][n] = (f32x4){fmaf(2.0f, acc[m][n][0], lo2f(p0)), fmaf(2.0f, acc[m][n][1], hi2f(p0)), fmaf(2.0f, acc[m][n][2], lo2f(p1)), fmaf(2.0f, acc[m][n][3], hi2f(p1))};
            PIN(acc[m][n]);
        }
    if (wid == 0) TSN(2, 1);
    const rsrc_t ro = mkrsrc(out, S_ * D_ * 4);
    BAR();
    if (wid == 0) TSN(2, 2);
#pragma unroll
    for (int n = 0; n < 2; ++n) {
        const int row = 16 * (32 * wid + 16 * n + c) + s2;
#pragma unroll
        for (int mp = 0; mp < 2; ++mp) {
            const u32x4 ga = *reinterpret_cast<const u32x4*>(smem + B_GA + ((wid * 2 + n) * 2 + mp) * 1024 + lane * 16);
#pragma unroll
            for (int mm = 0; mm < 2; ++mm) {
                const int m = 2 * mp + mm;
                const unsigned g0 = ga[2 * mm], g1 = ga[2 * mm + 1];
                const f32x4 o = {lo2f(g0) + omg[n] * acc[m][n][0], hi2f(g0) + omg[n] * acc[m][n][1], lo2f(g1) + omg[n] * acc[m][n][2], hi2f(g1) + omg[n] * acc[m][n][3]};
                st128<WTO>(__builtin_bit_cast(u32x4, o), ro, (row * D_ + 256 * m + col) * 4);
            }
        }
    }
    if (wid == 0) TSN(2, 3);
}

struct CM { u32x4 re, im; };
DEV CM cmat(float t0, float dt) {
    float cc[4], ss[4];
#pragma unroll
    for (int e = 0; e < 4; ++e) { const float f = __builtin_amdgcn_fractf(fmaf((float)e, dt, t0)); cc[e] = __builtin_amdgcn_cosf(f); ss[e] = __builtin_amdgcn_sinf(f); }
    const unsigned c0 = pkh(cc[0], cc[1]), c1 = pkh(cc[2], cc[3]), s0 = pkh(ss[0], ss[1]), s1 = pkh(ss[2], ss[3]);
    CM m; m.re = (u32x4){c0, c1, s0 ^ 0x80008000u, s1 ^ 0x80008000u}; m.im = (u32x4){s0, s1, c0, c1};
    return m;
}
DEV u32x4 pk8(f32x4 r, f32x4 i) { return (u32x4){pkh(r[0], r[1]), pkh(r[2], r[3]), pkh(i[0], i[1]), pkh(i[2], i[3])}; }
DEV u32x4 wmul(f32x4 r, f32x4 i, u32x4 w) {
    float yr[4], yi[4];
#pragma unroll
    for (int e = 0; e < 4; ++e) {
        const unsigned wu = w[e];
        const f16x2 h = __builtin_bit_cast(f16x2, wu);
        yr[e] = fmaf(r[e], (float)h[0], -i[e] * (float)h[1]); yi[e] = fmaf(r[e], (float)h[1], i[e] * (float)h[0]);
    }
    return (u32x4){pkh(yr[0], yr[1]), pkh(yr[2], yr[3]), pkh(yi[0], yi[1]), pkh(yi[2], yi[3])};
}
#define Z4 ((f32x4){0.f, 0.f, 0.f, 0.f})
#define PACE() do { if (nb < 32) { BAR(); ++nb; } } while (0)

DEV void kb_aux(char* ws, char* smem, int gb, int w, int lane) {
    int s2t, dat; tile_of(gb, s2t, dat);
    const int c = lane & 15, q = lane >> 4, mp = gb >> 2, j1 = gb & 3;
    const bool spc = mp == 0 && !(j1 & 1), xtr = gb == 0;
    const int k1 = mp ? mp : spc ? (w >> 1) * 64 : (j1 == 1 ? 0 : 64);
    const int ja0 = spc ? (4 * w) & 7 : 4 * w;
    const rsrc_t rs = mkrsrc(ws, WS_END);
    int nb = 0;
    if (w == 0) TSN(3, 0);
#pragma unroll
    for (int i = 0; i < 4; ++i) glds16(ws + OFF_U + gb * 16384 + (4 * w + i) * 1024 + lane * 16, smem + B_UT + (4 * w + i) * 1024 + lane * 16);
    const u32x4 mx4 = __builtin_amdgcn_raw_buffer_load_b128(rs, lane * 16, OFF_MX + w * 1024, 0);
    u32x4 wv[5];
#pragma unroll
    for (int i = 0; i < 4; ++i) wv[i] = __builtin_amdgcn_raw_buffer_load_b128(rs, lane * 16, OFF_W + (k1 * 4 + j1) * 16384 + (ja0 + i) * 1024, 0);
    wv[4] = __builtin_amdgcn_raw_buffer_load_b128(rs, xtr ? lane * 16 : 0x80000000u, OFF_W + (k1 * 4) * 16384 + 8 * 1024, 0);
    if (w == 0 && lane < 4) asm volatile("ds_write_b32 %0, %1" :: "v"(ldsa(smem + B_CNT) + lane * 4), "v"(0u) : "memory");
    SCHED();
    u32x4 mah[2], mal[2];
    {
        const float dt = -(float)(j1 + 4 * (spc ? c & 7 : c)) * (1.0f / 64.0f), t0 = dt * (float)(4 * q);
        float cc[4], ss[4];
#pragma unroll
        for (int e = 0; e < 4; ++e) { const float f = __builtin_amdgcn_fractf(fmaf((float)e, dt, t0)); cc[e] = __builtin_amdgcn_cosf(f); ss[e] = __builtin_amdgcn_sinf(f); }
        const unsigned c0 = pkb(cc[0], cc[1]), c1 = pkb(cc[2], cc[3]), s0 = pkb(ss[0], ss[1]), s1 = pkb(ss[2], ss[3]);
        const unsigned c0l = pkb(cc[0] - lo2f(c0), cc[1] - hi2f(c0)), c1l = pkb(cc[2] - lo2f(c1), cc[3] - hi2f(c1));
        const unsigned s0l = pkb(ss[0] - lo2f(s0), ss[1] - hi2f(s0)), s1l = pkb(ss[2] - lo2f(s1), ss[3] - hi2f(s1));
        mah[0] = (u32x4){c0, c1, s0 ^ 0x80008000u, s1 ^ 0x80008000u}; mah[1] = (u32x4){s0, s1, c0, c1};
        mal[0] = (u32x4){c0l, c1l, s0l ^ 0x80008000u, s1l ^ 0x80008000u}; mal[1] = (u32x4){s0l, s1l, c0l, c1l};
        if (spc) {
            mah[0] = c < 8 ? (u32x4){c0, c1, 0, 0} : (u32x4){0, 0, c0, c1}; mah[1] = c < 8 ? (u32x4){s0, s1, 0, 0} : (u32x4){0, 0, s0, s1};
            mal[0] = c < 8 ? (u32x4){c0l, c1l, 0, 0} : (u32x4){0, 0, c0l, c1l}; mal[1] = c < 8 ? (u32x4){s0l, s1l, 0, 0} : (u32x4){0, 0, s0l, s1l};
        }
    }
    CM gf[4];
#pragma unroll
    for (int i = 0; i < 4; ++i) { const float dt = -(float)(j1 + 4 * (ja0 + i) + 64 * c) * (1.0f / 1024.0f); gf[i] = cmat(dt * (float)(4 * q), dt); }
    const float dts = -(float)(k1 + 128 * c) * (1.0f / 2048.0f);
    CM msf = cmat(dts * (float)(4 * q), dts);
    CM msi = cmat((float)((k1 + 512 * q) * c) * (1.0f / 2048.0f), (float)c * (1.0f / 16.0f));
#pragma unroll
    for (int i = 0; i < 4; ++i) { PIN(gf[i].re); PIN(gf[i].im); }
    PIN(msf.re); PIN(msf.im); PIN(msi.re); PIN(msi.im); PIN(mah[0]); PIN(mah[1]); PIN(mal[0]); PIN(mal[1]);
    {
        const unsigned m = wave_pkmax(pkmax(pkmax(mx4.x, mx4.y), pkmax(mx4.z, mx4.w)));
        if (lane == 0) asm volatile("ds_write_b32 %0, %1" :: "v"(ldsa(smem + B_MX) + w * 4), "v"(m) : "memory");
    }
    PIN(wv[4]);
    LDS_WAIT();
    if (w == 0) TSN(3, 1);
    PACE();
    int eu, ew;
    {
        const u32x4 m4 = *reinterpret_cast<const u32x4*>(smem + B_MX);
        const unsigned m = __builtin_amdgcn_readfirstlane(pkmax(pkmax(m4.x, m4.y), pkmax(m4.z, m4.w)));
        eu = min(max((int)(m >> 23), 4), 254); ew = min((int)((m & 0xffffu) >> 7), 245);
    }
    const float su = __uint_as_float((unsigned)(258 - eu) << 23), sw = __uint_as_float((unsigned)(246 - ew) << 23);
#pragma unroll
    for (int i = 0; i < 4; ++i) {
        const u32x4 ua = trfrag(smem + B_UT + (4 * w + i) * 1024 + (lane >> 2) * 64 + (lane & 3) * 8, 32);
        f32x4 r = mfb(ua, mal[0], Z4), m = mfb(ua, mal[1], Z4);
        r = mfb(ua, mah[0], r); m = mfb(ua, mah[1], m);
        *reinterpret_cast<u32x4*>(smem + B_E1 + c * E1P + q * 256 + (4 * w + i) * 16) = pk8(r * su, m * su);
    }
    if (xtr) {
#pragma unroll
        for (int i = 0; i < 4; ++i) {
            const u32x4 ua = trfrag(smem + B_UT + (4 * w + i) * 1024 + (lane >> 2) * 64 + (lane & 3) * 8, 32);
            const unsigned sg = 0xbf803f80u;
            const f32x4 e = mfb(ua, c == 0 ? (u32x4){sg, sg, 0, 0} : (u32x4){0, 0, sg, sg}, Z4);
            if (c < 2) *reinterpret_cast<u32x4*>(smem + B_E1 + (16 + c) * E1P + q * 256 + (4 * w + i) * 16) = pk8(e * su, Z4);
        }
    }
#pragma unroll
    for (int i = 0; i < 5; ++i)
#pragma unroll
        for (int e = 0; e < 4; ++e) wv[i][e] = pkh(lo2f(wv[i][e]) * sw, hi2f(wv[i][e]) * sw);
#pragma unroll
    for (int i = 0; i < 5; ++i) PIN(wv[i]);
    LDS_WAIT();
    if (w == 0) TSN(3, 2);
    PACE();
    u32x4 dv[4];
#pragma unroll
    for (int i = 0; i < 4; ++i) dv[i] = *reinterpret_cast<const u32x4*>(smem + B_E1 + (4 * w + i) * E1P + lane * 16);
#pragma unroll
    for (int i = 0; i < 4; ++i) dv[i] = pk8(mfh(dv[i], gf[i].re, Z4), mfh(dv[i], gf[i].im, Z4));
#pragma unroll
    for (int i = 0; i < 4; ++i) dv[i] = wmul(mfh(msf.re, dv[i], Z4), mfh(msf.im, dv[i], Z4), wv[i]);
#pragma unroll
    for (int i = 0; i < 4; ++i) PIN(dv[i]);
    PACE();
    float cf = (float)c; PIN(cf);
    CM gi[4];
#pragma unroll
    for (int i = 0; i < 4; ++i) gi[i] = cmat(cf * (float)(j1 + 4 * (ja0 + i) + 256 * q) * (1.0f / 1024.0f), cf * (1.0f / 16.0f));
#pragma unroll
    for (int i = 0; i < 4; ++i) dv[i] = pk8(mfh(dv[i], msi.re, Z4), mfh(dv[i], msi.im, Z4));
#pragma unroll
    for (int i = 0; i < 4; ++i) { PIN(dv[i]); PIN(gi[i].re); PIN(gi[i].im); }
    PACE();
    {
        f32x4 r[4], m[4];
#pragma unroll
        for (int i = 0; i < 4; ++i) { r[i] = mfh(dv[i], gi[i].re, Z4); m[i] = mfh(dv[i], gi[i].im, Z4); }
#pragma unroll
        for (int e = 0; e < 4; ++e)
            *reinterpret_cast<u32x4*>(smem + B_E2 + (4 * q + e) * 1024 + w * 256 + c * 16) =
                (u32x4){pkh(r[0][e], r[1][e]), pkh(r[2][e], r[3][e]), pkh(m[0][e], m[1][e]), pkh(m[2][e], m[3][e])};
    }
    if (xtr && !(w & 1)) {
        const float dt = -(float)(32 + 64 * c) * (1.0f / 1024.0f);
        const CM f = cmat(dt * (float)(4 * q), dt), b = cmat(cf * (float)(32 + 256 * q) * (1.0f / 1024.0f), cf * (1.0f / 16.0f));
        u32x4 d = *reinterpret_cast<const u32x4*>(smem + B_E1 + (16 + (w >> 1)) * E1P + lane * 16);
        d = pk8(mfh(d, f.re, Z4), mfh(d, f.im, Z4));
        d = wmul(mfh(msf.re, d, Z4), mfh(msf.im, d, Z4), wv[4]);
        d = pk8(mfh(d, msi.re, Z4), mfh(d, msi.im, Z4));
        const f32x4 r = mfh(d, b.re, Z4);
#pragma unroll
        for (int e = 0; e < 4; ++e) reinterpret_cast<float*>(smem + B_EX)[(w >> 1) * 256 + (4 * q + e) * 16 + c] = r[e];
    }
    u32x4 mia, mib;
    {
        const int jq = spc ? (4 * q) & 7 : 4 * q;
        float cc[4], ss[4];
#pragma unroll
        for (int e = 0; e < 4; ++e) {
            const float f = __builtin_amdgcn_fractf(cf * (float)(j1 + 4 * (jq + e)) * (1.0f / 64.0f));
            const float wg = !spc || (xtr && jq + e == 0) ? 0.125f : 0.25f;
            cc[e] = __builtin_amdgcn_cosf(f) * wg; ss[e] = -__builtin_amdgcn_sinf(f) * wg;
        }
        const unsigned c0 = pkh(cc[0], cc[1]), c1 = pkh(cc[2], cc[3]), n0 = pkh(ss[0], ss[1]), n1 = pkh(ss[2], ss[3]);
        mia = (u32x4){c0, c1, n0, n1}; mib = (u32x4){n0, n1, c0 ^ 0x80008000u, c1 ^ 0x80008000u};
        if (spc) { mib = q < 2 ? (u32x4){0, 0, 0, 0} : mia ^ 0x80008000u; if (q >= 2) mia = (u32x4){0, 0, 0, 0}; }
        PIN(mia); PIN(mib);
    }
    LDS_WAIT();
    if (w == 0) TSN(3, 3);
    PACE();
    f32x4 xa[4], xb[4];
#pragma unroll
    for (int i = 0; i < 4; ++i) { xa[i] = Z4; xb[i] = Z4; }
    if (xtr) {
        const float sg = (c & 1) ? -0.125f : 0.125f;
#pragma unroll
        for (int i = 0; i < 4; ++i) {
            xa[i] = *reinterpret_cast<const f32x4*>(smem + B_EX + (4 * w + i) * 64 + q * 16) * sg;
            xb[i] = *reinterpret_cast<const f32x4*>(smem + B_EX + 1024 + (4 * w + i) * 64 + q * 16) * -sg;
        }
    }
#pragma unroll
    for (int i = 0; i < 4; ++i) {
        const int s2 = 4 * w + i;
        const u32x4 ea = *reinterpret_cast<const u32x4*>(smem + B_E2 + s2 * 1024 + lane * 16);
        const f32x4 ta = mfh(ea, mia, xa[i]), tb = mfh(ea, mib, xb[i]);
        st128<SC1>(to16(pkh(ta[0], ta[1]), pkh(ta[2], ta[3]), pkh(tb[0], tb[1]), pkh(tb[2], tb[3])), rs,
                   c * 16384 + (q & 1) * 32 + (q >> 1) * 16 + OFF_T + s2 * 262144 + j1 * 4096 + mp * 64);
    }
    if (w == 0) TSN(3, 4);
    float gg[2];
#pragma unroll
    for (int n = 0; n < 2; ++n) gg[n] = __uint_as_float(__builtin_amdgcn_raw_buffer_load_b32(rs, (32 * w + 16 * n + c) * 128, OFF_G + s2t * 8, 0));
    PACE();
    u32x4 fc[2][2], fs[2][2];
#pragma unroll
    for (int n = 0; n < 2; ++n) {
        float dt = (float)(32 * w + 16 * n + c) * (1.0f / 128.0f); PIN(dt);
#pragma unroll
        for (int h = 0; h < 2; ++h) {
            const CM a = cmat(dt * (float)(32 * h + 4 * q), dt), b = cmat(dt * (float)(32 * h + 16 + 4 * q), dt);
            fc[n][h] = (u32x4){a.re.x, a.re.y, b.re.x, b.re.y}; fs[n][h] = (u32x4){a.im.x, a.im.y, b.im.x, b.im.y};
            PIN(fc[n][h]); PIN(fs[n][h]);
        }
        PACE();
    }
    PACE();
    wait_vm<0>();
    if (w == 0) TSN(3, 5);
    {
        u32x4 tv[4];
#pragma unroll 1
        for (int spin = 0; spin < (1 << 18); ++spin) {
            unsigned toff = lane * 16; PIN(toff);
#pragma unroll
            for (int i = 0; i < 4; ++i) tv[i] = __builtin_amdgcn_raw_buffer_load_b128(rs, toff, OFF_T + (s2t * 16 + dat) * 16384 + w * 4096 + i * 1024, SC1);
            SCHED();
            PACE(); PACE(); PACE();
            SCHED();
            bool ok = true;
#pragma unroll
            for (int i = 0; i < 4; ++i) ok = ok & (tv[i].x != ~0u) & (tv[i].y != ~0u) & (tv[i].z != ~0u) & (tv[i].w != ~0u);
            if (__all(ok)) break;
            asm volatile("" ::: "memory");
        }
#pragma unroll
        for (int i = 0; i < 4; ++i)
            *reinterpret_cast<u32x4*>(smem + B_UT + w * 4096 + ((lane >> 1) & 1) * 2048 + (16 * i + (lane >> 2)) * 32 + (lane & 1) * 16) = tv[i];
        LDS_WAIT();
        const unsigned cnt = ldsa(smem + B_CNT);
        if (lane == 0) lds_add(cnt);
        if (w == 0) TSN(3, 6);
#pragma unroll 1
        for (int spin = 0; spin < (1 << 22); ++spin) { if (lds_peek(cnt) >= 4u) break; PACE(); }
    }
    if (w == 0) TSN(3, 7);
    f32x4 P0[2], P1[2], P2[2], P3a[2], P3b[2];
#pragma unroll
    for (int n = 0; n < 2; ++n) { P0[n] = Z4; P1[n] = Z4; P2[n] = Z4; P3a[n] = Z4; P3b[n] = Z4; }
    const char* tt = smem + B_UT + lane * 8;
#pragma unroll
    for (int h = 0; h < 2; ++h) {
        const u32x4 a1 = trfrag(tt + 1 * 4096 + h * 1024, 512), b1 = trfrag(tt + 1 * 4096 + 2048 + h * 1024, 512);
        const u32x4 a3 = trfrag(tt + 3 * 4096 + h * 1024, 512), b3 = trfrag(tt + 3 * 4096 + 2048 + h * 1024, 512);
        const u32x4 a0 = trfrag(tt + h * 1024, 512), b0 = trfrag(tt + 2048 + h * 1024, 512);
        const u32x4 a2 = trfrag(tt + 2 * 4096 + h * 1024, 512), b2 = trfrag(tt + 2 * 4096 + 2048 + h * 1024, 512);
#pragma unroll
        for (int n = 0; n < 2; ++n) {
            u32x4 cw = fc[n][h], sw_ = fs[n][h];
            P1[n] = mfh(a1, cw, P1[n]); P3a[n] = mfh(a1, sw_, P3a[n]); P3b[n] = mfh(b1, cw, P3b[n]); P1[n] = mfh(b1, sw_, P1[n]);
            if (h == 0 && q == 0) cw.x = (cw.x & 0xffff0000u) | ((c & 1) ? 0xbc00u : 0x3c00u);
            P3a[n] = mfh(b3, cw, P3a[n]); P3b[n] = mfh(a3, sw_, P3b[n]); P1[n] = mfh(a3, cw, P1[n]);
            P1[n] = mfh(b3, sw_, P1[n]);
            if (h == 0 && q == 0) { cw.x = (cw.x & 0xffff0000u) | 0x3800u; sw_.x = (sw_.x & 0xffff0000u) | ((c & 1) ? 0x3800u : 0xb800u); }
            P0[n] = mfh(a0, cw, P0[n]); P2[n] = mfh(a2, cw, P2[n]); P0[n] = mfh(b0, sw_, P0[n]); P2[n] = mfh(b2, sw_, P2[n]);
        }
#pragma unroll
        for (int n = 0; n < 2; ++n) { PIN(P0[n]); PIN(P1[n]); PIN(P2[n]); PIN(P3a[n]); PIN(P3b[n]); }
        PACE();
    }
    const int fe = eu + ew - 267;
#pragma unroll
    for (int n = 0; n < 2; ++n) {
        const f32x4 sp = P0[n] + P2[n], sm = P0[n] - P2[n], p3 = P3a[n] - P3b[n];
        const f32x4 av[4] = {sp + P1[n], sm - p3, sp - P1[n], sm + p3};
        unsigned o[8];
#pragma unroll
        for (int m = 0; m < 4; ++m) {
            const unsigned b0 = pkb(ldexpf(av[m][0], fe), ldexpf(av[m][1], fe)), b1 = pkb(ldexpf(av[m][2], fe), ldexpf(av[m][3], fe));
            o[2 * m] = pkb(gg[n] * lo2f(b0), gg[n] * hi2f(b0)); o[2 * m + 1] = pkb(gg[n] * lo2f(b1), gg[n] * hi2f(b1));
        }
        *reinterpret_cast<u32x4*>(smem + B_GA + ((w * 2 + n) * 2 + 0) * 1024 + lane * 16) = (u32x4){o[0], o[1], o[2], o[3]};
        *reinterpret_cast<u32x4*>(smem + B_GA + ((w * 2 + n) * 2 + 1) * 1024 + lane * 16) = (u32x4){o[4], o[5], o[6], o[7]};
        if (n == 0) PACE();
    }
    LDS_WAIT();
    if (w == 0) TSN(3, 8);
    while (nb < 32) { BAR(); ++nb; }
    BAR();
}

NOPK __global__ __launch_bounds__(768, 1) void kb_kernel(const u16* __restrict__ x, const u16* __restrict__ Cre, const u16* __restrict__ Cim,
                                                         const u16* __restrict__ Dskip, char* ws, float* __restrict__ out) {
    extern __shared__ __attribute__((aligned(16))) char smem[];
    const int wid = __builtin_amdgcn_readfirstlane(threadIdx.x >> 6), lane = threadIdx.x & 63;
    if (wid < 4) kb_mma(x, Dskip, ws, out, smem, blockIdx.x, wid, lane);
    else if (wid < 8) kb_load(Cre, Cim, ws, smem, blockIdx.x, wid - 4, lane);
    else kb_aux(ws, smem, blockIdx.x, wid - 8, lane);
}

extern "C" void kernel_launch(void* const* d_in, const int* in_sizes, int n_in, void* d_out, int out_size, void* d_ws, size_t ws_size,
                              hipStream_t stream) {
    const u16* x = (const u16*)d_in[0];
    const u16* w_r = (const u16*)d_in[1];
    const u16* w_i = (const u16*)d_in[2];
    const u16* lam_re = (const u16*)d_in[4];
    const u16* lam_im = (const u16*)d_in[5];
    const u16* Bre = (const u16*)d_in[6];
    const u16* Bim = (const u16*)d_in[7];
    const u16* Cre = (const u16*)d_in[8];
    const u16* Cim = (const u16*)d_in[9];
    const u16* log_dt = (const u16*)d_in[10];
    const u16* Dskip = (const u16*)d_in[11];
    const u16* gate_w = (const u16*)d_in[12];
    char* ws = (char*)d_ws;
    (void)hipFuncSetAttribute((const void*)ka_kernel, hipFuncAttributeMaxDynamicSharedMemorySize, KA_SMEM);
    (void)hipFuncSetAttribute((const void*)kb_kernel, hipFuncAttributeMaxDynamicSharedMemorySize, KB_SMEM);
    ka_kernel<<<256, 512, KA_SMEM, stream>>>(x, w_r, w_i, Bre, Bim, Cre, Cim, lam_re, lam_im, log_dt, gate_w, ws);
    kb_kernel<<<256, 768, KB_SMEM, stream>>>(x, Cre, Cim, Dskip, ws, (float*)d_out);
}
```

```cpp
#include <hip/hip_runtime.h>
#include <stdint.h>

typedef __attribute__((ext_vector_type(8))) __bf16 bf16x8;
typedef __attribute__((ext_vector_type(2))) __bf16 bf16x2;
typedef __attribute__((ext_vector_type(8))) _Float16 f16x8;
typedef __attribute__((ext_vector_type(2))) _Float16 f16x2;
typedef __attribute__((ext_vector_type(4))) float f32x4;
typedef __attribute__((ext_vector_type(2))) float f32x2;
typedef __attribute__((ext_vector_type(4))) unsigned u32x4;
typedef __attribute__((ext_vector_type(2))) unsigned u32x2;
typedef __attribute__((ext_vector_type(4))) short s16x4;
typedef __attribute__((ext_vector_type(2))) unsigned short u16x2;
typedef unsigned short u16;
typedef unsigned long long u64;
#define GAS __attribute__((address_space(1)))
#define LAS __attribute__((address_space(3)))
#define NOPK __attribute__((target("no-packed-fp32-ops")))
#define RLX_AGENT __ATOMIC_RELAXED, __HIP_MEMORY_SCOPE_AGENT
#define DEV __device__ __forceinline__

constexpr int S_ = 2048, D_ = 1024, KF = 513;

constexpr unsigned OFF_H = 0;
constexpr unsigned OFF_U = OFF_H + (8u << 20);
constexpr unsigned OFF_T = OFF_U + (4u << 20);
constexpr unsigned OFF_W = OFF_T + (4u << 20);
constexpr unsigned OFF_G = OFF_W + 65u * 65536u;
constexpr unsigned OFF_MX = OFF_G + 16384u;
constexpr unsigned OFF_GRAN = OFF_MX + 4096u;
constexpr unsigned OFF_E = OFF_GRAN + 16u * 1024u * 16u;
constexpr unsigned WS_END = OFF_E + 16u * 128u;

#define TSD()
#define TS(i)
#define TSE(slot, on)

DEV float bf2f(u16 h) { return __uint_as_float((unsigned)h << 16); }
DEV unsigned pkb(float a, float b) { f32x2 v = {a, b}; return __builtin_bit_cast(unsigned, __builtin_convertvector(v, bf16x2)); }
DEV unsigned pkh(float a, float b) { f32x2 v = {a, b}; return __builtin_bit_cast(unsigned, __builtin_convertvector(v, f16x2)); }
DEV float lo2f(unsigned p) { return __uint_as_float(p << 16); }
DEV float hi2f(unsigned p) { return __uint_as_float(p & 0xffff0000u); }
DEV float bfr(float f) { return lo2f(pkb(f, f)); }
DEV float2 cmul(float2 a, float2 b) { return make_float2(a.x * b.x - a.y * b.y, a.x * b.y + a.y * b.x); }
DEV float2 cfma(float2 a, float2 b, float2 c) { return make_float2(fmaf(a.x, b.x, fmaf(-a.y, b.y, c.x)), fmaf(a.x, b.y, fmaf(a.y, b.x, c.y))); }
DEV f32x4 mfb(u32x4 a, u32x4 b, f32x4 c) { return __builtin_amdgcn_mfma_f32_16x16x32_bf16(__builtin_bit_cast(bf16x8, a), __builtin_bit_cast(bf16x8, b), c, 0, 0, 0); }
DEV f32x4 mfh(u32x4 a, u32x4 b, f32x4 c) { return __builtin_amdgcn_mfma_f32_16x16x32_f16(__builtin_bit_cast(f16x8, a), __builtin_bit_cast(f16x8, b), c, 0, 0, 0); }
DEV unsigned pkmax(unsigned a, unsigned b) { return __builtin_bit_cast(unsigned, __builtin_elementwise_max(__builtin_bit_cast(u16x2, a), __builtin_bit_cast(u16x2, b))); }
DEV float vcos(float r) { return __builtin_amdgcn_cosf(__builtin_amdgcn_fractf(r)); }
DEV float vsin(float r) { return __builtin_amdgcn_sinf(__builtin_amdgcn_fractf(r)); }
DEV unsigned ldsa(const void* p) { return (unsigned)(uintptr_t)(LAS const char*)p; }
DEV u32x2 trread(const char* p) { return __builtin_bit_cast(u32x2, __builtin_amdgcn_ds_read_tr16_b64_v4i16((LAS s16x4*)p)); }
DEV u32x4 trfrag(const char* p, int second) { const u32x2 a = trread(p), b = trread(p + second); return (u32x4){a.x, a.y, b.x, b.y}; }
DEV void glds16(const void* g, char* l) { __builtin_amdgcn_global_load_lds((const GAS unsigned*)g, (LAS unsigned*)l, 16, 0, 0); }
DEV u32x4 ldfrag(const char* tile, int row, int chunk) { return *reinterpret_cast<const u32x4*>(tile + row * 128 + ((chunk ^ (row & 7)) << 4)); }
template <int N> DEV void wait_vm() { asm volatile("s_waitcnt vmcnt(%0)" :: "i"(N) : "memory"); }
#define LDS_WAIT() asm volatile("s_waitcnt lgkmcnt(0)" ::: "memory")
#define SCHED() __builtin_amdgcn_sched_barrier(0)
#define BAR() do { __builtin_amdgcn_s_barrier(); asm volatile("" ::: "memory"); } while (0)
#define PIN(x) asm volatile("" : "+v"(x))
typedef __amdgpu_buffer_rsrc_t rsrc_t;
DEV rsrc_t mkrsrc(void* p, unsigned n) { return __builtin_amdgcn_make_buffer_rsrc(p, 0, n, 0x00020000); }
constexpr int SC1 = 16;
#define WT 16
#define WTO 16
template <int AUX> DEV void st128(u32x4 v, rsrc_t rs, unsigned off) { __builtin_amdgcn_raw_buffer_store_b128(v, rs, off, 0, AUX); }
DEV void lds_add(unsigned a) { asm volatile("ds_add_u32 %0, %1" :: "v"(a), "v"(1u) : "memory"); }
DEV unsigned lds_peek(unsigned a) {
    unsigned v;
    asm volatile("ds_read_b32 %0, %1\n\ts_waitcnt lgkmcnt(0)" : "=v"(v) : "v"(a) : "memory");
    return __builtin_amdgcn_readfirstlane(v);
}
DEV void tile_of(int gb, int& ta, int& tb) {
    const int xcd = gb & 7, loc = gb >> 3;
    ta = (xcd >> 1) * 4 + (loc >> 3);
    tb = (xcd & 1) * 8 + (loc & 7);
}
DEV void rows4(float a, float& a0, float& a1, float& a2, float& a3) {
    const unsigned u = __float_as_uint(a);
    const auto r = __builtin_amdgcn_permlane32_swap(u, u, false, false);
    const auto p = __builtin_amdgcn_permlane16_swap(r[0], r[0], false, false);
    const auto s = __builtin_amdgcn_permlane16_swap(r[1], r[1], false, false);
    a0 = __uint_as_float(p[0]); a1 = __uint_as_float(p[1]); a2 = __uint_as_float(s[0]); a3 = __uint_as_float(s[1]);
}
DEV float rowsum4(float a) {
    const unsigned u = __float_as_uint(a);
    const auto r = __builtin_amdgcn_permlane32_swap(u, u, false, false);
    const unsigned t = __float_as_uint(__uint_as_float(r[0]) + __uint_as_float(r[1]));
    const auto p = __builtin_amdgcn_permlane16_swap(t, t, false, false);
    return __uint_as_float(p[0]) + __uint_as_float(p[1]);
}
DEV float2 sel4(int q, float2 a, float2 b, float2 c, float2 d) {
    PIN(d.x); PIN(d.y);
    const float2 e = (q & 1) ? b : a, f = (q & 1) ? d : c;
    return (q & 2) ? f : e;
}
DEV unsigned wave_pkmax(unsigned m) {
#pragma unroll
    for (int s = 32; s >= 1; s >>= 1) m = pkmax(m, (unsigned)__shfl_xor((int)m, s));
    return m;
}
DEV u32x4 to16(unsigned p0, unsigned p1, unsigned n0, unsigned n1) {
    const auto a = __builtin_amdgcn_permlane16_swap(p0, n0, false, false);
    const auto b = __builtin_amdgcn_permlane16_swap(p1, n1, false, false);
    return (u32x4){a[0], b[0], a[1], b[1]};
}

constexpr int A_STAGE = 32768, A_XT = 4 * A_STAGE, A_PRM = A_XT + 16384, A_AGG = A_PRM + 2048, A_FLG = A_AGG + 512, A_GW = A_FLG + 64,
              A_RAW = A_GW + 2048, KA_SMEM = A_RAW + 1024;

DEV void ka_mma(const u16* __restrict__ lam_re, const u16* __restrict__ lam_im, const u16* __restrict__ log_dt,
                const u16* __restrict__ gate_w, char* ws, char* smem, int gb, int wid, int lane) {
    int lt, pt; tile_of(gb, lt, pt);
    const int wr = wid >> 1, wc = wid & 1, c = lane & 15, q = lane >> 4;
    const rsrc_t rs = mkrsrc(ws, WS_END);
    TSD(); TS(0);
    GAS u64* Ep = (GAS u64*)(ws + OFF_E + pt * 128);
    u64 tag = __hip_atomic_load(Ep, RLX_AGENT) + 0x9E3779B97F4A7C15ull;
    const u32x2 gwv = *reinterpret_cast<const u32x2*>(gate_w + wid * 256 + lane * 4);
    const unsigned praw = (wid == 0 ? lam_re : wid == 1 ? lam_im : log_dt)[pt * 64 + lane];
#pragma unroll
    for (int i = 0; i < 4; ++i)
        st128<WT>((u32x4){~0u, ~0u, ~0u, ~0u}, rs, wid * 1024 + lane * 16 + OFF_T + gb * 16384 + i * 4096);
    if (wid == 0 && lane < 8) reinterpret_cast<unsigned*>(smem + A_FLG)[lane] = 0u;
    *reinterpret_cast<u32x2*>(smem + A_GW + wid * 512 + lane * 8) = gwv;
    reinterpret_cast<unsigned*>(smem + A_RAW)[lane * 4 + wid] = praw;
    PIN(tag);
    LDS_WAIT();
    TS(7);
    f32x4 acc[4][4];
#pragma unroll
    for (int m = 0; m < 4; ++m)
#pragma unroll
        for (int n = 0; n < 4; ++n) acc[m][n] = (f32x4){0.f, 0.f, 0.f, 0.f};
    u32x4 af[2][4], bf_[2][4];
#pragma unroll
    for (int m = 0; m < 4; ++m) { af[1][m] = (u32x4){0, 0, 0, 0}; bf_[1][m] = af[1][m]; }
#pragma unroll 4
    for (int kt = 0; kt < 16; ++kt) {
        BAR();
        TS(8 + kt);
        const char* tA = smem + (kt & 3) * A_STAGE;
        const char* tB = tA + 16384;
#pragma unroll
        for (int m = 0; m < 4; ++m) af[0][m] = ldfrag(tA, wr * 64 + m * 16 + c, q);
#pragma unroll
        for (int n = 0; n < 4; ++n) bf_[0][n] = ldfrag(tB, wc * 64 + n * 16 + c, q);
        SCHED();
#pragma unroll
        for (int m = 0; m < 4; ++m)
#pragma unroll
            for (int n = 0; n < 4; ++n) acc[m][n] = mfb(af[1][m], bf_[1][n], acc[m][n]);
        SCHED();
#pragma unroll
        for (int m = 0; m < 2; ++m)
#pragma unroll
            for (int n = 0; n < 4; ++n) acc[m][n] = mfb(af[0][m], bf_[0][n], acc[m][n]);
        SCHED();
#pragma unroll
        for (int m = 0; m < 4; ++m) af[1][m] = ldfrag(tA, wr * 64 + m * 16 + c, 4 + q);
#pragma unroll
        for (int n = 0; n < 4; ++n) bf_[1][n] = ldfrag(tB, wc * 64 + n * 16 + c, 4 + q);
        SCHED();
#pragma unroll
        for (int m = 2; m < 4; ++m)
#pragma unroll
            for (int n = 0; n < 4; ++n) acc[m][n] = mfb(af[0][m], bf_[0][n], acc[m][n]);
        SCHED();
        LDS_WAIT();
    }
    float2 lam[2], coef[2], l16[2], l128[2];
#pragma unroll
    for (int n = 0; n < 2; ++n) {
        const f32x4* pp = reinterpret_cast<const f32x4*>(smem + A_PRM + (wc * 32 + 2 * c + n) * 32);
        const f32x4 a = pp[0], b = pp[1];
        lam[n] = make_float2(a[0], a[1]); coef[n] = make_float2(a[2], a[3]); l16[n] = make_float2(b[0], b[1]); l128[n] = make_float2(b[2], b[3]);
    }
#pragma unroll
    for (int m = 0; m < 4; ++m)
#pragma unroll
        for (int n = 0; n < 4; ++n) acc[m][n] = mfb(af[1][m], bf_[1][n], acc[m][n]);
    TS(1);
    float2 h[2][16];
#pragma unroll
    for (int n = 0; n < 2; ++n) h[n][0] = make_float2(acc[0][n][0], acc[0][2 + n][0]);
#pragma unroll
    for (int t = 1; t < 16; ++t)
#pragma unroll
        for (int n = 0; n < 2; ++n) h[n][t] = cfma(lam[n], h[n][t - 1], make_float2(acc[t >> 2][n][t & 3], acc[t >> 2][2 + n][t & 3]));
    float2 loc[2], wag[2], wq[2];
#pragma unroll
    for (int n = 0; n < 2; ++n) {
        float2 a0, a1, a2, a3;
        rows4(h[n][15].x, a0.x, a1.x, a2.x, a3.x);
        rows4(h[n][15].y, a0.y, a1.y, a2.y, a3.y);
        const float2 s2 = cfma(l16[n], a0, a1), s3 = cfma(l16[n], s2, a2);
        wag[n] = cfma(l16[n], s3, a3);
        loc[n] = sel4(q, make_float2(0.f, 0.f), a0, s2, s3);
    }
    f32x4* aggp = reinterpret_cast<f32x4*>(smem + A_AGG) + wc * 16 + c;
    const unsigned flg = ldsa(smem + A_FLG) + wc * 4;
    if (wr == 0) {
        if (q == 0) *aggp = (f32x4){wag[0].x, wag[0].y, wag[1].x, wag[1].y};
        LDS_WAIT();
        if (lane == 0) lds_add(flg);
    }
#pragma unroll
    for (int n = 0; n < 2; ++n) {
        const float2 l32 = cmul(l16[n], l16[n]);
        wq[n] = sel4(q, make_float2(1.f, 0.f), l16[n], l32, cmul(l32, l16[n]));
        if (wr == 1) l16[n] = cmul(l32, l32);
    }
    if (wr == 1) {
        for (int spin = 0; spin < (1 << 20); ++spin) { if (lds_peek(flg)) break; }
        const f32x4 w0 = *aggp;
        const float2 W0[2] = {make_float2(w0[0], w0[1]), make_float2(w0[2], w0[3])};
        const float2 g0 = cfma(l16[0], W0[0], wag[0]), g1 = cfma(l16[1], W0[1], wag[1]);
        if (lt < 15 && q < 2) {
            const float2 gg = q == 0 ? g0 : g1;
            st128<SC1>((u32x4){__float_as_uint(gg.x), (unsigned)tag, __float_as_uint(gg.y), (unsigned)(tag >> 32)},
                       rs, (2 * c + q) * 16 + OFF_GRAN + (lt * 1024 + pt * 64 + wc * 32) * 16);
        }
#pragma unroll
        for (int n = 0; n < 2; ++n) { loc[n] = cfma(wq[n], W0[n], loc[n]); wq[n] = cmul(wq[n], l16[n]); }
    }
    TS(2);
    float2 pw[2][16];
#pragma unroll
    for (int n = 0; n < 2; ++n) pw[n][0] = cmul(coef[n], lam[n]);
#pragma unroll
    for (int t = 1; t < 16; ++t)
#pragma unroll
        for (int n = 0; n < 2; ++n) pw[n][t] = cmul(pw[n][t - 1], lam[n]);
#pragma unroll
    for (int t = 0; t < 16; ++t)
#pragma unroll
        for (int n = 0; n < 2; ++n) h[n][t] = cmul(coef[n], h[n][t]);
    float2 lq[2], l512[2];
#pragma unroll
    for (int n = 0; n < 2; ++n) {
        const float2 l256 = cmul(l128[n], l128[n]);
        lq[n] = sel4(q, make_float2(1.f, 0.f), l128[n], l256, cmul(l256, l128[n]));
        l512[n] = cmul(l256, l256);
    }
#pragma unroll
    for (int t = 0; t < 16; ++t) { PIN(h[0][t].x); PIN(h[0][t].y); PIN(h[1][t].x); PIN(h[1][t].y); PIN(pw[0][t].x); PIN(pw[0][t].y); PIN(pw[1][t].x); PIN(pw[1][t].y); }
    TS(3);
    if (lt > 0) {
        u32x4 gr[4][2];
#pragma unroll 1
        for (int spin = 0; spin < (1 << 20); ++spin) {
            bool ok = true;
#pragma unroll
            for (int j = 0; j < 4; ++j) {
                const int cc = lt - 1 - (4 * j + q);
                unsigned off = cc >= 0 ? (unsigned)(OFF_GRAN + (cc * 1024 + pt * 64 + wc * 32 + 2 * c) * 16) : 0x80000000u;
                PIN(off);
                gr[j][0] = __builtin_amdgcn_raw_buffer_load_b128(rs, off, 0, SC1);
                gr[j][1] = __builtin_amdgcn_raw_buffer_load_b128(rs, off + 16, 0, SC1);
            }
#pragma unroll
            for (int j = 0; j < 4; ++j) {
                const bool v = lt - 1 - (4 * j + q) >= 0;
#pragma unroll
                for (int n = 0; n < 2; ++n) ok = ok && (!v || (gr[j][n].y == (unsigned)tag && gr[j][n].w == (unsigned)(tag >> 32)));
            }
            if (__all(ok)) break;
            asm volatile("" ::: "memory");
        }
        if (lt == 15 && wid == 0 && lane == 0) __hip_atomic_store(Ep, tag, RLX_AGENT);
#pragma unroll
        for (int n = 0; n < 2; ++n) {
            float2 cs = make_float2(__uint_as_float(gr[3][n].x), __uint_as_float(gr[3][n].z));
#pragma unroll
            for (int j = 2; j >= 0; --j) cs = cfma(l512[n], cs, make_float2(__uint_as_float(gr[j][n].x), __uint_as_float(gr[j][n].z)));
            cs = cmul(cs, lq[n]);
            cs.x = rowsum4(cs.x); cs.y = rowsum4(cs.y);
            loc[n] = cfma(wq[n], cs, loc[n]);
        }
    }
    TS(4);
    const unsigned hoff = (unsigned)((lt * 8 + wr * 4 + q) * 128 + wc * 64 + c * 4);
#pragma unroll
    for (int t = 0; t < 16; ++t) {
        const float2 v0 = cfma(pw[0][t], loc[0], h[0][t]), v1 = cfma(pw[1][t], loc[1], h[1][t]);
#define HST 0
        __builtin_amdgcn_raw_buffer_store_b32(pkb(v0.x, v1.x), rs, HST == 1 ? hoff | 0x80000000u : hoff, OFF_H + (t * 32 + pt) * 16384, HST == 2 ? 16 : HST == 3 ? 2 : 0);
        __builtin_amdgcn_raw_buffer_store_b32(pkb(-v0.y, -v1.y), rs, HST == 1 ? hoff | 0x80000000u : hoff, OFF_H + (t * 32 + 16 + pt) * 16384, HST == 2 ? 16 : HST == 3 ? 2 : 0);
    }
    TS(5);
    TSE(0, wid == 0);
}

struct __attribute__((packed, aligned(2))) U64U { u32x2 v; };
DEV void w_load(const u16* __restrict__ w_r, const u16* __restrict__ w_i, int k1, int qw, int lw, int lane, u32x2 (&R)[4], u32x2 (&I)[4], unsigned& sp) {
    const int kd0 = 4 * (lane & 15) + 64 * (4 * lw + (lane >> 4));
    const int col = lw < 2 ? kd0 : 1021 - kd0;
#pragma unroll
    for (int r = 0; r < 4; ++r) {
        const int ks = k1 + 128 * (4 * qw + r), row = lw < 2 ? ks : (S_ - ks) & (S_ - 1);
        R[r] = reinterpret_cast<const U64U*>(w_r + row * KF + col)->v;
        I[r] = reinterpret_cast<const U64U*>(w_i + row * KF + col)->v;
    }
    const int ks = k1 + 128 * (4 * qw + (lane & 3)), row = lw < 2 ? (S_ - ks) & (S_ - 1) : ks;
    sp = ((lane & 4) ? w_i : w_r)[row * KF + (lw < 2 ? 0 : 512)];
}
DEV unsigned w_store(rsrc_t rs, int k1, int qw, int lw, int lane, u32x2 (&R)[4], u32x2 (&I)[4], unsigned sp) {
    unsigned o[4][4], mx = 0;
    const unsigned cj = lw < 2 ? 0u : 0x80008000u;
#pragma unroll
    for (int r = 0; r < 4; ++r) {
        mx = pkmax(mx, pkmax(pkmax(R[r].x & 0x7fff7fffu, R[r].y & 0x7fff7fffu), pkmax(I[r].x & 0x7fff7fffu, I[r].y & 0x7fff7fffu)));
        const unsigned i0 = I[r].x ^ cj, i1 = I[r].y ^ cj;
        o[0][r] = __builtin_amdgcn_perm(i0, R[r].x, 0x05040100u); o[1][r] = __builtin_amdgcn_perm(i0, R[r].x, 0x07060302u);
        o[2][r] = __builtin_amdgcn_perm(i1, R[r].y, 0x05040100u); o[3][r] = __builtin_amdgcn_perm(i1, R[r].y, 0x07060302u);
    }
    mx = pkmax(mx, sp & 0x7fffu);
    const bool f0 = lane == 0 && lw == 0, f3 = lane == 0 && lw == 2;
#pragma unroll
    for (int r = 0; r < 4; ++r) {
        const float pr = lo2f(__builtin_amdgcn_readlane(sp, r)), pi = lo2f(__builtin_amdgcn_readlane(sp, 4 + r) ^ (lw < 2 ? 0x8000u : 0u));
        const unsigned t = lw < 2 ? o[0][r] : o[3][r];
        unsigned v = pkb(0.5f * (lo2f(t) + pr), 0.5f * (hi2f(t) + pi));
        PIN(v);
        o[0][r] = f0 ? v : o[0][r]; o[3][r] = f3 ? v : o[3][r];
    }
#pragma unroll
    for (int e = 0; e < 4; ++e)
        st128<WT>((u32x4){o[e][0], o[e][1], o[e][2], o[e][3]}, rs, (lane & 15) * 1024 + (lane >> 4) * 16 +
                  OFF_W + (k1 * 4 + (lw < 2 ? e : 3 - e)) * 16384 + qw * 256 + lw * 64);
    return mx;
}

DEV void ka_load(const u16* __restrict__ x, const u16* __restrict__ w_r, const u16* __restrict__ w_i, const u16* __restrict__ Bre,
                 const u16* __restrict__ Bim, const u16* __restrict__ Cre, const u16* __restrict__ Cim, char* ws, char* smem, int gb, int lw, int lane) {
    int lt, pt; tile_of(gb, lt, pt);
    const int c = lane & 15, q = lane >> 4;
    const int srow = lane >> 3, schunk = (lane & 7) ^ srow;
    const int rot = (lt + pt) & 15;
    const rsrc_t rs = mkrsrc(ws, WS_END);
    const char* gA = (const char*)x + (size_t)(lt * 128 + 64 * (lw >> 1) + 8 * (lw & 1) + 16 * (srow >> 2) + (srow & 3)) * 2048 + schunk * 16;
    const char* gB = (const char*)((lw & 1) ? Bim : Bre) + (size_t)(pt * 64 + 32 * (lw >> 1) + 2 * srow) * 2048 + schunk * 16;
    auto stage = [&](int kk) {
        const int ko = ((kk + rot) & 15) * 128;
        char* l = smem + (kk & 3) * A_STAGE + lw * 4096 + lane * 16;
#pragma unroll
        for (int i = 0; i < 4; ++i) glds16(gA + (32 * (i & 1) + 4 * (i >> 1)) * 2048 + ko, l + i * 1024);
#pragma unroll
        for (int i = 0; i < 4; ++i) glds16(gB + (16 * (i & 1) + (i >> 1)) * 2048 + ko, l + 16384 + i * 1024);
    };
    TSD(); TS(0);
    stage(0); stage(1); stage(2);
    SCHED();
    TS(6);
    const int gi = 8 * pt + 2 * lw + (lane >> 5);
    const unsigned gaddr = ldsa(smem) + gi * 128 + ((((lane & 31) >> 2) ^ (gi & 7)) << 4) + (lane & 3) * 4;
    const unsigned gwaddr = ldsa(smem + A_GW) + (lane & 31) * 4;
    float gsum = 0.f;
    u32x4 fch[4], fcl[4], fsh[4], fsl[4];
    const float mrev = (float)(16 * lw + c) * (1.0f / 128.0f);
    const bool m0 = 16 * lw + c == 0;
    u32x2 R[4], I[4]; unsigned sp;
    constexpr int NAUX = 13;
#pragma unroll
    for (int kt = 0; kt < 16; ++kt) {
        if (kt <= 12) wait_vm<16>(); else if (kt == 13) wait_vm<16 + NAUX>(); else if (kt == 14) wait_vm<8 + NAUX>(); else wait_vm<NAUX>();
        TS(8 + kt);
        BAR();
        if (kt + 3 < 16) stage(kt + 3);
        if (kt == 12) {
            SCHED();
            const char* gx = (const char*)x + (size_t)(16 * (lane >> 1) + lt) * 2048 + lw * 512 + pt * 32 + (lane & 1) * 16;
#pragma unroll
            for (int i = 0; i < 4; ++i) glds16(gx + (size_t)i * 512 * 2048, smem + A_XT + lw * 4096 + i * 1024 + lane * 16);
            w_load(w_r, w_i, gb >> 2, gb & 3, lw, lane, R, I, sp);
        }
        SCHED();
        unsigned xv, gw;
        asm volatile("ds_read_b32 %0, %2\n\tds_read_b32 %1, %3\n\ts_waitcnt lgkmcnt(0)" : "=&v"(xv), "=&v"(gw)
                     : "v"(gaddr + (kt & 3) * A_STAGE), "v"(gwaddr + ((kt + rot) & 15) * 128) : "memory");
        const unsigned pr = pkb(lo2f(xv) * lo2f(gw), hi2f(xv) * hi2f(gw));
        gsum += lo2f(pr); gsum += hi2f(pr); PIN(gsum);
        {
            float th = mrev; PIN(th);
            const float t0 = th * (float)(32 * (kt >> 2) + 16 * ((kt >> 1) & 1) + 2 * (kt & 1) + 4 * q), t1 = t0 + th;
            const float c0 = vcos(t0), c1 = vcos(t1), s0 = m0 ? 1.f : -vsin(t0), s1 = m0 ? -1.f : -vsin(t1);
            const unsigned ch = pkb(c0, c1), sh = pkb(s0, s1);
            fch[kt >> 2][kt & 3] = ch; fcl[kt >> 2][kt & 3] = pkb(c0 - lo2f(ch), c1 - hi2f(ch));
            fsh[kt >> 2][kt & 3] = sh; fsl[kt >> 2][kt & 3] = pkb(s0 - lo2f(sh), s1 - hi2f(sh));
            PIN(fch[kt >> 2][kt & 3]); PIN(fcl[kt >> 2][kt & 3]); PIN(fsh[kt >> 2][kt & 3]); PIN(fsl[kt >> 2][kt & 3]);
        }
        if (kt == 2 && lw == 1) {
            u32x4 raw;
            asm volatile("ds_read_b128 %0, %1\n\ts_waitcnt lgkmcnt(0)" : "=v"(raw) : "v"(ldsa(smem + A_RAW) + lane * 16) : "memory");
            const float lr = lo2f(raw.x), li = lo2f(raw.y);
            const float dt = bfr(expf(lo2f(raw.z)));
            const float er = expf(dt * lr);
            float sn, cs; sincosf(dt * li, &sn, &cs);
            const float2 lam = make_float2(er * cs, er * sn);
            const float nr = lam.x - 1.0f, ni = lam.y, den = lr * lr + li * li;
            const float2 coef = make_float2((nr * lr + ni * li) / den, (ni * lr - nr * li) / den);
            float2 l16 = lam;
#pragma unroll
            for (int i = 0; i < 4; ++i) l16 = cmul(l16, l16);
            float2 l128 = l16;
#pragma unroll
            for (int i = 0; i < 3; ++i) l128 = cmul(l128, l128);
            const unsigned pa = ldsa(smem + A_PRM) + lane * 32;
            asm volatile("ds_write_b128 %0, %1\n\tds_write_b128 %0, %2 offset:16\n\ts_waitcnt lgkmcnt(0)" ::
                         "v"(pa), "v"((f32x4){lam.x, lam.y, coef.x, coef.y}), "v"((f32x4){l16.x, l16.y, l128.x, l128.y}) : "memory");
        }
        SCHED();
    }
    TS(5);
    PIN(sp);
    const unsigned cnt = ldsa(smem + A_FLG) + 8;
    if (lane == 0) lds_add(cnt);
#pragma unroll
    for (int s = 16; s >= 1; s >>= 1) gsum += __shfl_xor(gsum, s);
    {
        const float g = bfr(1.0f / (1.0f + expf(-bfr(gsum))));
        const int tau = (gi & 64) | ((gi & 12) << 2) | ((gi & 48) >> 2) | (gi & 3);
        if ((lane & 31) == 0)
            __builtin_amdgcn_raw_buffer_store_b64((u32x2){__float_as_uint(g), __float_as_uint(bfr(1.0f - g))}, rs, (lt * 128 + tau) * 8, OFF_G, 0);
    }
    TS(1);
    unsigned mx = w_store(rs, gb >> 2, gb & 3, lw, lane, R, I, sp);
    if (gb < 4) w_load(w_r, w_i, 64, gb & 3, lw, lane, R, I, sp);
    const unsigned ctouch = *reinterpret_cast<const unsigned*>((const char*)((lw & 1) ? Cim : Cre) + (size_t)gb * 8192 + (lane & 31) * 128 + (lw >> 1) * 4096);
    for (int spin = 0; spin < (1 << 20); ++spin) { if (lds_peek(cnt) >= 4u) break; }
    TS(2);
    f32x4 vr[4], vi[4];
#pragma unroll
    for (int d1 = 0; d1 < 4; ++d1) { vr[d1] = (f32x4){0.f, 0.f, 0.f, 0.f}; vi[d1] = vr[d1]; }
#pragma unroll
    for (int ks = 0; ks < 4; ++ks) {
        u32x4 xa[4];
#pragma unroll
        for (int d1 = 0; d1 < 4; ++d1) xa[d1] = trfrag(smem + A_XT + d1 * 4096 + ks * 1024 + lane * 8, 512);
#pragma unroll
        for (int d1 = 0; d1 < 4; ++d1) {
            vr[d1] = mfb(xa[d1], fcl[ks], vr[d1]); vi[d1] = mfb(xa[d1], fsl[ks], vi[d1]);
            vr[d1] = mfb(xa[d1], fch[ks], vr[d1]); vi[d1] = mfb(xa[d1], fsh[ks], vi[d1]);
        }
    }
    f32x4 ur[4], ui[4];
    {
        const f32x4 t0r = vr[0] + vr[2], t1r = vr[0] - vr[2], t2r = vr[1] + vr[3], t3r = vr[1] - vr[3];
        const f32x4 t0i = vi[0] + vi[2], t1i = vi[0] - vi[2], t2i = vi[1] + vi[3], t3i = vi[1] - vi[3];
        ur[0] = t0r + t2r; ui[0] = t0i + t2i; ur[2] = t0r - t2r; ui[2] = t0i - t2i;
        ur[1] = t1r + t3i; ui[1] = t1i - t3r; ur[3] = t1r - t3i; ui[3] = t1i + t3r;
        if (m0) { ur[1] = t1r; ui[1] = -t3r; ur[3] = t1i; ui[3] = t3i; }
    }
    unsigned um = 0;
#pragma unroll
    for (int j1 = 0; j1 < 4; ++j1) {
        const unsigned p0 = pkb(ur[j1][0], ur[j1][1]), p1 = pkb(ur[j1][2], ur[j1][3]), n0 = pkb(ui[j1][0], ui[j1][1]), n1 = pkb(ui[j1][2], ui[j1][3]);
        st128<WT>(to16(p0, p1, n0, n1), rs, c * 65536 + (q & 1) * 32 + (q >> 1) * 16 + OFF_U + (lw * 64 + j1) * 16384 + lt * 1024 + pt * 64);
        um = pkmax(um, pkmax(pkmax(p0 & 0x7fff7fffu, p1 & 0x7fff7fffu), pkmax(n0 & 0x7fff7fffu, n1 & 0x7fff7fffu)));
    }
    if (gb < 4) mx = pkmax(mx, w_store(rs, 64, gb & 3, lw, lane, R, I, sp));
    um = pkmax(um, um << 16) & 0xffff0000u;
    mx = pkmax(mx, mx >> 16) & 0xffffu;
    um = wave_pkmax(um | mx);
    if (lane == 0) __builtin_amdgcn_raw_buffer_store_b32(um, rs, 0, OFF_MX + (gb * 4 + lw) * 4, 0);
    asm volatile("" :: "v"(ctouch));
    TS(3);
    TSE(1, lw == 0);
}

NOPK __global__ __launch_bounds__(512, 1) void ka_kernel(const u16* __restrict__ x, const u16* __restrict__ w_r, const u16* __restrict__ w_i,
                                                         const u16* __restrict__ Bre, const u16* __restrict__ Bim, const u16* __restrict__ Cre,
                                                         const u16* __restrict__ Cim, const u16* __restrict__ lam_re,
                                                         const u16* __restrict__ lam_im, const u16* __restrict__ log_dt,
                                                         const u16* __restrict__ gate_w, char* ws) {
    extern __shared__ __attribute__((aligned(16))) char smem[];
    const int wid = __builtin_amdgcn_readfirstlane(threadIdx.x >> 6), lane = threadIdx.x & 63;
    if (wid < 4) ka_mma(lam_re, lam_im, log_dt, gate_w, ws, smem, blockIdx.x, wid, lane);
    else ka_load(x, w_r, w_i, Bre, Bim, Cre, Cim, ws, smem, blockIdx.x, wid - 4, lane);
}

constexpr int B_STAGE = 24576, B_UT = 4 * B_STAGE, B_E1 = B_UT + 16384, E1P = 1040, B_E2 = B_E1 + 18 * E1P + 32, B_EX = B_E2 + 16384,
              B_MX = B_EX + 2048, B_CNT = B_MX + 16, KB_SMEM = B_CNT + 16;
constexpr int B_GA = B_E1;

DEV void kb_load(const u16* __restrict__ Cre, const u16* __restrict__ Cim, char* ws, char* smem, int gb, int lw, int lane) {
    int s2, da; tile_of(gb, s2, da);
    const int srow = lane >> 3, schunk = (lane & 7) ^ srow;
    const size_t offA = (size_t)(256 * lw + 16 * da + srow) * 2048 + schunk * 16;
    const char* gB = ws + OFF_H + (size_t)s2 * 32 * 16384 + (32 * lw + srow) * 128 + schunk * 16;
    auto stage = [&](int kt) {
        char* l = smem + (kt & 3) * B_STAGE + lane * 16;
        const char* gA = (const char*)(kt < 16 ? Cre : Cim) + offA + (kt & 15) * 128;
        glds16(gA, l + lw * 2048);
        glds16(gA + 8 * 2048, l + lw * 2048 + 1024);
#pragma unroll
        for (int i = 0; i < 4; ++i) glds16(gB + kt * 16384 + i * 1024, l + 8192 + lw * 4096 + i * 1024);
    };
    TSD(); TS(0);
    stage(0); stage(1); stage(2);
    TS(1);
#pragma unroll 1
    for (int kt = 0; kt < 29; ++kt) { wait_vm<12>(); TS(8 + kt); BAR(); stage(kt + 3); }
    wait_vm<12>(); TS(37); BAR();
    wait_vm<6>(); TS(38); BAR();
    wait_vm<0>(); TS(39); BAR();
    TSE(4, lw == 0);
}

DEV void kb_mma(const u16* __restrict__ x, const u16* __restrict__ Dskip, char* ws, float* __restrict__ out, char* smem,
                int gb, int wid, int lane) {
    int s2, da; tile_of(gb, s2, da);
    const int c = lane & 15, q = lane >> 4;
    TSD(); TS(0);
    f32x4 acc[4][2];
#pragma unroll
    for (int m = 0; m < 4; ++m) { acc[m][0] = (f32x4){0.f, 0.f, 0.f, 0.f}; acc[m][1] = acc[m][0]; }
    u32x4 af[2][4], bf_[2][2];
#pragma unroll
    for (int m = 0; m < 4; ++m) af[1][m] = (u32x4){0, 0, 0, 0};
    bf_[1][0] = af[1][0]; bf_[1][1] = af[1][0];
    u32x2 xv[4][2], dk[4];
    float omg[2];
    auto step = [&](int kt) {
        BAR();
        TS(8 + kt);
        const char* tA = smem + (kt & 3) * B_STAGE;
        const char* tB = tA + 8192;
#pragma unroll
        for (int m = 0; m < 4; ++m) af[0][m] = ldfrag(tA, m * 16 + c, q);
#pragma unroll
        for (int n = 0; n < 2; ++n) bf_[0][n] = ldfrag(tB, wid * 32 + n * 16 + c, q);
        SCHED();
#pragma unroll
        for (int m = 0; m < 4; ++m)
#pragma unroll
            for (int n = 0; n < 2; ++n) acc[m][n] = mfb(af[1][m], bf_[1][n], acc[m][n]);
        SCHED();
#pragma unroll
        for (int m = 0; m < 2; ++m)
#pragma unroll
            for (int n = 0; n < 2; ++n) acc[m][n] = mfb(af[0][m], bf_[0][n], acc[m][n]);
        SCHED();
#pragma unroll
        for (int m = 0; m < 4; ++m) af[1][m] = ldfrag(tA, m * 16 + c, 4 + q);
#pragma unroll
        for (int n = 0; n < 2; ++n) bf_[1][n] = ldfrag(tB, wid * 32 + n * 16 + c, 4 + q);
        SCHED();
#pragma unroll
        for (int m = 2; m < 4; ++m)
#pragma unroll
            for (int n = 0; n < 2; ++n) acc[m][n] = mfb(af[0][m], bf_[0][n], acc[m][n]);
        SCHED();
        LDS_WAIT();
    };
#pragma unroll 4
    for (int kt = 0; kt < 24; ++kt) step(kt);
    const int col = 16 * da + 4 * q;
#pragma unroll
    for (int n = 0; n < 2; ++n) {
        const int row = 16 * (32 * wid + 16 * n + c) + s2;
#pragma unroll
        for (int m = 0; m < 4; ++m) xv[m][n] = *reinterpret_cast<const u32x2*>(x + (size_t)row * D_ + 256 * m + col);
        omg[n] = reinterpret_cast<const float*>(ws + OFF_G)[2 * row + 1];
    }
#pragma unroll
    for (int m = 0; m < 4; ++m) dk[m] = *reinterpret_cast<const u32x2*>(Dskip + 256 * m + col);
    SCHED();
#pragma unroll 4
    for (int kt = 24; kt < 32; ++kt) step(kt);
#pragma unroll
    for (int m = 0; m < 4; ++m)
#pragma unroll
        for (int n = 0; n < 2; ++n) acc[m][n] = mfb(af[1][m], bf_[1][n], acc[m][n]);
#pragma unroll
    for (int m = 0; m < 4; ++m)
#pragma unroll
        for (int n = 0; n < 2; ++n) {
            const unsigned p0 = pkb(lo2f(xv[m][n].x) * lo2f(dk[m].x), hi2f(xv[m][n].x) * hi2f(dk[m].x));
            const unsigned p1 = pkb(lo2f(xv[m][n].y) * lo2f(dk[m].y), hi2f(xv[m][n].y) * hi2f(dk[m].y));
            acc[m][n] = (f32x4){fmaf(2.0f, acc[m][n][0], lo2f(p0)), fmaf(2.0f, acc[m][n][1], hi2f(p0)), fmaf(2.0f, acc[m][n][2], lo2f(p1)), fmaf(2.0f, acc[m][n][3], hi2f(p1))};
            PIN(acc[m][n]);
        }
    TS(1);
    const rsrc_t ro = mkrsrc(out, S_ * D_ * 4);
    BAR();
    TS(2);
#pragma unroll
    for (int n = 0; n < 2; ++n) {
        const int row = 16 * (32 * wid + 16 * n + c) + s2;
#pragma unroll
        for (int mp = 0; mp < 2; ++mp) {
            const u32x4 ga = *reinterpret_cast<const u32x4*>(smem + B_GA + ((wid * 2 + n) * 2 + mp) * 1024 + lane * 16);
#pragma unroll
            for (int mm = 0; mm < 2; ++mm) {
                const int m = 2 * mp + mm;
                const unsigned g0 = ga[2 * mm], g1 = ga[2 * mm + 1];
                const f32x4 o = {lo2f(g0) + omg[n] * acc[m][n][0], hi2f(g0) + omg[n] * acc[m][n][1], lo2f(g1) + omg[n] * acc[m][n][2], hi2f(g1) + omg[n] * acc[m][n][3]};
                st128<WTO>(__builtin_bit_cast(u32x4, o), ro, (row * D_ + 256 * m + col) * 4);
            }
        }
    }
    TS(3);
    TSE(2, wid == 0);
}

struct CM { u32x4 re, im; };
DEV CM cmat(float t0, float dt) {
    float cc[4], ss[4];
#pragma unroll
    for (int e = 0; e < 4; ++e) { const float f = __builtin_amdgcn_fractf(fmaf((float)e, dt, t0)); cc[e] = __builtin_amdgcn_cosf(f); ss[e] = __builtin_amdgcn_sinf(f); }
    const unsigned c0 = pkh(cc[0], cc[1]), c1 = pkh(cc[2], cc[3]), s0 = pkh(ss[0], ss[1]), s1 = pkh(ss[2], ss[3]);
    CM m; m.re = (u32x4){c0, c1, s0 ^ 0x80008000u, s1 ^ 0x80008000u}; m.im = (u32x4){s0, s1, c0, c1};
    return m;
}
DEV u32x4 pk8(f32x4 r, f32x4 i) { return (u32x4){pkh(r[0], r[1]), pkh(r[2], r[3]), pkh(i[0], i[1]), pkh(i[2], i[3])}; }
DEV u32x4 wmul(f32x4 r, f32x4 i, u32x4 w) {
    float yr[4], yi[4];
#pragma unroll
    for (int e = 0; e < 4; ++e) {
        const unsigned wu = w[e];
        const f16x2 h = __builtin_bit_cast(f16x2, wu);
        yr[e] = fmaf(r[e], (float)h[0], -i[e] * (float)h[1]); yi[e] = fmaf(r[e], (float)h[1], i[e] * (float)h[0]);
    }
    return (u32x4){pkh(yr[0], yr[1]), pkh(yr[2], yr[3]), pkh(yi[0], yi[1]), pkh(yi[2], yi[3])};
}
#define Z4 ((f32x4){0.f, 0.f, 0.f, 0.f})
#define PACE() do { if (nb < 32) { BAR(); ++nb; } } while (0)

DEV void kb_aux(char* ws, char* smem, int gb, int w, int lane) {
    int s2t, dat; tile_of(gb, s2t, dat);
    const int c = lane & 15, q = lane >> 4, mp = gb >> 2, j1 = gb & 3;
    const bool spc = mp == 0 && !(j1 & 1), xtr = gb == 0;
    const int k1 = mp ? mp : spc ? (w >> 1) * 64 : (j1 == 1 ? 0 : 64);
    const int ja0 = spc ? (4 * w) & 7 : 4 * w;
    const rsrc_t rs = mkrsrc(ws, WS_END);
    int nb = 0;
    TSD(); TS(0);
#pragma unroll
    for (int i = 0; i < 4; ++i) glds16(ws + OFF_U + gb * 16384 + (4 * w + i) * 1024 + lane * 16, smem + B_UT + (4 * w + i) * 1024 + lane * 16);
    const u32x4 mx4 = __builtin_amdgcn_raw_buffer_load_b128(rs, lane * 16, OFF_MX + w * 1024, 0);
    u32x4 wv[5];
#pragma unroll
    for (int i = 0; i < 4; ++i) wv[i] = __builtin_amdgcn_raw_buffer_load_b128(rs, lane * 16, OFF_W + (k1 * 4 + j1) * 16384 + (ja0 + i) * 1024, 0);
    wv[4] = __builtin_amdgcn_raw_buffer_load_b128(rs, xtr ? lane * 16 : 0x80000000u, OFF_W + (k1 * 4) * 16384 + 8 * 1024, 0);
    if (w == 0 && lane < 4) asm volatile("ds_write_b32 %0, %1" :: "v"(ldsa(smem + B_CNT) + lane * 4), "v"(0u) : "memory");
    SCHED();
    u32x4 mah[2], mal[2];
    {
        const float dt = -(float)(j1 + 4 * (spc ? c & 7 : c)) * (1.0f / 64.0f), t0 = dt * (float)(4 * q);
        float cc[4], ss[4];
#pragma unroll
        for (int e = 0; e < 4; ++e) { const float f = __builtin_amdgcn_fractf(fmaf((float)e, dt, t0)); cc[e] = __builtin_amdgcn_cosf(f); ss[e] = __builtin_amdgcn_sinf(f); }
        const unsigned c0 = pkb(cc[0], cc[1]), c1 = pkb(cc[2], cc[3]), s0 = pkb(ss[0], ss[1]), s1 = pkb(ss[2], ss[3]);
        const unsigned c0l = pkb(cc[0] - lo2f(c0), cc[1] - hi2f(c0)), c1l = pkb(cc[2] - lo2f(c1), cc[3] - hi2f(c1));
        const unsigned s0l = pkb(ss[0] - lo2f(s0), ss[1] - hi2f(s0)), s1l = pkb(ss[2] - lo2f(s1), ss[3] - hi2f(s1));
        mah[0] = (u32x4){c0, c1, s0 ^ 0x80008000u, s1 ^ 0x80008000u}; mah[1] = (u32x4){s0, s1, c0, c1};
        mal[0] = (u32x4){c0l, c1l, s0l ^ 0x80008000u, s1l ^ 0x80008000u}; mal[1] = (u32x4){s0l, s1l, c0l, c1l};
        if (spc) {
            mah[0] = c < 8 ? (u32x4){c0, c1, 0, 0} : (u32x4){0, 0, c0, c1}; mah[1] = c < 8 ? (u32x4){s0, s1, 0, 0} : (u32x4){0, 0, s0, s1};
            mal[0] = c < 8 ? (u32x4){c0l, c1l, 0, 0} : (u32x4){0, 0, c0l, c1l}; mal[1] = c < 8 ? (u32x4){s0l, s1l, 0, 0} : (u32x4){0, 0, s0l, s1l};
        }
    }
    CM gf[4];
#pragma unroll
    for (int i = 0; i < 4; ++i) { const float dt = -(float)(j1 + 4 * (ja0 + i) + 64 * c) * (1.0f / 1024.0f); gf[i] = cmat(dt * (float)(4 * q), dt); }
    const float dts = -(float)(k1 + 128 * c) * (1.0f / 2048.0f);
    CM msf = cmat(dts * (float)(4 * q), dts);
    CM msi = cmat((float)((k1 + 512 * q) * c) * (1.0f / 2048.0f), (float)c * (1.0f / 16.0f));
#pragma unroll
    for (int i = 0; i < 4; ++i) { PIN(gf[i].re); PIN(gf[i].im); }
    PIN(msf.re); PIN(msf.im); PIN(msi.re); PIN(msi.im); PIN(mah[0]); PIN(mah[1]); PIN(mal[0]); PIN(mal[1]);
    {
        const unsigned m = wave_pkmax(pkmax(pkmax(mx4.x, mx4.y), pkmax(mx4.z, mx4.w)));
        if (lane == 0) asm volatile("ds_write_b32 %0, %1" :: "v"(ldsa(smem + B_MX) + w * 4), "v"(m) : "memory");
    }
    PIN(wv[4]);
    LDS_WAIT();
    TS(1);
    PACE();
    int eu, ew;
    {
        const u32x4 m4 = *reinterpret_cast<const u32x4*>(smem + B_MX);
        const unsigned m = __builtin_amdgcn_readfirstlane(pkmax(pkmax(m4.x, m4.y), pkmax(m4.z, m4.w)));
        eu = min(max((int)(m >> 23), 4), 254); ew = min((int)((m & 0xffffu) >> 7), 245);
    }
    const float su = __uint_as_float((unsigned)(258 - eu) << 23), sw = __uint_as_float((unsigned)(246 - ew) << 23);
#pragma unroll
    for (int i = 0; i < 4; ++i) {
        const u32x4 ua = trfrag(smem + B_UT + (4 * w + i) * 1024 + (lane >> 2) * 64 + (lane & 3) * 8, 32);
        f32x4 r = mfb(ua, mal[0], Z4), m = mfb(ua, mal[1], Z4);
        r = mfb(ua, mah[0], r); m = mfb(ua, mah[1], m);
        *reinterpret_cast<u32x4*>(smem + B_E1 + c * E1P + q * 256 + (4 * w + i) * 16) = pk8(r * su, m * su);
    }
    if (xtr) {
#pragma unroll
        for (int i = 0; i < 4; ++i) {
            const u32x4 ua = trfrag(smem + B_UT + (4 * w + i) * 1024 + (lane >> 2) * 64 + (lane & 3) * 8, 32);
            const unsigned sg = 0xbf803f80u;
            const f32x4 e = mfb(ua, c == 0 ? (u32x4){sg, sg, 0, 0} : (u32x4){0, 0, sg, sg}, Z4);
            if (c < 2) *reinterpret_cast<u32x4*>(smem + B_E1 + (16 + c) * E1P + q * 256 + (4 * w + i) * 16) = pk8(e * su, Z4);
        }
    }
#pragma unroll
    for (int i = 0; i < 5; ++i)
#pragma unroll
        for (int e = 0; e < 4; ++e) wv[i][e] = pkh(lo2f(wv[i][e]) * sw, hi2f(wv[i][e]) * sw);
#pragma unroll
    for (int i = 0; i < 5; ++i) PIN(wv[i]);
    LDS_WAIT();
    TS(2);
    PACE();
    u32x4 dv[4];
#pragma unroll
    for (int i = 0; i < 4; ++i) dv[i] = *reinterpret_cast<const u32x4*>(smem + B_E1 + (4 * w + i) * E1P + lane * 16);
#pragma unroll
    for (int i = 0; i < 4; ++i) dv[i] = pk8(mfh(dv[i], gf[i].re, Z4), mfh(dv[i], gf[i].im, Z4));
#pragma unroll
    for (int i = 0; i < 4; ++i) dv[i] = wmul(mfh(msf.re, dv[i], Z4), mfh(msf.im, dv[i], Z4), wv[i]);
#pragma unroll
    for (int i = 0; i < 4; ++i) PIN(dv[i]);
    PACE();
    float cf = (float)c; PIN(cf);
    CM gi[4];
#pragma unroll
    for (int i = 0; i < 4; ++i) gi[i] = cmat(cf * (float)(j1 + 4 * (ja0 + i) + 256 * q) * (1.0f / 1024.0f), cf * (1.0f / 16.0f));
#pragma unroll
    for (int i = 0; i < 4; ++i) dv[i] = pk8(mfh(dv[i], msi.re, Z4), mfh(dv[i], msi.im, Z4));
#pragma unroll
    for (int i = 0; i < 4; ++i) { PIN(dv[i]); PIN(gi[i].re); PIN(gi[i].im); }
    PACE();
    {
        f32x4 r[4], m[4];
#pragma unroll
        for (int i = 0; i < 4; ++i) { r[i] = mfh(dv[i], gi[i].re, Z4); m[i] = mfh(dv[i], gi[i].im, Z4); }
#pragma unroll
        for (int e = 0; e < 4; ++e)
            *reinterpret_cast<u32x4*>(smem + B_E2 + (4 * q + e) * 1024 + w * 256 + c * 16) =
                (u32x4){pkh(r[0][e], r[1][e]), pkh(r[2][e], r[3][e]), pkh(m[0][e], m[1][e]), pkh(m[2][e], m[3][e])};
    }
    if (xtr && !(w & 1)) {
        const float dt = -(float)(32 + 64 * c) * (1.0f / 1024.0f);
        const CM f = cmat(dt * (float)(4 * q), dt), b = cmat(cf * (float)(32 + 256 * q) * (1.0f / 1024.0f), cf * (1.0f / 16.0f));
        u32x4 d = *reinterpret_cast<const u32x4*>(smem + B_E1 + (16 + (w >> 1)) * E1P + lane * 16);
        d = pk8(mfh(d, f.re, Z4), mfh(d, f.im, Z4));
        d = wmul(mfh(msf.re, d, Z4), mfh(msf.im, d, Z4), wv[4]);
        d = pk8(mfh(d, msi.re, Z4), mfh(d, msi.im, Z4));
        const f32x4 r = mfh(d, b.re, Z4);
#pragma unroll
        for (int e = 0; e < 4; ++e) reinterpret_cast<float*>(smem + B_EX)[(w >> 1) * 256 + (4 * q + e) * 16 + c] = r[e];
    }
    u32x4 mia, mib;
    {
        const int jq = spc ? (4 * q) & 7 : 4 * q;
        float cc[4], ss[4];
#pragma unroll
        for (int e = 0; e < 4; ++e) {
            const float f = __builtin_amdgcn_fractf(cf * (float)(j1 + 4 * (jq + e)) * (1.0f / 64.0f));
            const float wg = !spc || (xtr && jq + e == 0) ? 0.125f : 0.25f;
            cc[e] = __builtin_amdgcn_cosf(f) * wg; ss[e] = -__builtin_amdgcn_sinf(f) * wg;
        }
        const unsigned c0 = pkh(cc[0], cc[1]), c1 = pkh(cc[2], cc[3]), n0 = pkh(ss[0], ss[1]), n1 = pkh(ss[2], ss[3]);
        mia = (u32x4){c0, c1, n0, n1}; mib = (u32x4){n0, n1, c0 ^ 0x80008000u, c1 ^ 0x80008000u};
        if (spc) { mib = q < 2 ? (u32x4){0, 0, 0, 0} : mia ^ 0x80008000u; if (q >= 2) mia = (u32x4){0, 0, 0, 0}; }
        PIN(mia); PIN(mib);
    }
    LDS_WAIT();
    TS(3);
    PACE();
    f32x4 xa[4], xb[4];
#pragma unroll
    for (int i = 0; i < 4; ++i) { xa[i] = Z4; xb[i] = Z4; }
    if (xtr) {
        const float sg = (c & 1) ? -0.125f : 0.125f;
#pragma unroll
        for (int i = 0; i < 4; ++i) {
            xa[i] = *reinterpret_cast<const f32x4*>(smem + B_EX + (4 * w + i) * 64 + q * 16) * sg;
            xb[i] = *reinterpret_cast<const f32x4*>(smem + B_EX + 1024 + (4 * w + i) * 64 + q * 16) * -sg;
        }
    }
#pragma unroll
    for (int i = 0; i < 4; ++i) {
        const int s2 = 4 * w + i;
        const u32x4 ea = *reinterpret_cast<const u32x4*>(smem + B_E2 + s2 * 1024 + lane * 16);
        const f32x4 ta = mfh(ea, mia, xa[i]), tb = mfh(ea, mib, xb[i]);
        st128<SC1>(to16(pkh(ta[0], ta[1]), pkh(ta[2], ta[3]), pkh(tb[0], tb[1]), pkh(tb[2], tb[3])), rs,
                   c * 16384 + (q & 1) * 32 + (q >> 1) * 16 + OFF_T + s2 * 262144 + j1 * 4096 + mp * 64);
    }
    TS(4);
    float gg[2];
#pragma unroll
    for (int n = 0; n < 2; ++n) gg[n] = __uint_as_float(__builtin_amdgcn_raw_buffer_load_b32(rs, (32 * w + 16 * n + c) * 128, OFF_G + s2t * 8, 0));
    PACE();
    u32x4 fc[2][2], fs[2][2];
#pragma unroll
    for (int n = 0; n < 2; ++n) {
        float dt = (float)(32 * w + 16 * n + c) * (1.0f / 128.0f); PIN(dt);
#pragma unroll
        for (int h = 0; h < 2; ++h) {
            const CM a = cmat(dt * (float)(32 * h + 4 * q), dt), b = cmat(dt * (float)(32 * h + 16 + 4 * q), dt);
            fc[n][h] = (u32x4){a.re.x, a.re.y, b.re.x, b.re.y}; fs[n][h] = (u32x4){a.im.x, a.im.y, b.im.x, b.im.y};
            PIN(fc[n][h]); PIN(fs[n][h]);
        }
        PACE();
    }
    PACE();
    wait_vm<0>();
    TS(5);
    {
        u32x4 tv[4];
#pragma unroll 1
        for (int spin = 0; spin < (1 << 18); ++spin) {
            unsigned toff = lane * 16; PIN(toff);
#pragma unroll
            for (int i = 0; i < 4; ++i) tv[i] = __builtin_amdgcn_raw_buffer_load_b128(rs, toff, OFF_T + (s2t * 16 + dat) * 16384 + w * 4096 + i * 1024, SC1);
            SCHED();
            PACE(); PACE(); PACE();
            SCHED();
            bool ok = true;
#pragma unroll
            for (int i = 0; i < 4; ++i) ok = ok & (tv[i].x != ~0u) & (tv[i].y != ~0u) & (tv[i].z != ~0u) & (tv[i].w != ~0u);
            if (__all(ok)) break;
            asm volatile("" ::: "memory");
        }
#pragma unroll
        for (int i = 0; i < 4; ++i)
            *reinterpret_cast<u32x4*>(smem + B_UT + w * 4096 + ((lane >> 1) & 1) * 2048 + (16 * i + (lane >> 2)) * 32 + (lane & 1) * 16) = tv[i];
        LDS_WAIT();
        const unsigned cnt = ldsa(smem + B_CNT);
        if (lane == 0) lds_add(cnt);
        TS(6);
#pragma unroll 1
        for (int spin = 0; spin < (1 << 22); ++spin) { if (lds_peek(cnt) >= 4u) break; PACE(); }
    }
    TS(7);
    f32x4 P0[2], P1[2], P2[2], P3a[2], P3b[2];
#pragma unroll
    for (int n = 0; n < 2; ++n) { P0[n] = Z4; P1[n] = Z4; P2[n] = Z4; P3a[n] = Z4; P3b[n] = Z4; }
    const char* tt = smem + B_UT + lane * 8;
#pragma unroll
    for (int h = 0; h < 2; ++h) {
        const u32x4 a1 = trfrag(tt + 1 * 4096 + h * 1024, 512), b1 = trfrag(tt + 1 * 4096 + 2048 + h * 1024, 512);
        const u32x4 a3 = trfrag(tt + 3 * 4096 + h * 1024, 512), b3 = trfrag(tt + 3 * 4096 + 2048 + h * 1024, 512);
        const u32x4 a0 = trfrag(tt + h * 1024, 512), b0 = trfrag(tt + 2048 + h * 1024, 512);
        const u32x4 a2 = trfrag(tt + 2 * 4096 + h * 1024, 512), b2 = trfrag(tt + 2 * 4096 + 2048 + h * 1024, 512);
#pragma unroll
        for (int n = 0; n < 2; ++n) {
            u32x4 cw = fc[n][h], sw_ = fs[n][h];
            P1[n] = mfh(a1, cw, P1[n]); P3a[n] = mfh(a1, sw_, P3a[n]); P3b[n] = mfh(b1, cw, P3b[n]); P1[n] = mfh(b1, sw_, P1[n]);
            if (h == 0 && q == 0) cw.x = (cw.x & 0xffff0000u) | ((c & 1) ? 0xbc00u : 0x3c00u);
            P3a[n] = mfh(b3, cw, P3a[n]); P3b[n] = mfh(a3, sw_, P3b[n]); P1[n] = mfh(a3, cw, P1[n]);
            P1[n] = mfh(b3, sw_, P1[n]);
            if (h == 0 && q == 0) { cw.x = (cw.x & 0xffff0000u) | 0x3800u; sw_.x = (sw_.x & 0xffff0000u) | ((c & 1) ? 0x3800u : 0xb800u); }
            P0[n] = mfh(a0, cw, P0[n]); P2[n] = mfh(a2, cw, P2[n]); P0[n] = mfh(b0, sw_, P0[n]); P2[n] = mfh(b2, sw_, P2[n]);
        }
#pragma unroll
        for (int n = 0; n < 2; ++n) { PIN(P0[n]); PIN(P1[n]); PIN(P2[n]); PIN(P3a[n]); PIN(P3b[n]); }
        PACE();
    }
    const int fe = eu + ew - 267;
#pragma unroll
    for (int n = 0; n < 2; ++n) {
        const f32x4 sp = P0[n] + P2[n], sm = P0[n] - P2[n], p3 = P3a[n] - P3b[n];
        const f32x4 av[4] = {sp + P1[n], sm - p3, sp - P1[n], sm + p3};
        unsigned o[8];
#pragma unroll
        for (int m = 0; m < 4; ++m) {
            const unsigned b0 = pkb(ldexpf(av[m][0], fe), ldexpf(av[m][1], fe)), b1 = pkb(ldexpf(av[m][2], fe), ldexpf(av[m][3], fe));
            o[2 * m] = pkb(gg[n] * lo2f(b0), gg[n] * hi2f(b0)); o[2 * m + 1] = pkb(gg[n] * lo2f(b1), gg[n] * hi2f(b1));
        }
        *reinterpret_cast<u32x4*>(smem + B_GA + ((w * 2 + n) * 2 + 0) * 1024 + lane * 16) = (u32x4){o[0], o[1], o[2], o[3]};
        *reinterpret_cast<u32x4*>(smem + B_GA + ((w * 2 + n) * 2 + 1) * 1024 + lane * 16) = (u32x4){o[4], o[5], o[6], o[7]};
        if (n == 0) PACE();
    }
    LDS_WAIT();
    TS(8);
    TSE(3, w == 0);
    while (nb < 32) { BAR(); ++nb; }
    BAR();
}

NOPK __global__ __launch_bounds__(768, 1) void kb_kernel(const u16* __restrict__ x, const u16* __restrict__ Cre, const u16* __restrict__ Cim,
                                                         const u16* __restrict__ Dskip, char* ws, float* __restrict__ out) {
    extern __shared__ __attribute__((aligned(16))) char smem[];
    const int wid = __builtin_amdgcn_readfirstlane(threadIdx.x >> 6), lane = threadIdx.x & 63;
    if (wid < 4) kb_mma(x, Dskip, ws, out, smem, blockIdx.x, wid, lane);
    else if (wid < 8) kb_load(Cre, Cim, ws, smem, blockIdx.x, wid - 4, lane);
    else kb_aux(ws, smem, blockIdx.x, wid - 8, lane);
}

extern "C" void kernel_launch(void* const* d_in, const int* in_sizes, int n_in, void* d_out, int out_size, void* d_ws, size_t ws_size,
                              hipStream_t stream) {
    const u16* x = (const u16*)d_in[0];
    const u16* w_r = (const u16*)d_in[1];
    const u16* w_i = (const u16*)d_in[2];
    const u16* lam_re = (const u16*)d_in[4];
    const u16* lam_im = (const u16*)d_in[5];
    const u16* Bre = (const u16*)d_in[6];
    const u16* Bim = (const u16*)d_in[7];
    const u16* Cre = (const u16*)d_in[8];
    const u16* Cim = (const u16*)d_in[9];
    const u16* log_dt = (const u16*)d_in[10];
    const u16* Dskip = (const u16*)d_in[11];
    const u16* gate_w = (const u16*)d_in[12];
    char* ws = (char*)d_ws;
    (void)hipFuncSetAttribute((const void*)ka_kernel, hipFuncAttributeMaxDynamicSharedMemorySize, KA_SMEM);
    (void)hipFuncSetAttribute((const void*)kb_kernel, hipFuncAttributeMaxDynamicSharedMemorySize, KB_SMEM);
    ka_kernel<<<256, 512, KA_SMEM, stream>>>(x, w_r, w_i, Bre, Bim, Cre, Cim, lam_re, lam_im, log_dt, gate_w, ws);
    kb_kernel<<<256, 768, KB_SMEM, stream>>>(x, Cre, Cim, Dskip, ws, (float*)d_out);
}
```

```cpp
#include <hip/hip_runtime.h>
#include <stdint.h>

typedef __attribute__((ext_vector_type(8))) __bf16 bf16x8;
typedef __attribute__((ext_vector_type(2))) __bf16 bf16x2;
typedef __attribute__((ext_vector_type(8))) _Float16 f16x8;
typedef __attribute__((ext_vector_type(2))) _Float16 f16x2;
typedef __attribute__((ext_vector_type(4))) float f32x4;
typedef __attribute__((ext_vector_type(2))) float f32x2;
typedef __attribute__((ext_vector_type(4))) unsigned u32x4;
typedef __attribute__((ext_vector_type(2))) unsigned u32x2;
typedef __attribute__((ext_vector_type(4))) short s16x4;
typedef __attribute__((ext_vector_type(2))) unsigned short u16x2;
typedef unsigned short u16;
typedef unsigned long long u64;
#define GAS __attribute__((address_space(1)))
#define LAS __attribute__((address_space(3)))
#define NOPK __attribute__((target("no-packed-fp32-ops")))
#define RLX_AGENT __ATOMIC_RELAXED, __HIP_MEMORY_SCOPE_AGENT
#define DEV __device__ __forceinline__

constexpr int S_ = 2048, D_ = 1024, KF = 513;

constexpr unsigned OFF_H = 0;
constexpr unsigned OFF_U = OFF_H + (8u << 20);
constexpr unsigned OFF_T = OFF_U + (4u << 20);
constexpr unsigned OFF_W = OFF_T + (4u << 20);
constexpr unsigned OFF_G = OFF_W + 65u * 65536u;
constexpr unsigned OFF_MX = OFF_G + 16384u;
constexpr unsigned OFF_GRAN = OFF_MX + 4096u;
constexpr unsigned OFF_E = OFF_GRAN + 16u * 1024u * 16u;
constexpr unsigned OFF_X = OFF_E + 16u * 128u;
constexpr unsigned WS_END = OFF_X + (4u << 20);

#define TSD()
#define TS(i)
#define TSE(slot, on)

DEV float bf2f(u16 h) { return __uint_as_float((unsigned)h << 16); }
DEV unsigned pkb(float a, float b) { f32x2 v = {a, b}; return __builtin_bit_cast(unsigned, __builtin_convertvector(v, bf16x2)); }
DEV unsigned pkh(float a, float b) { f32x2 v = {a, b}; return __builtin_bit_cast(unsigned, __builtin_convertvector(v, f16x2)); }
DEV float lo2f(unsigned p) { return __uint_as_float(p << 16); }
DEV float hi2f(unsigned p) { return __uint_as_float(p & 0xffff0000u); }
DEV float bfr(float f) { return lo2f(pkb(f, f)); }
DEV float2 cmul(float2 a, float2 b) { return make_float2(a.x * b.x - a.y * b.y, a.x * b.y + a.y * b.x); }
DEV float2 cfma(float2 a, float2 b, float2 c) { return make_float2(fmaf(a.x, b.x, fmaf(-a.y, b.y, c.x)), fmaf(a.x, b.y, fmaf(a.y, b.x, c.y))); }
DEV f32x4 mfb(u32x4 a, u32x4 b, f32x4 c) { return __builtin_amdgcn_mfma_f32_16x16x32_bf16(__builtin_bit_cast(bf16x8, a), __builtin_bit_cast(bf16x8, b), c, 0, 0, 0); }
DEV f32x4 mfh(u32x4 a, u32x4 b, f32x4 c) { return __builtin_amdgcn_mfma_f32_16x16x32_f16(__builtin_bit_cast(f16x8, a), __builtin_bit_cast(f16x8, b), c, 0, 0, 0); }
DEV unsigned pkmax(unsigned a, unsigned b) { return __builtin_bit_cast(unsigned, __builtin_elementwise_max(__builtin_bit_cast(u16x2, a), __builtin_bit_cast(u16x2, b))); }
DEV float vcos(float r) { return __builtin_amdgcn_cosf(__builtin_amdgcn_fractf(r)); }
DEV float vsin(float r) { return __builtin_amdgcn_sinf(__builtin_amdgcn_fractf(r)); }
typedef LAS char* lptr;
DEV unsigned ldsa(lptr p) { return (unsigned)(uintptr_t)p; }
template <class T> DEV T lld(lptr p) { return *(LAS const T*)p; }
template <class T> DEV void lst(lptr p, T v) { *(LAS T*)p = v; }
DEV u32x2 trread(lptr p) { return __builtin_bit_cast(u32x2, __builtin_amdgcn_ds_read_tr16_b64_v4i16((LAS s16x4*)p)); }
DEV u32x4 trfrag(lptr p, int second) { const u32x2 a = trread(p), b = trread(p + second); return (u32x4){a.x, a.y, b.x, b.y}; }
DEV void glds16(const void* g, lptr l) { __builtin_amdgcn_global_load_lds((const GAS unsigned*)g, (LAS unsigned*)l, 16, 0, 0); }
DEV u32x4 ldfrag(lptr tile, int row, int chunk) { return lld<u32x4>(tile + row * 128 + ((chunk ^ (row & 7)) << 4)); }
template <int N> DEV void wait_vm() { asm volatile("s_waitcnt vmcnt(%0)" :: "i"(N) : "memory"); }
#define LDS_WAIT() asm volatile("s_waitcnt lgkmcnt(0)" ::: "memory")
#define SCHED() __builtin_amdgcn_sched_barrier(0)
#define BAR() do { __builtin_amdgcn_s_barrier(); asm volatile("" ::: "memory"); } while (0)
#define PIN(x) asm volatile("" : "+v"(x))
typedef __amdgpu_buffer_rsrc_t rsrc_t;
DEV rsrc_t mkrsrc(void* p, unsigned n) { return __builtin_amdgcn_make_buffer_rsrc(p, 0, n, 0x00020000); }
constexpr int SC1 = 16;
#define WT 16
#define WTO 16
template <int AUX> DEV void st128(u32x4 v, rsrc_t rs, unsigned off) { __builtin_amdgcn_raw_buffer_store_b128(v, rs, off, 0, AUX); }
DEV void lds_add(unsigned a) { asm volatile("ds_add_u32 %0, %1" :: "v"(a), "v"(1u) : "memory"); }
DEV unsigned lds_peek(unsigned a) {
    unsigned v;
    asm volatile("ds_read_b32 %0, %1\n\ts_waitcnt lgkmcnt(0)" : "=v"(v) : "v"(a) : "memory");
    return __builtin_amdgcn_readfirstlane(v);
}
DEV void tile_of(int gb, int& ta, int& tb) {
    const int xcd = gb & 7, loc = gb >> 3;
    ta = (xcd >> 1) * 4 + (loc >> 3);
    tb = (xcd & 1) * 8 + (loc & 7);
}
DEV void rows4(float a, float& a0, float& a1, float& a2, float& a3) {
    const unsigned u = __float_as_uint(a);
    const auto r = __builtin_amdgcn_permlane32_swap(u, u, false, false);
    const auto p = __builtin_amdgcn_permlane16_swap(r[0], r[0], false, false);
    const auto s = __builtin_amdgcn_permlane16_swap(r[1], r[1], false, false);
    a0 = __uint_as_float(p[0]); a1 = __uint_as_float(p[1]); a2 = __uint_as_float(s[0]); a3 = __uint_as_float(s[1]);
}
DEV float rowsum4(float a) {
    const unsigned u = __float_as_uint(a);
    const auto r = __builtin_amdgcn_permlane32_swap(u, u, false, false);
    const unsigned t = __float_as_uint(__uint_as_float(r[0]) + __uint_as_float(r[1]));
    const auto p = __builtin_amdgcn_permlane16_swap(t, t, false, false);
    return __uint_as_float(p[0]) + __uint_as_float(p[1]);
}
DEV float2 sel4(int q, float2 a, float2 b, float2 c, float2 d) {
    PIN(d.x); PIN(d.y);
    const float2 e = (q & 1) ? b : a, f = (q & 1) ? d : c;
    return (q & 2) ? f : e;
}
template <int R> DEV unsigned ror16(unsigned v) { return __builtin_amdgcn_update_dpp(0u, v, 0x120 + R, 0xf, 0xf, false); }
DEV unsigned wave_pkmax(unsigned m) {
    m = pkmax(m, ror16<8>(m)); m = pkmax(m, ror16<4>(m)); m = pkmax(m, ror16<2>(m)); m = pkmax(m, ror16<1>(m));
    const auto r = __builtin_amdgcn_permlane32_swap(m, m, false, false);
    m = pkmax(r[0], r[1]);
    const auto p = __builtin_amdgcn_permlane16_swap(m, m, false, false);
    return pkmax(p[0], p[1]);
}
DEV float half_sum(float v) {
    v += __uint_as_float(ror16<8>(__float_as_uint(v))); v += __uint_as_float(ror16<4>(__float_as_uint(v)));
    v += __uint_as_float(ror16<2>(__float_as_uint(v))); v += __uint_as_float(ror16<1>(__float_as_uint(v)));
    const auto p = __builtin_amdgcn_permlane16_swap(__float_as_uint(v), __float_as_uint(v), false, false);
    return __uint_as_float(p[0]) + __uint_as_float(p[1]);
}
DEV u32x4 to16(unsigned p0, unsigned p1, unsigned n0, unsigned n1) {
    const auto a = __builtin_amdgcn_permlane16_swap(p0, n0, false, false);
    const auto b = __builtin_amdgcn_permlane16_swap(p1, n1, false, false);
    return (u32x4){a[0], b[0], a[1], b[1]};
}

#define Z4 ((f32x4){0.f, 0.f, 0.f, 0.f})
constexpr int A_STAGE = 32768, A_XT = 4 * A_STAGE, A_PRM = A_XT + 16384, A_AGG = A_PRM + 2048, A_FLG = A_AGG + 512, A_GW = A_FLG + 64,
              KA_SMEM = A_GW + 2048;

DEV void ka_mma(char* ws, lptr smem, int gb, int wid, int lane) {
    int lt, pt; tile_of(gb, lt, pt);
    const int wr = wid >> 1, wc = wid & 1, c = lane & 15, q = lane >> 4;
    const rsrc_t rs = mkrsrc(ws, WS_END);
    TSD(); TS(0);
    GAS u64* Ep = (GAS u64*)(ws + OFF_E + pt * 128);
    u64 tag = __hip_atomic_load(Ep, RLX_AGENT) + 0x9E3779B97F4A7C15ull;
    if (wid == 0 && lane < 8) lst<unsigned>(smem + A_FLG + lane * 4, 0u);
    f32x4 acc[4][4];
#pragma unroll
    for (int m = 0; m < 4; ++m)
#pragma unroll
        for (int n = 0; n < 4; ++n) acc[m][n] = Z4;
    u32x4 af[2][4], bf_[2][4];
#pragma unroll
    for (int m = 0; m < 4; ++m) { af[1][m] = (u32x4){0, 0, 0, 0}; bf_[1][m] = af[1][m]; }
    PIN(tag);
    tag = (u64)(unsigned)__builtin_amdgcn_readfirstlane((unsigned)tag) | (u64)(unsigned)__builtin_amdgcn_readfirstlane((unsigned)(tag >> 32)) << 32;
    LDS_WAIT();
#pragma unroll 4
    for (int kt = 0; kt < 16; ++kt) {
        BAR();
        TS(8 + kt);
        lptr tA = smem + (kt & 3) * A_STAGE;
        lptr tB = tA + 16384;
#pragma unroll
        for (int m = 0; m < 4; ++m) af[0][m] = ldfrag(tA, wr * 64 + m * 16 + c, q);
#pragma unroll
        for (int n = 0; n < 4; ++n) bf_[0][n] = ldfrag(tB, wc * 64 + n * 16 + c, q);
        SCHED();
#pragma unroll
        for (int m = 0; m < 4; ++m)
#pragma unroll
            for (int n = 0; n < 4; ++n) acc[m][n] = mfb(af[1][m], bf_[1][n], acc[m][n]);
        SCHED();
#pragma unroll
        for (int n = 0; n < 4; ++n) acc[0][n] = mfb(af[0][0], bf_[0][n], acc[0][n]);
        SCHED();
#pragma unroll
        for (int m = 0; m < 4; ++m) af[1][m] = ldfrag(tA, wr * 64 + m * 16 + c, 4 + q);
#pragma unroll
        for (int n = 0; n < 4; ++n) bf_[1][n] = ldfrag(tB, wc * 64 + n * 16 + c, 4 + q);
        SCHED();
#pragma unroll
        for (int m = 1; m < 4; ++m)
#pragma unroll
            for (int n = 0; n < 4; ++n) acc[m][n] = mfb(af[0][m], bf_[0][n], acc[m][n]);
        SCHED();
        LDS_WAIT();
    }
    float2 lam[2], coef[2], l16[2], l128[2];
#pragma unroll
    for (int n = 0; n < 2; ++n) {
        const f32x4 a = lld<f32x4>(smem + A_PRM + (wc * 32 + 2 * c + n) * 32), b = lld<f32x4>(smem + A_PRM + (wc * 32 + 2 * c + n) * 32 + 16);
        lam[n] = make_float2(a[0], a[1]); coef[n] = make_float2(a[2], a[3]); l16[n] = make_float2(b[0], b[1]); l128[n] = make_float2(b[2], b[3]);
    }
#pragma unroll
    for (int m = 0; m < 4; ++m)
#pragma unroll
        for (int n = 0; n < 4; ++n) acc[m][n] = mfb(af[1][m], bf_[1][n], acc[m][n]);
    float2 l32[2], l64[2];
#pragma unroll
    for (int n = 0; n < 2; ++n) { l32[n] = cmul(l16[n], l16[n]); l64[n] = cmul(l32[n], l32[n]); }
    TS(1);
    float2 h[2][16];
#pragma unroll
    for (int n = 0; n < 2; ++n) h[n][0] = make_float2(acc[0][n][0], acc[0][2 + n][0]);
#pragma unroll
    for (int t = 1; t < 16; ++t)
#pragma unroll
        for (int n = 0; n < 2; ++n) h[n][t] = cfma(lam[n], h[n][t - 1], make_float2(acc[t >> 2][n][t & 3], acc[t >> 2][2 + n][t & 3]));
    float2 loc[2], wag[2], wq[2];
#pragma unroll
    for (int n = 0; n < 2; ++n) {
        float2 a0, a1, a2, a3;
        rows4(h[n][15].x, a0.x, a1.x, a2.x, a3.x);
        rows4(h[n][15].y, a0.y, a1.y, a2.y, a3.y);
        const float2 s2 = cfma(l16[n], a0, a1), s3 = cfma(l16[n], s2, a2);
        wag[n] = cfma(l16[n], s3, a3);
        loc[n] = sel4(q, make_float2(0.f, 0.f), a0, s2, s3);
    }
    lptr aggp = smem + A_AGG + (wc * 16 + c) * 16;
    const unsigned flg = ldsa(smem + A_FLG) + wc * 4;
    float2 W0[2] = {make_float2(0.f, 0.f), make_float2(0.f, 0.f)};
    if (wr == 0) {
        if (q == 0) lst<f32x4>(aggp, (f32x4){wag[0].x, wag[0].y, wag[1].x, wag[1].y});
        asm volatile("" ::: "memory");
        if (lane == 0) lds_add(flg);
    } else {
#pragma unroll 1
        for (int spin = 0; spin < (1 << 20); ++spin) { if (lds_peek(flg)) break; }
        const f32x4 w0 = lld<f32x4>(aggp);
        W0[0] = make_float2(w0[0], w0[1]); W0[1] = make_float2(w0[2], w0[3]);
        const float2 g0 = cfma(l64[0], W0[0], wag[0]), g1 = cfma(l64[1], W0[1], wag[1]);
        if (lt < 15 && q < 2) {
            const float2 gg = q == 0 ? g0 : g1;
            st128<SC1>((u32x4){__float_as_uint(gg.x), (unsigned)tag, __float_as_uint(gg.y), (unsigned)(tag >> 32)},
                       rs, (2 * c + q) * 16 + OFF_GRAN + (lt * 1024 + pt * 64 + wc * 32) * 16);
        }
    }
    TS(2);
#pragma unroll
    for (int n = 0; n < 2; ++n) { PIN(lam[n].x); PIN(lam[n].y); PIN(coef[n].x); PIN(coef[n].y); PIN(l16[n].x); PIN(l16[n].y); PIN(l128[n].x); PIN(l128[n].y); }
    float2 pw[2][8], l8[2], lq[2], l512[2];
#pragma unroll
    for (int n = 0; n < 2; ++n) {
        wq[n] = sel4(q, make_float2(1.f, 0.f), l16[n], l32[n], cmul(l32[n], l16[n]));
        loc[n] = cfma(wq[n], W0[n], loc[n]);
        if (wr) wq[n] = cmul(wq[n], l64[n]);
        pw[n][0] = cmul(coef[n], lam[n]);
        l8[n] = cmul(lam[n], lam[n]); l8[n] = cmul(l8[n], l8[n]); l8[n] = cmul(l8[n], l8[n]);
        const float2 l256 = cmul(l128[n], l128[n]);
        lq[n] = sel4(q, make_float2(1.f, 0.f), l128[n], l256, cmul(l256, l128[n]));
        l512[n] = cmul(l256, l256);
    }
#pragma unroll
    for (int t = 1; t < 8; ++t)
#pragma unroll
        for (int n = 0; n < 2; ++n) pw[n][t] = cmul(pw[n][t - 1], lam[n]);
#pragma unroll
    for (int t = 0; t < 16; ++t)
#pragma unroll
        for (int n = 0; n < 2; ++n) h[n][t] = cmul(coef[n], h[n][t]);
#pragma unroll
    for (int t = 0; t < 16; ++t) { PIN(h[0][t].x); PIN(h[0][t].y); PIN(h[1][t].x); PIN(h[1][t].y); }
#pragma unroll
    for (int t = 0; t < 8; ++t) { PIN(pw[0][t].x); PIN(pw[0][t].y); PIN(pw[1][t].x); PIN(pw[1][t].y); }
    TS(3);
    if (lt > 0) {
        u32x4 gr[4][2];
#pragma unroll 1
        for (int spin = 0; spin < (1 << 20); ++spin) {
            bool ok = true;
#pragma unroll
            for (int j = 0; j < 4; ++j) {
                const int cc = lt - 1 - (4 * j + q);
                unsigned off = cc >= 0 ? (unsigned)(OFF_GRAN + (cc * 1024 + pt * 64 + wc * 32 + 2 * c) * 16) : 0x80000000u;
                PIN(off);
                gr[j][0] = __builtin_amdgcn_raw_buffer_load_b128(rs, off, 0, SC1);
                gr[j][1] = __builtin_amdgcn_raw_buffer_load_b128(rs, off + 16, 0, SC1);
            }
#pragma unroll
            for (int j = 0; j < 4; ++j) {
                const bool v = lt - 1 - (4 * j + q) >= 0;
#pragma unroll
                for (int n = 0; n < 2; ++n) ok = ok & (!v | ((gr[j][n].y == (unsigned)tag) & (gr[j][n].w == (unsigned)(tag >> 32))));
            }
            if (__all(ok)) break;
            asm volatile("" ::: "memory");
        }
        if (lt == 15 && wid == 0 && lane == 0) __hip_atomic_store(Ep, tag, RLX_AGENT);
#pragma unroll
        for (int n = 0; n < 2; ++n) {
            float2 cs = make_float2(__uint_as_float(gr[3][n].x), __uint_as_float(gr[3][n].z));
#pragma unroll
            for (int j = 2; j >= 0; --j) cs = cfma(l512[n], cs, make_float2(__uint_as_float(gr[j][n].x), __uint_as_float(gr[j][n].z)));
            cs = cmul(cs, lq[n]);
            cs.x = rowsum4(cs.x); cs.y = rowsum4(cs.y);
            loc[n] = cfma(wq[n], cs, loc[n]);
        }
    }
    TS(4);
    lptr tb = smem + wid * 8192;
#pragma unroll
    for (int n = 0; n < 2; ++n)
#pragma unroll
        for (int hf = 0; hf < 2; ++hf) {
            const float2 s = hf ? cmul(l8[n], loc[n]) : loc[n];
            float2 v[8];
#pragma unroll
            for (int t = 0; t < 8; ++t) v[t] = cfma(pw[n][t], s, h[n][8 * hf + t]);
            lst<u32x4>(tb + (n * 2 + hf) * 1024 + lane * 16, (u32x4){pkb(v[0].x, v[1].x), pkb(v[2].x, v[3].x), pkb(v[4].x, v[5].x), pkb(v[6].x, v[7].x)});
            lst<u32x4>(tb + 4096 + (n * 2 + hf) * 1024 + lane * 16, (u32x4){pkb(-v[0].y, -v[1].y), pkb(-v[2].y, -v[3].y), pkb(-v[4].y, -v[5].y), pkb(-v[6].y, -v[7].y)});
        }
    asm volatile("" ::: "memory");
    {
        lptr rb = tb + ((c >> 2) & 1) * 2048 + ((c >> 1) & 1) * 1024 + (4 * q + (c >> 3)) * 16 + (c & 1) * 8;
        const unsigned hoff = OFF_H + (c * 32 + pt) * 16384 + (lt * 8 + wr * 4) * 128 + wc * 64 + q * 16;
#pragma unroll
        for (int pl = 0; pl < 2; ++pl)
#pragma unroll
            for (int qq = 0; qq < 4; ++qq) st128<WT>(trfrag(rb + pl * 4096 + qq * 256, 32), rs, hoff + pl * 16 * 16384 + qq * 128);
    }
    TS(5);
    TSE(0, wid == 0);
}

struct __attribute__((packed, aligned(2))) U64U { u32x2 v; };
DEV void w_load(const u16* __restrict__ w_r, const u16* __restrict__ w_i, int k1, int qw, int lw, int lane, u32x2 (&R)[4], u32x2 (&I)[4], unsigned& sp) {
    const int kd0 = 4 * (lane & 15) + 64 * (4 * lw + (lane >> 4));
    const int col = lw < 2 ? kd0 : 1021 - kd0;
#pragma unroll
    for (int r = 0; r < 4; ++r) {
        const int ks = k1 + 128 * (4 * qw + r), row = lw < 2 ? ks : (S_ - ks) & (S_ - 1);
        R[r] = reinterpret_cast<const U64U*>(w_r + row * KF + col)->v;
        I[r] = reinterpret_cast<const U64U*>(w_i + row * KF + col)->v;
    }
    const int ks = k1 + 128 * (4 * qw + (lane & 3)), row = lw < 2 ? (S_ - ks) & (S_ - 1) : ks;
    sp = ((lane & 4) ? w_i : w_r)[row * KF + (lw < 2 ? 0 : 512)];
}
DEV unsigned w_store(rsrc_t rs, int k1, int qw, int lw, int lane, u32x2 (&R)[4], u32x2 (&I)[4], unsigned sp) {
    unsigned o[4][4], mx = 0;
    const unsigned cj = lw < 2 ? 0u : 0x80008000u;
#pragma unroll
    for (int r = 0; r < 4; ++r) {
        const unsigned i0 = I[r].x ^ cj, i1 = I[r].y ^ cj;
        o[0][r] = __builtin_amdgcn_perm(i0, R[r].x, 0x05040100u); o[1][r] = __builtin_amdgcn_perm(i0, R[r].x, 0x07060302u);
        o[2][r] = __builtin_amdgcn_perm(i1, R[r].y, 0x05040100u); o[3][r] = __builtin_amdgcn_perm(i1, R[r].y, 0x07060302u);
    }
    const bool f0 = lane == 0 && lw == 0, f3 = lane == 0 && lw == 2;
#pragma unroll
    for (int r = 0; r < 4; ++r) {
        const float pr = lo2f(__builtin_amdgcn_readlane(sp, r)), pi = lo2f(__builtin_amdgcn_readlane(sp, 4 + r) ^ (lw < 2 ? 0x8000u : 0u));
        const unsigned t = lw < 2 ? o[0][r] : o[3][r];
        unsigned v = pkb(0.5f * (lo2f(t) + pr), 0.5f * (hi2f(t) + pi));
        PIN(v);
        o[0][r] = f0 ? v : o[0][r]; o[3][r] = f3 ? v : o[3][r];
    }
#pragma unroll
    for (int e = 0; e < 4; ++e)
        st128<WT>((u32x4){o[e][0], o[e][1], o[e][2], o[e][3]}, rs, (lane & 15) * 1024 + (lane >> 4) * 16 +
                  OFF_W + (k1 * 4 + (lw < 2 ? e : 3 - e)) * 16384 + qw * 256 + lw * 64);
#pragma unroll
    for (int r = 0; r < 4; ++r)
        mx = pkmax(mx, pkmax(pkmax(R[r].x & 0x7fff7fffu, R[r].y & 0x7fff7fffu), pkmax(I[r].x & 0x7fff7fffu, I[r].y & 0x7fff7fffu)));
    return pkmax(mx, sp & 0x7fffu);
}

DEV void ka_load(const u16* __restrict__ x, const u16* __restrict__ w_r, const u16* __restrict__ w_i, const u16* __restrict__ Bre,
                 const u16* __restrict__ Bim, const u16* __restrict__ Cre, const u16* __restrict__ Cim, char* ws, lptr smem, int gb, int lw, int lane) {
    int lt, pt; tile_of(gb, lt, pt);
    const int srow = lane >> 3, schunk = (lane & 7) ^ srow;
    const int rot = (lt + pt) & 15;
    const char* gA = (const char*)x + (size_t)(lt * 128 + 64 * (lw >> 1) + 8 * (lw & 1) + 16 * (srow >> 2) + (srow & 3)) * 2048 + schunk * 16;
    const char* gB = (const char*)((lw & 1) ? Bim : Bre) + (size_t)(pt * 64 + 32 * (lw >> 1) + 2 * srow) * 2048 + schunk * 16;
    auto stage = [&](int kk) {
        const int ko = ((kk + rot) & 15) * 128;
        lptr l = smem + (kk & 3) * A_STAGE + lw * 4096 + lane * 16;
#pragma unroll
        for (int i = 0; i < 4; ++i) glds16(gA + (32 * (i & 1) + 4 * (i >> 1)) * 2048 + ko, l + i * 1024);
#pragma unroll
        for (int i = 0; i < 4; ++i) glds16(gB + (16 * (i & 1) + (i >> 1)) * 2048 + ko, l + 16384 + i * 1024);
    };
    TSD(); TS(0);
    stage(0); stage(1); stage(2);
    TS(6);
#pragma unroll 1
    for (int kt = 0; kt < 13; ++kt) { wait_vm<16>(); TS(8 + kt); BAR(); stage(kt + 3); }
    wait_vm<16>(); TS(21); BAR();
    wait_vm<8>(); TS(22); BAR();
    wait_vm<0>(); TS(23); BAR();
    const rsrc_t rs = mkrsrc(ws, WS_END);
    u32x2 R[4], I[4]; unsigned sp;
    w_load(w_r, w_i, gb >> 2, gb & 3, lw, lane, R, I, sp);
    const unsigned ctouch = *reinterpret_cast<const unsigned*>((const char*)((lw & 1) ? Cim : Cre) + (size_t)gb * 8192 + (lane & 31) * 128 + (lw >> 1) * 4096);
    TS(1);
    unsigned mx = w_store(rs, gb >> 2, gb & 3, lw, lane, R, I, sp);
    TS(2);
    if (gb < 4) {
        w_load(w_r, w_i, 64, gb & 3, lw, lane, R, I, sp);
        mx = pkmax(mx, w_store(rs, 64, gb & 3, lw, lane, R, I, sp));
    }
    mx = wave_pkmax(pkmax(mx, mx >> 16));
    if (lane == 0) __builtin_amdgcn_raw_buffer_store_b16((u16)mx, rs, 0, OFF_MX + (gb * 4 + lw) * 4, 0);
    asm volatile("" :: "v"(ctouch));
    TS(3);
    TSE(1, lw == 0);
}

DEV void ka_aux(const u16* __restrict__ x, const u16* __restrict__ lam_re, const u16* __restrict__ lam_im, const u16* __restrict__ log_dt,
                const u16* __restrict__ gate_w, char* ws, lptr smem, int gb, int aw, int lane) {
    int lt, pt; tile_of(gb, lt, pt);
    const int c = lane & 15, q = lane >> 4;
    const int rot = (lt + pt) & 15;
    const rsrc_t rs = mkrsrc(ws, WS_END);
    TSD(); TS(0);
    const u32x2 gwv = *reinterpret_cast<const u32x2*>(gate_w + aw * 256 + lane * 4);
    unsigned lru = lam_re[pt * 64 + lane], liu = lam_im[pt * 64 + lane], ldu = log_dt[pt * 64 + lane];
    SCHED();
    const int gi = 8 * pt + 2 * aw + (lane >> 5);
    const unsigned gaddr = ldsa(smem) + gi * 128 + ((((lane & 31) >> 2) ^ (gi & 7)) << 4) + (lane & 3) * 4;
    const unsigned gwaddr = ldsa(smem + A_GW) + (lane & 31) * 4;
    float gsum = 0.f;
    u32x4 fch[4], fcl[4], fsh[4], fsl[4];
    const float mrev = (float)(16 * aw + c) * (1.0f / 128.0f);
    const bool m0 = 16 * aw + c == 0;
    auto fgen = [&](int i) {
        float th = mrev; PIN(th);
        const float t0 = __builtin_amdgcn_fractf(th * (float)(32 * (i >> 2) + 16 * ((i >> 1) & 1) + 2 * (i & 1) + 4 * q)), t1 = t0 + th;
        const float c0 = __builtin_amdgcn_cosf(t0), c1 = __builtin_amdgcn_cosf(t1);
        const float s0 = m0 ? 1.f : -__builtin_amdgcn_sinf(t0), s1 = m0 ? -1.f : -__builtin_amdgcn_sinf(t1);
        const unsigned ch = pkb(c0, c1), sh = pkb(s0, s1);
        fch[i >> 2][i & 3] = ch; fcl[i >> 2][i & 3] = pkb(c0 - lo2f(ch), c1 - hi2f(ch));
        fsh[i >> 2][i & 3] = sh; fsl[i >> 2][i & 3] = pkb(s0 - lo2f(sh), s1 - hi2f(sh));
        PIN(fch[i >> 2][i & 3]); PIN(fcl[i >> 2][i & 3]); PIN(fsh[i >> 2][i & 3]); PIN(fsl[i >> 2][i & 3]);
    };
#pragma unroll
    for (int i = 0; i < 6; ++i) fgen(i);
    PIN(lru); PIN(liu); PIN(ldu);
    lst<u32x2>(smem + A_GW + aw * 512 + lane * 8, gwv);
    const float lr = lo2f(lru), li = lo2f(liu), ld = lo2f(ldu);
    LDS_WAIT();
    float dt = 0.f, er = 0.f; float2 lam = make_float2(0.f, 0.f), coef = lam;
#pragma unroll
    for (int kt = 0; kt < 16; ++kt) {
        TS(8 + kt);
        BAR();
        if (kt == 13 || kt == 14) {
            const char* gx = (const char*)x + (size_t)(16 * (lane >> 1) + lt) * 2048 + aw * 512 + pt * 32 + (lane & 1) * 16;
#pragma unroll
            for (int i = 2 * (kt - 13); i < 2 * (kt - 13) + 2; ++i) glds16(gx + (size_t)i * 512 * 2048, smem + A_XT + aw * 4096 + i * 1024 + lane * 16);
        }
        if (kt >= 4 && kt < 8) st128<WT>((u32x4){~0u, ~0u, ~0u, ~0u}, rs, aw * 1024 + lane * 16 + OFF_T + gb * 16384 + (kt - 4) * 4096);
        unsigned xv, gw;
        asm volatile("ds_read_b32 %0, %2\n\tds_read_b32 %1, %3\n\ts_waitcnt lgkmcnt(0)" : "=&v"(xv), "=&v"(gw)
                     : "v"(gaddr + (kt & 3) * A_STAGE), "v"(gwaddr + ((kt + rot) & 15) * 128) : "memory");
        const unsigned pr = pkb(lo2f(xv) * lo2f(gw), hi2f(xv) * hi2f(gw));
        gsum += lo2f(pr); gsum += hi2f(pr); PIN(gsum);
        if (kt >= 3 && kt < 13) fgen(kt + 3);
        if (aw == 1) {
            if (kt == 0) { dt = bfr(expf(ld)); er = expf(dt * lr); PIN(dt); PIN(er); }
            if (kt == 1) { float sn, cs; sincosf(dt * li, &sn, &cs); lam = make_float2(er * cs, er * sn); PIN(lam.x); PIN(lam.y); }
            if (kt == 2) {
                const float nr = lam.x - 1.0f, ni = lam.y, den = lr * lr + li * li;
                coef = make_float2((nr * lr + ni * li) / den, (ni * lr - nr * li) / den);
                float2 l16 = lam;
#pragma unroll
                for (int i = 0; i < 4; ++i) l16 = cmul(l16, l16);
                float2 l128 = l16;
#pragma unroll
                for (int i = 0; i < 3; ++i) l128 = cmul(l128, l128);
                lst<f32x4>(smem + A_PRM + lane * 32, (f32x4){lam.x, lam.y, coef.x, coef.y});
                lst<f32x4>(smem + A_PRM + lane * 32 + 16, (f32x4){l16.x, l16.y, l128.x, l128.y});
                LDS_WAIT();
            }
        }
        SCHED();
    }
    TS(1);
    gsum = half_sum(gsum);
    const float g = bfr(1.0f / (1.0f + expf(-bfr(gsum))));
    float omg = bfr(1.0f - g);
    PIN(omg);
    __builtin_amdgcn_s_waitcnt(0x0F70);
    const unsigned cnt = ldsa(smem + A_FLG) + 8;
    if (lane == 0) lds_add(cnt);
    {
        const int tau = (gi & 64) | ((gi & 12) << 2) | ((gi & 48) >> 2) | (gi & 3);
        if ((lane & 31) == 0)
            __builtin_amdgcn_raw_buffer_store_b64((u32x2){__float_as_uint(g), __float_as_uint(omg)}, rs, (tau & 15) * 1024 + (lt * 8 + (tau >> 4)) * 8, OFF_G, 0);
    }
    u32x4 xc[4];
#pragma unroll
    for (int i = 0; i < 4; ++i) xc[i] = lld<u32x4>(smem + A_XT + aw * 4096 + i * 1024 + lane * 16);
    TS(2);
#pragma unroll 1
    for (int spin = 0; spin < (1 << 20); ++spin) { if (lds_peek(cnt) >= 4u) break; }
    TS(3);
    u32x4 xa[4][4];
#pragma unroll
    for (int ks = 0; ks < 4; ++ks)
#pragma unroll
        for (int d1 = 0; d1 < 4; ++d1) xa[ks][d1] = trfrag(smem + A_XT + d1 * 4096 + ks * 1024 + lane * 8, 512);
#pragma unroll
    for (int i = 0; i < 4; ++i) st128<WT>(xc[i], rs, OFF_X + (lt * 16 + pt) * 16384 + aw * 4096 + i * 1024 + lane * 16);
    SCHED();
    f32x4 vr[4], vi[4];
#pragma unroll
    for (int d1 = 0; d1 < 4; ++d1) { vr[d1] = Z4; vi[d1] = Z4; }
#pragma unroll
    for (int ks = 0; ks < 4; ++ks) {
#pragma unroll
        for (int d1 = 0; d1 < 4; ++d1) { vr[d1] = mfb(xa[ks][d1], fcl[ks], vr[d1]); vi[d1] = mfb(xa[ks][d1], fsl[ks], vi[d1]); }
#pragma unroll
        for (int d1 = 0; d1 < 4; ++d1) { vr[d1] = mfb(xa[ks][d1], fch[ks], vr[d1]); vi[d1] = mfb(xa[ks][d1], fsh[ks], vi[d1]); }
        SCHED();
    }
    f32x4 ur[4], ui[4];
    {
        const f32x4 t0r = vr[0] + vr[2], t1r = vr[0] - vr[2], t2r = vr[1] + vr[3], t3r = vr[1] - vr[3];
        const f32x4 t0i = vi[0] + vi[2], t1i = vi[0] - vi[2], t2i = vi[1] + vi[3], t3i = vi[1] - vi[3];
        ur[0] = t0r + t2r; ui[0] = t0i + t2i; ur[2] = t0r - t2r; ui[2] = t0i - t2i;
        ur[1] = t1r + t3i; ui[1] = t1i - t3r; ur[3] = t1r - t3i; ui[3] = t1i + t3r;
        if (m0) { ur[1] = t1r; ui[1] = -t3r; ur[3] = t1i; ui[3] = t3i; }
    }
    TS(4);
    unsigned um = 0;
#pragma unroll
    for (int j1 = 0; j1 < 4; ++j1) {
        const u32x4 o = to16(pkb(ur[j1][0], ur[j1][1]), pkb(ur[j1][2], ur[j1][3]), pkb(ui[j1][0], ui[j1][1]), pkb(ui[j1][2], ui[j1][3]));
        st128<WT>(o, rs, c * 65536 + (q & 1) * 32 + (q >> 1) * 16 + OFF_U + (aw * 64 + j1) * 16384 + lt * 1024 + pt * 64);
        um = pkmax(um, pkmax(pkmax(o.x & 0x7fff7fffu, o.y & 0x7fff7fffu), pkmax(o.z & 0x7fff7fffu, o.w & 0x7fff7fffu)));
    }
    um = wave_pkmax(pkmax(um, um >> 16));
    if (lane == 0) __builtin_amdgcn_raw_buffer_store_b16((u16)um, rs, 0, OFF_MX + (gb * 4 + aw) * 4 + 2, 0);
    TS(5);
    TSE(5, aw == 0);
}

NOPK __global__ __launch_bounds__(768, 1) void ka_kernel(const u16* __restrict__ x, const u16* __restrict__ w_r, const u16* __restrict__ w_i,
                                                         const u16* __restrict__ Bre, const u16* __restrict__ Bim, const u16* __restrict__ Cre,
                                                         const u16* __restrict__ Cim, const u16* __restrict__ lam_re,
                                                         const u16* __restrict__ lam_im, const u16* __restrict__ log_dt,
                                                         const u16* __restrict__ gate_w, char* ws) {
    extern __shared__ __attribute__((aligned(16))) char smem_[];
    lptr smem = (lptr)smem_;
    const int wid = __builtin_amdgcn_readfirstlane(threadIdx.x >> 6), lane = threadIdx.x & 63;
    if (wid < 4) ka_mma(ws, smem, blockIdx.x, wid, lane);
    else if (wid < 8) ka_load(x, w_r, w_i, Bre, Bim, Cre, Cim, ws, smem, blockIdx.x, wid - 4, lane);
    else ka_aux(x, lam_re, lam_im, log_dt, gate_w, ws, smem, blockIdx.x, wid - 8, lane);
}

constexpr int B_STAGE = 24576, B_UT = 4 * B_STAGE, B_E1 = B_UT + 16384, E1P = 1040, B_E2 = B_E1 + 18 * E1P + 32, B_EX = B_E2 + 16384,
              B_MX = B_EX + 2048, B_CNT = B_MX + 32, KB_SMEM = B_CNT + 16;
constexpr int B_R0 = B_E1;

DEV void kb_load(const u16* __restrict__ Cre, const u16* __restrict__ Cim, char* ws, lptr smem, int gb, int lw, int lane) {
    int s2, da; tile_of(gb, s2, da);
    const int srow = lane >> 3, schunk = (lane & 7) ^ srow;
    const size_t offA = (size_t)(256 * lw + 16 * da + srow) * 2048 + schunk * 16;
    const char* gB = ws + OFF_H + (size_t)s2 * 32 * 16384 + (32 * lw + srow) * 128 + schunk * 16;
    auto stage = [&](int kt) {
        lptr l = smem + (kt & 3) * B_STAGE + lane * 16;
        const char* gA = (const char*)(kt < 16 ? Cre : Cim) + offA + (kt & 15) * 128;
        glds16(gA, l + lw * 2048);
        glds16(gA + 8 * 2048, l + lw * 2048 + 1024);
#pragma unroll
        for (int i = 0; i < 4; ++i) glds16(gB + kt * 16384 + i * 1024, l + 8192 + lw * 4096 + i * 1024);
    };
    TSD(); TS(0);
    stage(0); stage(1); stage(2);
    TS(1);
#pragma unroll 1
    for (int kt = 0; kt < 29; ++kt) { wait_vm<12>(); TS(8 + kt); BAR(); stage(kt + 3); }
    wait_vm<12>(); TS(37); BAR();
    wait_vm<6>(); TS(38); BAR();
    wait_vm<0>(); TS(39); BAR();
    TSE(4, lw == 0);
}

DEV void kb_mma(char* ws, float* __restrict__ out, lptr smem, int gb, int wid, int lane) {
    int s2, da; tile_of(gb, s2, da);
    const int c = lane & 15, q = lane >> 4;
    TSD(); TS(0);
    float omg[2];
#pragma unroll
    for (int n = 0; n < 2; ++n) omg[n] = reinterpret_cast<const float*>(ws + OFF_G)[(s2 * 128 + 32 * wid + 16 * n + c) * 2 + 1];
    f32x4 acc[4][2];
#pragma unroll
    for (int m = 0; m < 4; ++m) { acc[m][0] = Z4; acc[m][1] = Z4; }
    u32x4 af[2][4], bf_[2][2];
#pragma unroll
    for (int m = 0; m < 4; ++m) af[1][m] = (u32x4){0, 0, 0, 0};
    bf_[1][0] = af[1][0]; bf_[1][1] = af[1][0];
#pragma unroll 4
    for (int kt = 0; kt < 32; ++kt) {
        BAR();
        TS(8 + kt);
        lptr tA = smem + (kt & 3) * B_STAGE;
        lptr tB = tA + 8192;
#pragma unroll
        for (int m = 0; m < 4; ++m) af[0][m] = ldfrag(tA, m * 16 + c, q);
#pragma unroll
        for (int n = 0; n < 2; ++n) bf_[0][n] = ldfrag(tB, wid * 32 + n * 16 + c, q);
        SCHED();
#pragma unroll
        for (int m = 0; m < 4; ++m)
#pragma unroll
            for (int n = 0; n < 2; ++n) acc[m][n] = mfb(af[1][m], bf_[1][n], acc[m][n]);
        SCHED();
#pragma unroll
        for (int m = 0; m < 4; ++m) af[1][m] = ldfrag(tA, m * 16 + c, 4 + q);
#pragma unroll
        for (int n = 0; n < 2; ++n) bf_[1][n] = ldfrag(tB, wid * 32 + n * 16 + c, 4 + q);
        SCHED();
#pragma unroll
        for (int m = 0; m < 4; ++m)
#pragma unroll
            for (int n = 0; n < 2; ++n) acc[m][n] = mfb(af[0][m], bf_[0][n], acc[m][n]);
        SCHED();
        LDS_WAIT();
    }
#pragma unroll
    for (int m = 0; m < 4; ++m)
#pragma unroll
        for (int n = 0; n < 2; ++n) acc[m][n] = mfb(af[1][m], bf_[1][n], acc[m][n]);
    TS(1);
    const rsrc_t ro = mkrsrc(out, S_ * D_ * 4);
    BAR();
    TS(2);
#pragma unroll
    for (int n = 0; n < 2; ++n) {
        const int row = 16 * (32 * wid + 16 * n + c) + s2;
        const float o2 = 2.0f * omg[n];
#pragma unroll
        for (int m = 0; m < 4; ++m) {
            const f32x4 r0 = lld<f32x4>(smem + B_R0 + ((wid * 2 + n) * 4 + m) * 1024 + lane * 16);
            const f32x4 o = {fmaf(o2, acc[m][n][0], r0[0]), fmaf(o2, acc[m][n][1], r0[1]), fmaf(o2, acc[m][n][2], r0[2]), fmaf(o2, acc[m][n][3], r0[3])};
            st128<WTO>(__builtin_bit_cast(u32x4, o), ro, (row * D_ + 256 * m + 16 * da + 4 * q) * 4);
        }
    }
    TS(3);
    TSE(2, wid == 0);
}

struct CM { u32x4 re, im; };
DEV CM cmat(float t0, float dt) {
    float cc[4], ss[4];
    t0 = __builtin_amdgcn_fractf(t0);
#pragma unroll
    for (int e = 0; e < 4; ++e) { const float f = fmaf((float)e, dt, t0); cc[e] = __builtin_amdgcn_cosf(f); ss[e] = __builtin_amdgcn_sinf(f); }
    const unsigned c0 = pkh(cc[0], cc[1]), c1 = pkh(cc[2], cc[3]), s0 = pkh(ss[0], ss[1]), s1 = pkh(ss[2], ss[3]);
    CM m; m.re = (u32x4){c0, c1, s0 ^ 0x80008000u, s1 ^ 0x80008000u}; m.im = (u32x4){s0, s1, c0, c1};
    PIN(m.re); PIN(m.im);
    return m;
}
DEV u32x4 pk8(f32x4 r, f32x4 i) { return (u32x4){pkh(r[0], r[1]), pkh(r[2], r[3]), pkh(i[0], i[1]), pkh(i[2], i[3])}; }
DEV u32x4 wmul(f32x4 r, f32x4 i, u32x4 w) {
    float yr[4], yi[4];
#pragma unroll
    for (int e = 0; e < 4; ++e) {
        const unsigned wu = w[e];
        const f16x2 h = __builtin_bit_cast(f16x2, wu);
        yr[e] = fmaf(r[e], (float)h[0], -i[e] * (float)h[1]); yi[e] = fmaf(r[e], (float)h[1], i[e] * (float)h[0]);
    }
    return (u32x4){pkh(yr[0], yr[1]), pkh(yr[2], yr[3]), pkh(yi[0], yi[1]), pkh(yi[2], yi[3])};
}
DEV u32x4 wcvt(u32x4 w, float sw) {
#pragma unroll
    for (int e = 0; e < 4; ++e) w[e] = pkh(lo2f(w[e]) * sw, hi2f(w[e]) * sw);
    PIN(w);
    return w;
}
#define PACE() do { if (nb < 32) { BAR(); ++nb; } } while (0)

DEV void kb_aux(const u16* __restrict__ Dskip, char* ws, lptr smem, int gb, int w, int lane) {
    int s2t, dat; tile_of(gb, s2t, dat);
    const int c = lane & 15, q = lane >> 4, mp = gb >> 2, j1 = gb & 3;
    const bool spc = mp == 0 && !(j1 & 1), xtr = gb == 0;
    const int k1 = mp ? mp : spc ? (w >> 2) * 64 : (j1 == 1 ? 0 : 64);
    const int ja0 = spc ? (2 * w) & 7 : 2 * w;
    const rsrc_t rs = mkrsrc(ws, WS_END);
    int nb = 0;
    TSD(); TS(0);
#pragma unroll
    for (int i = 0; i < 2; ++i) glds16(ws + OFF_U + gb * 16384 + (2 * w + i) * 1024 + lane * 16, smem + B_UT + (2 * w + i) * 1024 + lane * 16);
    u32x2 mx2 = __builtin_amdgcn_raw_buffer_load_b64(rs, lane * 8, OFF_MX + w * 512, 0);
    u32x4 wv[3];
#pragma unroll
    for (int i = 0; i < 2; ++i) wv[i] = __builtin_amdgcn_raw_buffer_load_b128(rs, lane * 16, OFF_W + (k1 * 4 + j1) * 16384 + (ja0 + i) * 1024, 0);
    wv[2] = __builtin_amdgcn_raw_buffer_load_b128(rs, xtr ? lane * 16 : 0x80000000u, OFF_W + (k1 * 4) * 16384 + 8 * 1024, 0);
    if (w == 0 && lane < 4) asm volatile("ds_write_b32 %0, %1" :: "v"(ldsa(smem + B_CNT) + lane * 4), "v"(0u) : "memory");
    SCHED();
    float cf = (float)c;
    u32x4 mah[2], mal[2];
    {
        const float dt = -(float)(j1 + 4 * (spc ? c & 7 : c)) * (1.0f / 64.0f), t0 = __builtin_amdgcn_fractf(dt * (float)(4 * q));
        float cc[4], ss[4];
#pragma unroll
        for (int e = 0; e < 4; ++e) { const float f = fmaf((float)e, dt, t0); cc[e] = __builtin_amdgcn_cosf(f); ss[e] = __builtin_amdgcn_sinf(f); }
        const unsigned c0 = pkb(cc[0], cc[1]), c1 = pkb(cc[2], cc[3]), s0 = pkb(ss[0], ss[1]), s1 = pkb(ss[2], ss[3]);
        const unsigned c0l = pkb(cc[0] - lo2f(c0), cc[1] - hi2f(c0)), c1l = pkb(cc[2] - lo2f(c1), cc[3] - hi2f(c1));
        const unsigned s0l = pkb(ss[0] - lo2f(s0), ss[1] - hi2f(s0)), s1l = pkb(ss[2] - lo2f(s1), ss[3] - hi2f(s1));
        mah[0] = (u32x4){c0, c1, s0 ^ 0x80008000u, s1 ^ 0x80008000u}; mah[1] = (u32x4){s0, s1, c0, c1};
        mal[0] = (u32x4){c0l, c1l, s0l ^ 0x80008000u, s1l ^ 0x80008000u}; mal[1] = (u32x4){s0l, s1l, c0l, c1l};
        if (spc) {
            mah[0] = c < 8 ? (u32x4){c0, c1, 0, 0} : (u32x4){0, 0, c0, c1}; mah[1] = c < 8 ? (u32x4){s0, s1, 0, 0} : (u32x4){0, 0, s0, s1};
            mal[0] = c < 8 ? (u32x4){c0l, c1l, 0, 0} : (u32x4){0, 0, c0l, c1l}; mal[1] = c < 8 ? (u32x4){s0l, s1l, 0, 0} : (u32x4){0, 0, s0l, s1l};
        }
        PIN(mah[0]); PIN(mah[1]); PIN(mal[0]); PIN(mal[1]);
    }
    CM gf[2], gi[2];
#pragma unroll
    for (int i = 0; i < 2; ++i) { const float dt = -(float)(j1 + 4 * (ja0 + i) + 64 * c) * (1.0f / 1024.0f); gf[i] = cmat(dt * (float)(4 * q), dt); }
    const float dts = -(float)(k1 + 128 * c) * (1.0f / 2048.0f);
    const CM msf = cmat(dts * (float)(4 * q), dts);
    const CM msi = cmat((float)(k1 + 512 * q) * cf * (1.0f / 2048.0f), cf * (1.0f / 16.0f));
    TS(1);
    PACE();
    PIN(cf);
#pragma unroll
    for (int i = 0; i < 2; ++i) gi[i] = cmat(cf * (float)(j1 + 4 * (ja0 + i) + 256 * q) * (1.0f / 1024.0f), cf * (1.0f / 16.0f));
    PACE();
    PIN(mx2);
    {
        const unsigned m = wave_pkmax(pkmax(mx2.x, mx2.y));
        if (lane == 0) asm volatile("ds_write_b32 %0, %1" :: "v"(ldsa(smem + B_MX) + w * 4), "v"(m) : "memory");
    }
    PIN(wv[2]);
    f32x4 er[2], ei[2];
#pragma unroll
    for (int i = 0; i < 2; ++i) {
        const u32x4 ua = trfrag(smem + B_UT + (2 * w + i) * 1024 + (lane >> 2) * 64 + (lane & 3) * 8, 32);
        er[i] = mfb(ua, mal[0], Z4); ei[i] = mfb(ua, mal[1], Z4);
        er[i] = mfb(ua, mah[0], er[i]); ei[i] = mfb(ua, mah[1], ei[i]);
        PIN(er[i]); PIN(ei[i]);
    }
    LDS_WAIT();
    TS(2);
    PACE();
    int eu, ew;
    {
        const u32x4 m4 = lld<u32x4>(smem + B_MX), m5 = lld<u32x4>(smem + B_MX + 16);
        const unsigned m = __builtin_amdgcn_readfirstlane(pkmax(pkmax(pkmax(m4.x, m4.y), pkmax(m4.z, m4.w)), pkmax(pkmax(m5.x, m5.y), pkmax(m5.z, m5.w))));
        eu = min(max((int)(m >> 23), 4), 254); ew = min((int)((m & 0xffffu) >> 7), 245);
    }
    const float su = __uint_as_float((unsigned)(258 - eu) << 23), sw = __uint_as_float((unsigned)(246 - ew) << 23);
#pragma unroll
    for (int i = 0; i < 2; ++i) lst<u32x4>(smem + B_E1 + c * E1P + q * 256 + (2 * w + i) * 16, pk8(er[i] * su, ei[i] * su));
    if (xtr) {
#pragma unroll
        for (int i = 0; i < 2; ++i) {
            const u32x4 ua = trfrag(smem + B_UT + (2 * w + i) * 1024 + (lane >> 2) * 64 + (lane & 3) * 8, 32);
            const unsigned sg = 0xbf803f80u;
            const f32x4 e = mfb(ua, c == 0 ? (u32x4){sg, sg, 0, 0} : (u32x4){0, 0, sg, sg}, Z4);
            if (c < 2) lst<u32x4>(smem + B_E1 + (16 + c) * E1P + q * 256 + (2 * w + i) * 16, pk8(e * su, Z4));
        }
    }
    wv[0] = wcvt(wv[0], sw); wv[1] = wcvt(wv[1], sw);
    LDS_WAIT();
    TS(3);
    PACE();
    u32x4 dv[2];
#pragma unroll
    for (int i = 0; i < 2; ++i) dv[i] = lld<u32x4>(smem + B_E1 + (2 * w + i) * E1P + lane * 16);
#pragma unroll
    for (int i = 0; i < 2; ++i) dv[i] = pk8(mfh(dv[i], gf[i].re, Z4), mfh(dv[i], gf[i].im, Z4));
#pragma unroll
    for (int i = 0; i < 2; ++i) dv[i] = wmul(mfh(msf.re, dv[i], Z4), mfh(msf.im, dv[i], Z4), wv[i]);
#pragma unroll
    for (int i = 0; i < 2; ++i) PIN(dv[i]);
    PACE();
#pragma unroll
    for (int i = 0; i < 2; ++i) dv[i] = pk8(mfh(dv[i], msi.re, Z4), mfh(dv[i], msi.im, Z4));
    {
        f32x4 r[2], m[2];
#pragma unroll
        for (int i = 0; i < 2; ++i) { r[i] = mfh(dv[i], gi[i].re, Z4); m[i] = mfh(dv[i], gi[i].im, Z4); }
#pragma unroll
        for (int e = 0; e < 4; ++e)
            lst<u32x2>(smem + B_E2 + (4 * q + e) * 1024 + (w >> 1) * 256 + c * 16 + (w & 1) * 8, (u32x2){pkh(r[0][e], r[1][e]), pkh(m[0][e], m[1][e])});
    }
    if (xtr && !(w & 3)) {
        const float dt = -(float)(32 + 64 * c) * (1.0f / 1024.0f);
        const CM f = cmat(dt * (float)(4 * q), dt), b = cmat(cf * (float)(32 + 256 * q) * (1.0f / 1024.0f), cf * (1.0f / 16.0f));
        u32x4 d = lld<u32x4>(smem + B_E1 + (16 + (w >> 2)) * E1P + lane * 16);
        d = pk8(mfh(d, f.re, Z4), mfh(d, f.im, Z4));
        d = wmul(mfh(msf.re, d, Z4), mfh(msf.im, d, Z4), wcvt(wv[2], sw));
        d = pk8(mfh(d, msi.re, Z4), mfh(d, msi.im, Z4));
        const f32x4 r = mfh(d, b.re, Z4);
#pragma unroll
        for (int e = 0; e < 4; ++e) lst<float>(smem + B_EX + ((w >> 2) * 256 + (4 * q + e) * 16 + c) * 4, r[e]);
    }
    u32x4 mia, mib;
    {
        const int jq = spc ? (4 * q) & 7 : 4 * q;
        float cc[4], ss[4];
        const float t0 = __builtin_amdgcn_fractf(cf * (float)(j1 + 4 * jq) * (1.0f / 64.0f));
#pragma unroll
        for (int e = 0; e < 4; ++e) {
            const float f = fmaf(cf, (float)e * (1.0f / 16.0f), t0);
            const float wg = !spc || (xtr && jq + e == 0) ? 0.125f : 0.25f;
            cc[e] = __builtin_amdgcn_cosf(f) * wg; ss[e] = -__builtin_amdgcn_sinf(f) * wg;
        }
        const unsigned c0 = pkh(cc[0], cc[1]), c1 = pkh(cc[2], cc[3]), n0 = pkh(ss[0], ss[1]), n1 = pkh(ss[2], ss[3]);
        mia = (u32x4){c0, n0, c1, n1}; mib = (u32x4){n0, c0 ^ 0x80008000u, n1, c1 ^ 0x80008000u};
        if (spc) { mib = q < 2 ? (u32x4){0, 0, 0, 0} : mia ^ 0x80008000u; if (q >= 2) mia = (u32x4){0, 0, 0, 0}; }
        PIN(mia); PIN(mib);
    }
    LDS_WAIT();
    TS(4);
    PACE();
    {
        f32x4 xa[2], xb[2];
#pragma unroll
        for (int i = 0; i < 2; ++i) { xa[i] = Z4; xb[i] = Z4; }
        if (xtr) {
            const float sg = (c & 1) ? -0.125f : 0.125f;
#pragma unroll
            for (int i = 0; i < 2; ++i) {
                xa[i] = lld<f32x4>(smem + B_EX + (2 * w + i) * 64 + q * 16) * sg;
                xb[i] = lld<f32x4>(smem + B_EX + 1024 + (2 * w + i) * 64 + q * 16) * -sg;
            }
        }
#pragma unroll
        for (int i = 0; i < 2; ++i) {
            const int s2 = 2 * w + i;
            const u32x4 ea = lld<u32x4>(smem + B_E2 + s2 * 1024 + lane * 16);
            const f32x4 ta = mfh(ea, mia, xa[i]), tb = mfh(ea, mib, xb[i]);
            st128<SC1>(to16(pkh(ta[0], ta[1]), pkh(ta[2], ta[3]), pkh(tb[0], tb[1]), pkh(tb[2], tb[3])), rs,
                       c * 16384 + (q & 1) * 32 + (q >> 1) * 16 + OFF_T + s2 * 262144 + j1 * 4096 + mp * 64);
        }
    }
    TS(5);
    PACE();
    const u32x2 gg = __builtin_amdgcn_raw_buffer_load_b64(rs, c * 8, OFF_G + s2t * 1024 + w * 128, 0);
    u32x2 xv[4], dk[4];
#pragma unroll
    for (int m = 0; m < 4; ++m) {
        xv[m] = __builtin_amdgcn_raw_buffer_load_b64(rs, c * 32 + q * 8, OFF_X + (s2t * 16 + dat) * 16384 + m * 4096 + w * 512, 0);
        dk[m] = *reinterpret_cast<const u32x2*>(Dskip + 256 * m + 16 * dat + 4 * q);
    }
    SCHED();
    u32x4 fc[2], fs[2];
#pragma unroll
    for (int h = 0; h < 2; ++h) {
        float dt = (float)(16 * w + c) * (1.0f / 128.0f); PIN(dt);
        float cc[8], ss[8];
        const float t0 = __builtin_amdgcn_fractf(dt * (float)(32 * h + 4 * q)), t1 = __builtin_amdgcn_fractf(dt * (float)(32 * h + 16 + 4 * q));
#pragma unroll
        for (int e = 0; e < 8; ++e) { const float f = fmaf((float)(e & 3), dt, e < 4 ? t0 : t1); cc[e] = __builtin_amdgcn_cosf(f); ss[e] = __builtin_amdgcn_sinf(f); }
        fc[h] = (u32x4){pkh(cc[0], cc[1]), pkh(cc[2], cc[3]), pkh(cc[4], cc[5]), pkh(cc[6], cc[7])};
        fs[h] = (u32x4){pkh(ss[0], ss[1]), pkh(ss[2], ss[3]), pkh(ss[4], ss[5]), pkh(ss[6], ss[7])};
        PIN(fc[h]); PIN(fs[h]);
        PACE();
    }
    PACE(); PACE();
    f32x4 r0[4];
#pragma unroll
    for (int m = 0; m < 4; ++m) {
        PIN(xv[m]); PIN(dk[m]);
        const unsigned p0 = pkb(lo2f(xv[m].x) * lo2f(dk[m].x), hi2f(xv[m].x) * hi2f(dk[m].x));
        const unsigned p1 = pkb(lo2f(xv[m].y) * lo2f(dk[m].y), hi2f(xv[m].y) * hi2f(dk[m].y));
        r0[m] = (f32x4){lo2f(p0), hi2f(p0), lo2f(p1), hi2f(p1)} * __uint_as_float(gg.y);
        PIN(r0[m]);
    }
    TS(6);
    {
        u32x4 tv[2];
#pragma unroll 1
        for (int spin = 0; spin < (1 << 18); ++spin) {
            unsigned toff = lane * 16; PIN(toff);
#pragma unroll
            for (int i = 0; i < 2; ++i) tv[i] = __builtin_amdgcn_raw_buffer_load_b128(rs, toff, OFF_T + (s2t * 16 + dat) * 16384 + w * 2048 + i * 1024, SC1);
            SCHED();
            PACE(); PACE(); PACE(); PACE();
            SCHED();
            bool ok = true;
#pragma unroll
            for (int i = 0; i < 2; ++i) ok = ok & (tv[i].x != ~0u) & (tv[i].y != ~0u) & (tv[i].z != ~0u) & (tv[i].w != ~0u);
            if (__all(ok)) break;
            asm volatile("" ::: "memory");
        }
#pragma unroll
        for (int i = 0; i < 2; ++i)
            lst<u32x4>(smem + B_UT + (w >> 1) * 4096 + ((lane >> 1) & 1) * 2048 + (32 * (w & 1) + 16 * i + (lane >> 2)) * 32 + (lane & 1) * 16, tv[i]);
        asm volatile("" ::: "memory");
        const unsigned cnt = ldsa(smem + B_CNT);
        if (lane == 0) lds_add(cnt);
        TS(7);
#pragma unroll 1
        for (int spin = 0; spin < (1 << 22); ++spin) { PACE(); if (lds_peek(cnt) >= 8u) break; }
    }
    TS(8);
    f32x4 P0 = Z4, P1 = Z4, P2 = Z4, P3a = Z4, P3b = Z4;
    lptr tt = smem + B_UT + lane * 8;
#pragma unroll
    for (int h = 0; h < 2; ++h) {
        const u32x4 a1 = trfrag(tt + 1 * 4096 + h * 1024, 512), b1 = trfrag(tt + 1 * 4096 + 2048 + h * 1024, 512);
        const u32x4 a3 = trfrag(tt + 3 * 4096 + h * 1024, 512), b3 = trfrag(tt + 3 * 4096 + 2048 + h * 1024, 512);
        const u32x4 a0 = trfrag(tt + h * 1024, 512), b0 = trfrag(tt + 2048 + h * 1024, 512);
        const u32x4 a2 = trfrag(tt + 2 * 4096 + h * 1024, 512), b2 = trfrag(tt + 2 * 4096 + 2048 + h * 1024, 512);
        u32x4 cw = fc[h], sw_ = fs[h];
        P1 = mfh(a1, cw, P1); P3a = mfh(a1, sw_, P3a); P3b = mfh(b1, cw, P3b); P1 = mfh(b1, sw_, P1);
        if (h == 0) cw.x = q == 0 ? (cw.x & 0xffff0000u) | ((c & 1) ? 0xbc00u : 0x3c00u) : cw.x;
        P3a = mfh(b3, cw, P3a); P3b = mfh(a3, sw_, P3b); P1 = mfh(a3, cw, P1); P1 = mfh(b3, sw_, P1);
        if (h == 0) { cw.x = q == 0 ? (cw.x & 0xffff0000u) | 0x3800u : cw.x; sw_.x = q == 0 ? (sw_.x & 0xffff0000u) | ((c & 1) ? 0x3800u : 0xb800u) : sw_.x; }
        P0 = mfh(a0, cw, P0); P2 = mfh(a2, cw, P2); P0 = mfh(b0, sw_, P0); P2 = mfh(b2, sw_, P2);
        PIN(P0); PIN(P1); PIN(P2); PIN(P3a); PIN(P3b);
        PACE();
    }
    const int fe = eu + ew - 267;
    {
        const float g = __uint_as_float(gg.x);
        const f32x4 sp = P0 + P2, sm = P0 - P2, p3 = P3a - P3b;
        const f32x4 av[4] = {sp + P1, sm - p3, sp - P1, sm + p3};
#pragma unroll
        for (int m = 0; m < 4; ++m) {
            const unsigned b0 = pkb(ldexpf(av[m][0], fe), ldexpf(av[m][1], fe)), b1 = pkb(ldexpf(av[m][2], fe), ldexpf(av[m][3], fe));
            const unsigned o0 = pkb(g * lo2f(b0), g * hi2f(b0)), o1 = pkb(g * lo2f(b1), g * hi2f(b1));
            lst<f32x4>(smem + B_R0 + (w * 4 + m) * 1024 + lane * 16, r0[m] + (f32x4){lo2f(o0), hi2f(o0), lo2f(o1), hi2f(o1)});
            if (m == 1) PACE();
        }
    }
    LDS_WAIT();
    TS(9);
    TSE(3, w == 0);
    while (nb < 32) { BAR(); ++nb; }
    BAR();
}

NOPK __global__ __launch_bounds__(1024, 1) void kb_kernel(const u16* __restrict__ Cre, const u16* __restrict__ Cim,
                                                          const u16* __restrict__ Dskip, char* ws, float* __restrict__ out) {
    extern __shared__ __attribute__((aligned(16))) char smem_[];
    lptr smem = (lptr)smem_;
    const int wid = __builtin_amdgcn_readfirstlane(threadIdx.x >> 6), lane = threadIdx.x & 63;
    if (wid < 4) kb_mma(ws, out, smem, blockIdx.x, wid, lane);
    else if (wid < 8) kb_load(Cre, Cim, ws, smem, blockIdx.x, wid - 4, lane);
    else kb_aux(Dskip, ws, smem, blockIdx.x, wid - 8, lane);
}

extern "C" void kernel_launch(void* const* d_in, const int* in_sizes, int n_in, void* d_out, int out_size, void* d_ws, size_t ws_size,
                              hipStream_t stream) {
    const u16* x = (const u16*)d_in[0];
    const u16* w_r = (const u16*)d_in[1];
    const u16* w_i = (const u16*)d_in[2];
    const u16* lam_re = (const u16*)d_in[4];
    const u16* lam_im = (const u16*)d_in[5];
    const u16* Bre = (const u16*)d_in[6];
    const u16* Bim = (const u16*)d_in[7];
    const u16* Cre = (const u16*)d_in[8];
    const u16* Cim = (const u16*)d_in[9];
    const u16* log_dt = (const u16*)d_in[10];
    const u16* Dskip = (const u16*)d_in[11];
    const u16* gate_w = (const u16*)d_in[12];
    char* ws = (char*)d_ws;
    (void)hipFuncSetAttribute((const void*)ka_kernel, hipFuncAttributeMaxDynamicSharedMemorySize, KA_SMEM);
    (void)hipFuncSetAttribute((const void*)kb_kernel, hipFuncAttributeMaxDynamicSharedMemorySize, KB_SMEM);
    ka_kernel<<<256, 768, KA_SMEM, stream>>>(x, w_r, w_i, Bre, Bim, Cre, Cim, lam_re, lam_im, log_dt, gate_w, ws);
    kb_kernel<<<256, 1024, KB_SMEM, stream>>>(Cre, Cim, Dskip, ws, (float*)d_out);
}
```

```cpp
#include <hip/hip_runtime.h>
#include <stdint.h>

typedef __attribute__((ext_vector_type(8))) __bf16 bf16x8;
typedef __attribute__((ext_vector_type(2))) __bf16 bf16x2;
typedef __attribute__((ext_vector_type(8))) _Float16 f16x8;
typedef __attribute__((ext_vector_type(2))) _Float16 f16x2;
typedef __attribute__((ext_vector_type(4))) float f32x4;
typedef __attribute__((ext_vector_type(2))) float f32x2;
typedef __attribute__((ext_vector_type(4))) unsigned u32x4;
typedef __attribute__((ext_vector_type(2))) unsigned u32x2;
typedef __attribute__((ext_vector_type(4))) short s16x4;
typedef __attribute__((ext_vector_type(2))) unsigned short u16x2;
typedef unsigned short u16;
typedef unsigned long long u64;
#define GAS __attribute__((address_space(1)))
#define LAS __attribute__((address_space(3)))
#define NOPK __attribute__((target("no-packed-fp32-ops")))
#define RLX_AGENT __ATOMIC_RELAXED, __HIP_MEMORY_SCOPE_AGENT
#define DEV __device__ __forceinline__

constexpr int S_ = 2048, D_ = 1024, KF = 513;

constexpr unsigned OFF_H = 0;
constexpr unsigned OFF_U = OFF_H + (8u << 20);
constexpr unsigned OFF_T = OFF_U + (4u << 20);
constexpr unsigned OFF_W = OFF_T + (4u << 20);
constexpr unsigned OFF_G = OFF_W + 65u * 65536u;
constexpr unsigned OFF_MX = OFF_G + 16384u;
constexpr unsigned OFF_GRAN = OFF_MX + 4096u;
constexpr unsigned OFF_E = OFF_GRAN + 16u * 1024u * 16u;
constexpr unsigned OFF_X = OFF_E + 16u * 128u;
constexpr unsigned OFF_C = OFF_X + (4u << 20);
constexpr unsigned WS_END = OFF_C + (4u << 20);

#define TSD()
#define TS(i)
#define TC(i)
#define TSE(slot, on)

DEV float bf2f(u16 h) { return __uint_as_float((unsigned)h << 16); }
DEV unsigned pkb(float a, float b) { f32x2 v = {a, b}; return __builtin_bit_cast(unsigned, __builtin_convertvector(v, bf16x2)); }
DEV unsigned pkh(float a, float b) { f32x2 v = {a, b}; return __builtin_bit_cast(unsigned, __builtin_convertvector(v, f16x2)); }
DEV float lo2f(unsigned p) { return __uint_as_float(p << 16); }
DEV float hi2f(unsigned p) { return __uint_as_float(p & 0xffff0000u); }
DEV float bfr(float f) { return lo2f(pkb(f, f)); }
DEV float2 cmul(float2 a, float2 b) { return make_float2(a.x * b.x - a.y * b.y, a.x * b.y + a.y * b.x); }
DEV float2 cfma(float2 a, float2 b, float2 c) { return make_float2(fmaf(a.x, b.x, fmaf(-a.y, b.y, c.x)), fmaf(a.x, b.y, fmaf(a.y, b.x, c.y))); }
DEV f32x4 mfb(u32x4 a, u32x4 b, f32x4 c) { return __builtin_amdgcn_mfma_f32_16x16x32_bf16(__builtin_bit_cast(bf16x8, a), __builtin_bit_cast(bf16x8, b), c, 0, 0, 0); }
DEV f32x4 mfh(u32x4 a, u32x4 b, f32x4 c) { return __builtin_amdgcn_mfma_f32_16x16x32_f16(__builtin_bit_cast(f16x8, a), __builtin_bit_cast(f16x8, b), c, 0, 0, 0); }
DEV unsigned pkmax(unsigned a, unsigned b) { return __builtin_bit_cast(unsigned, __builtin_elementwise_max(__builtin_bit_cast(u16x2, a), __builtin_bit_cast(u16x2, b))); }
DEV float vcos(float r) { return __builtin_amdgcn_cosf(__builtin_amdgcn_fractf(r)); }
DEV float vsin(float r) { return __builtin_amdgcn_sinf(__builtin_amdgcn_fractf(r)); }
typedef LAS char* lptr;
DEV unsigned ldsa(lptr p) { return (unsigned)(uintptr_t)p; }
template <class T> DEV T lld(lptr p) { return *(LAS const T*)p; }
template <class T> DEV void lst(lptr p, T v) { *(LAS T*)p = v; }
DEV u32x2 trread(lptr p) { return __builtin_bit_cast(u32x2, __builtin_amdgcn_ds_read_tr16_b64_v4i16((LAS s16x4*)p)); }
DEV u32x4 trfrag(lptr p, int second) { const u32x2 a = trread(p), b = trread(p + second); return (u32x4){a.x, a.y, b.x, b.y}; }
DEV void glds16(const void* g, lptr l) { __builtin_amdgcn_global_load_lds((const GAS unsigned*)g, (LAS unsigned*)l, 16, 0, 0); }
typedef __amdgpu_buffer_rsrc_t rsrc_t;
template <int AUX = 0> DEV void blds16(rsrc_t rs, lptr l, unsigned vo, unsigned so) { __builtin_amdgcn_raw_ptr_buffer_load_lds(rs, (LAS void*)l, 16, vo, so, 0, AUX); }
DEV u32x4 ldfrag(lptr tile, int row, int chunk) { return lld<u32x4>(tile + row * 128 + ((chunk ^ (row & 7)) << 4)); }
template <int N> DEV void wait_vm() { asm volatile("s_waitcnt vmcnt(%0)" :: "i"(N) : "memory"); }
#define LDS_WAIT() asm volatile("s_waitcnt lgkmcnt(0)" ::: "memory")
#define SCHED() __builtin_amdgcn_sched_barrier(0)
#define BAR() do { __builtin_amdgcn_s_barrier(); asm volatile("" ::: "memory"); } while (0)
#define PIN(x) asm volatile("" : "+v"(x))
DEV rsrc_t mkrsrc(const void* p, unsigned n) { return __builtin_amdgcn_make_buffer_rsrc((void*)p, 0, n, 0x00020000); }
constexpr int SC1 = 16;
#define WT 16
#define WTO 16
template <int AUX> DEV void st128(u32x4 v, rsrc_t rs, unsigned off) { __builtin_amdgcn_raw_buffer_store_b128(v, rs, off, 0, AUX); }
DEV void lds_add(unsigned a) { asm volatile("ds_add_u32 %0, %1" :: "v"(a), "v"(1u) : "memory"); }
DEV unsigned lds_peek(unsigned a) {
    unsigned v;
    asm volatile("ds_read_b32 %0, %1\n\ts_waitcnt lgkmcnt(0)" : "=v"(v) : "v"(a) : "memory");
    return __builtin_amdgcn_readfirstlane(v);
}
DEV void tile_of(int gb, int& ta, int& tb) {
    const int xcd = gb & 7, loc = gb >> 3;
    ta = (xcd >> 1) * 4 + (loc >> 3);
    tb = (xcd & 1) * 8 + (loc & 7);
}
DEV void rows4(float a, float& a0, float& a1, float& a2, float& a3) {
    const unsigned u = __float_as_uint(a);
    const auto r = __builtin_amdgcn_permlane32_swap(u, u, false, false);
    const auto p = __builtin_amdgcn_permlane16_swap(r[0], r[0], false, false);
    const auto s = __builtin_amdgcn_permlane16_swap(r[1], r[1], false, false);
    a0 = __uint_as_float(p[0]); a1 = __uint_as_float(p[1]); a2 = __uint_as_float(s[0]); a3 = __uint_as_float(s[1]);
}
DEV float rowsum4(float a) {
    const unsigned u = __float_as_uint(a);
    const auto r = __builtin_amdgcn_permlane32_swap(u, u, false, false);
    const unsigned t = __float_as_uint(__uint_as_float(r[0]) + __uint_as_float(r[1]));
    const auto p = __builtin_amdgcn_permlane16_swap(t, t, false, false);
    return __uint_as_float(p[0]) + __uint_as_float(p[1]);
}
DEV float2 sel4(int q, float2 a, float2 b, float2 c, float2 d) {
    PIN(d.x); PIN(d.y);
    const float2 e = (q & 1) ? b : a, f = (q & 1) ? d : c;
    return (q & 2) ? f : e;
}
template <int R> DEV unsigned ror16(unsigned v) { return __builtin_amdgcn_update_dpp(0u, v, 0x120 + R, 0xf, 0xf, false); }
DEV unsigned wave_pkmax(unsigned m) {
    m = pkmax(m, ror16<8>(m)); m = pkmax(m, ror16<4>(m)); m = pkmax(m, ror16<2>(m)); m = pkmax(m, ror16<1>(m));
    const auto r = __builtin_amdgcn_permlane32_swap(m, m, false, false);
    m = pkmax(r[0], r[1]);
    const auto p = __builtin_amdgcn_permlane16_swap(m, m, false, false);
    return pkmax(p[0], p[1]);
}
DEV float half_sum(float v) {
    v += __uint_as_float(ror16<8>(__float_as_uint(v))); v += __uint_as_float(ror16<4>(__float_as_uint(v)));
    v += __uint_as_float(ror16<2>(__float_as_uint(v))); v += __uint_as_float(ror16<1>(__float_as_uint(v)));
    const auto p = __builtin_amdgcn_permlane16_swap(__float_as_uint(v), __float_as_uint(v), false, false);
    return __uint_as_float(p[0]) + __uint_as_float(p[1]);
}
DEV u32x4 to16(unsigned p0, unsigned p1, unsigned n0, unsigned n1) {
    const auto a = __builtin_amdgcn_permlane16_swap(p0, n0, false, false);
    const auto b = __builtin_amdgcn_permlane16_swap(p1, n1, false, false);
    return (u32x4){a[0], b[0], a[1], b[1]};
}

#define PRIOA 3
#define FLO 1
#define WSTEP 2
#define WSPL 1
#define XSTEP 4
#define PRIOT 3
#define RDPAT {0, 1, 2, -1, 3, 4, -1, 5, 6, -1, 7, -1, -1, -1, -1, -1}
#define Z4 ((f32x4){0.f, 0.f, 0.f, 0.f})
struct CM { u32x4 re, im; };
DEV CM cmat(float t0, float dt) {
    float cc[4], ss[4];
    t0 = __builtin_amdgcn_fractf(t0);
#pragma unroll
    for (int e = 0; e < 4; ++e) { const float f = fmaf((float)e, dt, t0); cc[e] = __builtin_amdgcn_cosf(f); ss[e] = __builtin_amdgcn_sinf(f); }
    const unsigned c0 = pkh(cc[0], cc[1]), c1 = pkh(cc[2], cc[3]), s0 = pkh(ss[0], ss[1]), s1 = pkh(ss[2], ss[3]);
    CM m; m.re = (u32x4){c0, c1, s0 ^ 0x80008000u, s1 ^ 0x80008000u}; m.im = (u32x4){s0, s1, c0, c1};
    PIN(m.re); PIN(m.im);
    return m;
}
DEV u32x4 pk8(f32x4 r, f32x4 i) { return (u32x4){pkh(r[0], r[1]), pkh(r[2], r[3]), pkh(i[0], i[1]), pkh(i[2], i[3])}; }
DEV u32x4 wmul(f32x4 r, f32x4 i, u32x4 w) {
    float yr[4], yi[4];
#pragma unroll
    for (int e = 0; e < 4; ++e) {
        const unsigned wu = w[e];
        const f16x2 h = __builtin_bit_cast(f16x2, wu);
        yr[e] = fmaf(r[e], (float)h[0], -i[e] * (float)h[1]); yi[e] = fmaf(r[e], (float)h[1], i[e] * (float)h[0]);
    }
    return (u32x4){pkh(yr[0], yr[1]), pkh(yr[2], yr[3]), pkh(yi[0], yi[1]), pkh(yi[2], yi[3])};
}
DEV u32x4 wcvt(u32x4 w, float sw) {
#pragma unroll
    for (int e = 0; e < 4; ++e) w[e] = pkh(lo2f(w[e]) * sw, hi2f(w[e]) * sw);
    PIN(w);
    return w;
}
DEV void aux_sync(unsigned cnt, unsigned target, int lane) {
    asm volatile("" ::: "memory");
    if (lane == 0) lds_add(cnt);
#pragma unroll 1
    for (int spin = 0; spin < (1 << 24); ++spin) { if (lds_peek(cnt) >= target) break; }
}

constexpr int A_STAGE = 32768, A_XT = 4 * A_STAGE, A_PRM = A_XT + 16384, A_AGG = A_PRM + 2048, A_FLG = A_AGG + 512, A_GW = A_FLG + 64,
              A_MAT = A_GW + 2048, KA_SMEM = A_MAT + 10240;
constexpr int A_UT = 16384, A_E1 = 2 * A_STAGE, E1P = 1040, A_EX = A_E1 + 18 * E1P + 32, A_E2 = A_XT;

DEV void ka_mma(char* ws, lptr smem, int gb, int wid, int lane) {
    int lt, pt; tile_of(gb, lt, pt);
    const int wr = wid >> 1, wc = wid & 1, c = lane & 15, q = lane >> 4;
    const rsrc_t rs = mkrsrc(ws, WS_END);
    TSD(); TS(0);
    u64 tag = __hip_atomic_load((GAS u64*)(ws + OFF_E), __ATOMIC_RELAXED, __HIP_MEMORY_SCOPE_WORKGROUP) + 0x9E3779B97F4A7C15ull;
    if (wid == 0 && lane < 8) lst<unsigned>(smem + A_FLG + lane * 4, 0u);
    f32x4 acc[4][4];
#pragma unroll
    for (int m = 0; m < 4; ++m)
#pragma unroll
        for (int n = 0; n < 4; ++n) acc[m][n] = Z4;
    u32x4 af[2][4], bf_[2][4];
#pragma unroll
    for (int m = 0; m < 4; ++m) { af[1][m] = (u32x4){0, 0, 0, 0}; bf_[1][m] = af[1][m]; }
    PIN(tag);
    if (wid == 0 && lane == 0) lst<u64>(smem + A_FLG + 32, tag);
    tag = (u64)(unsigned)__builtin_amdgcn_readfirstlane((unsigned)tag) | (u64)(unsigned)__builtin_amdgcn_readfirstlane((unsigned)(tag >> 32)) << 32;
    LDS_WAIT();
#pragma unroll 4
    for (int kt = 0; kt < 16; ++kt) {
        BAR();
        lptr tA = smem + (kt & 3) * A_STAGE;
        lptr tB = tA + 16384;
#pragma unroll
        for (int hf = 0; hf < 2; ++hf)
#pragma unroll
            for (int i = 0; i < 16; ++i) {
                acc[i >> 2][i & 3] = mfb(af[1 - hf][i >> 2], bf_[1 - hf][i & 3], acc[i >> 2][i & 3]);
                constexpr int RD[16] = RDPAT;
                const int r = RD[i];
                if (r == 0) af[hf][0] = ldfrag(tA, wr * 64 + c, 4 * hf + q);
                else if (r > 0 && r < 5) bf_[hf][r - 1] = ldfrag(tB, wc * 64 + (r - 1) * 16 + c, 4 * hf + q);
                else if (r >= 5) af[hf][r - 4] = ldfrag(tA, wr * 64 + (r - 4) * 16 + c, 4 * hf + q);
                SCHED();
            }
        LDS_WAIT();
    }
    __builtin_amdgcn_s_setprio(PRIOT);
    float2 lam[2], coef[2], l16[2], l128[2];
#pragma unroll
    for (int n = 0; n < 2; ++n) {
        const f32x4 a = lld<f32x4>(smem + A_PRM + (wc * 32 + 2 * c + n) * 32), b = lld<f32x4>(smem + A_PRM + (wc * 32 + 2 * c + n) * 32 + 16);
        lam[n] = make_float2(a[0], a[1]); coef[n] = make_float2(a[2], a[3]); l16[n] = make_float2(b[0], b[1]); l128[n] = make_float2(b[2], b[3]);
    }
#pragma unroll
    for (int m = 0; m < 4; ++m)
#pragma unroll
        for (int n = 0; n < 4; ++n) acc[m][n] = mfb(af[1][m], bf_[1][n], acc[m][n]);
    float2 l32[2], l64[2];
#pragma unroll
    for (int n = 0; n < 2; ++n) { l32[n] = cmul(l16[n], l16[n]); l64[n] = cmul(l32[n], l32[n]); }
    TS(1);
    float2 h[2][16];
#pragma unroll
    for (int n = 0; n < 2; ++n) h[n][0] = make_float2(acc[0][n][0], acc[0][2 + n][0]);
#pragma unroll
    for (int t = 1; t < 16; ++t)
#pragma unroll
        for (int n = 0; n < 2; ++n) h[n][t] = cfma(lam[n], h[n][t - 1], make_float2(acc[t >> 2][n][t & 3], acc[t >> 2][2 + n][t & 3]));
    float2 loc[2], wag[2], wq[2];
#pragma unroll
    for (int n = 0; n < 2; ++n) {
        float2 a0, a1, a2, a3;
        rows4(h[n][15].x, a0.x, a1.x, a2.x, a3.x);
        rows4(h[n][15].y, a0.y, a1.y, a2.y, a3.y);
        const float2 s2 = cfma(l16[n], a0, a1), s3 = cfma(l16[n], s2, a2);
        wag[n] = cfma(l16[n], s3, a3);
        loc[n] = sel4(q, make_float2(0.f, 0.f), a0, s2, s3);
    }
    lptr aggp = smem + A_AGG + (wc * 16 + c) * 16;
    const unsigned flg = ldsa(smem + A_FLG) + wc * 4;
    float2 W0[2] = {make_float2(0.f, 0.f), make_float2(0.f, 0.f)};
    if (wr == 0) {
        if (q == 0) lst<f32x4>(aggp, (f32x4){wag[0].x, wag[0].y, wag[1].x, wag[1].y});
        asm volatile("" ::: "memory");
        if (lane == 0) lds_add(flg);
    } else {
#pragma unroll 1
        for (int spin = 0; spin < (1 << 20); ++spin) { if (lds_peek(flg)) break; }
        const f32x4 w0 = lld<f32x4>(aggp);
        W0[0] = make_float2(w0[0], w0[1]); W0[1] = make_float2(w0[2], w0[3]);
        const float2 g0 = cfma(l64[0], W0[0], wag[0]), g1 = cfma(l64[1], W0[1], wag[1]);
        if (lt < 15 && q < 2) {
            const float2 gg = q == 0 ? g0 : g1;
            st128<SC1>((u32x4){__float_as_uint(gg.x), (unsigned)tag, __float_as_uint(gg.y), (unsigned)(tag >> 32)},
                       rs, (2 * c + q) * 16 + OFF_GRAN + (lt * 1024 + pt * 64 + wc * 32) * 16);
        }
    }
    TS(2);
#pragma unroll
    for (int n = 0; n < 2; ++n) { PIN(lam[n].x); PIN(lam[n].y); PIN(coef[n].x); PIN(coef[n].y); PIN(l16[n].x); PIN(l16[n].y); PIN(l128[n].x); PIN(l128[n].y); }
    float2 pw[2][8], l8[2], lq[2], l512[2];
#pragma unroll
    for (int n = 0; n < 2; ++n) {
        wq[n] = sel4(q, make_float2(1.f, 0.f), l16[n], l32[n], cmul(l32[n], l16[n]));
        loc[n] = cfma(wq[n], W0[n], loc[n]);
        if (wr) wq[n] = cmul(wq[n], l64[n]);
        pw[n][0] = cmul(coef[n], lam[n]);
        l8[n] = cmul(lam[n], lam[n]); l8[n] = cmul(l8[n], l8[n]); l8[n] = cmul(l8[n], l8[n]);
        const float2 l256 = cmul(l128[n], l128[n]);
        lq[n] = sel4(q, make_float2(1.f, 0.f), l128[n], l256, cmul(l256, l128[n]));
        l512[n] = cmul(l256, l256);
    }
#pragma unroll
    for (int t = 1; t < 8; ++t)
#pragma unroll
        for (int n = 0; n < 2; ++n) pw[n][t] = cmul(pw[n][t - 1], lam[n]);
#pragma unroll
    for (int t = 0; t < 16; ++t)
#pragma unroll
        for (int n = 0; n < 2; ++n) h[n][t] = cmul(coef[n], h[n][t]);
#pragma unroll
    for (int t = 0; t < 16; ++t) { PIN(h[0][t].x); PIN(h[0][t].y); PIN(h[1][t].x); PIN(h[1][t].y); }
#pragma unroll
    for (int t = 0; t < 8; ++t) { PIN(pw[0][t].x); PIN(pw[0][t].y); PIN(pw[1][t].x); PIN(pw[1][t].y); }
    TS(3);
    if (lt > 0) {
        u32x4 gr[4][2];
#pragma unroll 1
        for (int spin = 0; spin < (1 << 20); ++spin) {
            bool ok = true;
#pragma unroll
            for (int j = 0; j < 4; ++j) {
                const int cc = lt - 1 - (4 * j + q);
                unsigned off = cc >= 0 ? (unsigned)(OFF_GRAN + (cc * 1024 + pt * 64 + wc * 32 + 2 * c) * 16) : 0x80000000u;
                PIN(off);
                gr[j][0] = __builtin_amdgcn_raw_buffer_load_b128(rs, off, 0, SC1);
                gr[j][1] = __builtin_amdgcn_raw_buffer_load_b128(rs, off + 16, 0, SC1);
            }
#pragma unroll
            for (int j = 0; j < 4; ++j) {
                const bool v = lt - 1 - (4 * j + q) >= 0;
#pragma unroll
                for (int n = 0; n < 2; ++n) ok = ok & (!v | ((gr[j][n].y == (unsigned)tag) & (gr[j][n].w == (unsigned)(tag >> 32))));
            }
            if (__all(ok)) break;
            asm volatile("" ::: "memory");
        }
#pragma unroll
        for (int n = 0; n < 2; ++n) {
            float2 cs = make_float2(__uint_as_float(gr[3][n].x), __uint_as_float(gr[3][n].z));
#pragma unroll
            for (int j = 2; j >= 0; --j) cs = cfma(l512[n], cs, make_float2(__uint_as_float(gr[j][n].x), __uint_as_float(gr[j][n].z)));
            cs = cmul(cs, lq[n]);
            cs.x = rowsum4(cs.x); cs.y = rowsum4(cs.y);
            loc[n] = cfma(wq[n], cs, loc[n]);
        }
    }
    TS(4);
    lptr tb = smem + A_STAGE + wid * 8192;
    PIN(lane);
#pragma unroll
    for (int hf = 0; hf < 2; ++hf) {
        const float2 s0 = hf ? cmul(l8[0], loc[0]) : loc[0], s1 = hf ? cmul(l8[1], loc[1]) : loc[1];
#pragma unroll
        for (int t = 0; t < 8; ++t) {
            const float2 v0 = cfma(pw[0][t], s0, h[0][8 * hf + t]), v1 = cfma(pw[1][t], s1, h[1][8 * hf + t]);
            lst<unsigned>(tb + (8 * hf + t) * 256 + lane * 4, pkb(v0.x, v1.x));
            lst<unsigned>(tb + 4096 + (8 * hf + t) * 256 + lane * 4, pkb(-v0.y, -v1.y));
        }
        asm volatile("" ::: "memory");
        lptr rb = tb + (lane >> 4) * 256 + (lane & 3) * 64 + ((lane >> 2) & 3) * 16;
        const unsigned hoff = OFF_H + ((lane >> 4) * 64 + pt * 2 + wc) * 8192 + lt * 512 + wr * 256 + (lane & 15) * 16;
#pragma unroll
        for (int v = 2 * hf; v < 2 * hf + 2; ++v)
#pragma unroll
            for (int pl = 0; pl < 2; ++pl) st128<WT>(lld<u32x4>(rb + pl * 4096 + v * 1024), rs, hoff + (v * 256 + pl * 32) * 8192);
    }
    TS(5);
    TSE(0, wid == 0);
}

struct __attribute__((packed, aligned(2))) U64U { u32x2 v; };
DEV void w_load(const u16* __restrict__ w, int k1, int qw, int lw, int lane, u32x2 (&R)[4]) {
    const int kd0 = 4 * (lane & 15) + 64 * (4 * lw + (lane >> 4));
    const int col = lw < 2 ? kd0 : 1021 - kd0;
#pragma unroll
    for (int r = 0; r < 4; ++r) {
        const int ks = k1 + 128 * (4 * qw + r), row = lw < 2 ? ks : (S_ - ks) & (S_ - 1);
        R[r] = reinterpret_cast<const U64U*>(w + row * KF + col)->v;
    }
}
DEV unsigned w_part(const u16* __restrict__ w_r, const u16* __restrict__ w_i, int k1, int qw, int lw, int lane) {
    const int ks = k1 + 128 * (4 * qw + (lane & 3)), row = lw < 2 ? (S_ - ks) & (S_ - 1) : ks;
    return ((lane & 4) ? w_i : w_r)[row * KF + (lw < 2 ? 0 : 512)];
}
DEV void w_row(int r, int lw, int lane, u32x2 R, u32x2 I, unsigned sp, u32x4 (&o)[4], unsigned& mx) {
    const unsigned cj = lw < 2 ? 0u : 0x80008000u;
    const unsigned i0 = I.x ^ cj, i1 = I.y ^ cj;
    unsigned o0 = __builtin_amdgcn_perm(i0, R.x, 0x05040100u), o3 = __builtin_amdgcn_perm(i1, R.y, 0x07060302u);
    o[1][r] = __builtin_amdgcn_perm(i0, R.x, 0x07060302u); o[2][r] = __builtin_amdgcn_perm(i1, R.y, 0x05040100u);
    const float pr = lo2f(__builtin_amdgcn_readlane(sp, r)), pi = lo2f(__builtin_amdgcn_readlane(sp, 4 + r) ^ (lw < 2 ? 0x8000u : 0u));
    const unsigned t = lw < 2 ? o0 : o3;
    unsigned v = pkb(0.5f * (lo2f(t) + pr), 0.5f * (hi2f(t) + pi));
    PIN(v);
    o[0][r] = (lane == 0 && lw == 0) ? v : o0; o[3][r] = (lane == 0 && lw == 2) ? v : o3;
    mx = pkmax(mx, pkmax(pkmax(R.x & 0x7fff7fffu, R.y & 0x7fff7fffu), pkmax(I.x & 0x7fff7fffu, I.y & 0x7fff7fffu)));
}
DEV void w_store(rsrc_t rs, int k1, int qw, int lw, int lane, u32x4 (&o)[4], int e0, int e1) {
#pragma unroll
    for (int e = e0; e < e1; ++e)
        st128<SC1>(o[e], rs, (lane & 15) * 1024 + (lane >> 4) * 16 + OFF_W + (k1 * 4 + (lw < 2 ? e : 3 - e)) * 16384 + qw * 256 + lw * 64);
}

#define TSARG
#define TSPASS
DEV void ka_load(const u16* __restrict__ x, const u16* __restrict__ Bre, const u16* __restrict__ Bim, lptr smem, int gb, int lw, int lane TSARG) {
    int lt, pt; tile_of(gb, lt, pt);
    const int srow = lane >> 3, schunk = (lane & 7) ^ srow;
    const int rot = (lt + pt) & 15;
    const rsrc_t rx = mkrsrc(x, S_ * D_ * 2), rb = mkrsrc((lw & 1) ? Bim : Bre, D_ * D_ * 2);
    const unsigned vA = (16 * (srow >> 2) + (srow & 3)) * 2048 + schunk * 16, sA = (lt * 128 + 64 * (lw >> 1) + 8 * (lw & 1)) * 2048;
    const unsigned vB = 2 * srow * 2048 + schunk * 16, sB = (pt * 64 + 32 * (lw >> 1)) * 2048;
    auto stage = [&](int kk) {
        const int ko = ((kk + rot) & 15) * 128;
        lptr l = smem + (kk & 3) * A_STAGE + lw * 4096;
#pragma unroll
        for (int i = 0; i < 4; ++i) blds16(rx, l + i * 1024, vA, sA + (32 * (i & 1) + 4 * (i >> 1)) * 2048 + ko);
#pragma unroll
        for (int i = 0; i < 4; ++i) blds16(rb, l + 16384 + i * 1024, vB, sB + (16 * (i & 1) + (i >> 1)) * 2048 + ko);
    };
    TS(0);
    stage(0);
#pragma unroll
    for (int i = 0; i < 4; ++i) blds16(rx, smem + A_XT + lw * 4096 + i * 1024, (lane >> 1) * 32768 + (lane & 1) * 16, (i * 512 + lt) * 2048 + lw * 512 + pt * 32);
    stage(1); stage(2);
    TS(6);
#pragma unroll 1
    for (int kt = 0; kt < 13; ++kt) { wait_vm<16>(); TS(8 + kt); BAR(); TS(28 + kt); if (kt == 0) TC(60); stage(kt + 3); }
}

DEV void gate_step(float& gsum, unsigned xaddr, unsigned waddr) {
    unsigned xv, gw;
    asm volatile("ds_read_b32 %0, %2\n\tds_read_b32 %1, %3\n\ts_waitcnt lgkmcnt(0)" : "=&v"(xv), "=&v"(gw) : "v"(xaddr), "v"(waddr) : "memory");
    const unsigned pr = pkb(lo2f(xv) * lo2f(gw), hi2f(xv) * hi2f(gw));
    gsum += lo2f(pr); gsum += hi2f(pr); PIN(gsum);
}
DEV void gate_addr(lptr smem, int pt, int aw, int lane, int& gi, unsigned& gaddr, unsigned& gwaddr) {
    gi = 8 * pt + 2 * aw + (lane >> 5);
    gaddr = ldsa(smem) + gi * 128 + ((((lane & 31) >> 2) ^ (gi & 7)) << 4) + (lane & 3) * 4;
    gwaddr = ldsa(smem + A_GW) + (lane & 31) * 4;
}
DEV CM mat_sf(int k1, int c, int q) { const float dt = -(float)(k1 + 128 * c) * (1.0f / 2048.0f); return cmat(dt * (float)(4 * q), dt); }
DEV CM mat_si(int k1, int c, int q) { return cmat((float)((k1 + 512 * q) * c) * (1.0f / 2048.0f), (float)c * (1.0f / 16.0f)); }

DEV float ka_aux(const u16* __restrict__ w_r, const u16* __restrict__ w_i, const u16* __restrict__ lam_re, const u16* __restrict__ lam_im,
                 const u16* __restrict__ log_dt, const u16* __restrict__ gate_w, char* ws, lptr smem, int gb, int aw, int lane TSARG) {
    int lt, pt; tile_of(gb, lt, pt);
    const int c = lane & 15, q = lane >> 4;
    const int rot = (lt + pt) & 15;
    const rsrc_t rs = mkrsrc(ws, WS_END);
    TS(0);
    __builtin_amdgcn_s_setprio(PRIOA);
    const u32x2 gwv = *reinterpret_cast<const u32x2*>(gate_w + aw * 256 + lane * 4);
    unsigned lru = lam_re[pt * 64 + lane], liu = lam_im[pt * 64 + lane], ldu = log_dt[pt * 64 + lane];
    SCHED();
    int gi; unsigned gaddr, gwaddr; gate_addr(smem, pt, aw, lane, gi, gaddr, gwaddr);
    float gsum = 0.f;
    u32x4 fch, fcl, fsh, fsl;
    const bool m0 = 16 * aw + c == 0;
    {
        const float th = (float)(16 * aw + c) * (1.0f / 128.0f);
#pragma unroll
        for (int i = 0; i < 4; ++i) {
            const float t0 = __builtin_amdgcn_fractf(th * (float)(16 * (i >> 1) + 2 * (i & 1) + 4 * q)), t1 = t0 + th;
            const float c0 = __builtin_amdgcn_cosf(t0), c1 = __builtin_amdgcn_cosf(t1);
            const float s0 = m0 ? 1.f : -__builtin_amdgcn_sinf(t0), s1 = m0 ? -1.f : -__builtin_amdgcn_sinf(t1);
            const unsigned ch = pkb(c0, c1), sh = pkb(s0, s1);
            fch[i] = ch; fcl[i] = pkb(c0 - lo2f(ch), c1 - hi2f(ch));
            fsh[i] = sh; fsl[i] = pkb(s0 - lo2f(sh), s1 - hi2f(sh));
        }
        PIN(fch); PIN(fcl); PIN(fsh); PIN(fsl);
    }
    {
        const int mp = gb >> 2, j1 = gb & 3;
        const bool spc = mp == 0 && !(j1 & 1);
        const int k1 = mp ? mp : (j1 == 3 ? 64 : 0);
        lptr mt = smem + A_MAT + lane * 16;
        if (aw == 0) {
            const float dt = -(float)(j1 + 4 * (spc ? c & 7 : c)) * (1.0f / 64.0f), t0 = __builtin_amdgcn_fractf(dt * (float)(4 * q));
            float cc[4], ss[4];
#pragma unroll
            for (int e = 0; e < 4; ++e) { const float f = fmaf((float)e, dt, t0); cc[e] = __builtin_amdgcn_cosf(f); ss[e] = __builtin_amdgcn_sinf(f); }
            const unsigned c0 = pkb(cc[0], cc[1]), c1 = pkb(cc[2], cc[3]), s0 = pkb(ss[0], ss[1]), s1 = pkb(ss[2], ss[3]);
            const unsigned c0l = pkb(cc[0] - lo2f(c0), cc[1] - hi2f(c0)), c1l = pkb(cc[2] - lo2f(c1), cc[3] - hi2f(c1));
            const unsigned s0l = pkb(ss[0] - lo2f(s0), ss[1] - hi2f(s0)), s1l = pkb(ss[2] - lo2f(s1), ss[3] - hi2f(s1));
            u32x4 m[4] = {(u32x4){c0, c1, s0 ^ 0x80008000u, s1 ^ 0x80008000u}, (u32x4){s0, s1, c0, c1},
                          (u32x4){c0l, c1l, s0l ^ 0x80008000u, s1l ^ 0x80008000u}, (u32x4){s0l, s1l, c0l, c1l}};
            if (spc) {
                m[0] = c < 8 ? (u32x4){c0, c1, 0, 0} : (u32x4){0, 0, c0, c1}; m[1] = c < 8 ? (u32x4){s0, s1, 0, 0} : (u32x4){0, 0, s0, s1};
                m[2] = c < 8 ? (u32x4){c0l, c1l, 0, 0} : (u32x4){0, 0, c0l, c1l}; m[3] = c < 8 ? (u32x4){s0l, s1l, 0, 0} : (u32x4){0, 0, s0l, s1l};
            }
#pragma unroll
            for (int i = 0; i < 4; ++i) lst<u32x4>(mt + i * 1024, m[i]);
        } else if (aw == 1) {
            const int jq = spc ? (4 * q) & 7 : 4 * q;
            float cc[4], ss[4];
            const float t0 = __builtin_amdgcn_fractf((float)(c * (j1 + 4 * jq)) * (1.0f / 64.0f));
#pragma unroll
            for (int e = 0; e < 4; ++e) {
                const float f = fmaf((float)c, (float)e * (1.0f / 16.0f), t0);
                const float wg = !spc || (gb == 0 && jq + e == 0) ? 0.125f : 0.25f;
                cc[e] = __builtin_amdgcn_cosf(f) * wg; ss[e] = -__builtin_amdgcn_sinf(f) * wg;
            }
            const unsigned c0 = pkh(cc[0], cc[1]), c1 = pkh(cc[2], cc[3]), n0 = pkh(ss[0], ss[1]), n1 = pkh(ss[2], ss[3]);
            u32x4 mia = (u32x4){c0, n0, c1, n1}, mib = (u32x4){n0, c0 ^ 0x80008000u, n1, c1 ^ 0x80008000u};
            if (spc) { mib = q < 2 ? (u32x4){0, 0, 0, 0} : mia ^ 0x80008000u; if (q >= 2) mia = (u32x4){0, 0, 0, 0}; }
            lst<u32x4>(mt + 8 * 1024, mia); lst<u32x4>(mt + 9 * 1024, mib);
        } else {
            const CM m = aw == 2 ? mat_sf(k1, c, q) : mat_si(k1, c, q);
            lst<u32x4>(mt + 2 * aw * 1024, m.re); lst<u32x4>(mt + (2 * aw + 1) * 1024, m.im);
        }
    }
    const unsigned sg64 = (c & 1) ? 0x80008000u : 0u, sgA = (c & 2) ? 0x80008000u : 0u, sgB = ((c + 1) & 2) ? 0x80008000u : 0u;
    PIN(lru); PIN(liu); PIN(ldu);
    lst<u32x2>(smem + A_GW + aw * 512 + lane * 8, gwv);
    LDS_WAIT();
    u32x2 R[4], I[4], R2[4], I2[4]; unsigned sp = 0, sp2 = 0, mx = 0, um = 0;
#pragma unroll
    for (int r = 0; r < 4; ++r) { R2[r] = (u32x2){0, 0}; I2[r] = R2[r]; }
    u32x4 wo[4], uo[4];
    f32x4 vr[4], vi[4];
    float dt = 0.f, er = 0.f; float2 lam = make_float2(0.f, 0.f);
#pragma unroll
    for (int d1 = 0; d1 < 4; ++d1) { vr[d1] = Z4; vi[d1] = Z4; }
#pragma unroll
    for (int kt = 0; kt < 13; ++kt) {
        TS(8 + kt);
        BAR();
        u32x4 xa[2];
        if (kt < 8) {
            constexpr int KSO[4] = {0, 2, 3, 1};
#pragma unroll
            for (int i = 0; i < 2; ++i) xa[i] = trfrag(smem + A_XT + (2 * (kt & 1) + i) * 4096 + KSO[kt >> 1] * 1024 + lane * 8, 512);
        }
        u32x2 xc[4];
        if (kt == XSTEP || kt == XSTEP + 1) {
#pragma unroll
            for (int i = 0; i < 4; ++i) xc[i] = lld<u32x2>(smem + A_XT + i * 4096 + (32 * aw + 16 * (kt - XSTEP) + c) * 32 + q * 8);
        }
        gate_step(gsum, gaddr + (kt & 3) * A_STAGE, gwaddr + ((kt + rot) & 15) * 128);
        if (kt < 8) {
#pragma unroll
            for (int i = 0; i < 2; ++i) { const int d1 = 2 * (kt & 1) + i; vr[d1] = mfb(xa[i], fcl, vr[d1]); vi[d1] = mfb(xa[i], fsl, vi[d1]); }
#pragma unroll
            for (int i = 0; i < 2; ++i) { const int d1 = 2 * (kt & 1) + i; vr[d1] = mfb(xa[i], fch, vr[d1]); vi[d1] = mfb(xa[i], fsh, vi[d1]); PIN(vr[d1]); PIN(vi[d1]); }
        }
        if (kt == 1 || kt == 5) { fch ^= sg64; fsh ^= sg64; PIN(fch); PIN(fsh); if (FLO) { fcl ^= sg64; fsl ^= sg64; PIN(fcl); PIN(fsl); } }
        if (kt == 3) {
            const bool od = c & 1;
            const u32x4 a = (od ? fsh : fch) ^ sgA, b = (od ? fch : fsh) ^ sgB, al = (od ? fsl : fcl) ^ sgA, bl = (od ? fcl : fsl) ^ sgB;
            fch = a; fsh = b; PIN(fch); PIN(fsh);
            if (FLO) { fcl = al; fsl = bl; PIN(fcl); PIN(fsl); }
        }
        if (kt == WSTEP) {
            if (gb < 4) { w_load(w_r, 64, gb & 3, aw, lane, R2); w_load(w_i, 64, gb & 3, aw, lane, I2); sp2 = w_part(w_r, w_i, 64, gb & 3, aw, lane); }
            w_load(w_r, gb >> 2, gb & 3, aw, lane, R);
            sp = w_part(w_r, w_i, gb >> 2, gb & 3, aw, lane);
        }
        if (kt == WSTEP + WSPL) w_load(w_i, gb >> 2, gb & 3, aw, lane, I);
        if (kt == XSTEP || kt == XSTEP + 1) {
#pragma unroll
            for (int i = 0; i < 2; ++i)
                st128<WT>((u32x4){xc[2 * i].x, xc[2 * i].y, xc[2 * i + 1].x, xc[2 * i + 1].y}, rs,
                          OFF_X + (lt * 16 + pt) * 16384 + aw * 4096 + (2 * (kt - XSTEP) + i) * 1024 + lane * 16);
        }
        if (kt == 6 || kt == 7) {
            if (kt == 6) { PIN(sp); mx = sp & 0x7fffu; }
#pragma unroll
            for (int r = 2 * (kt - 6); r < 2 * (kt - 6) + 2; ++r) {
                PIN(R[r]); PIN(I[r]);
                w_row(r, aw, lane, R[r], I[r], sp, wo, mx);
                PIN(wo[0][r]); PIN(wo[1][r]); PIN(wo[2][r]); PIN(wo[3][r]); PIN(mx);
            }
        }
        if (kt == 8) {
            f32x4 ur[4], ui[4];
            const f32x4 t0r = vr[0] + vr[2], t1r = vr[0] - vr[2], t2r = vr[1] + vr[3], t3r = vr[1] - vr[3];
            const f32x4 t0i = vi[0] + vi[2], t1i = vi[0] - vi[2], t2i = vi[1] + vi[3], t3i = vi[1] - vi[3];
            ur[0] = t0r + t2r; ui[0] = t0i + t2i; ur[2] = t0r - t2r; ui[2] = t0i - t2i;
            ur[1] = t1r + t3i; ui[1] = t1i - t3r; ur[3] = t1r - t3i; ui[3] = t1i + t3r;
            if (m0) { ur[1] = t1r; ui[1] = -t3r; ur[3] = t1i; ui[3] = t3i; }
#pragma unroll
            for (int j1 = 0; j1 < 4; ++j1) {
                uo[j1] = (u32x4){pkb(ur[j1][0], ur[j1][1]), pkb(ur[j1][2], ur[j1][3]), pkb(ui[j1][0], ui[j1][1]), pkb(ui[j1][2], ui[j1][3])};
                PIN(uo[j1]);
            }
        }
        if (kt >= 8 && kt < 12) {
            const int j1 = kt - 8;
            PIN(uo[j1]);
            const u32x4 o = to16(uo[j1].x, uo[j1].y, uo[j1].z, uo[j1].w);
            um = pkmax(um, pkmax(pkmax(o.x & 0x7fff7fffu, o.y & 0x7fff7fffu), pkmax(o.z & 0x7fff7fffu, o.w & 0x7fff7fffu)));
            PIN(um);
            if (aw == 1) {
                const float lr = lo2f(lru), li = lo2f(liu);
                if (kt == 9) { dt = bfr(expf(lo2f(ldu))); er = expf(dt * lr); PIN(dt); PIN(er); }
                if (kt == 10) { float sn, cs; sincosf(dt * li, &sn, &cs); lam = make_float2(er * cs, er * sn); PIN(lam.x); PIN(lam.y); }
                if (kt == 11) {
                    const float nr = lam.x - 1.0f, ni = lam.y, den = lr * lr + li * li;
                    const float2 coef = make_float2((nr * lr + ni * li) / den, (ni * lr - nr * li) / den);
                    float2 l16 = lam;
#pragma unroll
                    for (int i = 0; i < 4; ++i) l16 = cmul(l16, l16);
                    float2 l128 = l16;
#pragma unroll
                    for (int i = 0; i < 3; ++i) l128 = cmul(l128, l128);
                    lst<f32x4>(smem + A_PRM + lane * 32, (f32x4){lam.x, lam.y, coef.x, coef.y});
                    lst<f32x4>(smem + A_PRM + lane * 32 + 16, (f32x4){l16.x, l16.y, l128.x, l128.y});
                }
            }
            st128<SC1>(o, rs, c * 65536 + (q & 1) * 32 + (q >> 1) * 16 + OFF_U + (aw * 64 + j1) * 16384 + lt * 1024 + pt * 64);
            w_store(rs, gb >> 2, gb & 3, aw, lane, wo, j1, j1 + 1);
        }
        if (kt == 12) {
            if (gb < 4) {
                mx = pkmax(mx, sp2 & 0x7fffu);
#pragma unroll
                for (int r = 0; r < 4; ++r) w_row(r, aw, lane, R2[r], I2[r], sp2, wo, mx);
                w_store(rs, 64, gb & 3, aw, lane, wo, 0, 4);
            }
            mx = wave_pkmax(__builtin_amdgcn_perm(pkmax(um, um >> 16), pkmax(mx, mx >> 16), 0x05040100u));
            if (lane == 0) lst<unsigned>(smem + A_FLG + 16 + aw * 4, mx);
            LDS_WAIT();
        }
        SCHED();
    }
    return gsum;
}

DEV void ka_mid(const u16* __restrict__ Cre, const u16* __restrict__ Cim, char* ws, lptr smem, int gb, int w, int lane, float gsum TSARG) {
    int lt, pt; tile_of(gb, lt, pt);
    const int c = lane & 15, q = lane >> 4, mp = gb >> 2, j1 = gb & 3, rot = (lt + pt) & 15;
    const bool spc = mp == 0 && !(j1 & 1), xtr = gb == 0, ld = w >= 4;
    const int k1 = mp ? mp : spc ? (w >> 2) * 64 : (j1 == 1 ? 0 : 64);
    const int ja0 = spc ? (2 * w) & 7 : 2 * w;
    const rsrc_t rs = mkrsrc(ws, WS_END);
    int gi; unsigned gaddr, gwaddr; gate_addr(smem, pt, w, lane, gi, gaddr, gwaddr);
    float cf = (float)c;
    CM gf[2], gi_[2];
    u32x2 tag;
#pragma unroll
    for (int kt = 13; kt < 16; ++kt) {
        if (ld) { if (kt == 13) wait_vm<16>(); else if (kt == 14) wait_vm<12>(); else wait_vm<4>(); }
        else if (kt == 14) wait_vm<0>();
        TS(8 + kt);
        BAR();
        TS(28 + kt);
        if (ld) {
            if (kt == 13) {
                const rsrc_t rc = mkrsrc((gb & 8) ? Cim : Cre, D_ * D_ * 2);
#pragma unroll
                for (int i = 0; i < 4; ++i)
                    blds16(rc, smem + (w - 4) * 4096 + i * 1024, q * 2048 + (c ^ (4 * q)) * 16, (256 * (w - 4) + (gb & ~15) + 4 * i) * 2048 + (gb & 7) * 256);
            }
        } else gate_step(gsum, gaddr + (kt & 3) * A_STAGE, gwaddr + ((kt + rot) & 15) * 128);
        if (kt == 13) asm volatile("ds_read_b64 %0, %1\n\ts_waitcnt lgkmcnt(0)" : "=v"(tag) : "v"(ldsa(smem + A_FLG + 32)) : "memory");
        if (kt == 14 && w == 0) {
            const u32x4 m4 = lld<u32x4>(smem + A_FLG + 16);
            const unsigned m = pkmax(pkmax(m4.x, m4.y), pkmax(m4.z, m4.w));
            if (lane == 0) __builtin_amdgcn_raw_buffer_store_b64((u32x2){(tag.x & 0xffff0000u) | ((m >> 15) & 0xff00u) | ((m & 0xffffu) >> 7), tag.y}, rs, 0, OFF_MX + gb * 8, SC1);
        }
        if (kt < 15) {
            PIN(cf);
            const float dt = fmaf(cf, -1.0f / 16.0f, -(float)(j1 + 4 * (ja0 + kt - 13)) * (1.0f / 1024.0f));
            gf[kt - 13] = cmat(dt * (float)(4 * q), dt);
        }
        SCHED();
    }
    TC(61);
    __builtin_amdgcn_s_setprio(0);
    u32x4 fl[2];
    unsigned foff = lane * 16; PIN(foff);
#pragma unroll
    for (int i = 0; i < 2; ++i) fl[i] = __builtin_amdgcn_raw_buffer_load_b128(rs, foff + i * 1024, OFF_MX, SC1);
    SCHED();
    PIN(cf);
#pragma unroll
    for (int i = 0; i < 2; ++i) gi_[i] = cmat(cf * (float)(j1 + 4 * (ja0 + i) + 256 * q) * (1.0f / 1024.0f), cf * (1.0f / 16.0f));
    TS(1);
    int spin = 0;
#pragma unroll 1
    for (; spin < (1 << 20); ++spin) {
        bool ok = true;
#pragma unroll
        for (int i = 0; i < 2; ++i) ok = ok & ((fl[i].x ^ tag.x) < 0x10000u) & (fl[i].y == tag.y) & ((fl[i].z ^ tag.x) < 0x10000u) & (fl[i].w == tag.y);
        if (__all(ok)) break;
        PIN(foff);
#pragma unroll
        for (int i = 0; i < 2; ++i) fl[i] = __builtin_amdgcn_raw_buffer_load_b128(rs, foff + i * 1024, OFF_MX, SC1);
    }
    asm volatile("" ::: "memory");
    TS(2);
#pragma unroll
    for (int i = 0; i < 2; ++i) blds16<SC1>(rs, smem + A_UT + (2 * w + i) * 1024, lane * 16, OFF_U + gb * 16384 + (2 * w + i) * 1024);
    u32x4 wv[3];
#pragma unroll
    for (int i = 0; i < 2; ++i) wv[i] = __builtin_amdgcn_raw_buffer_load_b128(rs, lane * 16, OFF_W + (k1 * 4 + j1) * 16384 + (ja0 + i) * 1024, SC1);
    wv[2] = (u32x4){0, 0, 0, 0};
    if (xtr) wv[2] = __builtin_amdgcn_raw_buffer_load_b128(rs, lane * 16, OFF_W + (k1 * 4) * 16384 + 8 * 1024, SC1);
    SCHED();
    int eu, ew;
    {
        constexpr unsigned SP = 0x0c010c00u;
        const unsigned m = __builtin_amdgcn_readfirstlane(wave_pkmax(pkmax(pkmax(__builtin_amdgcn_perm(0u, fl[0].x, SP), __builtin_amdgcn_perm(0u, fl[0].z, SP)),
                                                                           pkmax(__builtin_amdgcn_perm(0u, fl[1].x, SP), __builtin_amdgcn_perm(0u, fl[1].z, SP)))));
        eu = min(max((int)(m >> 16), 4), 254); ew = min((int)(m & 0xffffu), 245);
    }
    const float su = __uint_as_float((unsigned)(258 - eu) << 23), sw = __uint_as_float((unsigned)(246 - ew) << 23);
    u32x4 mt[10];
    asm volatile("ds_read_b128 %0, %10\n\tds_read_b128 %1, %10 offset:1024\n\tds_read_b128 %2, %10 offset:2048\n\tds_read_b128 %3, %10 offset:3072\n\t"
                 "ds_read_b128 %4, %10 offset:4096\n\tds_read_b128 %5, %10 offset:5120\n\tds_read_b128 %6, %10 offset:6144\n\tds_read_b128 %7, %10 offset:7168\n\t"
                 "ds_read_b128 %8, %10 offset:8192\n\tds_read_b128 %9, %10 offset:9216\n\ts_waitcnt lgkmcnt(0)"
                 : "=&v"(mt[0]), "=&v"(mt[1]), "=&v"(mt[2]), "=&v"(mt[3]), "=&v"(mt[4]), "=&v"(mt[5]), "=&v"(mt[6]), "=&v"(mt[7]), "=&v"(mt[8]), "=&v"(mt[9])
                 : "v"(ldsa(smem + A_MAT) + lane * 16) : "memory");
    if (spc && ld) { const CM a = mat_sf(64, c, q), b = mat_si(64, c, q); mt[4] = a.re; mt[5] = a.im; mt[6] = b.re; mt[7] = b.im; }
    u32x4 cv[4];
    if (ld) {
        const unsigned ca = ldsa(smem) + (w - 4) * 4096 + q * 1024 + (lane & 3) * 256 + ((lane >> 2) & 3) * 16;
        asm volatile("ds_read_b128 %0, %4\n\tds_read_b128 %1, %5\n\tds_read_b128 %2, %6\n\tds_read_b128 %3, %7\n\ts_waitcnt lgkmcnt(0)"
                     : "=&v"(cv[0]), "=&v"(cv[1]), "=&v"(cv[2]), "=&v"(cv[3])
                     : "v"(ca + (lane & 3) * 64), "v"(ca + ((lane & 3) ^ 1) * 64), "v"(ca + ((lane & 3) ^ 2) * 64), "v"(ca + ((lane & 3) ^ 3) * 64) : "memory");
    } else {
        gsum = half_sum(gsum);
        const float g = bfr(1.0f / (1.0f + expf(-bfr(gsum))));
        cv[0].x = __float_as_uint(g); cv[0].y = __float_as_uint(bfr(1.0f - g));
        PIN(cv[0]);
    }
    SCHED();
    PIN(wv[0]); PIN(wv[1]); PIN(wv[2]);
    TS(3);
    const unsigned cnt = ldsa(smem + A_FLG) + 8;
    {
        u32x2 ua[4];
        asm volatile("ds_read_b64_tr_b16 %0, %4\n\tds_read_b64_tr_b16 %1, %4 offset:32\n\tds_read_b64_tr_b16 %2, %4 offset:1024\n\tds_read_b64_tr_b16 %3, %4 offset:1056\n\t"
                     "s_waitcnt lgkmcnt(0)" : "=&v"(ua[0]), "=&v"(ua[1]), "=&v"(ua[2]), "=&v"(ua[3])
                     : "v"(ldsa(smem + A_UT) + 2 * w * 1024 + (lane >> 2) * 64 + (lane & 3) * 8) : "memory");
        const unsigned ea = ldsa(smem + A_E1) + c * E1P + q * 256 + 2 * w * 16;
#pragma unroll
        for (int i = 0; i < 2; ++i) {
            const u32x4 u = {ua[2 * i].x, ua[2 * i].y, ua[2 * i + 1].x, ua[2 * i + 1].y};
            f32x4 r = mfb(u, mt[2], Z4), m = mfb(u, mt[3], Z4);
            r = mfb(u, mt[0], r); m = mfb(u, mt[1], m);
            asm volatile("ds_write_b128 %0, %1 offset:%2" :: "v"(ea), "v"(pk8(r * su, m * su)), "i"(i * 16) : "memory");
            if (xtr) {
                const unsigned sg = 0xbf803f80u;
                const f32x4 e = mfb(u, c == 0 ? (u32x4){sg, sg, 0, 0} : (u32x4){0, 0, sg, sg}, Z4);
                if (c < 2) asm volatile("ds_write_b128 %0, %1 offset:%2" :: "v"(ea), "v"(pk8(e * su, Z4)), "i"(16 * E1P + i * 16) : "memory");
            }
        }
    }
    if (lane == 0) lds_add(cnt);
    if (ld) {
#pragma unroll
        for (int j = 0; j < 4; ++j) st128<WT>(cv[j], rs, OFF_C + ((gb * 4 + j) * 4 + w - 4) * 1024 + lane * 16);
    } else {
        const int tau = (gi & 64) | ((gi & 12) << 2) | ((gi & 48) >> 2) | (gi & 3);
        if ((lane & 31) == 0) __builtin_amdgcn_raw_buffer_store_b64((u32x2){cv[0].x, cv[0].y}, rs, (tau & 15) * 1024 + (lt * 8 + (tau >> 4)) * 8, OFF_G, 0);
        if (xtr && w == 0 && lane == 0) {
            __builtin_amdgcn_raw_buffer_store_b64(tag, rs, 0, OFF_E, SC1);
            __builtin_amdgcn_raw_buffer_store_b32(eu + ew - 267, rs, 0, OFF_E + 128, 0);
        }
    }
    wv[0] = wcvt(wv[0], sw); wv[1] = wcvt(wv[1], sw);
    TS(4);
#pragma unroll 1
    for (int sp_ = 0; sp_ < (1 << 24); ++sp_) { if (lds_peek(cnt) >= 8u) break; }
    u32x4 dv[2];
#pragma unroll
    for (int i = 0; i < 2; ++i) dv[i] = lld<u32x4>(smem + A_E1 + (2 * w + i) * E1P + lane * 16);
#pragma unroll
    for (int i = 0; i < 2; ++i) dv[i] = pk8(mfh(dv[i], gf[i].re, Z4), mfh(dv[i], gf[i].im, Z4));
#pragma unroll
    for (int i = 0; i < 2; ++i) dv[i] = wmul(mfh(mt[4], dv[i], Z4), mfh(mt[5], dv[i], Z4), wv[i]);
#pragma unroll
    for (int i = 0; i < 2; ++i) dv[i] = pk8(mfh(dv[i], mt[6], Z4), mfh(dv[i], mt[7], Z4));
    {
        f32x4 r[2], m[2];
#pragma unroll
        for (int i = 0; i < 2; ++i) { r[i] = mfh(dv[i], gi_[i].re, Z4); m[i] = mfh(dv[i], gi_[i].im, Z4); }
#pragma unroll
        for (int e = 0; e < 4; ++e)
            lst<u32x2>(smem + A_E2 + (4 * q + e) * 1024 + (w >> 1) * 256 + c * 16 + (w & 1) * 8, (u32x2){pkh(r[0][e], r[1][e]), pkh(m[0][e], m[1][e])});
    }
    if (xtr && !(w & 3)) {
        const float dt = -(float)(32 + 64 * c) * (1.0f / 1024.0f);
        const CM f = cmat(dt * (float)(4 * q), dt), b = cmat(cf * (float)(32 + 256 * q) * (1.0f / 1024.0f), cf * (1.0f / 16.0f));
        u32x4 d = lld<u32x4>(smem + A_E1 + (16 + (w >> 2)) * E1P + lane * 16);
        d = pk8(mfh(d, f.re, Z4), mfh(d, f.im, Z4));
        d = wmul(mfh(mt[4], d, Z4), mfh(mt[5], d, Z4), wcvt(wv[2], sw));
        d = pk8(mfh(d, mt[6], Z4), mfh(d, mt[7], Z4));
        const f32x4 r = mfh(d, b.re, Z4);
#pragma unroll
        for (int e = 0; e < 4; ++e) lst<float>(smem + A_EX + ((w >> 2) * 256 + (4 * q + e) * 16 + c) * 4, r[e]);
    }
    TS(5);
    aux_sync(cnt, 16u, lane);
    {
        f32x4 xa[2], xb[2];
#pragma unroll
        for (int i = 0; i < 2; ++i) { xa[i] = Z4; xb[i] = Z4; }
        if (xtr) {
            const float sg = (c & 1) ? -0.125f : 0.125f;
#pragma unroll
            for (int i = 0; i < 2; ++i) {
                xa[i] = lld<f32x4>(smem + A_EX + (2 * w + i) * 64 + q * 16) * sg;
                xb[i] = lld<f32x4>(smem + A_EX + 1024 + (2 * w + i) * 64 + q * 16) * -sg;
            }
        }
#pragma unroll
        for (int i = 0; i < 2; ++i) {
            const int s2 = 2 * w + i;
            const u32x4 ea = lld<u32x4>(smem + A_E2 + s2 * 1024 + lane * 16);
            const f32x4 ta = mfh(ea, mt[8], xa[i]), tb = mfh(ea, mt[9], xb[i]);
            st128<WT>(to16(pkh(ta[0], ta[1]), pkh(ta[2], ta[3]), pkh(tb[0], tb[1]), pkh(tb[2], tb[3])), rs,
                      c * 16384 + (q & 1) * 32 + (q >> 1) * 16 + OFF_T + s2 * 262144 + j1 * 4096 + mp * 64);
        }
    }
    TS(7);
    TSE(w ? 1 : 5, (w & 3) == 0);
}

NOPK __global__ __launch_bounds__(768, 1) void ka_kernel(const u16* __restrict__ x, const u16* __restrict__ w_r, const u16* __restrict__ w_i,
                                                         const u16* __restrict__ Bre, const u16* __restrict__ Bim, const u16* __restrict__ Cre,
                                                         const u16* __restrict__ Cim, const u16* __restrict__ lam_re,
                                                         const u16* __restrict__ lam_im, const u16* __restrict__ log_dt,
                                                         const u16* __restrict__ gate_w, char* ws) {
    extern __shared__ __attribute__((aligned(16))) char smem_[];
    lptr smem = (lptr)smem_;
    const int wid = __builtin_amdgcn_readfirstlane(threadIdx.x >> 6), lane = threadIdx.x & 63;
    if (wid < 4) ka_mma(ws, smem, blockIdx.x, wid, lane);
    else {
        TSD();
        float gsum = 0.f;
        if (wid < 8) ka_load(x, Bre, Bim, smem, blockIdx.x, wid - 4, lane TSPASS);
        else gsum = ka_aux(w_r, w_i, lam_re, lam_im, log_dt, gate_w, ws, smem, blockIdx.x, wid - 8, lane TSPASS);
        int w = wid & 7;
        asm volatile("" : "+s"(w));
        ka_mid(Cre, Cim, ws, smem, blockIdx.x, w, lane, gsum TSPASS);
    }
}

constexpr int B_UT = 0, B_R0 = B_UT + 16384, B_CNT = B_R0 + 32768, B_DMP = B_CNT + 64, KB_SMEM = B_DMP + 12 * 8192;

DEV void kb_mma(char* ws, float* __restrict__ out, lptr smem, int gb, int wid, int lane) {
    int s2, da; tile_of(gb, s2, da);
    const int c = lane & 15, q = lane >> 4;
    const rsrc_t rs = mkrsrc(ws, WS_END);
    TSD(); TS(0);
    const unsigned vo = (c >> 2) * 256 + q * 64 + (c & 3) * 16;
    const unsigned sC = OFF_C + (da * 64 + 16 * wid) * 4096, sH = OFF_H + (s2 * 64 + 16 * wid) * 8192;
    auto ldA = [&](int i, int m) { return __builtin_amdgcn_raw_buffer_load_b128(rs, vo + m * 1024, sC + i * 4096, 0); };
    auto ldB = [&](int i, int n) { return __builtin_amdgcn_raw_buffer_load_b128(rs, vo + (n & 1) * 1024, sH + i * 8192 + (((n >> 1) + wid + 1) & 3) * 2048, 0); };
    u32x4 A[3][4], B[2][8];
#pragma unroll
    for (int i = 0; i < 2; ++i) {
#pragma unroll
        for (int m = 0; m < 4; ++m) A[i][m] = ldA(i, m);
#pragma unroll
        for (int n = 0; n < 8; ++n) B[i][n] = ldB(i, n);
        SCHED();
        if (i == 0) BAR();
    }
    f32x4 acc[4][8];
#pragma unroll
    for (int m = 0; m < 4; ++m)
#pragma unroll
        for (int n = 0; n < 8; ++n) acc[m][n] = Z4;
    float omg[2];
#pragma unroll
    for (int i = 0; i < 16; ++i) {
        if (i + 2 < 16) {
#pragma unroll
            for (int m = 0; m < 4; ++m) A[(i + 2) % 3][m] = ldA(i + 2, m);
        }
        if (i == 14) {
#pragma unroll
            for (int n = 0; n < 2; ++n) omg[n] = __uint_as_float(__builtin_amdgcn_raw_buffer_load_b32(rs, c * 8 + n * 128 + 4, OFF_G + s2 * 1024 + wid * 256, 0));
        }
        SCHED();
#pragma unroll
        for (int n = 0; n < 8; ++n) {
#pragma unroll
            for (int m = 0; m < 4; ++m) acc[m][n] = mfb(A[i % 3][m], B[i & 1][n], acc[m][n]);
            if (i + 2 < 16) B[i & 1][n] = ldB(i + 2, n);
            if (i == 15 && n >= 1 && n < 7) {
                const int j = (n - 1) >> 1;
#pragma unroll
                for (int m = 0; m < 4; ++m)
                    lst<f32x4>(smem + B_DMP + (((j + wid + 1) & 3) * 3 + j) * 8192 + (((n - 1) & 1) * 4 + m) * 1024 + lane * 16, acc[m][n - 1]);
            }
            SCHED();
        }
        if (i == 0) { TS(4); TC(60); }
        if (i == 7) TS(5);
        if (i == 14) { TS(6); TC(61); }
    }
    TS(1);
    const rsrc_t ro = mkrsrc(out, S_ * D_ * 4);
    LDS_WAIT();
    BAR();
    TS(2);
#pragma unroll
    for (int n = 0; n < 2; ++n) {
        const int row = 16 * (32 * wid + 16 * n + c) + s2;
        const float o2 = 2.0f * omg[n];
#pragma unroll
        for (int m = 0; m < 4; ++m) {
            f32x4 a = acc[m][6 + n];
#pragma unroll
            for (int j = 0; j < 3; ++j) a += lld<f32x4>(smem + B_DMP + (wid * 3 + j) * 8192 + (n * 4 + m) * 1024 + lane * 16);
            const f32x4 r0 = lld<f32x4>(smem + B_R0 + ((wid * 2 + n) * 4 + m) * 1024 + lane * 16);
            const f32x4 o = {fmaf(o2, a[0], r0[0]), fmaf(o2, a[1], r0[1]), fmaf(o2, a[2], r0[2]), fmaf(o2, a[3], r0[3])};
            st128<WTO>(__builtin_bit_cast(u32x4, o), ro, (row * D_ + 256 * m + 16 * da + 4 * q) * 4);
        }
    }
    TS(3);
    TSE(2, wid == 0);
}

#define PB_HI 3
#define PB_LO 0
#define PHI() __builtin_amdgcn_s_setprio(PB_HI)
#define PLO() __builtin_amdgcn_s_setprio(PB_LO)
#define LD128 __builtin_amdgcn_raw_buffer_load_b128
#define LD64 __builtin_amdgcn_raw_buffer_load_b64
#define LD32 __builtin_amdgcn_raw_buffer_load_b32
#define BLDS blds16
#define STT st128<SC1>
#define KSLP 8
DEV void kb_aux(const u16* __restrict__ Dskip, char* ws, lptr smem, int gb, int w, int lane) {
    int s2t, dat; tile_of(gb, s2t, dat);
    const int c = lane & 15, q = lane >> 4;
    const rsrc_t rs = mkrsrc(ws, WS_END), rd = mkrsrc(Dskip, D_ * 2);
    const unsigned cnt = ldsa(smem + B_CNT);
    TSD(); TS(0);
    if (w == 0) asm volatile("ds_write_b32 %0, %1\n\ts_waitcnt lgkmcnt(0)" :: "v"(cnt), "v"(0u) : "memory");
    BAR();
    u32x4 fc[2][2], fs[2][2];
#pragma unroll
    for (int n = 0; n < 2; ++n) {
        float dt = (float)(32 * w + 16 * n + c) * (1.0f / 128.0f); PIN(dt);
        float cc[8], ss[8];
        const float t0 = __builtin_amdgcn_fractf(dt * (float)(4 * q)), t1 = __builtin_amdgcn_fractf(dt * (float)(16 + 4 * q));
#pragma unroll
        for (int e = 0; e < 8; ++e) { const float f = fmaf((float)(e & 3), dt, e < 4 ? t0 : t1); cc[e] = __builtin_amdgcn_cosf(f); ss[e] = __builtin_amdgcn_sinf(f); }
        fc[n][0] = (u32x4){pkh(cc[0], cc[1]), pkh(cc[2], cc[3]), pkh(cc[4], cc[5]), pkh(cc[6], cc[7])};
        fs[n][0] = (u32x4){pkh(ss[0], ss[1]), pkh(ss[2], ss[3]), pkh(ss[4], ss[5]), pkh(ss[6], ss[7])};
        fc[n][1] = ((c & 1) ? fs[n][0] : fc[n][0]) ^ (((c + 1) & 2) ? 0x80008000u : 0u);
        fs[n][1] = ((c & 1) ? fc[n][0] : fs[n][0]) ^ ((c & 2) ? 0x80008000u : 0u);
        PIN(fc[n][0]); PIN(fs[n][0]); PIN(fc[n][1]); PIN(fs[n][1]);
    }
    TS(1);
    u32x2 gg[2], dk[4];
    u32x4 xv[4];
    PHI();
#pragma unroll
    for (int n = 0; n < 2; ++n) gg[n] = LD64(rs, c * 8 + n * 128, OFF_G + s2t * 1024 + w * 256, 0);
#pragma unroll
    for (int i = 0; i < 4; ++i) dk[i] = LD64(rd, q * 8 + i * 512, dat * 32, 0);
    int fe = LD32(rs, 0, OFF_E + 128, 0);
#pragma unroll
    for (int i = 0; i < 4; ++i) {
        __builtin_amdgcn_s_sleep(KSLP);
        xv[i] = LD128(rs, lane * 16 + i * 1024, OFF_X + (s2t * 16 + dat) * 16384 + w * 4096, 0);
        SCHED();
    }
#pragma unroll
    for (int i = 0; i < 4; ++i) {
        __builtin_amdgcn_s_sleep(KSLP);
        BLDS(rs, smem + B_UT + w * 4096 + i * 1024, (lane >> 1) * 64 + (lane & 1) * 16, OFF_T + (s2t * 16 + dat) * 16384 + w * 4096 + (i & 1) * 2048 + (i >> 1) * 32);
        SCHED();
    }
    PLO();
    TS(2);
    f32x4 r0[2][4];
#pragma unroll
    for (int i = 0; i < 4; ++i) PIN(dk[i]);
#pragma unroll
    for (int i = 0; i < 4; ++i) {
        PIN(xv[i]);
#pragma unroll
        for (int p = 0; p < 2; ++p) {
            const int m = 2 * (i & 1) + p;
            const unsigned x0 = xv[i][2 * p], x1 = xv[i][2 * p + 1];
            const unsigned p0 = pkb(lo2f(x0) * lo2f(dk[m].x), hi2f(x0) * hi2f(dk[m].x)), p1 = pkb(lo2f(x1) * lo2f(dk[m].y), hi2f(x1) * hi2f(dk[m].y));
            r0[i >> 1][m] = (f32x4){lo2f(p0), hi2f(p0), lo2f(p1), hi2f(p1)} * __uint_as_float(gg[i >> 1].y);
            PIN(r0[i >> 1][m]);
        }
    }
    TS(5);
    __builtin_amdgcn_s_waitcnt(0x0F70);
    TS(6);
    aux_sync(cnt, 4u, lane);
    TS(7);
    f32x4 P0[2], P1[2], P2[2], P3a[2], P3b[2];
#pragma unroll
    for (int n = 0; n < 2; ++n) { P0[n] = Z4; P1[n] = Z4; P2[n] = Z4; P3a[n] = Z4; P3b[n] = Z4; }
    lptr tt = smem + B_UT + lane * 8;
#pragma unroll
    for (int h = 0; h < 2; ++h) {
        const u32x4 a1 = trfrag(tt + 1 * 4096 + h * 1024, 512), b1 = trfrag(tt + 1 * 4096 + 2048 + h * 1024, 512);
        const u32x4 a3 = trfrag(tt + 3 * 4096 + h * 1024, 512), b3 = trfrag(tt + 3 * 4096 + 2048 + h * 1024, 512);
        const u32x4 a0 = trfrag(tt + h * 1024, 512), b0 = trfrag(tt + 2048 + h * 1024, 512);
        const u32x4 a2 = trfrag(tt + 2 * 4096 + h * 1024, 512), b2 = trfrag(tt + 2 * 4096 + 2048 + h * 1024, 512);
#pragma unroll
        for (int n = 0; n < 2; ++n) {
            u32x4 cw = fc[n][h], sw_ = fs[n][h];
            P1[n] = mfh(a1, cw, P1[n]); P3a[n] = mfh(a1, sw_, P3a[n]); P3b[n] = mfh(b1, cw, P3b[n]); P1[n] = mfh(b1, sw_, P1[n]);
            if (h == 0) cw.x = q == 0 ? (cw.x & 0xffff0000u) | ((c & 1) ? 0xbc00u : 0x3c00u) : cw.x;
            P3a[n] = mfh(b3, cw, P3a[n]); P3b[n] = mfh(a3, sw_, P3b[n]); P1[n] = mfh(a3, cw, P1[n]); P1[n] = mfh(b3, sw_, P1[n]);
            if (h == 0) { cw.x = q == 0 ? (cw.x & 0xffff0000u) | 0x3800u : cw.x; sw_.x = q == 0 ? (sw_.x & 0xffff0000u) | ((c & 1) ? 0x3800u : 0xb800u) : sw_.x; }
            P0[n] = mfh(a0, cw, P0[n]); P2[n] = mfh(a2, cw, P2[n]); P0[n] = mfh(b0, sw_, P0[n]); P2[n] = mfh(b2, sw_, P2[n]);
        }
    }
#pragma unroll
    for (int n = 0; n < 2; ++n) {
        const float g = __uint_as_float(gg[n].x);
        const f32x4 sp = P0[n] + P2[n], sm = P0[n] - P2[n], p3 = P3a[n] - P3b[n];
        const f32x4 av[4] = {sp + P1[n], sm - p3, sp - P1[n], sm + p3};
#pragma unroll
        for (int m = 0; m < 4; ++m) {
            const unsigned b0 = pkb(ldexpf(av[m][0], fe), ldexpf(av[m][1], fe)), b1 = pkb(ldexpf(av[m][2], fe), ldexpf(av[m][3], fe));
            const unsigned o0 = pkb(g * lo2f(b0), g * hi2f(b0)), o1 = pkb(g * lo2f(b1), g * hi2f(b1));
            lst<f32x4>(smem + B_R0 + ((w * 2 + n) * 4 + m) * 1024 + lane * 16, r0[n][m] + (f32x4){lo2f(o0), hi2f(o0), lo2f(o1), hi2f(o1)});
        }
    }
    LDS_WAIT();
    TS(8);
    TSE(3, w == 0);
    BAR();
}

NOPK __global__ __launch_bounds__(512, 1) void kb_kernel(const u16* __restrict__ Dskip, char* ws, float* __restrict__ out) {
    extern __shared__ __attribute__((aligned(16))) char smem_[];
    lptr smem = (lptr)smem_;
    const int wid = __builtin_amdgcn_readfirstlane(threadIdx.x >> 6), lane = threadIdx.x & 63;
    if (wid < 4) kb_mma(ws, out, smem, blockIdx.x, wid, lane);
    else kb_aux(Dskip, ws, smem, blockIdx.x, wid - 4, lane);
}

extern "C" void kernel_launch(void* const* d_in, const int* in_sizes, int n_in, void* d_out, int out_size, void* d_ws, size_t ws_size,
                              hipStream_t stream) {
    const u16* x = (const u16*)d_in[0];
    const u16* w_r = (const u16*)d_in[1];
    const u16* w_i = (const u16*)d_in[2];
    const u16* lam_re = (const u16*)d_in[4];
    const u16* lam_im = (const u16*)d_in[5];
    const u16* Bre = (const u16*)d_in[6];
    const u16* Bim = (const u16*)d_in[7];
    const u16* Cre = (const u16*)d_in[8];
    const u16* Cim = (const u16*)d_in[9];
    const u16* log_dt = (const u16*)d_in[10];
    const u16* Dskip = (const u16*)d_in[11];
    const u16* gate_w = (const u16*)d_in[12];
    char* ws = (char*)d_ws;
    (void)hipFuncSetAttribute((const void*)ka_kernel, hipFuncAttributeMaxDynamicSharedMemorySize, KA_SMEM);
    (void)hipFuncSetAttribute((const void*)kb_kernel, hipFuncAttributeMaxDynamicSharedMemorySize, KB_SMEM);
    ka_kernel<<<256, 768, KA_SMEM, stream>>>(x, w_r, w_i, Bre, Bim, Cre, Cim, lam_re, lam_im, log_dt, gate_w, ws);
    kb_kernel<<<256, 512, KB_SMEM, stream>>>(Dskip, ws, (float*)d_out);
}
```

```cpp
#define PROBE 0
#include <hip/hip_runtime.h>
#include <stdint.h>

typedef __attribute__((ext_vector_type(8))) __bf16 bf16x8;
typedef __attribute__((ext_vector_type(2))) __bf16 bf16x2;
typedef __attribute__((ext_vector_type(8))) _Float16 f16x8;
typedef __attribute__((ext_vector_type(2))) _Float16 f16x2;
typedef __attribute__((ext_vector_type(4))) float f32x4;
typedef __attribute__((ext_vector_type(2))) float f32x2;
typedef __attribute__((ext_vector_type(4))) unsigned u32x4;
typedef __attribute__((ext_vector_type(2))) unsigned u32x2;
typedef __attribute__((ext_vector_type(4))) short s16x4;
typedef __attribute__((ext_vector_type(2))) unsigned short u16x2;
typedef unsigned short u16;
typedef unsigned long long u64;
#define GAS __attribute__((address_space(1)))
#define LAS __attribute__((address_space(3)))
#define NOPK __attribute__((target("no-packed-fp32-ops")))
#define RLX_AGENT __ATOMIC_RELAXED, __HIP_MEMORY_SCOPE_AGENT
#define DEV __device__ __forceinline__

constexpr int S_ = 2048, D_ = 1024, KF = 513;

constexpr unsigned OFF_H = 0;
constexpr unsigned OFF_U = OFF_H + (8u << 20);
constexpr unsigned OFF_T = OFF_U + (4u << 20);
constexpr unsigned OFF_W = OFF_T + (4u << 20);
constexpr unsigned OFF_G = OFF_W + 65u * 65536u;
constexpr unsigned OFF_MX = OFF_G + 16384u;
constexpr unsigned OFF_GRAN = OFF_MX + 4096u;
constexpr unsigned OFF_E = OFF_GRAN + 16u * 1024u * 16u;
constexpr unsigned OFF_X = OFF_E + 16u * 128u;
constexpr unsigned OFF_C = OFF_X + (4u << 20);
constexpr unsigned WS_END = OFF_C + (4u << 20);

#define TSD()
#define TS(i)
#define TC(i)
#define TSE(slot, on)

DEV float bf2f(u16 h) { return __uint_as_float((unsigned)h << 16); }
DEV unsigned pkb(float a, float b) { f32x2 v = {a, b}; return __builtin_bit_cast(unsigned, __builtin_convertvector(v, bf16x2)); }
DEV unsigned pkh(float a, float b) { f32x2 v = {a, b}; return __builtin_bit_cast(unsigned, __builtin_convertvector(v, f16x2)); }
DEV float lo2f(unsigned p) { return __uint_as_float(p << 16); }
DEV float hi2f(unsigned p) { return __uint_as_float(p & 0xffff0000u); }
DEV float bfr(float f) { return lo2f(pkb(f, f)); }
DEV float2 cmul(float2 a, float2 b) { return make_float2(a.x * b.x - a.y * b.y, a.x * b.y + a.y * b.x); }
DEV float2 cfma(float2 a, float2 b, float2 c) { return make_float2(fmaf(a.x, b.x, fmaf(-a.y, b.y, c.x)), fmaf(a.x, b.y, fmaf(a.y, b.x, c.y))); }
DEV f32x4 mfb(u32x4 a, u32x4 b, f32x4 c) { return __builtin_amdgcn_mfma_f32_16x16x32_bf16(__builtin_bit_cast(bf16x8, a), __builtin_bit_cast(bf16x8, b), c, 0, 0, 0); }
DEV f32x4 mfh(u32x4 a, u32x4 b, f32x4 c) { return __builtin_amdgcn_mfma_f32_16x16x32_f16(__builtin_bit_cast(f16x8, a), __builtin_bit_cast(f16x8, b), c, 0, 0, 0); }
DEV unsigned pkmax(unsigned a, unsigned b) { return __builtin_bit_cast(unsigned, __builtin_elementwise_max(__builtin_bit_cast(u16x2, a), __builtin_bit_cast(u16x2, b))); }
DEV float vcos(float r) { return __builtin_amdgcn_cosf(__builtin_amdgcn_fractf(r)); }
DEV float vsin(float r) { return __builtin_amdgcn_sinf(__builtin_amdgcn_fractf(r)); }
typedef LAS char* lptr;
DEV unsigned ldsa(lptr p) { return (unsigned)(uintptr_t)p; }
template <class T> DEV T lld(lptr p) { return *(LAS const T*)p; }
template <class T> DEV void lst(lptr p, T v) { *(LAS T*)p = v; }
DEV u32x2 trread(lptr p) { return __builtin_bit_cast(u32x2, __builtin_amdgcn_ds_read_tr16_b64_v4i16((LAS s16x4*)p)); }
DEV u32x4 trfrag(lptr p, int second) { const u32x2 a = trread(p), b = trread(p + second); return (u32x4){a.x, a.y, b.x, b.y}; }
DEV void glds16(const void* g, lptr l) { __builtin_amdgcn_global_load_lds((const GAS unsigned*)g, (LAS unsigned*)l, 16, 0, 0); }
typedef __amdgpu_buffer_rsrc_t rsrc_t;
template <int AUX = 0> DEV void blds16(rsrc_t rs, lptr l, unsigned vo, unsigned so) { __builtin_amdgcn_raw_ptr_buffer_load_lds(rs, (LAS void*)l, 16, vo, so, 0, AUX); }
DEV u32x4 ldfrag(lptr tile, int row, int chunk) { return lld<u32x4>(tile + row * 128 + ((chunk ^ (row & 7)) << 4)); }
template <int N> DEV void wait_vm() { asm volatile("s_waitcnt vmcnt(%0)" :: "i"(N) : "memory"); }
#define LDS_WAIT() asm volatile("s_waitcnt lgkmcnt(0)" ::: "memory")
#define SCHED() __builtin_amdgcn_sched_barrier(0)
#define BAR() do { __builtin_amdgcn_s_barrier(); asm volatile("" ::: "memory"); } while (0)
#define PIN(x) asm volatile("" : "+v"(x))
DEV rsrc_t mkrsrc(const void* p, unsigned n) { return __builtin_amdgcn_make_buffer_rsrc((void*)p, 0, n, 0x00020000); }
constexpr int SC1 = 16;
#define WT 16
#define WTO 16
template <int AUX> DEV void st128(u32x4 v, rsrc_t rs, unsigned off) { __builtin_amdgcn_raw_buffer_store_b128(v, rs, off, 0, AUX); }
DEV void lds_add(unsigned a) { asm volatile("ds_add_u32 %0, %1" :: "v"(a), "v"(1u) : "memory"); }
DEV unsigned lds_peek(unsigned a) {
    unsigned v;
    asm volatile("ds_read_b32 %0, %1\n\ts_waitcnt lgkmcnt(0)" : "=v"(v) : "v"(a) : "memory");
    return __builtin_amdgcn_readfirstlane(v);
}
DEV void tile_of(int gb, int& ta, int& tb) {
    const int xcd = gb & 7, loc = gb >> 3;
    ta = (xcd >> 1) * 4 + (loc >> 3);
    tb = (xcd & 1) * 8 + (loc & 7);
}
DEV void rows4(float a, float& a0, float& a1, float& a2, float& a3) {
    const unsigned u = __float_as_uint(a);
    const auto r = __builtin_amdgcn_permlane32_swap(u, u, false, false);
    const auto p = __builtin_amdgcn_permlane16_swap(r[0], r[0], false, false);
    const auto s = __builtin_amdgcn_permlane16_swap(r[1], r[1], false, false);
    a0 = __uint_as_float(p[0]); a1 = __uint_as_float(p[1]); a2 = __uint_as_float(s[0]); a3 = __uint_as_float(s[1]);
}
DEV float rowsum4(float a) {
    const unsigned u = __float_as_uint(a);
    const auto r = __builtin_amdgcn_permlane32_swap(u, u, false, false);
    const unsigned t = __float_as_uint(__uint_as_float(r[0]) + __uint_as_float(r[1]));
    const auto p = __builtin_amdgcn_permlane16_swap(t, t, false, false);
    return __uint_as_float(p[0]) + __uint_as_float(p[1]);
}
DEV float2 sel4(int q, float2 a, float2 b, float2 c, float2 d) {
    PIN(d.x); PIN(d.y);
    const float2 e = (q & 1) ? b : a, f = (q & 1) ? d : c;
    return (q & 2) ? f : e;
}
template <int R> DEV unsigned ror16(unsigned v) { return __builtin_amdgcn_update_dpp(0u, v, 0x120 + R, 0xf, 0xf, false); }
DEV unsigned wave_pkmax(unsigned m) {
    m = pkmax(m, ror16<8>(m)); m = pkmax(m, ror16<4>(m)); m = pkmax(m, ror16<2>(m)); m = pkmax(m, ror16<1>(m));
    const auto r = __builtin_amdgcn_permlane32_swap(m, m, false, false);
    m = pkmax(r[0], r[1]);
    const auto p = __builtin_amdgcn_permlane16_swap(m, m, false, false);
    return pkmax(p[0], p[1]);
}
DEV float half_sum(float v) {
    v += __uint_as_float(ror16<8>(__float_as_uint(v))); v += __uint_as_float(ror16<4>(__float_as_uint(v)));
    v += __uint_as_float(ror16<2>(__float_as_uint(v))); v += __uint_as_float(ror16<1>(__float_as_uint(v)));
    const auto p = __builtin_amdgcn_permlane16_swap(__float_as_uint(v), __float_as_uint(v), false, false);
    return __uint_as_float(p[0]) + __uint_as_float(p[1]);
}
DEV u32x4 to16(unsigned p0, unsigned p1, unsigned n0, unsigned n1) {
    const auto a = __builtin_amdgcn_permlane16_swap(p0, n0, false, false);
    const auto b = __builtin_amdgcn_permlane16_swap(p1, n1, false, false);
    return (u32x4){a[0], b[0], a[1], b[1]};
}

#define PRIOA 3
#define FLO 1
#define WSTEP 2
#define WSPL 1
#define XSTEP 4
#define PRIOT 3
#define RDPAT {0, 1, 2, -1, 3, 4, -1, 5, 6, -1, 7, -1, -1, -1, -1, -1}
#define Z4 ((f32x4){0.f, 0.f, 0.f, 0.f})
struct CM { u32x4 re, im; };
DEV CM cmat(float t0, float dt) {
    float cc[4], ss[4];
    t0 = __builtin_amdgcn_fractf(t0);
#pragma unroll
    for (int e = 0; e < 4; ++e) { const float f = fmaf((float)e, dt, t0); cc[e] = __builtin_amdgcn_cosf(f); ss[e] = __builtin_amdgcn_sinf(f); }
    const unsigned c0 = pkh(cc[0], cc[1]), c1 = pkh(cc[2], cc[3]), s0 = pkh(ss[0], ss[1]), s1 = pkh(ss[2], ss[3]);
    CM m; m.re = (u32x4){c0, c1, s0 ^ 0x80008000u, s1 ^ 0x80008000u}; m.im = (u32x4){s0, s1, c0, c1};
    PIN(m.re); PIN(m.im);
    return m;
}
DEV u32x4 pk8(f32x4 r, f32x4 i) { return (u32x4){pkh(r[0], r[1]), pkh(r[2], r[3]), pkh(i[0], i[1]), pkh(i[2], i[3])}; }
DEV u32x4 wmul(f32x4 r, f32x4 i, u32x4 w) {
    float yr[4], yi[4];
#pragma unroll
    for (int e = 0; e < 4; ++e) {
        const unsigned wu = w[e];
        const f16x2 h = __builtin_bit_cast(f16x2, wu);
        yr[e] = fmaf(r[e], (float)h[0], -i[e] * (float)h[1]); yi[e] = fmaf(r[e], (float)h[1], i[e] * (float)h[0]);
    }
    return (u32x4){pkh(yr[0], yr[1]), pkh(yr[2], yr[3]), pkh(yi[0], yi[1]), pkh(yi[2], yi[3])};
}
DEV u32x4 wcvt(u32x4 w, float sw) {
#pragma unroll
    for (int e = 0; e < 4; ++e) w[e] = pkh(lo2f(w[e]) * sw, hi2f(w[e]) * sw);
    PIN(w);
    return w;
}
DEV void aux_sync(unsigned cnt, unsigned target, int lane) {
    asm volatile("" ::: "memory");
    if (lane == 0) lds_add(cnt);
#pragma unroll 1
    for (int spin = 0; spin < (1 << 24); ++spin) { if (lds_peek(cnt) >= target) break; }
}

constexpr unsigned OFF_P = 64u << 20;
#define PT() ((unsigned)__builtin_amdgcn_s_memrealtime())
DEV void pput(char* ws, int slot, unsigned t) { __builtin_amdgcn_raw_buffer_store_b32(t, mkrsrc(ws + OFF_P, 65536), 0, (slot * 256 + blockIdx.x) * 4, 0); }
constexpr int A_STAGE = 32768, A_XT = 4 * A_STAGE, A_PRM = A_XT + 16384, A_AGG = A_PRM + 2048, A_FLG = A_AGG + 512, A_GW = A_FLG + 64,
              A_MAT = A_GW + 2048, KA_SMEM = A_MAT + 10240;
constexpr int A_UT = 16384, A_E1 = 2 * A_STAGE, E1P = 1040, A_EX = A_E1 + 18 * E1P + 32, A_E2 = A_XT;

DEV void ka_mma(char* ws, lptr smem, int gb, int wid, int lane) {
    int lt, pt; tile_of(gb, lt, pt);
    const int wr = wid >> 1, wc = wid & 1, c = lane & 15, q = lane >> 4;
    const rsrc_t rs = mkrsrc(ws, WS_END);
    TSD(); TS(0);
    const unsigned p0 = PT();
    u64 tag = __hip_atomic_load((GAS u64*)(ws + OFF_E), __ATOMIC_RELAXED, __HIP_MEMORY_SCOPE_WORKGROUP) + 0x9E3779B97F4A7C15ull;
    if (wid == 0 && lane < 8) lst<unsigned>(smem + A_FLG + lane * 4, 0u);
    f32x4 acc[4][4];
#pragma unroll
    for (int m = 0; m < 4; ++m)
#pragma unroll
        for (int n = 0; n < 4; ++n) acc[m][n] = Z4;
    u32x4 af[2][4], bf_[2][4];
#pragma unroll
    for (int m = 0; m < 4; ++m) { af[1][m] = (u32x4){0, 0, 0, 0}; bf_[1][m] = af[1][m]; }
    PIN(tag);
    if (wid == 0 && lane == 0) lst<u64>(smem + A_FLG + 32, tag);
    tag = (u64)(unsigned)__builtin_amdgcn_readfirstlane((unsigned)tag) | (u64)(unsigned)__builtin_amdgcn_readfirstlane((unsigned)(tag >> 32)) << 32;
    LDS_WAIT();
#pragma unroll 4
    for (int kt = 0; kt < 16; ++kt) {
        BAR();
        lptr tA = smem + (kt & 3) * A_STAGE;
        lptr tB = tA + 16384;
#pragma unroll
        for (int hf = 0; hf < 2; ++hf)
#pragma unroll
            for (int i = 0; i < 16; ++i) {
                acc[i >> 2][i & 3] = mfb(af[1 - hf][i >> 2], bf_[1 - hf][i & 3], acc[i >> 2][i & 3]);
                constexpr int RD[16] = RDPAT;
                const int r = RD[i];
                if (r == 0) af[hf][0] = ldfrag(tA, wr * 64 + c, 4 * hf + q);
                else if (r > 0 && r < 5) bf_[hf][r - 1] = ldfrag(tB, wc * 64 + (r - 1) * 16 + c, 4 * hf + q);
                else if (r >= 5) af[hf][r - 4] = ldfrag(tA, wr * 64 + (r - 4) * 16 + c, 4 * hf + q);
                SCHED();
            }
        LDS_WAIT();
    }
    const unsigned p3 = PT();
    __builtin_amdgcn_s_setprio(PRIOT);
    float2 lam[2], coef[2], l16[2], l128[2];
#pragma unroll
    for (int n = 0; n < 2; ++n) {
        const f32x4 a = lld<f32x4>(smem + A_PRM + (wc * 32 + 2 * c + n) * 32), b = lld<f32x4>(smem + A_PRM + (wc * 32 + 2 * c + n) * 32 + 16);
        lam[n] = make_float2(a[0], a[1]); coef[n] = make_float2(a[2], a[3]); l16[n] = make_float2(b[0], b[1]); l128[n] = make_float2(b[2], b[3]);
    }
#pragma unroll
    for (int m = 0; m < 4; ++m)
#pragma unroll
        for (int n = 0; n < 4; ++n) acc[m][n] = mfb(af[1][m], bf_[1][n], acc[m][n]);
    float2 l32[2], l64[2];
#pragma unroll
    for (int n = 0; n < 2; ++n) { l32[n] = cmul(l16[n], l16[n]); l64[n] = cmul(l32[n], l32[n]); }
    TS(1);
    float2 h[2][16];
#pragma unroll
    for (int n = 0; n < 2; ++n) h[n][0] = make_float2(acc[0][n][0], acc[0][2 + n][0]);
#pragma unroll
    for (int t = 1; t < 16; ++t)
#pragma unroll
        for (int n = 0; n < 2; ++n) h[n][t] = cfma(lam[n], h[n][t - 1], make_float2(acc[t >> 2][n][t & 3], acc[t >> 2][2 + n][t & 3]));
    float2 loc[2], wag[2], wq[2];
#pragma unroll
    for (int n = 0; n < 2; ++n) {
        float2 a0, a1, a2, a3;
        rows4(h[n][15].x, a0.x, a1.x, a2.x, a3.x);
        rows4(h[n][15].y, a0.y, a1.y, a2.y, a3.y);
        const float2 s2 = cfma(l16[n], a0, a1), s3 = cfma(l16[n], s2, a2);
        wag[n] = cfma(l16[n], s3, a3);
        loc[n] = sel4(q, make_float2(0.f, 0.f), a0, s2, s3);
    }
    lptr aggp = smem + A_AGG + (wc * 16 + c) * 16;
    const unsigned flg = ldsa(smem + A_FLG) + wc * 4;
    float2 W0[2] = {make_float2(0.f, 0.f), make_float2(0.f, 0.f)};
    if (wr == 0) {
        if (q == 0) lst<f32x4>(aggp, (f32x4){wag[0].x, wag[0].y, wag[1].x, wag[1].y});
        asm volatile("" ::: "memory");
        if (lane == 0) lds_add(flg);
    } else {
#pragma unroll 1
        for (int spin = 0; spin < (1 << 20); ++spin) { if (lds_peek(flg)) break; }
        const f32x4 w0 = lld<f32x4>(aggp);
        W0[0] = make_float2(w0[0], w0[1]); W0[1] = make_float2(w0[2], w0[3]);
        const float2 g0 = cfma(l64[0], W0[0], wag[0]), g1 = cfma(l64[1], W0[1], wag[1]);
        if (lt < 15 && q < 2) {
            const float2 gg = q == 0 ? g0 : g1;
            st128<SC1>((u32x4){__float_as_uint(gg.x), (unsigned)tag, __float_as_uint(gg.y), (unsigned)(tag >> 32)},
                       rs, (2 * c + q) * 16 + OFF_GRAN + ((15 - lt) * 1024 + pt * 64 + wc * 32) * 16);
        }
    }
    TS(2);
#pragma unroll
    for (int n = 0; n < 2; ++n) { PIN(lam[n].x); PIN(lam[n].y); PIN(coef[n].x); PIN(coef[n].y); PIN(l16[n].x); PIN(l16[n].y); PIN(l128[n].x); PIN(l128[n].y); }
    float2 pw[2][8], l8[2], lq[2], l512[2];
#pragma unroll
    for (int n = 0; n < 2; ++n) {
        wq[n] = sel4(q, make_float2(1.f, 0.f), l16[n], l32[n], cmul(l32[n], l16[n]));
        loc[n] = cfma(wq[n], W0[n], loc[n]);
        if (wr) wq[n] = cmul(wq[n], l64[n]);
        pw[n][0] = cmul(coef[n], lam[n]);
        l8[n] = cmul(lam[n], lam[n]); l8[n] = cmul(l8[n], l8[n]); l8[n] = cmul(l8[n], l8[n]);
        const float2 l256 = cmul(l128[n], l128[n]);
        lq[n] = sel4(q, make_float2(1.f, 0.f), l128[n], l256, cmul(l256, l128[n]));
        l512[n] = cmul(l256, l256);
    }
#pragma unroll
    for (int n = 0; n < 2; ++n) {
        PIN(wq[n].x); PIN(wq[n].y); PIN(loc[n].x); PIN(loc[n].y); PIN(l8[n].x); PIN(l8[n].y); PIN(lq[n].x); PIN(lq[n].y); PIN(l512[n].x); PIN(l512[n].y);
    }
#pragma unroll
    for (int t = 0; t < 8; ++t) {
#pragma unroll
        for (int n = 0; n < 2; ++n) {
            if (t) pw[n][t] = cmul(pw[n][t - 1], lam[n]);
            h[n][2 * t] = cmul(coef[n], h[n][2 * t]); h[n][2 * t + 1] = cmul(coef[n], h[n][2 * t + 1]);
        }
#pragma unroll
        for (int n = 0; n < 2; ++n) { PIN(pw[n][t].x); PIN(pw[n][t].y); PIN(h[n][2 * t].x); PIN(h[n][2 * t].y); PIN(h[n][2 * t + 1].x); PIN(h[n][2 * t + 1].y); }
        SCHED();
    }
    TS(3);
    if (lt > 0) {
        u32x4 gr[4][2];
        unsigned off = ((16 - lt + q) * 1024 + pt * 64 + wc * 32 + 2 * c) * 16;
#pragma unroll 1
        for (int spin = 0; spin < (1 << 20); ++spin) {
            bool ok = true;
            PIN(off);
#pragma unroll
            for (int j = 0; j < 4; ++j) {
                const rsrc_t rg = mkrsrc(ws + OFF_GRAN + j * 65536, (4 - j) * 65536);
                gr[j][0] = __builtin_amdgcn_raw_buffer_load_b128(rg, off, 0, SC1);
                gr[j][1] = __builtin_amdgcn_raw_buffer_load_b128(rg, off + 16, 0, SC1);
            }
#pragma unroll
            for (int j = 0; j < 4; ++j) {
                const bool v = lt - 1 - (4 * j + q) >= 0;
#pragma unroll
                for (int n = 0; n < 2; ++n) ok = ok & (!v | ((gr[j][n].y == (unsigned)tag) & (gr[j][n].w == (unsigned)(tag >> 32))));
            }
            if (__all(ok)) break;
            asm volatile("" ::: "memory");
        }
#pragma unroll
        for (int n = 0; n < 2; ++n) {
            float2 cs = make_float2(__uint_as_float(gr[3][n].x), __uint_as_float(gr[3][n].z));
#pragma unroll
            for (int j = 2; j >= 0; --j) cs = cfma(l512[n], cs, make_float2(__uint_as_float(gr[j][n].x), __uint_as_float(gr[j][n].z)));
            cs = cmul(cs, lq[n]);
            cs.x = rowsum4(cs.x); cs.y = rowsum4(cs.y);
            loc[n] = cfma(wq[n], cs, loc[n]);
        }
    }
    const unsigned p5 = PT();
    lptr tb = smem + A_STAGE + wid * 8192;
    PIN(lane);
#pragma unroll
    for (int hf = 0; hf < 2; ++hf) {
        const float2 s0 = hf ? cmul(l8[0], loc[0]) : loc[0], s1 = hf ? cmul(l8[1], loc[1]) : loc[1];
#pragma unroll
        for (int t = 0; t < 8; ++t) {
            const float2 v0 = cfma(pw[0][t], s0, h[0][8 * hf + t]), v1 = cfma(pw[1][t], s1, h[1][8 * hf + t]);
            lst<unsigned>(tb + (8 * hf + t) * 256 + lane * 4, pkb(v0.x, v1.x));
            lst<unsigned>(tb + 4096 + (8 * hf + t) * 256 + lane * 4, pkb(-v0.y, -v1.y));
        }
        asm volatile("" ::: "memory");
        lptr rb = tb + (lane >> 4) * 256 + (lane & 3) * 64 + ((lane >> 2) & 3) * 16;
        const unsigned hoff = OFF_H + ((lane >> 4) * 64 + pt * 2 + wc) * 8192 + lt * 512 + wr * 256 + (lane & 15) * 16;
#pragma unroll
        for (int v = 2 * hf; v < 2 * hf + 2; ++v)
#pragma unroll
            for (int pl = 0; pl < 2; ++pl) st128<WT>(lld<u32x4>(rb + pl * 4096 + v * 1024), rs, hoff + (v * 256 + pl * 32) * 8192);
    }
    TS(5);
    wait_vm<0>();
    { const unsigned p1 = PT(); if (wid == 3) { pput(ws, 0, p0); pput(ws, 1, p1); pput(ws, 3, p3); pput(ws, 5, p5); } }
    TSE(0, wid == 0);
}

struct __attribute__((packed, aligned(2))) U64U { u32x2 v; };
DEV void w_load(const u16* __restrict__ w, int k1, int qw, int lw, int lane, u32x2 (&R)[4]) {
    const int kd0 = 4 * (lane & 15) + 64 * (4 * lw + (lane >> 4));
    const int col = lw < 2 ? kd0 : 1021 - kd0;
#pragma unroll
    for (int r = 0; r < 4; ++r) {
        const int ks = k1 + 128 * (4 * qw + r), row = lw < 2 ? ks : (S_ - ks) & (S_ - 1);
        R[r] = reinterpret_cast<const U64U*>(w + row * KF + col)->v;
    }
}
DEV unsigned w_part(const u16* __restrict__ w_r, const u16* __restrict__ w_i, int k1, int qw, int lw, int lane) {
    const int ks = k1 + 128 * (4 * qw + (lane & 3)), row = lw < 2 ? (S_ - ks) & (S_ - 1) : ks;
    return ((lane & 4) ? w_i : w_r)[row * KF + (lw < 2 ? 0 : 512)];
}
DEV void w_row(int r, int lw, int lane, u32x2 R, u32x2 I, unsigned sp, u32x4 (&o)[4], unsigned& mx) {
    const unsigned cj = lw < 2 ? 0u : 0x80008000u;
    const unsigned i0 = I.x ^ cj, i1 = I.y ^ cj;
    unsigned o0 = __builtin_amdgcn_perm(i0, R.x, 0x05040100u), o3 = __builtin_amdgcn_perm(i1, R.y, 0x07060302u);
    o[1][r] = __builtin_amdgcn_perm(i0, R.x, 0x07060302u); o[2][r] = __builtin_amdgcn_perm(i1, R.y, 0x05040100u);
    const float pr = lo2f(__builtin_amdgcn_readlane(sp, r)), pi = lo2f(__builtin_amdgcn_readlane(sp, 4 + r) ^ (lw < 2 ? 0x8000u : 0u));
    const unsigned t = lw < 2 ? o0 : o3;
    unsigned v = pkb(0.5f * (lo2f(t) + pr), 0.5f * (hi2f(t) + pi));
    PIN(v);
    o[0][r] = (lane == 0 && lw == 0) ? v : o0; o[3][r] = (lane == 0 && lw == 2) ? v : o3;
    mx = pkmax(mx, pkmax(pkmax(R.x & 0x7fff7fffu, R.y & 0x7fff7fffu), pkmax(I.x & 0x7fff7fffu, I.y & 0x7fff7fffu)));
}
DEV void w_store(rsrc_t rs, int k1, int qw, int lw, int lane, u32x4 (&o)[4], int e0, int e1) {
#pragma unroll
    for (int e = e0; e < e1; ++e)
        st128<SC1>(o[e], rs, (lane & 15) * 1024 + (lane >> 4) * 16 + OFF_W + (k1 * 4 + (lw < 2 ? e : 3 - e)) * 16384 + qw * 256 + lw * 64);
}

#define TSARG
#define TSPASS
DEV void ka_load(const u16* __restrict__ x, const u16* __restrict__ Bre, const u16* __restrict__ Bim, lptr smem, int gb, int lw, int lane TSARG) {
    int lt, pt; tile_of(gb, lt, pt);
    const int srow = lane >> 3, schunk = (lane & 7) ^ srow;
    const int rot = (lt + pt) & 15;
    const rsrc_t rx = mkrsrc(x, S_ * D_ * 2), rb = mkrsrc((lw & 1) ? Bim : Bre, D_ * D_ * 2);
    const unsigned vA = (16 * (srow >> 2) + (srow & 3)) * 2048 + schunk * 16, sA = (lt * 128 + 64 * (lw >> 1) + 8 * (lw & 1)) * 2048;
    const unsigned vB = 2 * srow * 2048 + schunk * 16, sB = (pt * 64 + 32 * (lw >> 1)) * 2048;
    auto stage = [&](int kk) {
        const int ko = ((kk + rot) & 15) * 128;
        lptr l = smem + (kk & 3) * A_STAGE + lw * 4096;
#pragma unroll
        for (int i = 0; i < 4; ++i) blds16(rx, l + i * 1024, vA, sA + (32 * (i & 1) + 4 * (i >> 1)) * 2048 + ko);
#pragma unroll
        for (int i = 0; i < 4; ++i) blds16(rb, l + 16384 + i * 1024, vB, sB + (16 * (i & 1) + (i >> 1)) * 2048 + ko);
    };
    TS(0);
    stage(0);
#pragma unroll
    for (int i = 0; i < 4; ++i) blds16(rx, smem + A_XT + lw * 4096 + i * 1024, (lane >> 1) * 32768 + (lane & 1) * 16, (i * 512 + lt) * 2048 + lw * 512 + pt * 32);
    stage(1); stage(2);
    TS(6);
#pragma unroll 1
    for (int kt = 0; kt < 13; ++kt) { wait_vm<16>(); TS(8 + kt); BAR(); TS(28 + kt); if (kt == 0) TC(60); stage(kt + 3); }
}

DEV void gate_step(float& gsum, unsigned xaddr, unsigned waddr) {
    unsigned xv, gw;
    asm volatile("ds_read_b32 %0, %2\n\tds_read_b32 %1, %3\n\ts_waitcnt lgkmcnt(0)" : "=&v"(xv), "=&v"(gw) : "v"(xaddr), "v"(waddr) : "memory");
    const unsigned pr = pkb(lo2f(xv) * lo2f(gw), hi2f(xv) * hi2f(gw));
    gsum += lo2f(pr); gsum += hi2f(pr); PIN(gsum);
}
DEV void gate_addr(lptr smem, int pt, int aw, int lane, int& gi, unsigned& gaddr, unsigned& gwaddr) {
    gi = 8 * pt + 2 * aw + (lane >> 5);
    gaddr = ldsa(smem) + gi * 128 + ((((lane & 31) >> 2) ^ (gi & 7)) << 4) + (lane & 3) * 4;
    gwaddr = ldsa(smem + A_GW) + (lane & 31) * 4;
}
DEV CM mat_sf(int k1, int c, int q) { const float dt = -(float)(k1 + 128 * c) * (1.0f / 2048.0f); return cmat(dt * (float)(4 * q), dt); }
DEV CM mat_si(int k1, int c, int q) { return cmat((float)((k1 + 512 * q) * c) * (1.0f / 2048.0f), (float)c * (1.0f / 16.0f)); }

DEV float ka_aux(const u16* __restrict__ w_r, const u16* __restrict__ w_i, const u16* __restrict__ lam_re, const u16* __restrict__ lam_im,
                 const u16* __restrict__ log_dt, const u16* __restrict__ gate_w, char* ws, lptr smem, int gb, int aw, int lane TSARG) {
    int lt, pt; tile_of(gb, lt, pt);
    const int c = lane & 15, q = lane >> 4;
    const int rot = (lt + pt) & 15;
    const rsrc_t rs = mkrsrc(ws, WS_END);
    TS(0);
    __builtin_amdgcn_s_setprio(PRIOA);
    const u32x2 gwv = *reinterpret_cast<const u32x2*>(gate_w + aw * 256 + lane * 4);
    unsigned lru = lam_re[pt * 64 + lane], liu = lam_im[pt * 64 + lane], ldu = log_dt[pt * 64 + lane];
    SCHED();
    int gi; unsigned gaddr, gwaddr; gate_addr(smem, pt, aw, lane, gi, gaddr, gwaddr);
    float gsum = 0.f;
    u32x4 fch, fcl, fsh, fsl;
    const bool m0 = 16 * aw + c == 0;
    {
        const float th = (float)(16 * aw + c) * (1.0f / 128.0f);
#pragma unroll
        for (int i = 0; i < 4; ++i) {
            const float t0 = __builtin_amdgcn_fractf(th * (float)(16 * (i >> 1) + 2 * (i & 1) + 4 * q)), t1 = t0 + th;
            const float c0 = __builtin_amdgcn_cosf(t0), c1 = __builtin_amdgcn_cosf(t1);
            const float s0 = m0 ? 1.f : -__builtin_amdgcn_sinf(t0), s1 = m0 ? -1.f : -__builtin_amdgcn_sinf(t1);
            const unsigned ch = pkb(c0, c1), sh = pkb(s0, s1);
            fch[i] = ch; fcl[i] = pkb(c0 - lo2f(ch), c1 - hi2f(ch));
            fsh[i] = sh; fsl[i] = pkb(s0 - lo2f(sh), s1 - hi2f(sh));
        }
        PIN(fch); PIN(fcl); PIN(fsh); PIN(fsl);
    }
    {
        const int mp = gb >> 2, j1 = gb & 3;
        const bool spc = mp == 0 && !(j1 & 1);
        const int k1 = mp ? mp : (j1 == 3 ? 64 : 0);
        lptr mt = smem + A_MAT + lane * 16;
        if (aw == 0) {
            const float dt = -(float)(j1 + 4 * (spc ? c & 7 : c)) * (1.0f / 64.0f), t0 = __builtin_amdgcn_fractf(dt * (float)(4 * q));
            float cc[4], ss[4];
#pragma unroll
            for (int e = 0; e < 4; ++e) { const float f = fmaf((float)e, dt, t0); cc[e] = __builtin_amdgcn_cosf(f); ss[e] = __builtin_amdgcn_sinf(f); }
            const unsigned c0 = pkb(cc[0], cc[1]), c1 = pkb(cc[2], cc[3]), s0 = pkb(ss[0], ss[1]), s1 = pkb(ss[2], ss[3]);
            const unsigned c0l = pkb(cc[0] - lo2f(c0), cc[1] - hi2f(c0)), c1l = pkb(cc[2] - lo2f(c1), cc[3] - hi2f(c1));
            const unsigned s0l = pkb(ss[0] - lo2f(s0), ss[1] - hi2f(s0)), s1l = pkb(ss[2] - lo2f(s1), ss[3] - hi2f(s1));
            u32x4 m[4] = {(u32x4){c0, c1, s0 ^ 0x80008000u, s1 ^ 0x80008000u}, (u32x4){s0, s1, c0, c1},
                          (u32x4){c0l, c1l, s0l ^ 0x80008000u, s1l ^ 0x80008000u}, (u32x4){s0l, s1l, c0l, c1l}};
            if (spc) {
                m[0] = c < 8 ? (u32x4){c0, c1, 0, 0} : (u32x4){0, 0, c0, c1}; m[1] = c < 8 ? (u32x4){s0, s1, 0, 0} : (u32x4){0, 0, s0, s1};
                m[2] = c < 8 ? (u32x4){c0l, c1l, 0, 0} : (u32x4){0, 0, c0l, c1l}; m[3] = c < 8 ? (u32x4){s0l, s1l, 0, 0} : (u32x4){0, 0, s0l, s1l};
            }
#pragma unroll
            for (int i = 0; i < 4; ++i) lst<u32x4>(mt + i * 1024, m[i]);
        } else if (aw == 1) {
            const int jq = spc ? (4 * q) & 7 : 4 * q;
            float cc[4], ss[4];
            const float t0 = __builtin_amdgcn_fractf((float)(c * (j1 + 4 * jq)) * (1.0f / 64.0f));
#pragma unroll
            for (int e = 0; e < 4; ++e) {
                const float f = fmaf((float)c, (float)e * (1.0f / 16.0f), t0);
                const float wg = !spc || (gb == 0 && jq + e == 0) ? 0.125f : 0.25f;
                cc[e] = __builtin_amdgcn_cosf(f) * wg; ss[e] = -__builtin_amdgcn_sinf(f) * wg;
            }
            const unsigned c0 = pkh(cc[0], cc[1]), c1 = pkh(cc[2], cc[3]), n0 = pkh(ss[0], ss[1]), n1 = pkh(ss[2], ss[3]);
            u32x4 mia = (u32x4){c0, n0, c1, n1}, mib = (u32x4){n0, c0 ^ 0x80008000u, n1, c1 ^ 0x80008000u};
            if (spc) { mib = q < 2 ? (u32x4){0, 0, 0, 0} : mia ^ 0x80008000u; if (q >= 2) mia = (u32x4){0, 0, 0, 0}; }
            lst<u32x4>(mt + 8 * 1024, mia); lst<u32x4>(mt + 9 * 1024, mib);
        } else {
            const CM m = aw == 2 ? mat_sf(k1, c, q) : mat_si(k1, c, q);
            lst<u32x4>(mt + 2 * aw * 1024, m.re); lst<u32x4>(mt + (2 * aw + 1) * 1024, m.im);
        }
    }
    const unsigned sg64 = (c & 1) ? 0x80008000u : 0u, sgA = (c & 2) ? 0x80008000u : 0u, sgB = ((c + 1) & 2) ? 0x80008000u : 0u;
    PIN(lru); PIN(liu); PIN(ldu);
    lst<u32x2>(smem + A_GW + aw * 512 + lane * 8, gwv);
    LDS_WAIT();
    u32x2 R[4], I[4], R2[4], I2[4]; unsigned sp = 0, sp2 = 0, mx = 0, um = 0;
#pragma unroll
    for (int r = 0; r < 4; ++r) { R2[r] = (u32x2){0, 0}; I2[r] = R2[r]; }
    u32x4 wo[4], uo[4];
    f32x4 vr[4], vi[4];
    float dt = 0.f, er = 0.f; float2 lam = make_float2(0.f, 0.f);
#pragma unroll
    for (int d1 = 0; d1 < 4; ++d1) { vr[d1] = Z4; vi[d1] = Z4; }
#pragma unroll
    for (int kt = 0; kt < 13; ++kt) {
        TS(8 + kt);
        BAR();
        if (kt == 0 || kt == 8) { const unsigned p4 = PT(); if (aw == 3) pput(ws, kt ? 7 : 4, p4); }
        u32x4 xa[2];
        if (kt < 8) {
            constexpr int KSO[4] = {0, 2, 3, 1};
#pragma unroll
            for (int i = 0; i < 2; ++i) xa[i] = trfrag(smem + A_XT + (2 * (kt & 1) + i) * 4096 + KSO[kt >> 1] * 1024 + lane * 8, 512);
        }
        u32x2 xc[4];
        if (kt == XSTEP || kt == XSTEP + 1) {
#pragma unroll
            for (int i = 0; i < 4; ++i) xc[i] = lld<u32x2>(smem + A_XT + i * 4096 + (32 * aw + 16 * (kt - XSTEP) + c) * 32 + q * 8);
        }
        gate_step(gsum, gaddr + (kt & 3) * A_STAGE, gwaddr + ((kt + rot) & 15) * 128);
        if (kt < 8) {
#pragma unroll
            for (int i = 0; i < 2; ++i) { const int d1 = 2 * (kt & 1) + i; vr[d1] = mfb(xa[i], fcl, vr[d1]); vi[d1] = mfb(xa[i], fsl, vi[d1]); }
#pragma unroll
            for (int i = 0; i < 2; ++i) { const int d1 = 2 * (kt & 1) + i; vr[d1] = mfb(xa[i], fch, vr[d1]); vi[d1] = mfb(xa[i], fsh, vi[d1]); PIN(vr[d1]); PIN(vi[d1]); }
        }
        if (kt == 1 || kt == 5) { fch ^= sg64; fsh ^= sg64; PIN(fch); PIN(fsh); if (FLO) { fcl ^= sg64; fsl ^= sg64; PIN(fcl); PIN(fsl); } }
        if (kt == 3) {
            const bool od = c & 1;
            const u32x4 a = (od ? fsh : fch) ^ sgA, b = (od ? fch : fsh) ^ sgB, al = (od ? fsl : fcl) ^ sgA, bl = (od ? fcl : fsl) ^ sgB;
            fch = a; fsh = b; PIN(fch); PIN(fsh);
            if (FLO) { fcl = al; fsl = bl; PIN(fcl); PIN(fsl); }
        }
        if (kt == WSTEP) {
            if (gb < 4) { w_load(w_r, 64, gb & 3, aw, lane, R2); w_load(w_i, 64, gb & 3, aw, lane, I2); sp2 = w_part(w_r, w_i, 64, gb & 3, aw, lane); }
            w_load(w_r, gb >> 2, gb & 3, aw, lane, R);
            sp = w_part(w_r, w_i, gb >> 2, gb & 3, aw, lane);
        }
        if (kt == WSTEP + WSPL) w_load(w_i, gb >> 2, gb & 3, aw, lane, I);
        if (kt == XSTEP || kt == XSTEP + 1) {
#pragma unroll
            for (int i = 0; i < 2; ++i)
                st128<WT>((u32x4){xc[2 * i].x, xc[2 * i].y, xc[2 * i + 1].x, xc[2 * i + 1].y}, rs,
                          OFF_X + (lt * 16 + pt) * 16384 + aw * 4096 + (2 * (kt - XSTEP) + i) * 1024 + lane * 16);
        }
        if (kt == 6 || kt == 7) {
            if (kt == 6) { PIN(sp); mx = sp & 0x7fffu; }
#pragma unroll
            for (int r = 2 * (kt - 6); r < 2 * (kt - 6) + 2; ++r) {
                PIN(R[r]); PIN(I[r]);
                w_row(r, aw, lane, R[r], I[r], sp, wo, mx);
                PIN(wo[0][r]); PIN(wo[1][r]); PIN(wo[2][r]); PIN(wo[3][r]); PIN(mx);
            }
        }
        if (kt == 8) {
            f32x4 ur[4], ui[4];
            const f32x4 t0r = vr[0] + vr[2], t1r = vr[0] - vr[2], t2r = vr[1] + vr[3], t3r = vr[1] - vr[3];
            const f32x4 t0i = vi[0] + vi[2], t1i = vi[0] - vi[2], t2i = vi[1] + vi[3], t3i = vi[1] - vi[3];
            ur[0] = t0r + t2r; ui[0] = t0i + t2i; ur[2] = t0r - t2r; ui[2] = t0i - t2i;
            ur[1] = t1r + t3i; ui[1] = t1i - t3r; ur[3] = t1r - t3i; ui[3] = t1i + t3r;
            if (m0) { ur[1] = t1r; ui[1] = -t3r; ur[3] = t1i; ui[3] = t3i; }
#pragma unroll
            for (int j1 = 0; j1 < 4; ++j1) {
                uo[j1] = (u32x4){pkb(ur[j1][0], ur[j1][1]), pkb(ur[j1][2], ur[j1][3]), pkb(ui[j1][0], ui[j1][1]), pkb(ui[j1][2], ui[j1][3])};
                PIN(uo[j1]);
            }
        }
        if (kt >= 8 && kt < 12) {
            const int j1 = kt - 8;
            PIN(uo[j1]);
            const u32x4 o = to16(uo[j1].x, uo[j1].y, uo[j1].z, uo[j1].w);
            um = pkmax(um, pkmax(pkmax(o.x & 0x7fff7fffu, o.y & 0x7fff7fffu), pkmax(o.z & 0x7fff7fffu, o.w & 0x7fff7fffu)));
            PIN(um);
            if (aw == 1) {
                const float lr = lo2f(lru), li = lo2f(liu);
                if (kt == 9) { dt = bfr(expf(lo2f(ldu))); er = expf(dt * lr); PIN(dt); PIN(er); }
                if (kt == 10) { float sn, cs; sincosf(dt * li, &sn, &cs); lam = make_float2(er * cs, er * sn); PIN(lam.x); PIN(lam.y); }
                if (kt == 11) {
                    const float nr = lam.x - 1.0f, ni = lam.y, den = lr * lr + li * li;
                    const float2 coef = make_float2((nr * lr + ni * li) / den, (ni * lr - nr * li) / den);
                    float2 l16 = lam;
#pragma unroll
                    for (int i = 0; i < 4; ++i) l16 = cmul(l16, l16);
                    float2 l128 = l16;
#pragma unroll
                    for (int i = 0; i < 3; ++i) l128 = cmul(l128, l128);
                    lst<f32x4>(smem + A_PRM + lane * 32, (f32x4){lam.x, lam.y, coef.x, coef.y});
                    lst<f32x4>(smem + A_PRM + lane * 32 + 16, (f32x4){l16.x, l16.y, l128.x, l128.y});
                }
            }
            st128<SC1>(o, rs, c * 65536 + (q & 1) * 32 + (q >> 1) * 16 + OFF_U + (aw * 64 + j1) * 16384 + lt * 1024 + pt * 64);
            w_store(rs, gb >> 2, gb & 3, aw, lane, wo, j1, j1 + 1);
        }
        if (kt == 12) {
            if (gb < 4) {
                mx = pkmax(mx, sp2 & 0x7fffu);
#pragma unroll
                for (int r = 0; r < 4; ++r) w_row(r, aw, lane, R2[r], I2[r], sp2, wo, mx);
                w_store(rs, 64, gb & 3, aw, lane, wo, 0, 4);
            }
            mx = wave_pkmax(__builtin_amdgcn_perm(pkmax(um, um >> 16), pkmax(mx, mx >> 16), 0x05040100u));
            if (lane == 0) lst<unsigned>(smem + A_FLG + 16 + aw * 4, mx);
            LDS_WAIT();
        }
        SCHED();
    }
    return gsum;
}

DEV void ka_mid(const u16* __restrict__ Cre, const u16* __restrict__ Cim, char* ws, lptr smem, int gb, int w, int lane, float gsum TSARG) {
    int lt, pt; tile_of(gb, lt, pt);
    const int c = lane & 15, q = lane >> 4, mp = gb >> 2, j1 = gb & 3, rot = (lt + pt) & 15;
    const bool spc = mp == 0 && !(j1 & 1), xtr = gb == 0, ld = w >= 4;
    const int k1 = mp ? mp : spc ? (w >> 2) * 64 : (j1 == 1 ? 0 : 64);
    const int ja0 = spc ? (2 * w) & 7 : 2 * w;
    const rsrc_t rs = mkrsrc(ws, WS_END);
    int gi; unsigned gaddr, gwaddr; gate_addr(smem, pt, w, lane, gi, gaddr, gwaddr);
    float cf = (float)c;
    CM gf[2], gi_[2];
    u32x2 tag;
#pragma unroll
    for (int kt = 13; kt < 16; ++kt) {
        if (ld) { if (kt == 13) wait_vm<16>(); else if (kt == 14) wait_vm<12>(); else wait_vm<4>(); }
        else if (kt == 14) wait_vm<0>();
        TS(8 + kt);
        BAR();
        TS(28 + kt);
        if (ld) {
            if (kt == 13) {
                const rsrc_t rc = mkrsrc((gb & 8) ? Cim : Cre, D_ * D_ * 2);
#pragma unroll
                for (int i = 0; i < 4; ++i)
                    blds16(rc, smem + (w - 4) * 4096 + i * 1024, q * 2048 + (c ^ (4 * q)) * 16, (256 * (w - 4) + (gb & ~15) + 4 * i) * 2048 + (gb & 7) * 256);
            }
        } else gate_step(gsum, gaddr + (kt & 3) * A_STAGE, gwaddr + ((kt + rot) & 15) * 128);
        if (kt == 13) asm volatile("ds_read_b64 %0, %1\n\ts_waitcnt lgkmcnt(0)" : "=v"(tag) : "v"(ldsa(smem + A_FLG + 32)) : "memory");
        if (kt == 14 && w == 0) {
            const u32x4 m4 = lld<u32x4>(smem + A_FLG + 16);
            const unsigned m = pkmax(pkmax(m4.x, m4.y), pkmax(m4.z, m4.w));
            if (lane == 0) __builtin_amdgcn_raw_buffer_store_b64((u32x2){(tag.x & 0xffff0000u) | ((m >> 15) & 0xff00u) | ((m & 0xffffu) >> 7), tag.y}, rs, 0, OFF_MX + gb * 8, SC1);
        }
        if (kt < 15) {
            PIN(cf);
            const float dt = fmaf(cf, -1.0f / 16.0f, -(float)(j1 + 4 * (ja0 + kt - 13)) * (1.0f / 1024.0f));
            gf[kt - 13] = cmat(dt * (float)(4 * q), dt);
        }
        SCHED();
    }
    TC(61);
    __builtin_amdgcn_s_setprio(0);
    u32x4 fl[2];
    unsigned foff = lane * 16; PIN(foff);
#pragma unroll
    for (int i = 0; i < 2; ++i) fl[i] = __builtin_amdgcn_raw_buffer_load_b128(rs, foff + i * 1024, OFF_MX, SC1);
    SCHED();
    PIN(cf);
#pragma unroll
    for (int i = 0; i < 2; ++i) gi_[i] = cmat(cf * (float)(j1 + 4 * (ja0 + i) + 256 * q) * (1.0f / 1024.0f), cf * (1.0f / 16.0f));
    TS(1);
    int spin = 0;
#pragma unroll 1
    for (; spin < (1 << 20); ++spin) {
        bool ok = true;
#pragma unroll
        for (int i = 0; i < 2; ++i) ok = ok & ((fl[i].x ^ tag.x) < 0x10000u) & (fl[i].y == tag.y) & ((fl[i].z ^ tag.x) < 0x10000u) & (fl[i].w == tag.y);
        if (__all(ok)) break;
        PIN(foff);
#pragma unroll
        for (int i = 0; i < 2; ++i) fl[i] = __builtin_amdgcn_raw_buffer_load_b128(rs, foff + i * 1024, OFF_MX, SC1);
    }
    asm volatile("" ::: "memory");
    const unsigned p6 = PT();
#pragma unroll
    for (int i = 0; i < 2; ++i) blds16<SC1>(rs, smem + A_UT + (2 * w + i) * 1024, lane * 16, OFF_U + gb * 16384 + (2 * w + i) * 1024);
    u32x4 wv[3];
#pragma unroll
    for (int i = 0; i < 2; ++i) wv[i] = __builtin_amdgcn_raw_buffer_load_b128(rs, lane * 16, OFF_W + (k1 * 4 + j1) * 16384 + (ja0 + i) * 1024, SC1);
    wv[2] = (u32x4){0, 0, 0, 0};
    if (xtr) wv[2] = __builtin_amdgcn_raw_buffer_load_b128(rs, lane * 16, OFF_W + (k1 * 4) * 16384 + 8 * 1024, SC1);
    SCHED();
    int eu, ew;
    {
        constexpr unsigned SP = 0x0c010c00u;
        const unsigned m = __builtin_amdgcn_readfirstlane(wave_pkmax(pkmax(pkmax(__builtin_amdgcn_perm(0u, fl[0].x, SP), __builtin_amdgcn_perm(0u, fl[0].z, SP)),
                                                                           pkmax(__builtin_amdgcn_perm(0u, fl[1].x, SP), __builtin_amdgcn_perm(0u, fl[1].z, SP)))));
        eu = min(max((int)(m >> 16), 4), 254); ew = min((int)(m & 0xffffu), 245);
    }
    const float su = __uint_as_float((unsigned)(258 - eu) << 23), sw = __uint_as_float((unsigned)(246 - ew) << 23);
    u32x4 mt[10];
    asm volatile("ds_read_b128 %0, %10\n\tds_read_b128 %1, %10 offset:1024\n\tds_read_b128 %2, %10 offset:2048\n\tds_read_b128 %3, %10 offset:3072\n\t"
                 "ds_read_b128 %4, %10 offset:4096\n\tds_read_b128 %5, %10 offset:5120\n\tds_read_b128 %6, %10 offset:6144\n\tds_read_b128 %7, %10 offset:7168\n\t"
                 "ds_read_b128 %8, %10 offset:8192\n\tds_read_b128 %9, %10 offset:9216\n\ts_waitcnt lgkmcnt(0)"
                 : "=&v"(mt[0]), "=&v"(mt[1]), "=&v"(mt[2]), "=&v"(mt[3]), "=&v"(mt[4]), "=&v"(mt[5]), "=&v"(mt[6]), "=&v"(mt[7]), "=&v"(mt[8]), "=&v"(mt[9])
                 : "v"(ldsa(smem + A_MAT) + lane * 16) : "memory");
    if (spc && ld) { const CM a = mat_sf(64, c, q), b = mat_si(64, c, q); mt[4] = a.re; mt[5] = a.im; mt[6] = b.re; mt[7] = b.im; }
    u32x4 cv[4];
    if (ld) {
        const unsigned ca = ldsa(smem) + (w - 4) * 4096 + q * 1024 + (lane & 3) * 256 + ((lane >> 2) & 3) * 16;
        asm volatile("ds_read_b128 %0, %4\n\tds_read_b128 %1, %5\n\tds_read_b128 %2, %6\n\tds_read_b128 %3, %7\n\ts_waitcnt lgkmcnt(0)"
                     : "=&v"(cv[0]), "=&v"(cv[1]), "=&v"(cv[2]), "=&v"(cv[3])
                     : "v"(ca + (lane & 3) * 64), "v"(ca + ((lane & 3) ^ 1) * 64), "v"(ca + ((lane & 3) ^ 2) * 64), "v"(ca + ((lane & 3) ^ 3) * 64) : "memory");
    } else {
        gsum = half_sum(gsum);
        const float g = bfr(1.0f / (1.0f + expf(-bfr(gsum))));
        cv[0].x = __float_as_uint(g); cv[0].y = __float_as_uint(bfr(1.0f - g));
        PIN(cv[0]);
    }
    SCHED();
    PIN(wv[0]); PIN(wv[1]); PIN(wv[2]);
    TS(3);
    const unsigned cnt = ldsa(smem + A_FLG) + 8;
    {
        u32x2 ua[4];
        asm volatile("ds_read_b64_tr_b16 %0, %4\n\tds_read_b64_tr_b16 %1, %4 offset:32\n\tds_read_b64_tr_b16 %2, %4 offset:1024\n\tds_read_b64_tr_b16 %3, %4 offset:1056\n\t"
                     "s_waitcnt lgkmcnt(0)" : "=&v"(ua[0]), "=&v"(ua[1]), "=&v"(ua[2]), "=&v"(ua[3])
                     : "v"(ldsa(smem + A_UT) + 2 * w * 1024 + (lane >> 2) * 64 + (lane & 3) * 8) : "memory");
        const unsigned ea = ldsa(smem + A_E1) + c * E1P + q * 256 + 2 * w * 16;
#pragma unroll
        for (int i = 0; i < 2; ++i) {
            const u32x4 u = {ua[2 * i].x, ua[2 * i].y, ua[2 * i + 1].x, ua[2 * i + 1].y};
            f32x4 r = mfb(u, mt[2], Z4), m = mfb(u, mt[3], Z4);
            r = mfb(u, mt[0], r); m = mfb(u, mt[1], m);
            asm volatile("ds_write_b128 %0, %1 offset:%2" :: "v"(ea), "v"(pk8(r * su, m * su)), "i"(i * 16) : "memory");
            if (xtr) {
                const unsigned sg = 0xbf803f80u;
                const f32x4 e = mfb(u, c == 0 ? (u32x4){sg, sg, 0, 0} : (u32x4){0, 0, sg, sg}, Z4);
                if (c < 2) asm volatile("ds_write_b128 %0, %1 offset:%2" :: "v"(ea), "v"(pk8(e * su, Z4)), "i"(16 * E1P + i * 16) : "memory");
            }
        }
    }
    if (lane == 0) lds_add(cnt);
    if (ld) {
#pragma unroll
        for (int j = 0; j < 4; ++j) st128<WT>(cv[j], rs, OFF_C + ((gb * 4 + j) * 4 + w - 4) * 1024 + lane * 16);
    } else {
        const int tau = (gi & 64) | ((gi & 12) << 2) | ((gi & 48) >> 2) | (gi & 3);
        if ((lane & 31) == 0) __builtin_amdgcn_raw_buffer_store_b64((u32x2){cv[0].x, cv[0].y}, rs, (tau & 15) * 1024 + (lt * 8 + (tau >> 4)) * 8, OFF_G, 0);
        if (xtr && w == 0 && lane == 0) {
            __builtin_amdgcn_raw_buffer_store_b64(tag, rs, 0, OFF_E, SC1);
            __builtin_amdgcn_raw_buffer_store_b32(eu + ew - 267, rs, 0, OFF_E + 128, 0);
        }
    }
    wv[0] = wcvt(wv[0], sw); wv[1] = wcvt(wv[1], sw);
    TS(4);
#pragma unroll 1
    for (int sp_ = 0; sp_ < (1 << 24); ++sp_) { if (lds_peek(cnt) >= 8u) break; }
    u32x4 dv[2];
#pragma unroll
    for (int i = 0; i < 2; ++i) dv[i] = lld<u32x4>(smem + A_E1 + (2 * w + i) * E1P + lane * 16);
#pragma unroll
    for (int i = 0; i < 2; ++i) dv[i] = pk8(mfh(dv[i], gf[i].re, Z4), mfh(dv[i], gf[i].im, Z4));
#pragma unroll
    for (int i = 0; i < 2; ++i) dv[i] = wmul(mfh(mt[4], dv[i], Z4), mfh(mt[5], dv[i], Z4), wv[i]);
#pragma unroll
    for (int i = 0; i < 2; ++i) dv[i] = pk8(mfh(dv[i], mt[6], Z4), mfh(dv[i], mt[7], Z4));
    {
        f32x4 r[2], m[2];
#pragma unroll
        for (int i = 0; i < 2; ++i) { r[i] = mfh(dv[i], gi_[i].re, Z4); m[i] = mfh(dv[i], gi_[i].im, Z4); }
#pragma unroll
        for (int e = 0; e < 4; ++e)
            lst<u32x2>(smem + A_E2 + (4 * q + e) * 1024 + (w >> 1) * 256 + c * 16 + (w & 1) * 8, (u32x2){pkh(r[0][e], r[1][e]), pkh(m[0][e], m[1][e])});
    }
    if (xtr && !(w & 3)) {
        const float dt = -(float)(32 + 64 * c) * (1.0f / 1024.0f);
        const CM f = cmat(dt * (float)(4 * q), dt), b = cmat(cf * (float)(32 + 256 * q) * (1.0f / 1024.0f), cf * (1.0f / 16.0f));
        u32x4 d = lld<u32x4>(smem + A_E1 + (16 + (w >> 2)) * E1P + lane * 16);
        d = pk8(mfh(d, f.re, Z4), mfh(d, f.im, Z4));
        d = wmul(mfh(mt[4], d, Z4), mfh(mt[5], d, Z4), wcvt(wv[2], sw));
        d = pk8(mfh(d, mt[6], Z4), mfh(d, mt[7], Z4));
        const f32x4 r = mfh(d, b.re, Z4);
#pragma unroll
        for (int e = 0; e < 4; ++e) lst<float>(smem + A_EX + ((w >> 2) * 256 + (4 * q + e) * 16 + c) * 4, r[e]);
    }
    TS(5);
    aux_sync(cnt, 16u, lane);
    {
        f32x4 xa[2], xb[2];
#pragma unroll
        for (int i = 0; i < 2; ++i) { xa[i] = Z4; xb[i] = Z4; }
        if (xtr) {
            const float sg = (c & 1) ? -0.125f : 0.125f;
#pragma unroll
            for (int i = 0; i < 2; ++i) {
                xa[i] = lld<f32x4>(smem + A_EX + (2 * w + i) * 64 + q * 16) * sg;
                xb[i] = lld<f32x4>(smem + A_EX + 1024 + (2 * w + i) * 64 + q * 16) * -sg;
            }
        }
#pragma unroll
        for (int i = 0; i < 2; ++i) {
            const int s2 = 2 * w + i;
            const u32x4 ea = lld<u32x4>(smem + A_E2 + s2 * 1024 + lane * 16);
            const f32x4 ta = mfh(ea, mt[8], xa[i]), tb = mfh(ea, mt[9], xb[i]);
            st128<WT>(to16(pkh(ta[0], ta[1]), pkh(ta[2], ta[3]), pkh(tb[0], tb[1]), pkh(tb[2], tb[3])), rs,
                      c * 16384 + (q & 1) * 32 + (q >> 1) * 16 + OFF_T + s2 * 262144 + j1 * 4096 + mp * 64);
        }
    }
    TS(7);
    wait_vm<0>();
    { const unsigned p2 = PT(); if (w == 7) { pput(ws, 2, p2); pput(ws, 6, p6); } }
    TSE(w ? 1 : 5, (w & 3) == 0);
}

NOPK __global__ __launch_bounds__(768, 1) void ka_kernel(const u16* __restrict__ x, const u16* __restrict__ w_r, const u16* __restrict__ w_i,
                                                         const u16* __restrict__ Bre, const u16* __restrict__ Bim, const u16* __restrict__ Cre,
                                                         const u16* __restrict__ Cim, const u16* __restrict__ lam_re,
                                                         const u16* __restrict__ lam_im, const u16* __restrict__ log_dt,
                                                         const u16* __restrict__ gate_w, char* ws) {
    extern __shared__ __attribute__((aligned(16))) char smem_[];
    lptr smem = (lptr)smem_;
    const int wid = __builtin_amdgcn_readfirstlane(threadIdx.x >> 6), lane = threadIdx.x & 63;
    if (wid < 4) ka_mma(ws, smem, blockIdx.x, wid, lane);
    else {
        TSD();
        float gsum = 0.f;
        if (wid < 8) ka_load(x, Bre, Bim, smem, blockIdx.x, wid - 4, lane TSPASS);
        else gsum = ka_aux(w_r, w_i, lam_re, lam_im, log_dt, gate_w, ws, smem, blockIdx.x, wid - 8, lane TSPASS);
        int w = wid & 7;
        asm volatile("" : "+s"(w));
        ka_mid(Cre, Cim, ws, smem, blockIdx.x, w, lane, gsum TSPASS);
    }
}

constexpr int B_UT = 0, B_R0 = B_UT + 16384, B_CNT = B_R0 + 32768, B_DMP = B_CNT + 64, KB_SMEM = B_DMP + 12 * 8192;

DEV void kb_mma(char* ws, float* __restrict__ out, lptr smem, int gb, int wid, int lane) {
    int s2, da; tile_of(gb, s2, da);
    const int c = lane & 15, q = lane >> 4;
    const rsrc_t rs = mkrsrc(ws, WS_END);
    TSD(); TS(0);
    unsigned tkb = PT(), tk1 = 0;
    const unsigned vo = (c >> 2) * 256 + q * 64 + (c & 3) * 16;
    const unsigned sC = OFF_C + (da * 64 + 16 * wid) * 4096, sH = OFF_H + (s2 * 64 + 16 * wid) * 8192;
    auto ldA = [&](int i, int m) { return __builtin_amdgcn_raw_buffer_load_b128(rs, vo + m * 1024, sC + i * 4096, 0); };
    auto ldB = [&](int i, int n) { return __builtin_amdgcn_raw_buffer_load_b128(rs, vo + (n & 1) * 1024, sH + i * 8192 + (((n >> 1) + wid + 1) & 3) * 2048, 0); };
    u32x4 A[3][4], B[2][8];
#pragma unroll
    for (int i = 0; i < 2; ++i) {
#pragma unroll
        for (int m = 0; m < 4; ++m) A[i][m] = ldA(i, m);
#pragma unroll
        for (int n = 0; n < 8; ++n) B[i][n] = ldB(i, n);
        SCHED();
        if (i == 0) BAR();
    }
    f32x4 acc[4][8];
#pragma unroll
    for (int m = 0; m < 4; ++m)
#pragma unroll
        for (int n = 0; n < 8; ++n) acc[m][n] = Z4;
    float omg[2];
#pragma unroll
    for (int i = 0; i < 16; ++i) {
        if (i + 2 < 16) {
#pragma unroll
            for (int m = 0; m < 4; ++m) A[(i + 2) % 3][m] = ldA(i + 2, m);
        }
        if (i == 14) {
#pragma unroll
            for (int n = 0; n < 2; ++n) omg[n] = __uint_as_float(__builtin_amdgcn_raw_buffer_load_b32(rs, c * 8 + n * 128 + 4, OFF_G + s2 * 1024 + wid * 256, 0));
        }
        SCHED();
#pragma unroll
        for (int n = 0; n < 8; ++n) {
#pragma unroll
            for (int m = 0; m < 4; ++m) acc[m][n] = mfb(A[i % 3][m], B[i & 1][n], acc[m][n]);
            if (i + 2 < 16) B[i & 1][n] = ldB(i + 2, n);
            if (i == 15 && n >= 1 && n < 7) {
                const int j = (n - 1) >> 1;
#pragma unroll
                for (int m = 0; m < 4; ++m)
                    lst<f32x4>(smem + B_DMP + (((j + wid + 1) & 3) * 3 + j) * 8192 + (((n - 1) & 1) * 4 + m) * 1024 + lane * 16, acc[m][n - 1]);
            }
            SCHED();
        }
        if (i == 0) tk1 = PT();
        if (i == 7) TS(5);
        if (i == 14) { TS(6); TC(61); }
    }
    const unsigned tk2 = PT();
    const rsrc_t ro = mkrsrc(out, S_ * D_ * 4);
    LDS_WAIT();
    BAR();
    TS(2);
#pragma unroll
    for (int n = 0; n < 2; ++n) {
        const int row = 16 * (32 * wid + 16 * n + c) + s2;
        const float o2 = 2.0f * omg[n];
#pragma unroll
        for (int m = 0; m < 4; ++m) {
            f32x4 a = acc[m][6 + n];
#pragma unroll
            for (int j = 0; j < 3; ++j) a += lld<f32x4>(smem + B_DMP + (wid * 3 + j) * 8192 + (n * 4 + m) * 1024 + lane * 16);
            const f32x4 r0 = lld<f32x4>(smem + B_R0 + ((wid * 2 + n) * 4 + m) * 1024 + lane * 16);
            const f32x4 o = {fmaf(o2, a[0], r0[0]), fmaf(o2, a[1], r0[1]), fmaf(o2, a[2], r0[2]), fmaf(o2, a[3], r0[3])};
            st128<WTO>(__builtin_bit_cast(u32x4, o), ro, (row * D_ + 256 * m + 16 * da + 4 * q) * 4);
        }
    }
    TS(3);
    wait_vm<0>();
    if (PROBE == 100) {
        const unsigned c0 = (unsigned)__builtin_amdgcn_s_memtime();
        while ((unsigned)__builtin_amdgcn_s_memtime() - c0 < 40000u) __builtin_amdgcn_s_sleep(1);
    } else {
        unsigned ref = PROBE == 51 ? tk1 : PROBE == 52 ? tk2 : PROBE == 53 ? PT() : tkb;
        if (PROBE < 50) {
            const u32x4 v = reinterpret_cast<const u32x4*>(ws + OFF_P + (PROBE % 10) * 1024)[lane];
            int d = PROBE < 10 ? max(max((int)(tkb - v.x), (int)(tkb - v.y)), max((int)(tkb - v.z), (int)(tkb - v.w)))
                               : min(min((int)(tkb - v.x), (int)(tkb - v.y)), min((int)(tkb - v.z), (int)(tkb - v.w)));
            for (int o = 1; o < 64; o <<= 1) { const int e = __shfl_xor(d, o); d = PROBE < 10 ? max(d, e) : min(d, e); }
            ref = tkb - __builtin_amdgcn_readfirstlane(d);
        }
        while ((int)(PT() - ref) < 6000) __builtin_amdgcn_s_sleep(1);
    }
    TSE(2, wid == 0);
}

#define PB_HI 3
#define PB_LO 0
#define PHI() __builtin_amdgcn_s_setprio(PB_HI)
#define PLO() __builtin_amdgcn_s_setprio(PB_LO)
#define LD128 __builtin_amdgcn_raw_buffer_load_b128
#define LD64 __builtin_amdgcn_raw_buffer_load_b64
#define LD32 __builtin_amdgcn_raw_buffer_load_b32
#define BLDS blds16
#define STT st128<SC1>
#define KSLP 8
DEV void kb_aux(const u16* __restrict__ Dskip, char* ws, lptr smem, int gb, int w, int lane) {
    int s2t, dat; tile_of(gb, s2t, dat);
    const int c = lane & 15, q = lane >> 4;
    const rsrc_t rs = mkrsrc(ws, WS_END), rd = mkrsrc(Dskip, D_ * 2);
    const unsigned cnt = ldsa(smem + B_CNT);
    TSD(); TS(0);
    if (w == 0) asm volatile("ds_write_b32 %0, %1\n\ts_waitcnt lgkmcnt(0)" :: "v"(cnt), "v"(0u) : "memory");
    BAR();
    u32x4 fc[2][2], fs[2][2];
#pragma unroll
    for (int n = 0; n < 2; ++n) {
        float dt = (float)(32 * w + 16 * n + c) * (1.0f / 128.0f); PIN(dt);
        float cc[8], ss[8];
        const float t0 = __builtin_amdgcn_fractf(dt * (float)(4 * q)), t1 = __builtin_amdgcn_fractf(dt * (float)(16 + 4 * q));
#pragma unroll
        for (int e = 0; e < 8; ++e) { const float f = fmaf((float)(e & 3), dt, e < 4 ? t0 : t1); cc[e] = __builtin_amdgcn_cosf(f); ss[e] = __builtin_amdgcn_sinf(f); }
        fc[n][0] = (u32x4){pkh(cc[0], cc[1]), pkh(cc[2], cc[3]), pkh(cc[4], cc[5]), pkh(cc[6], cc[7])};
        fs[n][0] = (u32x4){pkh(ss[0], ss[1]), pkh(ss[2], ss[3]), pkh(ss[4], ss[5]), pkh(ss[6], ss[7])};
        fc[n][1] = ((c & 1) ? fs[n][0] : fc[n][0]) ^ (((c + 1) & 2) ? 0x80008000u : 0u);
        fs[n][1] = ((c & 1) ? fc[n][0] : fs[n][0]) ^ ((c & 2) ? 0x80008000u : 0u);
        PIN(fc[n][0]); PIN(fs[n][0]); PIN(fc[n][1]); PIN(fs[n][1]);
    }
    TS(1);
    u32x2 gg[2], dk[4];
    u32x4 xv[4];
    PHI();
#pragma unroll
    for (int n = 0; n < 2; ++n) gg[n] = LD64(rs, c * 8 + n * 128, OFF_G + s2t * 1024 + w * 256, 0);
#pragma unroll
    for (int i = 0; i < 4; ++i) dk[i] = LD64(rd, q * 8 + i * 512, dat * 32, 0);
    int fe = LD32(rs, 0, OFF_E + 128, 0);
#pragma unroll
    for (int i = 0; i < 4; ++i) {
        __builtin_amdgcn_s_sleep(KSLP);
        xv[i] = LD128(rs, lane * 16 + i * 1024, OFF_X + (s2t * 16 + dat) * 16384 + w * 4096, 0);
        SCHED();
    }
#pragma unroll
    for (int i = 0; i < 4; ++i) {
        __builtin_amdgcn_s_sleep(KSLP);
        BLDS(rs, smem + B_UT + w * 4096 + i * 1024, (lane >> 1) * 64 + (lane & 1) * 16, OFF_T + (s2t * 16 + dat) * 16384 + w * 4096 + (i & 1) * 2048 + (i >> 1) * 32);
        SCHED();
    }
    PLO();
    TS(2);
    f32x4 r0[2][4];
#pragma unroll
    for (int i = 0; i < 4; ++i) PIN(dk[i]);
#pragma unroll
    for (int i = 0; i < 4; ++i) {
        PIN(xv[i]);
#pragma unroll
        for (int p = 0; p < 2; ++p) {
            const int m = 2 * (i & 1) + p;
            const unsigned x0 = xv[i][2 * p], x1 = xv[i][2 * p + 1];
            const unsigned p0 = pkb(lo2f(x0) * lo2f(dk[m].x), hi2f(x0) * hi2f(dk[m].x)), p1 = pkb(lo2f(x1) * lo2f(dk[m].y), hi2f(x1) * hi2f(dk[m].y));
            r0[i >> 1][m] = (f32x4){lo2f(p0), hi2f(p0), lo2f(p1), hi2f(p1)} * __uint_as_float(gg[i >> 1].y);
            PIN(r0[i >> 1][m]);
        }
    }
    TS(5);
    __builtin_amdgcn_s_waitcnt(0x0F70);
    TS(6);
    aux_sync(cnt, 4u, lane);
    TS(7);
    f32x4 P0[2], P1[2], P2[2], P3a[2], P3b[2];
#pragma unroll
    for (int n = 0; n < 2; ++n) { P0[n] = Z4; P1[n] = Z4; P2[n] = Z4; P3a[n] = Z4; P3b[n] = Z4; }
    lptr tt = smem + B_UT + lane * 8;
#pragma unroll
    for (int h = 0; h < 2; ++h) {
        const u32x4 a1 = trfrag(tt + 1 * 4096 + h * 1024, 512), b1 = trfrag(tt + 1 * 4096 + 2048 + h * 1024, 512);
        const u32x4 a3 = trfrag(tt + 3 * 4096 + h * 1024, 512), b3 = trfrag(tt + 3 * 4096 + 2048 + h * 1024, 512);
        const u32x4 a0 = trfrag(tt + h * 1024, 512), b0 = trfrag(tt + 2048 + h * 1024, 512);
        const u32x4 a2 = trfrag(tt + 2 * 4096 + h * 1024, 512), b2 = trfrag(tt + 2 * 4096 + 2048 + h * 1024, 512);
#pragma unroll
        for (int n = 0; n < 2; ++n) {
            u32x4 cw = fc[n][h], sw_ = fs[n][h];
            P1[n] = mfh(a1, cw, P1[n]); P3a[n] = mfh(a1, sw_, P3a[n]); P3b[n] = mfh(b1, cw, P3b[n]); P1[n] = mfh(b1, sw_, P1[n]);
            if (h == 0) cw.x = q == 0 ? (cw.x & 0xffff0000u) | ((c & 1) ? 0xbc00u : 0x3c00u) : cw.x;
            P3a[n] = mfh(b3, cw, P3a[n]); P3b[n] = mfh(a3, sw_, P3b[n]); P1[n] = mfh(a3, cw, P1[n]); P1[n] = mfh(b3, sw_, P1[n]);
            if (h == 0) { cw.x = q == 0 ? (cw.x & 0xffff0000u) | 0x3800u : cw.x; sw_.x = q == 0 ? (sw_.x & 0xffff0000u) | ((c & 1) ? 0x3800u : 0xb800u) : sw_.x; }
            P0[n] = mfh(a0, cw, P0[n]); P2[n] = mfh(a2, cw, P2[n]); P0[n] = mfh(b0, sw_, P0[n]); P2[n] = mfh(b2, sw_, P2[n]);
        }
    }
#pragma unroll
    for (int n = 0; n < 2; ++n) {
        const float g = __uint_as_float(gg[n].x);
        const f32x4 sp = P0[n] + P2[n], sm = P0[n] - P2[n], p3 = P3a[n] - P3b[n];
        const f32x4 av[4] = {sp + P1[n], sm - p3, sp - P1[n], sm + p3};
#pragma unroll
        for (int m = 0; m < 4; ++m) {
            const unsigned b0 = pkb(ldexpf(av[m][0], fe), ldexpf(av[m][1], fe)), b1 = pkb(ldexpf(av[m][2], fe), ldexpf(av[m][3], fe));
            const unsigned o0 = pkb(g * lo2f(b0), g * hi2f(b0)), o1 = pkb(g * lo2f(b1), g * hi2f(b1));
            lst<f32x4>(smem + B_R0 + ((w * 2 + n) * 4 + m) * 1024 + lane * 16, r0[n][m] + (f32x4){lo2f(o0), hi2f(o0), lo2f(o1), hi2f(o1)});
        }
    }
    LDS_WAIT();
    TS(8);
    TSE(3, w == 0);
    BAR();
}

NOPK __global__ __launch_bounds__(512, 1) void kb_kernel(const u16* __restrict__ Dskip, char* ws, float* __restrict__ out) {
    extern __shared__ __attribute__((aligned(16))) char smem_[];
    lptr smem = (lptr)smem_;
    const int wid = __builtin_amdgcn_readfirstlane(threadIdx.x >> 6), lane = threadIdx.x & 63;
    if (wid < 4) kb_mma(ws, out, smem, blockIdx.x, wid, lane);
    else kb_aux(Dskip, ws, smem, blockIdx.x, wid - 4, lane);
}

extern "C" void kernel_launch(void* const* d_in, const int* in_sizes, int n_in, void* d_out, int out_size, void* d_ws, size_t ws_size,
                              hipStream_t stream) {
    const u16* x = (const u16*)d_in[0];
    const u16* w_r = (const u16*)d_in[1];
    const u16* w_i = (const u16*)d_in[2];
    const u16* lam_re = (const u16*)d_in[4];
    const u16* lam_im = (const u16*)d_in[5];
    const u16* Bre = (const u16*)d_in[6];
    const u16* Bim = (const u16*)d_in[7];
    const u16* Cre = (const u16*)d_in[8];
    const u16* Cim = (const u16*)d_in[9];
    const u16* log_dt = (const u16*)d_in[10];
    const u16* Dskip = (const u16*)d_in[11];
    const u16* gate_w = (const u16*)d_in[12];
    char* ws = (char*)d_ws;
    (void)hipFuncSetAttribute((const void*)ka_kernel, hipFuncAttributeMaxDynamicSharedMemorySize, KA_SMEM);
    (void)hipFuncSetAttribute((const void*)kb_kernel, hipFuncAttributeMaxDynamicSharedMemorySize, KB_SMEM);
    ka_kernel<<<256, 768, KA_SMEM, stream>>>(x, w_r, w_i, Bre, Bim, Cre, Cim, lam_re, lam_im, log_dt, gate_w, ws);
    kb_kernel<<<256, 512, KB_SMEM, stream>>>(Dskip, ws, (float*)d_out);
}
```

```cpp
#include <hip/hip_runtime.h>
#include <stdint.h>

typedef __attribute__((ext_vector_type(8))) __bf16 bf16x8;
typedef __attribute__((ext_vector_type(2))) __bf16 bf16x2;
typedef __attribute__((ext_vector_type(8))) _Float16 f16x8;
typedef __attribute__((ext_vector_type(2))) _Float16 f16x2;
typedef __attribute__((ext_vector_type(4))) float f32x4;
typedef __attribute__((ext_vector_type(2))) float f32x2;
typedef __attribute__((ext_vector_type(4))) unsigned u32x4;
typedef __attribute__((ext_vector_type(2))) unsigned u32x2;
typedef __attribute__((ext_vector_type(4))) short s16x4;
typedef __attribute__((ext_vector_type(2))) unsigned short u16x2;
typedef unsigned short u16;
typedef unsigned long long u64;
#define GAS __attribute__((address_space(1)))
#define LAS __attribute__((address_space(3)))
#define NOPK __attribute__((target("no-packed-fp32-ops")))
#define RLX_AGENT __ATOMIC_RELAXED, __HIP_MEMORY_SCOPE_AGENT
#define DEV __device__ __forceinline__

constexpr int S_ = 2048, D_ = 1024, KF = 513;

constexpr unsigned OFF_H = 0;
constexpr unsigned OFF_U = OFF_H + (8u << 20);
constexpr unsigned OFF_T = OFF_U + (4u << 20);
constexpr unsigned OFF_W = OFF_T + (4u << 20);
constexpr unsigned OFF_G = OFF_W + 65u * 65536u;
constexpr unsigned OFF_MX = OFF_G + 16384u;
constexpr unsigned OFF_GRAN = OFF_MX + 4096u;
constexpr unsigned OFF_E = OFF_GRAN + 16u * 1024u * 16u;
constexpr unsigned OFF_X = OFF_E + 16u * 128u;
constexpr unsigned OFF_C = OFF_X + (4u << 20);
constexpr unsigned WS_END = OFF_C + (4u << 20);

#define TSD()
#define TS(i)
#define TC(i)
#define TSE(slot, on)

DEV float bf2f(u16 h) { return __uint_as_float((unsigned)h << 16); }
DEV unsigned pkb(float a, float b) { f32x2 v = {a, b}; return __builtin_bit_cast(unsigned, __builtin_convertvector(v, bf16x2)); }
DEV unsigned pkh(float a, float b) { f32x2 v = {a, b}; return __builtin_bit_cast(unsigned, __builtin_convertvector(v, f16x2)); }
DEV float lo2f(unsigned p) { return __uint_as_float(p << 16); }
DEV float hi2f(unsigned p) { return __uint_as_float(p & 0xffff0000u); }
DEV float bfr(float f) { return lo2f(pkb(f, f)); }
DEV float2 cmul(float2 a, float2 b) { return make_float2(a.x * b.x - a.y * b.y, a.x * b.y + a.y * b.x); }
DEV float2 cfma(float2 a, float2 b, float2 c) { return make_float2(fmaf(a.x, b.x, fmaf(-a.y, b.y, c.x)), fmaf(a.x, b.y, fmaf(a.y, b.x, c.y))); }
DEV f32x4 mfb(u32x4 a, u32x4 b, f32x4 c) { return __builtin_amdgcn_mfma_f32_16x16x32_bf16(__builtin_bit_cast(bf16x8, a), __builtin_bit_cast(bf16x8, b), c, 0, 0, 0); }
DEV f32x4 mfh(u32x4 a, u32x4 b, f32x4 c) { return __builtin_amdgcn_mfma_f32_16x16x32_f16(__builtin_bit_cast(f16x8, a), __builtin_bit_cast(f16x8, b), c, 0, 0, 0); }
DEV unsigned pkmax(unsigned a, unsigned b) { return __builtin_bit_cast(unsigned, __builtin_elementwise_max(__builtin_bit_cast(u16x2, a), __builtin_bit_cast(u16x2, b))); }
DEV float vcos(float r) { return __builtin_amdgcn_cosf(__builtin_amdgcn_fractf(r)); }
DEV float vsin(float r) { return __builtin_amdgcn_sinf(__builtin_amdgcn_fractf(r)); }
typedef LAS char* lptr;
DEV unsigned ldsa(lptr p) { return (unsigned)(uintptr_t)p; }
template <class T> DEV T lld(lptr p) { return *(LAS const T*)p; }
template <class T> DEV void lst(lptr p, T v) { *(LAS T*)p = v; }
DEV u32x2 trread(lptr p) { return __builtin_bit_cast(u32x2, __builtin_amdgcn_ds_read_tr16_b64_v4i16((LAS s16x4*)p)); }
DEV u32x4 trfrag(lptr p, int second) { const u32x2 a = trread(p), b = trread(p + second); return (u32x4){a.x, a.y, b.x, b.y}; }
DEV void glds16(const void* g, lptr l) { __builtin_amdgcn_global_load_lds((const GAS unsigned*)g, (LAS unsigned*)l, 16, 0, 0); }
typedef __amdgpu_buffer_rsrc_t rsrc_t;
template <int AUX = 0> DEV void blds16(rsrc_t rs, lptr l, unsigned vo, unsigned so) { __builtin_amdgcn_raw_ptr_buffer_load_lds(rs, (LAS void*)l, 16, vo, so, 0, AUX); }
DEV u32x4 ldfrag(lptr tile, int row, int chunk) { return lld<u32x4>(tile + row * 128 + ((chunk ^ (row & 7)) << 4)); }
template <int N> DEV void wait_vm() { asm volatile("s_waitcnt vmcnt(%0)" :: "i"(N) : "memory"); }
#define LDS_WAIT() asm volatile("s_waitcnt lgkmcnt(0)" ::: "memory")
#define SCHED() __builtin_amdgcn_sched_barrier(0)
#define BAR() do { __builtin_amdgcn_s_barrier(); asm volatile("" ::: "memory"); } while (0)
#define PIN(x) asm volatile("" : "+v"(x))
DEV rsrc_t mkrsrc(const void* p, unsigned n) { return __builtin_amdgcn_make_buffer_rsrc((void*)p, 0, n, 0x00020000); }
constexpr int SC1 = 16;
#define WT 16
#define WTO 16
template <int AUX> DEV void st128(u32x4 v, rsrc_t rs, unsigned off) { __builtin_amdgcn_raw_buffer_store_b128(v, rs, off, 0, AUX); }
DEV void lds_add(unsigned a) { asm volatile("ds_add_u32 %0, %1" :: "v"(a), "v"(1u) : "memory"); }
DEV unsigned lds_peek(unsigned a) {
    unsigned v;
    asm volatile("ds_read_b32 %0, %1\n\ts_waitcnt lgkmcnt(0)" : "=v"(v) : "v"(a) : "memory");
    return __builtin_amdgcn_readfirstlane(v);
}
DEV void tile_of(int gb, int& ta, int& tb) {
    const int xcd = gb & 7, loc = gb >> 3;
    ta = (xcd >> 1) * 4 + (loc >> 3);
    tb = (xcd & 1) * 8 + (loc & 7);
}
DEV void rows4(float a, float& a0, float& a1, float& a2, float& a3) {
    const unsigned u = __float_as_uint(a);
    const auto r = __builtin_amdgcn_permlane32_swap(u, u, false, false);
    const auto p = __builtin_amdgcn_permlane16_swap(r[0], r[0], false, false);
    const auto s = __builtin_amdgcn_permlane16_swap(r[1], r[1], false, false);
    a0 = __uint_as_float(p[0]); a1 = __uint_as_float(p[1]); a2 = __uint_as_float(s[0]); a3 = __uint_as_float(s[1]);
}
DEV float rowsum4(float a) {
    const unsigned u = __float_as_uint(a);
    const auto r = __builtin_amdgcn_permlane32_swap(u, u, false, false);
    const unsigned t = __float_as_uint(__uint_as_float(r[0]) + __uint_as_float(r[1]));
    const auto p = __builtin_amdgcn_permlane16_swap(t, t, false, false);
    return __uint_as_float(p[0]) + __uint_as_float(p[1]);
}
DEV float2 sel4(int q, float2 a, float2 b, float2 c, float2 d) {
    PIN(d.x); PIN(d.y);
    const float2 e = (q & 1) ? b : a, f = (q & 1) ? d : c;
    return (q & 2) ? f : e;
}
template <int R> DEV unsigned ror16(unsigned v) { return __builtin_amdgcn_update_dpp(0u, v, 0x120 + R, 0xf, 0xf, false); }
DEV unsigned wave_pkmax(unsigned m) {
    m = pkmax(m, ror16<8>(m)); m = pkmax(m, ror16<4>(m)); m = pkmax(m, ror16<2>(m)); m = pkmax(m, ror16<1>(m));
    const auto r = __builtin_amdgcn_permlane32_swap(m, m, false, false);
    m = pkmax(r[0], r[1]);
    const auto p = __builtin_amdgcn_permlane16_swap(m, m, false, false);
    return pkmax(p[0], p[1]);
}
DEV float half_sum(float v) {
    v += __uint_as_float(ror16<8>(__float_as_uint(v))); v += __uint_as_float(ror16<4>(__float_as_uint(v)));
    v += __uint_as_float(ror16<2>(__float_as_uint(v))); v += __uint_as_float(ror16<1>(__float_as_uint(v)));
    const auto p = __builtin_amdgcn_permlane16_swap(__float_as_uint(v), __float_as_uint(v), false, false);
    return __uint_as_float(p[0]) + __uint_as_float(p[1]);
}
DEV u32x4 to16(unsigned p0, unsigned p1, unsigned n0, unsigned n1) {
    const auto a = __builtin_amdgcn_permlane16_swap(p0, n0, false, false);
    const auto b = __builtin_amdgcn_permlane16_swap(p1, n1, false, false);
    return (u32x4){a[0], b[0], a[1], b[1]};
}

#define PRIOA 3
#define FLO 1
#define WSTEP 2
#define WSPL 1
#define XSTEP 4
#define PRIOT 3
#define RDPAT {0, 1, 2, -1, 3, 4, -1, 5, 6, -1, 7, -1, -1, -1, -1, -1}
#define Z4 ((f32x4){0.f, 0.f, 0.f, 0.f})
struct CM { u32x4 re, im; };
DEV CM cmat(float t0, float dt) {
    float cc[4], ss[4];
    t0 = __builtin_amdgcn_fractf(t0);
#pragma unroll
    for (int e = 0; e < 4; ++e) { const float f = fmaf((float)e, dt, t0); cc[e] = __builtin_amdgcn_cosf(f); ss[e] = __builtin_amdgcn_sinf(f); }
    const unsigned c0 = pkh(cc[0], cc[1]), c1 = pkh(cc[2], cc[3]), s0 = pkh(ss[0], ss[1]), s1 = pkh(ss[2], ss[3]);
    CM m; m.re = (u32x4){c0, c1, s0 ^ 0x80008000u, s1 ^ 0x80008000u}; m.im = (u32x4){s0, s1, c0, c1};
    PIN(m.re); PIN(m.im);
    return m;
}
DEV u32x4 pk8(f32x4 r, f32x4 i) { return (u32x4){pkh(r[0], r[1]), pkh(r[2], r[3]), pkh(i[0], i[1]), pkh(i[2], i[3])}; }
DEV u32x4 wmul(f32x4 r, f32x4 i, u32x4 w) {
    float yr[4], yi[4];
#pragma unroll
    for (int e = 0; e < 4; ++e) {
        const unsigned wu = w[e];
        const f16x2 h = __builtin_bit_cast(f16x2, wu);
        yr[e] = fmaf(r[e], (float)h[0], -i[e] * (float)h[1]); yi[e] = fmaf(r[e], (float)h[1], i[e] * (float)h[0]);
    }
    return (u32x4){pkh(yr[0], yr[1]), pkh(yr[2], yr[3]), pkh(yi[0], yi[1]), pkh(yi[2], yi[3])};
}
DEV u32x4 wcvt(u32x4 w, float sw) {
#pragma unroll
    for (int e = 0; e < 4; ++e) w[e] = pkh(lo2f(w[e]) * sw, hi2f(w[e]) * sw);
    PIN(w);
    return w;
}
DEV void aux_sync(unsigned cnt, unsigned target, int lane) {
    asm volatile("" ::: "memory");
    if (lane == 0) lds_add(cnt);
#pragma unroll 1
    for (int spin = 0; spin < (1 << 24); ++spin) { if (lds_peek(cnt) >= target) break; }
}

constexpr int A_STAGE = 32768, A_XT = 4 * A_STAGE, A_PRM = A_XT + 16384, A_AGG = A_PRM + 2048, A_FLG = A_AGG + 512, A_GW = A_FLG + 64,
              A_MAT = A_GW + 2048, KA_SMEM = A_MAT + 10240;
constexpr int A_UT = 16384, A_E1 = 2 * A_STAGE, E1P = 1040, A_EX = A_E1 + 18 * E1P + 32, A_E2 = A_XT;

DEV void ka_mma(char* ws, lptr smem, int gb, int wid, int lane) {
    int lt, pt; tile_of(gb, lt, pt);
    const int wr = wid >> 1, wc = wid & 1, c = lane & 15, q = lane >> 4;
    const rsrc_t rs = mkrsrc(ws, WS_END);
    TSD(); TS(0);
    u64 tag = __hip_atomic_load((GAS u64*)(ws + OFF_E), __ATOMIC_RELAXED, __HIP_MEMORY_SCOPE_WORKGROUP) + 0x9E3779B97F4A7C15ull;
    if (wid == 0 && lane < 8) lst<unsigned>(smem + A_FLG + lane * 4, 0u);
    f32x4 acc[4][4];
#pragma unroll
    for (int m = 0; m < 4; ++m)
#pragma unroll
        for (int n = 0; n < 4; ++n) acc[m][n] = Z4;
    u32x4 af[2][4], bf_[2][4];
#pragma unroll
    for (int m = 0; m < 4; ++m) { af[1][m] = (u32x4){0, 0, 0, 0}; bf_[1][m] = af[1][m]; }
    PIN(tag);
    if (wid == 0 && lane == 0) lst<u64>(smem + A_FLG + 32, tag);
    tag = (u64)(unsigned)__builtin_amdgcn_readfirstlane((unsigned)tag) | (u64)(unsigned)__builtin_amdgcn_readfirstlane((unsigned)(tag >> 32)) << 32;
    LDS_WAIT();
#pragma unroll 4
    for (int kt = 0; kt < 16; ++kt) {
        BAR();
        lptr tA = smem + (kt & 3) * A_STAGE;
        lptr tB = tA + 16384;
#pragma unroll
        for (int hf = 0; hf < 2; ++hf)
#pragma unroll
            for (int i = 0; i < 16; ++i) {
                acc[i >> 2][i & 3] = mfb(af[1 - hf][i >> 2], bf_[1 - hf][i & 3], acc[i >> 2][i & 3]);
                constexpr int RD[16] = RDPAT;
                const int r = RD[i];
                if (r == 0) af[hf][0] = ldfrag(tA, wr * 64 + c, 4 * hf + q);
                else if (r > 0 && r < 5) bf_[hf][r - 1] = ldfrag(tB, wc * 64 + (r - 1) * 16 + c, 4 * hf + q);
                else if (r >= 5) af[hf][r - 4] = ldfrag(tA, wr * 64 + (r - 4) * 16 + c, 4 * hf + q);
                SCHED();
            }
        LDS_WAIT();
    }
    __builtin_amdgcn_s_setprio(PRIOT);
    float2 lam[2], coef[2], l16[2], l128[2];
#pragma unroll
    for (int n = 0; n < 2; ++n) {
        const f32x4 a = lld<f32x4>(smem + A_PRM + (wc * 32 + 2 * c + n) * 32), b = lld<f32x4>(smem + A_PRM + (wc * 32 + 2 * c + n) * 32 + 16);
        lam[n] = make_float2(a[0], a[1]); coef[n] = make_float2(a[2], a[3]); l16[n] = make_float2(b[0], b[1]); l128[n] = make_float2(b[2], b[3]);
    }
#pragma unroll
    for (int m = 0; m < 4; ++m)
#pragma unroll
        for (int n = 0; n < 4; ++n) acc[m][n] = mfb(af[1][m], bf_[1][n], acc[m][n]);
    float2 l32[2], l64[2];
#pragma unroll
    for (int n = 0; n < 2; ++n) { l32[n] = cmul(l16[n], l16[n]); l64[n] = cmul(l32[n], l32[n]); }
    TS(1);
    float2 h[2][16];
#pragma unroll
    for (int n = 0; n < 2; ++n) h[n][0] = make_float2(acc[0][n][0], acc[0][2 + n][0]);
#pragma unroll
    for (int t = 1; t < 16; ++t)
#pragma unroll
        for (int n = 0; n < 2; ++n) h[n][t] = cfma(lam[n], h[n][t - 1], make_float2(acc[t >> 2][n][t & 3], acc[t >> 2][2 + n][t & 3]));
    float2 loc[2], wag[2], wq[2];
#pragma unroll
    for (int n = 0; n < 2; ++n) {
        float2 a0, a1, a2, a3;
        rows4(h[n][15].x, a0.x, a1.x, a2.x, a3.x);
        rows4(h[n][15].y, a0.y, a1.y, a2.y, a3.y);
        const float2 s2 = cfma(l16[n], a0, a1), s3 = cfma(l16[n], s2, a2);
        wag[n] = cfma(l16[n], s3, a3);
        loc[n] = sel4(q, make_float2(0.f, 0.f), a0, s2, s3);
    }
    lptr aggp = smem + A_AGG + (wc * 16 + c) * 16;
    const unsigned flg = ldsa(smem + A_FLG) + wc * 4;
    float2 W0[2] = {make_float2(0.f, 0.f), make_float2(0.f, 0.f)};
    if (wr == 0) {
        if (q == 0) lst<f32x4>(aggp, (f32x4){wag[0].x, wag[0].y, wag[1].x, wag[1].y});
        asm volatile("" ::: "memory");
        if (lane == 0) lds_add(flg);
    } else {
#pragma unroll 1
        for (int spin = 0; spin < (1 << 20); ++spin) { if (lds_peek(flg)) break; }
        const f32x4 w0 = lld<f32x4>(aggp);
        W0[0] = make_float2(w0[0], w0[1]); W0[1] = make_float2(w0[2], w0[3]);
        const float2 g0 = cfma(l64[0], W0[0], wag[0]), g1 = cfma(l64[1], W0[1], wag[1]);
        if (lt < 15 && q < 2) {
            const float2 gg = q == 0 ? g0 : g1;
            st128<SC1>((u32x4){__float_as_uint(gg.x), (unsigned)tag, __float_as_uint(gg.y), (unsigned)(tag >> 32)},
                       rs, (2 * c + q) * 16 + OFF_GRAN + ((15 - lt) * 1024 + pt * 64 + wc * 32) * 16);
        }
    }
    TS(2);
#pragma unroll
    for (int n = 0; n < 2; ++n) { PIN(lam[n].x); PIN(lam[n].y); PIN(coef[n].x); PIN(coef[n].y); PIN(l16[n].x); PIN(l16[n].y); PIN(l128[n].x); PIN(l128[n].y); }
    float2 pw[2][8], l8[2], lq[2], l512[2];
#pragma unroll
    for (int n = 0; n < 2; ++n) {
        wq[n] = sel4(q, make_float2(1.f, 0.f), l16[n], l32[n], cmul(l32[n], l16[n]));
        loc[n] = cfma(wq[n], W0[n], loc[n]);
        if (wr) wq[n] = cmul(wq[n], l64[n]);
        pw[n][0] = cmul(coef[n], lam[n]);
        l8[n] = cmul(lam[n], lam[n]); l8[n] = cmul(l8[n], l8[n]); l8[n] = cmul(l8[n], l8[n]);
        const float2 l256 = cmul(l128[n], l128[n]);
        lq[n] = sel4(q, make_float2(1.f, 0.f), l128[n], l256, cmul(l256, l128[n]));
        l512[n] = cmul(l256, l256);
    }
#pragma unroll
    for (int n = 0; n < 2; ++n) {
        PIN(wq[n].x); PIN(wq[n].y); PIN(loc[n].x); PIN(loc[n].y); PIN(l8[n].x); PIN(l8[n].y); PIN(lq[n].x); PIN(lq[n].y); PIN(l512[n].x); PIN(l512[n].y);
    }
#pragma unroll
    for (int t = 0; t < 8; ++t) {
#pragma unroll
        for (int n = 0; n < 2; ++n) {
            if (t) pw[n][t] = cmul(pw[n][t - 1], lam[n]);
            h[n][2 * t] = cmul(coef[n], h[n][2 * t]); h[n][2 * t + 1] = cmul(coef[n], h[n][2 * t + 1]);
        }
#pragma unroll
        for (int n = 0; n < 2; ++n) { PIN(pw[n][t].x); PIN(pw[n][t].y); PIN(h[n][2 * t].x); PIN(h[n][2 * t].y); PIN(h[n][2 * t + 1].x); PIN(h[n][2 * t + 1].y); }
        SCHED();
    }
    TS(3);
    if (lt > 0) {
        u32x4 gr[4][2];
        unsigned off = ((16 - lt + q) * 1024 + pt * 64 + wc * 32 + 2 * c) * 16;
#pragma unroll 1
        for (int spin = 0; spin < (1 << 20); ++spin) {
            bool ok = true;
            PIN(off);
#pragma unroll
            for (int j = 0; j < 4; ++j) {
                const rsrc_t rg = mkrsrc(ws + OFF_GRAN + j * 65536, (4 - j) * 65536);
                gr[j][0] = __builtin_amdgcn_raw_buffer_load_b128(rg, off, 0, SC1);
                gr[j][1] = __builtin_amdgcn_raw_buffer_load_b128(rg, off + 16, 0, SC1);
            }
#pragma unroll
            for (int j = 0; j < 4; ++j) {
                const bool v = lt - 1 - (4 * j + q) >= 0;
#pragma unroll
                for (int n = 0; n < 2; ++n) ok = ok & (!v | ((gr[j][n].y == (unsigned)tag) & (gr[j][n].w == (unsigned)(tag >> 32))));
            }
            if (__all(ok)) break;
            asm volatile("" ::: "memory");
        }
#pragma unroll
        for (int n = 0; n < 2; ++n) {
            float2 cs = make_float2(__uint_as_float(gr[3][n].x), __uint_as_float(gr[3][n].z));
#pragma unroll
            for (int j = 2; j >= 0; --j) cs = cfma(l512[n], cs, make_float2(__uint_as_float(gr[j][n].x), __uint_as_float(gr[j][n].z)));
            cs = cmul(cs, lq[n]);
            cs.x = rowsum4(cs.x); cs.y = rowsum4(cs.y);
            loc[n] = cfma(wq[n], cs, loc[n]);
        }
    }
    TS(4);
    lptr tb = smem + A_STAGE + wid * 8192;
    PIN(lane);
#pragma unroll
    for (int hf = 0; hf < 2; ++hf) {
        const float2 s0 = hf ? cmul(l8[0], loc[0]) : loc[0], s1 = hf ? cmul(l8[1], loc[1]) : loc[1];
#pragma unroll
        for (int t = 0; t < 8; ++t) {
            const float2 v0 = cfma(pw[0][t], s0, h[0][8 * hf + t]), v1 = cfma(pw[1][t], s1, h[1][8 * hf + t]);
            lst<unsigned>(tb + (8 * hf + t) * 256 + lane * 4, pkb(v0.x, v1.x));
            lst<unsigned>(tb + 4096 + (8 * hf + t) * 256 + lane * 4, pkb(-v0.y, -v1.y));
        }
        asm volatile("" ::: "memory");
        lptr rb = tb + (lane >> 4) * 256 + (lane & 3) * 64 + ((lane >> 2) & 3) * 16;
        const unsigned hoff = OFF_H + ((lane >> 4) * 64 + pt * 2 + wc) * 8192 + lt * 512 + wr * 256 + (lane & 15) * 16;
#pragma unroll
        for (int v = 2 * hf; v < 2 * hf + 2; ++v)
#pragma unroll
            for (int pl = 0; pl < 2; ++pl) st128<WT>(lld<u32x4>(rb + pl * 4096 + v * 1024), rs, hoff + (v * 256 + pl * 32) * 8192);
    }
    TS(5);
    TSE(0, wid == 0);
}

struct __attribute__((packed, aligned(2))) U64U { u32x2 v; };
DEV void w_load(const u16* __restrict__ w, int k1, int qw, int lw, int lane, u32x2 (&R)[4]) {
    const int kd0 = 4 * (lane & 15) + 64 * (4 * lw + (lane >> 4));
    const int col = lw < 2 ? kd0 : 1021 - kd0;
#pragma unroll
    for (int r = 0; r < 4; ++r) {
        const int ks = k1 + 128 * (4 * qw + r), row = lw < 2 ? ks : (S_ - ks) & (S_ - 1);
        R[r] = reinterpret_cast<const U64U*>(w + row * KF + col)->v;
    }
}
DEV unsigned w_part(const u16* __restrict__ w_r, const u16* __restrict__ w_i, int k1, int qw, int lw, int lane) {
    const int ks = k1 + 128 * (4 * qw + (lane & 3)), row = lw < 2 ? (S_ - ks) & (S_ - 1) : ks;
    return ((lane & 4) ? w_i : w_r)[row * KF + (lw < 2 ? 0 : 512)];
}
DEV void w_row(int r, int lw, int lane, u32x2 R, u32x2 I, unsigned sp, u32x4 (&o)[4], unsigned& mx) {
    const unsigned cj = lw < 2 ? 0u : 0x80008000u;
    const unsigned i0 = I.x ^ cj, i1 = I.y ^ cj;
    unsigned o0 = __builtin_amdgcn_perm(i0, R.x, 0x05040100u), o3 = __builtin_amdgcn_perm(i1, R.y, 0x07060302u);
    o[1][r] = __builtin_amdgcn_perm(i0, R.x, 0x07060302u); o[2][r] = __builtin_amdgcn_perm(i1, R.y, 0x05040100u);
    const float pr = lo2f(__builtin_amdgcn_readlane(sp, r)), pi = lo2f(__builtin_amdgcn_readlane(sp, 4 + r) ^ (lw < 2 ? 0x8000u : 0u));
    const unsigned t = lw < 2 ? o0 : o3;
    unsigned v = pkb(0.5f * (lo2f(t) + pr), 0.5f * (hi2f(t) + pi));
    PIN(v);
    o[0][r] = (lane == 0 && lw == 0) ? v : o0; o[3][r] = (lane == 0 && lw == 2) ? v : o3;
    mx = pkmax(mx, pkmax(pkmax(R.x & 0x7fff7fffu, R.y & 0x7fff7fffu), pkmax(I.x & 0x7fff7fffu, I.y & 0x7fff7fffu)));
}
DEV void w_store(rsrc_t rs, int k1, int qw, int lw, int lane, u32x4 (&o)[4], int e0, int e1) {
#pragma unroll
    for (int e = e0; e < e1; ++e)
        st128<SC1>(o[e], rs, (lane & 15) * 1024 + (lane >> 4) * 16 + OFF_W + (k1 * 4 + (lw < 2 ? e : 3 - e)) * 16384 + qw * 256 + lw * 64);
}

#define TSARG
#define TSPASS
DEV void ka_load(const u16* __restrict__ x, const u16* __restrict__ Bre, const u16* __restrict__ Bim, lptr smem, int gb, int lw, int lane TSARG) {
    int lt, pt; tile_of(gb, lt, pt);
    const int srow = lane >> 3, schunk = (lane & 7) ^ srow;
    const int rot = (lt + pt) & 15;
    const rsrc_t rx = mkrsrc(x, S_ * D_ * 2), rb = mkrsrc((lw & 1) ? Bim : Bre, D_ * D_ * 2);
    const unsigned vA = (16 * (srow >> 2) + (srow & 3)) * 2048 + schunk * 16, sA = (lt * 128 + 64 * (lw >> 1) + 8 * (lw & 1)) * 2048;
    const unsigned vB = 2 * srow * 2048 + schunk * 16, sB = (pt * 64 + 32 * (lw >> 1)) * 2048;
    auto stage = [&](int kk) {
        const int ko = ((kk + rot) & 15) * 128;
        lptr l = smem + (kk & 3) * A_STAGE + lw * 4096;
#pragma unroll
        for (int i = 0; i < 4; ++i) blds16(rx, l + i * 1024, vA, sA + (32 * (i & 1) + 4 * (i >> 1)) * 2048 + ko);
#pragma unroll
        for (int i = 0; i < 4; ++i) blds16(rb, l + 16384 + i * 1024, vB, sB + (16 * (i & 1) + (i >> 1)) * 2048 + ko);
    };
    TS(0);
    stage(0);
#pragma unroll
    for (int i = 0; i < 4; ++i) blds16(rx, smem + A_XT + lw * 4096 + i * 1024, (lane >> 1) * 32768 + (lane & 1) * 16, (i * 512 + lt) * 2048 + lw * 512 + pt * 32);
    stage(1); stage(2);
    TS(6);
#pragma unroll 1
    for (int kt = 0; kt < 13; ++kt) { wait_vm<16>(); TS(8 + kt); BAR(); TS(28 + kt); if (kt == 0) TC(60); stage(kt + 3); }
}

DEV void gate_step(float& gsum, unsigned xaddr, unsigned waddr) {
    unsigned xv, gw;
    asm volatile("ds_read_b32 %0, %2\n\tds_read_b32 %1, %3\n\ts_waitcnt lgkmcnt(0)" : "=&v"(xv), "=&v"(gw) : "v"(xaddr), "v"(waddr) : "memory");
    const unsigned pr = pkb(lo2f(xv) * lo2f(gw), hi2f(xv) * hi2f(gw));
    gsum += lo2f(pr); gsum += hi2f(pr); PIN(gsum);
}
DEV void gate_addr(lptr smem, int pt, int aw, int lane, int& gi, unsigned& gaddr, unsigned& gwaddr) {
    gi = 8 * pt + 2 * aw + (lane >> 5);
    gaddr = ldsa(smem) + gi * 128 + ((((lane & 31) >> 2) ^ (gi & 7)) << 4) + (lane & 3) * 4;
    gwaddr = ldsa(smem + A_GW) + (lane & 31) * 4;
}
DEV CM mat_sf(int k1, int c, int q) { const float dt = -(float)(k1 + 128 * c) * (1.0f / 2048.0f); return cmat(dt * (float)(4 * q), dt); }
DEV CM mat_si(int k1, int c, int q) { return cmat((float)((k1 + 512 * q) * c) * (1.0f / 2048.0f), (float)c * (1.0f / 16.0f)); }

DEV float ka_aux(const u16* __restrict__ w_r, const u16* __restrict__ w_i, const u16* __restrict__ lam_re, const u16* __restrict__ lam_im,
                 const u16* __restrict__ log_dt, const u16* __restrict__ gate_w, char* ws, lptr smem, int gb, int aw, int lane TSARG) {
    int lt, pt; tile_of(gb, lt, pt);
    const int c = lane & 15, q = lane >> 4;
    const int rot = (lt + pt) & 15;
    const rsrc_t rs = mkrsrc(ws, WS_END);
    TS(0);
    __builtin_amdgcn_s_setprio(PRIOA);
    const u32x2 gwv = *reinterpret_cast<const u32x2*>(gate_w + aw * 256 + lane * 4);
    unsigned lru = lam_re[pt * 64 + lane], liu = lam_im[pt * 64 + lane], ldu = log_dt[pt * 64 + lane];
    SCHED();
    int gi; unsigned gaddr, gwaddr; gate_addr(smem, pt, aw, lane, gi, gaddr, gwaddr);
    float gsum = 0.f;
    u32x4 fch, fcl, fsh, fsl;
    const bool m0 = 16 * aw + c == 0;
    {
        const float th = (float)(16 * aw + c) * (1.0f / 128.0f);
#pragma unroll
        for (int i = 0; i < 4; ++i) {
            const float t0 = __builtin_amdgcn_fractf(th * (float)(16 * (i >> 1) + 2 * (i & 1) + 4 * q)), t1 = t0 + th;
            const float c0 = __builtin_amdgcn_cosf(t0), c1 = __builtin_amdgcn_cosf(t1);
            const float s0 = m0 ? 1.f : -__builtin_amdgcn_sinf(t0), s1 = m0 ? -1.f : -__builtin_amdgcn_sinf(t1);
            const unsigned ch = pkb(c0, c1), sh = pkb(s0, s1);
            fch[i] = ch; fcl[i] = pkb(c0 - lo2f(ch), c1 - hi2f(ch));
            fsh[i] = sh; fsl[i] = pkb(s0 - lo2f(sh), s1 - hi2f(sh));
        }
        PIN(fch); PIN(fcl); PIN(fsh); PIN(fsl);
    }
    {
        const int mp = gb >> 2, j1 = gb & 3;
        const bool spc = mp == 0 && !(j1 & 1);
        const int k1 = mp ? mp : (j1 == 3 ? 64 : 0);
        lptr mt = smem + A_MAT + lane * 16;
        if (aw == 0) {
            const float dt = -(float)(j1 + 4 * (spc ? c & 7 : c)) * (1.0f / 64.0f), t0 = __builtin_amdgcn_fractf(dt * (float)(4 * q));
            float cc[4], ss[4];
#pragma unroll
            for (int e = 0; e < 4; ++e) { const float f = fmaf((float)e, dt, t0); cc[e] = __builtin_amdgcn_cosf(f); ss[e] = __builtin_amdgcn_sinf(f); }
            const unsigned c0 = pkb(cc[0], cc[1]), c1 = pkb(cc[2], cc[3]), s0 = pkb(ss[0], ss[1]), s1 = pkb(ss[2], ss[3]);
            const unsigned c0l = pkb(cc[0] - lo2f(c0), cc[1] - hi2f(c0)), c1l = pkb(cc[2] - lo2f(c1), cc[3] - hi2f(c1));
            const unsigned s0l = pkb(ss[0] - lo2f(s0), ss[1] - hi2f(s0)), s1l = pkb(ss[2] - lo2f(s1), ss[3] - hi2f(s1));
            u32x4 m[4] = {(u32x4){c0, c1, s0 ^ 0x80008000u, s1 ^ 0x80008000u}, (u32x4){s0, s1, c0, c1},
                          (u32x4){c0l, c1l, s0l ^ 0x80008000u, s1l ^ 0x80008000u}, (u32x4){s0l, s1l, c0l, c1l}};
            if (spc) {
                m[0] = c < 8 ? (u32x4){c0, c1, 0, 0} : (u32x4){0, 0, c0, c1}; m[1] = c < 8 ? (u32x4){s0, s1, 0, 0} : (u32x4){0, 0, s0, s1};
                m[2] = c < 8 ? (u32x4){c0l, c1l, 0, 0} : (u32x4){0, 0, c0l, c1l}; m[3] = c < 8 ? (u32x4){s0l, s1l, 0, 0} : (u32x4){0, 0, s0l, s1l};
            }
#pragma unroll
            for (int i = 0; i < 4; ++i) lst<u32x4>(mt + i * 1024, m[i]);
        } else if (aw == 1) {
            const int jq = spc ? (4 * q) & 7 : 4 * q;
            float cc[4], ss[4];
            const float t0 = __builtin_amdgcn_fractf((float)(c * (j1 + 4 * jq)) * (1.0f / 64.0f));
#pragma unroll
            for (int e = 0; e < 4; ++e) {
                const float f = fmaf((float)c, (float)e * (1.0f / 16.0f), t0);
                const float wg = !spc || (gb == 0 && jq + e == 0) ? 0.125f : 0.25f;
                cc[e] = __builtin_amdgcn_cosf(f) * wg; ss[e] = -__builtin_amdgcn_sinf(f) * wg;
            }
            const unsigned c0 = pkh(cc[0], cc[1]), c1 = pkh(cc[2], cc[3]), n0 = pkh(ss[0], ss[1]), n1 = pkh(ss[2], ss[3]);
            u32x4 mia = (u32x4){c0, n0, c1, n1}, mib = (u32x4){n0, c0 ^ 0x80008000u, n1, c1 ^ 0x80008000u};
            if (spc) { mib = q < 2 ? (u32x4){0, 0, 0, 0} : mia ^ 0x80008000u; if (q >= 2) mia = (u32x4){0, 0, 0, 0}; }
            lst<u32x4>(mt + 8 * 1024, mia); lst<u32x4>(mt + 9 * 1024, mib);
        } else {
            const CM m = aw == 2 ? mat_sf(k1, c, q) : mat_si(k1, c, q);
            lst<u32x4>(mt + 2 * aw * 1024, m.re); lst<u32x4>(mt + (2 * aw + 1) * 1024, m.im);
        }
    }
    const unsigned sg64 = (c & 1) ? 0x80008000u : 0u, sgA = (c & 2) ? 0x80008000u : 0u, sgB = ((c + 1) & 2) ? 0x80008000u : 0u;
    PIN(lru); PIN(liu); PIN(ldu);
    lst<u32x2>(smem + A_GW + aw * 512 + lane * 8, gwv);
    LDS_WAIT();
    u32x2 R[4], I[4], R2[4], I2[4]; unsigned sp = 0, sp2 = 0, mx = 0, um = 0;
#pragma unroll
    for (int r = 0; r < 4; ++r) { R2[r] = (u32x2){0, 0}; I2[r] = R2[r]; }
    u32x4 wo[4], uo[4];
    f32x4 vr[4], vi[4];
    float dt = 0.f, er = 0.f; float2 lam = make_float2(0.f, 0.f);
#pragma unroll
    for (int d1 = 0; d1 < 4; ++d1) { vr[d1] = Z4; vi[d1] = Z4; }
#pragma unroll
    for (int kt = 0; kt < 13; ++kt) {
        TS(8 + kt);
        BAR();
        u32x4 xa[2];
        if (kt < 8) {
            constexpr int KSO[4] = {0, 2, 3, 1};
#pragma unroll
            for (int i = 0; i < 2; ++i) xa[i] = trfrag(smem + A_XT + (2 * (kt & 1) + i) * 4096 + KSO[kt >> 1] * 1024 + lane * 8, 512);
        }
        u32x2 xc[4];
        if (kt == XSTEP || kt == XSTEP + 1) {
#pragma unroll
            for (int i = 0; i < 4; ++i) xc[i] = lld<u32x2>(smem + A_XT + i * 4096 + (32 * aw + 16 * (kt - XSTEP) + c) * 32 + q * 8);
        }
        gate_step(gsum, gaddr + (kt & 3) * A_STAGE, gwaddr + ((kt + rot) & 15) * 128);
        if (kt < 8) {
#pragma unroll
            for (int i = 0; i < 2; ++i) { const int d1 = 2 * (kt & 1) + i; vr[d1] = mfb(xa[i], fcl, vr[d1]); vi[d1] = mfb(xa[i], fsl, vi[d1]); }
#pragma unroll
            for (int i = 0; i < 2; ++i) { const int d1 = 2 * (kt & 1) + i; vr[d1] = mfb(xa[i], fch, vr[d1]); vi[d1] = mfb(xa[i], fsh, vi[d1]); PIN(vr[d1]); PIN(vi[d1]); }
        }
        if (kt == 1 || kt == 5) { fch ^= sg64; fsh ^= sg64; PIN(fch); PIN(fsh); if (FLO) { fcl ^= sg64; fsl ^= sg64; PIN(fcl); PIN(fsl); } }
        if (kt == 3) {
            const bool od = c & 1;
            const u32x4 a = (od ? fsh : fch) ^ sgA, b = (od ? fch : fsh) ^ sgB, al = (od ? fsl : fcl) ^ sgA, bl = (od ? fcl : fsl) ^ sgB;
            fch = a; fsh = b; PIN(fch); PIN(fsh);
            if (FLO) { fcl = al; fsl = bl; PIN(fcl); PIN(fsl); }
        }
        if (kt == WSTEP) {
            if (gb < 4) { w_load(w_r, 64, gb & 3, aw, lane, R2); w_load(w_i, 64, gb & 3, aw, lane, I2); sp2 = w_part(w_r, w_i, 64, gb & 3, aw, lane); }
            w_load(w_r, gb >> 2, gb & 3, aw, lane, R);
            sp = w_part(w_r, w_i, gb >> 2, gb & 3, aw, lane);
        }
        if (kt == WSTEP + WSPL) w_load(w_i, gb >> 2, gb & 3, aw, lane, I);
        if (kt == XSTEP || kt == XSTEP + 1) {
#pragma unroll
            for (int i = 0; i < 2; ++i)
                st128<WT>((u32x4){xc[2 * i].x, xc[2 * i].y, xc[2 * i + 1].x, xc[2 * i + 1].y}, rs,
                          OFF_X + (lt * 16 + pt) * 16384 + aw * 4096 + (2 * (kt - XSTEP) + i) * 1024 + lane * 16);
        }
        if (kt == 6 || kt == 7) {
            if (kt == 6) { PIN(sp); mx = sp & 0x7fffu; }
#pragma unroll
            for (int r = 2 * (kt - 6); r < 2 * (kt - 6) + 2; ++r) {
                PIN(R[r]); PIN(I[r]);
                w_row(r, aw, lane, R[r], I[r], sp, wo, mx);
                PIN(wo[0][r]); PIN(wo[1][r]); PIN(wo[2][r]); PIN(wo[3][r]); PIN(mx);
            }
        }
        if (kt == 8) {
            f32x4 ur[4], ui[4];
            const f32x4 t0r = vr[0] + vr[2], t1r = vr[0] - vr[2], t2r = vr[1] + vr[3], t3r = vr[1] - vr[3];
            const f32x4 t0i = vi[0] + vi[2], t1i = vi[0] - vi[2], t2i = vi[1] + vi[3], t3i = vi[1] - vi[3];
            ur[0] = t0r + t2r; ui[0] = t0i + t2i; ur[2] = t0r - t2r; ui[2] = t0i - t2i;
            ur[1] = t1r + t3i; ui[1] = t1i - t3r; ur[3] = t1r - t3i; ui[3] = t1i + t3r;
            if (m0) { ur[1] = t1r; ui[1] = -t3r; ur[3] = t1i; ui[3] = t3i; }
#pragma unroll
            for (int j1 = 0; j1 < 4; ++j1) {
                uo[j1] = (u32x4){pkb(ur[j1][0], ur[j1][1]), pkb(ur[j1][2], ur[j1][3]), pkb(ui[j1][0], ui[j1][1]), pkb(ui[j1][2], ui[j1][3])};
                PIN(uo[j1]);
            }
        }
        if (kt >= 8 && kt < 12) {
            const int j1 = kt - 8;
            PIN(uo[j1]);
            const u32x4 o = to16(uo[j1].x, uo[j1].y, uo[j1].z, uo[j1].w);
            um = pkmax(um, pkmax(pkmax(o.x & 0x7fff7fffu, o.y & 0x7fff7fffu), pkmax(o.z & 0x7fff7fffu, o.w & 0x7fff7fffu)));
            PIN(um);
            if (aw == 1) {
                const float lr = lo2f(lru), li = lo2f(liu);
                if (kt == 9) { dt = bfr(expf(lo2f(ldu))); er = expf(dt * lr); PIN(dt); PIN(er); }
                if (kt == 10) { float sn, cs; sincosf(dt * li, &sn, &cs); lam = make_float2(er * cs, er * sn); PIN(lam.x); PIN(lam.y); }
                if (kt == 11) {
                    const float nr = lam.x - 1.0f, ni = lam.y, den = lr * lr + li * li;
                    const float2 coef = make_float2((nr * lr + ni * li) / den, (ni * lr - nr * li) / den);
                    float2 l16 = lam;
#pragma unroll
                    for (int i = 0; i < 4; ++i) l16 = cmul(l16, l16);
                    float2 l128 = l16;
#pragma unroll
                    for (int i = 0; i < 3; ++i) l128 = cmul(l128, l128);
                    lst<f32x4>(smem + A_PRM + lane * 32, (f32x4){lam.x, lam.y, coef.x, coef.y});
                    lst<f32x4>(smem + A_PRM + lane * 32 + 16, (f32x4){l16.x, l16.y, l128.x, l128.y});
                }
            }
            st128<SC1>(o, rs, c * 65536 + (q & 1) * 32 + (q >> 1) * 16 + OFF_U + (aw * 64 + j1) * 16384 + lt * 1024 + pt * 64);
            w_store(rs, gb >> 2, gb & 3, aw, lane, wo, j1, j1 + 1);
        }
        if (kt == 12) {
            if (gb < 4) {
                mx = pkmax(mx, sp2 & 0x7fffu);
#pragma unroll
                for (int r = 0; r < 4; ++r) w_row(r, aw, lane, R2[r], I2[r], sp2, wo, mx);
                w_store(rs, 64, gb & 3, aw, lane, wo, 0, 4);
            }
            mx = wave_pkmax(__builtin_amdgcn_perm(pkmax(um, um >> 16), pkmax(mx, mx >> 16), 0x05040100u));
            if (lane == 0) lst<unsigned>(smem + A_FLG + 16 + aw * 4, mx);
            LDS_WAIT();
        }
        SCHED();
    }
    return gsum;
}

DEV void ka_mid(const u16* __restrict__ Cre, const u16* __restrict__ Cim, char* ws, lptr smem, int gb, int w, int lane, float gsum TSARG) {
    int lt, pt; tile_of(gb, lt, pt);
    const int c = lane & 15, q = lane >> 4, mp = gb >> 2, j1 = gb & 3, rot = (lt + pt) & 15;
    const bool spc = mp == 0 && !(j1 & 1), xtr = gb == 0, ld = w >= 4;
    const int k1 = mp ? mp : spc ? (w >> 2) * 64 : (j1 == 1 ? 0 : 64);
    const int ja0 = spc ? (2 * w) & 7 : 2 * w;
    const rsrc_t rs = mkrsrc(ws, WS_END);
    int gi; unsigned gaddr, gwaddr; gate_addr(smem, pt, w, lane, gi, gaddr, gwaddr);
    float cf = (float)c;
    CM gf[2], gi_[2];
    u32x2 tag;
#pragma unroll
    for (int kt = 13; kt < 16; ++kt) {
        if (ld) { if (kt == 13) wait_vm<16>(); else if (kt == 14) wait_vm<12>(); else wait_vm<4>(); }
        else if (kt == 14) wait_vm<0>();
        TS(8 + kt);
        BAR();
        TS(28 + kt);
        if (ld) {
            if (kt == 13) {
                const rsrc_t rc = mkrsrc((gb & 8) ? Cim : Cre, D_ * D_ * 2);
#pragma unroll
                for (int i = 0; i < 4; ++i)
                    blds16(rc, smem + (w - 4) * 4096 + i * 1024, q * 2048 + (c ^ (4 * q)) * 16, (256 * (w - 4) + (gb & ~15) + 4 * i) * 2048 + (gb & 7) * 256);
            }
        } else gate_step(gsum, gaddr + (kt & 3) * A_STAGE, gwaddr + ((kt + rot) & 15) * 128);
        if (kt == 13) asm volatile("ds_read_b64 %0, %1\n\ts_waitcnt lgkmcnt(0)" : "=v"(tag) : "v"(ldsa(smem + A_FLG + 32)) : "memory");
        if (kt == 14 && w == 0) {
            const u32x4 m4 = lld<u32x4>(smem + A_FLG + 16);
            const unsigned m = pkmax(pkmax(m4.x, m4.y), pkmax(m4.z, m4.w));
            if (lane == 0) __builtin_amdgcn_raw_buffer_store_b64((u32x2){(tag.x & 0xffff0000u) | ((m >> 15) & 0xff00u) | ((m & 0xffffu) >> 7), tag.y}, rs, 0, OFF_MX + gb * 8, SC1);
        }
        if (kt < 15) {
            PIN(cf);
            const float dt = fmaf(cf, -1.0f / 16.0f, -(float)(j1 + 4 * (ja0 + kt - 13)) * (1.0f / 1024.0f));
            gf[kt - 13] = cmat(dt * (float)(4 * q), dt);
        }
        SCHED();
    }
    TC(61);
    __builtin_amdgcn_s_setprio(0);
    u32x4 fl[2];
    unsigned foff = lane * 16; PIN(foff);
#pragma unroll
    for (int i = 0; i < 2; ++i) fl[i] = __builtin_amdgcn_raw_buffer_load_b128(rs, foff + i * 1024, OFF_MX, SC1);
    SCHED();
    PIN(cf);
#pragma unroll
    for (int i = 0; i < 2; ++i) gi_[i] = cmat(cf * (float)(j1 + 4 * (ja0 + i) + 256 * q) * (1.0f / 1024.0f), cf * (1.0f / 16.0f));
    TS(1);
    int spin = 0;
#pragma unroll 1
    for (; spin < (1 << 20); ++spin) {
        bool ok = true;
#pragma unroll
        for (int i = 0; i < 2; ++i) ok = ok & ((fl[i].x ^ tag.x) < 0x10000u) & (fl[i].y == tag.y) & ((fl[i].z ^ tag.x) < 0x10000u) & (fl[i].w == tag.y);
        if (__all(ok)) break;
        PIN(foff);
#pragma unroll
        for (int i = 0; i < 2; ++i) fl[i] = __builtin_amdgcn_raw_buffer_load_b128(rs, foff + i * 1024, OFF_MX, SC1);
    }
    asm volatile("" ::: "memory");
    TS(2);
#pragma unroll
    for (int i = 0; i < 2; ++i) blds16<SC1>(rs, smem + A_UT + (2 * w + i) * 1024, lane * 16, OFF_U + gb * 16384 + (2 * w + i) * 1024);
    u32x4 wv[3];
#pragma unroll
    for (int i = 0; i < 2; ++i) wv[i] = __builtin_amdgcn_raw_buffer_load_b128(rs, lane * 16, OFF_W + (k1 * 4 + j1) * 16384 + (ja0 + i) * 1024, SC1);
    wv[2] = (u32x4){0, 0, 0, 0};
    if (xtr) wv[2] = __builtin_amdgcn_raw_buffer_load_b128(rs, lane * 16, OFF_W + (k1 * 4) * 16384 + 8 * 1024, SC1);
    SCHED();
    int eu, ew;
    {
        constexpr unsigned SP = 0x0c010c00u;
        const unsigned m = __builtin_amdgcn_readfirstlane(wave_pkmax(pkmax(pkmax(__builtin_amdgcn_perm(0u, fl[0].x, SP), __builtin_amdgcn_perm(0u, fl[0].z, SP)),
                                                                           pkmax(__builtin_amdgcn_perm(0u, fl[1].x, SP), __builtin_amdgcn_perm(0u, fl[1].z, SP)))));
        eu = min(max((int)(m >> 16), 4), 254); ew = min((int)(m & 0xffffu), 245);
    }
    const float su = __uint_as_float((unsigned)(258 - eu) << 23), sw = __uint_as_float((unsigned)(246 - ew) << 23);
    u32x4 mt[10];
    asm volatile("ds_read_b128 %0, %10\n\tds_read_b128 %1, %10 offset:1024\n\tds_read_b128 %2, %10 offset:2048\n\tds_read_b128 %3, %10 offset:3072\n\t"
                 "ds_read_b128 %4, %10 offset:4096\n\tds_read_b128 %5, %10 offset:5120\n\tds_read_b128 %6, %10 offset:6144\n\tds_read_b128 %7, %10 offset:7168\n\t"
                 "ds_read_b128 %8, %10 offset:8192\n\tds_read_b128 %9, %10 offset:9216\n\ts_waitcnt lgkmcnt(0)"
                 : "=&v"(mt[0]), "=&v"(mt[1]), "=&v"(mt[2]), "=&v"(mt[3]), "=&v"(mt[4]), "=&v"(mt[5]), "=&v"(mt[6]), "=&v"(mt[7]), "=&v"(mt[8]), "=&v"(mt[9])
                 : "v"(ldsa(smem + A_MAT) + lane * 16) : "memory");
    if (spc && ld) { const CM a = mat_sf(64, c, q), b = mat_si(64, c, q); mt[4] = a.re; mt[5] = a.im; mt[6] = b.re; mt[7] = b.im; }
    u32x4 cv[4];
    if (ld) {
        const unsigned ca = ldsa(smem) + (w - 4) * 4096 + q * 1024 + (lane & 3) * 256 + ((lane >> 2) & 3) * 16;
        asm volatile("ds_read_b128 %0, %4\n\tds_read_b128 %1, %5\n\tds_read_b128 %2, %6\n\tds_read_b128 %3, %7\n\ts_waitcnt lgkmcnt(0)"
                     : "=&v"(cv[0]), "=&v"(cv[1]), "=&v"(cv[2]), "=&v"(cv[3])
                     : "v"(ca + (lane & 3) * 64), "v"(ca + ((lane & 3) ^ 1) * 64), "v"(ca + ((lane & 3) ^ 2) * 64), "v"(ca + ((lane & 3) ^ 3) * 64) : "memory");
    } else {
        gsum = half_sum(gsum);
        const float g = bfr(1.0f / (1.0f + expf(-bfr(gsum))));
        cv[0].x = __float_as_uint(g); cv[0].y = __float_as_uint(bfr(1.0f - g));
        PIN(cv[0]);
    }
    SCHED();
    PIN(wv[0]); PIN(wv[1]); PIN(wv[2]);
    TS(3);
    const unsigned cnt = ldsa(smem + A_FLG) + 8;
    {
        u32x2 ua[4];
        asm volatile("ds_read_b64_tr_b16 %0, %4\n\tds_read_b64_tr_b16 %1, %4 offset:32\n\tds_read_b64_tr_b16 %2, %4 offset:1024\n\tds_read_b64_tr_b16 %3, %4 offset:1056\n\t"
                     "s_waitcnt lgkmcnt(0)" : "=&v"(ua[0]), "=&v"(ua[1]), "=&v"(ua[2]), "=&v"(ua[3])
                     : "v"(ldsa(smem + A_UT) + 2 * w * 1024 + (lane >> 2) * 64 + (lane & 3) * 8) : "memory");
        const unsigned ea = ldsa(smem + A_E1) + c * E1P + q * 256 + 2 * w * 16;
#pragma unroll
        for (int i = 0; i < 2; ++i) {
            const u32x4 u = {ua[2 * i].x, ua[2 * i].y, ua[2 * i + 1].x, ua[2 * i + 1].y};
            f32x4 r = mfb(u, mt[2], Z4), m = mfb(u, mt[3], Z4);
            r = mfb(u, mt[0], r); m = mfb(u, mt[1], m);
            asm volatile("ds_write_b128 %0, %1 offset:%2" :: "v"(ea), "v"(pk8(r * su, m * su)), "i"(i * 16) : "memory");
            if (xtr) {
                const unsigned sg = 0xbf803f80u;
                const f32x4 e = mfb(u, c == 0 ? (u32x4){sg, sg, 0, 0} : (u32x4){0, 0, sg, sg}, Z4);
                if (c < 2) asm volatile("ds_write_b128 %0, %1 offset:%2" :: "v"(ea), "v"(pk8(e * su, Z4)), "i"(16 * E1P + i * 16) : "memory");
            }
        }
    }
    if (lane == 0) lds_add(cnt);
    if (ld) {
#pragma unroll
        for (int j = 0; j < 4; ++j) st128<WT>(cv[j], rs, OFF_C + ((gb * 4 + j) * 4 + w - 4) * 1024 + lane * 16);
    } else {
        const int tau = (gi & 64) | ((gi & 12) << 2) | ((gi & 48) >> 2) | (gi & 3);
        if ((lane & 31) == 0) __builtin_amdgcn_raw_buffer_store_b64((u32x2){cv[0].x, cv[0].y}, rs, (tau & 15) * 1024 + (lt * 8 + (tau >> 4)) * 8, OFF_G, 0);
        if (xtr && w == 0 && lane == 0) {
            __builtin_amdgcn_raw_buffer_store_b64(tag, rs, 0, OFF_E, SC1);
            __builtin_amdgcn_raw_buffer_store_b32(eu + ew - 267, rs, 0, OFF_E + 128, 0);
        }
    }
    wv[0] = wcvt(wv[0], sw); wv[1] = wcvt(wv[1], sw);
    TS(4);
#pragma unroll 1
    for (int sp_ = 0; sp_ < (1 << 24); ++sp_) { if (lds_peek(cnt) >= 8u) break; }
    u32x4 dv[2];
#pragma unroll
    for (int i = 0; i < 2; ++i) dv[i] = lld<u32x4>(smem + A_E1 + (2 * w + i) * E1P + lane * 16);
#pragma unroll
    for (int i = 0; i < 2; ++i) dv[i] = pk8(mfh(dv[i], gf[i].re, Z4), mfh(dv[i], gf[i].im, Z4));
#pragma unroll
    for (int i = 0; i < 2; ++i) dv[i] = wmul(mfh(mt[4], dv[i], Z4), mfh(mt[5], dv[i], Z4), wv[i]);
#pragma unroll
    for (int i = 0; i < 2; ++i) dv[i] = pk8(mfh(dv[i], mt[6], Z4), mfh(dv[i], mt[7], Z4));
    {
        f32x4 r[2], m[2];
#pragma unroll
        for (int i = 0; i < 2; ++i) { r[i] = mfh(dv[i], gi_[i].re, Z4); m[i] = mfh(dv[i], gi_[i].im, Z4); }
#pragma unroll
        for (int e = 0; e < 4; ++e)
            lst<u32x2>(smem + A_E2 + (4 * q + e) * 1024 + (w >> 1) * 256 + c * 16 + (w & 1) * 8, (u32x2){pkh(r[0][e], r[1][e]), pkh(m[0][e], m[1][e])});
    }
    if (xtr && !(w & 3)) {
        const float dt = -(float)(32 + 64 * c) * (1.0f / 1024.0f);
        const CM f = cmat(dt * (float)(4 * q), dt), b = cmat(cf * (float)(32 + 256 * q) * (1.0f / 1024.0f), cf * (1.0f / 16.0f));
        u32x4 d = lld<u32x4>(smem + A_E1 + (16 + (w >> 2)) * E1P + lane * 16);
        d = pk8(mfh(d, f.re, Z4), mfh(d, f.im, Z4));
        d = wmul(mfh(mt[4], d, Z4), mfh(mt[5], d, Z4), wcvt(wv[2], sw));
        d = pk8(mfh(d, mt[6], Z4), mfh(d, mt[7], Z4));
        const f32x4 r = mfh(d, b.re, Z4);
#pragma unroll
        for (int e = 0; e < 4; ++e) lst<float>(smem + A_EX + ((w >> 2) * 256 + (4 * q + e) * 16 + c) * 4, r[e]);
    }
    TS(5);
    aux_sync(cnt, 16u, lane);
    {
        f32x4 xa[2], xb[2];
#pragma unroll
        for (int i = 0; i < 2; ++i) { xa[i] = Z4; xb[i] = Z4; }
        if (xtr) {
            const float sg = (c & 1) ? -0.125f : 0.125f;
#pragma unroll
            for (int i = 0; i < 2; ++i) {
                xa[i] = lld<f32x4>(smem + A_EX + (2 * w + i) * 64 + q * 16) * sg;
                xb[i] = lld<f32x4>(smem + A_EX + 1024 + (2 * w + i) * 64 + q * 16) * -sg;
            }
        }
#pragma unroll
        for (int i = 0; i < 2; ++i) {
            const int s2 = 2 * w + i;
            const u32x4 ea = lld<u32x4>(smem + A_E2 + s2 * 1024 + lane * 16);
            const f32x4 ta = mfh(ea, mt[8], xa[i]), tb = mfh(ea, mt[9], xb[i]);
            st128<WT>(to16(pkh(ta[0], ta[1]), pkh(ta[2], ta[3]), pkh(tb[0], tb[1]), pkh(tb[2], tb[3])), rs,
                      c * 16384 + (q & 1) * 32 + (q >> 1) * 16 + OFF_T + s2 * 262144 + j1 * 4096 + mp * 64);
        }
    }
    TS(7);
    TSE(w ? 1 : 5, (w & 3) == 0);
}

NOPK __global__ __launch_bounds__(768, 1) void ka_kernel(const u16* __restrict__ x, const u16* __restrict__ w_r, const u16* __restrict__ w_i,
                                                         const u16* __restrict__ Bre, const u16* __restrict__ Bim, const u16* __restrict__ Cre,
                                                         const u16* __restrict__ Cim, const u16* __restrict__ lam_re,
                                                         const u16* __restrict__ lam_im, const u16* __restrict__ log_dt,
                                                         const u16* __restrict__ gate_w, char* ws) {
    extern __shared__ __attribute__((aligned(16))) char smem_[];
    lptr smem = (lptr)smem_;
    const int wid = __builtin_amdgcn_readfirstlane(threadIdx.x >> 6), lane = threadIdx.x & 63;
    if (wid < 4) ka_mma(ws, smem, blockIdx.x, wid, lane);
    else {
        TSD();
        float gsum = 0.f;
        if (wid < 8) ka_load(x, Bre, Bim, smem, blockIdx.x, wid - 4, lane TSPASS);
        else gsum = ka_aux(w_r, w_i, lam_re, lam_im, log_dt, gate_w, ws, smem, blockIdx.x, wid - 8, lane TSPASS);
        int w = wid & 7;
        asm volatile("" : "+s"(w));
        ka_mid(Cre, Cim, ws, smem, blockIdx.x, w, lane, gsum TSPASS);
    }
}

constexpr int B_UT = 0, B_R0 = B_UT + 16384, B_CNT = B_R0 + 32768, B_DMP = B_CNT + 64, KB_SMEM = B_DMP + 12 * 8192;

DEV void kb_mma(char* ws, float* __restrict__ out, lptr smem, int gb, int wid, int lane) {
    int s2, da; tile_of(gb, s2, da);
    const int c = lane & 15, q = lane >> 4;
    const rsrc_t rs = mkrsrc(ws, WS_END);
    TSD(); TS(0);
    const unsigned vo = (c >> 2) * 256 + q * 64 + (c & 3) * 16;
    const unsigned sC = OFF_C + (da * 64 + 16 * wid) * 4096, sH = OFF_H + (s2 * 64 + 16 * wid) * 8192;
    auto ldA = [&](int i, int m) { return __builtin_amdgcn_raw_buffer_load_b128(rs, vo + m * 1024, sC + i * 4096, 0); };
    auto ldB = [&](int i, int n) { return __builtin_amdgcn_raw_buffer_load_b128(rs, vo + (n & 1) * 1024, sH + i * 8192 + (((n >> 1) + wid + 1) & 3) * 2048, 0); };
    u32x4 A[3][4], B[2][8];
#pragma unroll
    for (int i = 0; i < 2; ++i) {
#pragma unroll
        for (int m = 0; m < 4; ++m) A[i][m] = ldA(i, m);
#pragma unroll
        for (int n = 0; n < 8; ++n) B[i][n] = ldB(i, n);
        SCHED();
        if (i == 0) BAR();
    }
    f32x4 acc[4][8];
#pragma unroll
    for (int m = 0; m < 4; ++m)
#pragma unroll
        for (int n = 0; n < 8; ++n) acc[m][n] = Z4;
    float omg[2];
#pragma unroll
    for (int i = 0; i < 16; ++i) {
        if (i + 2 < 16) {
#pragma unroll
            for (int m = 0; m < 4; ++m) A[(i + 2) % 3][m] = ldA(i + 2, m);
        }
        if (i == 14) {
#pragma unroll
            for (int n = 0; n < 2; ++n) omg[n] = __uint_as_float(__builtin_amdgcn_raw_buffer_load_b32(rs, c * 8 + n * 128 + 4, OFF_G + s2 * 1024 + wid * 256, 0));
        }
        SCHED();
#pragma unroll
        for (int n = 0; n < 8; ++n) {
#pragma unroll
            for (int m = 0; m < 4; ++m) acc[m][n] = mfb(A[i % 3][m], B[i & 1][n], acc[m][n]);
            if (i + 2 < 16) B[i & 1][n] = ldB(i + 2, n);
            if (i == 15 && n >= 1 && n < 7) {
                const int j = (n - 1) >> 1;
#pragma unroll
                for (int m = 0; m < 4; ++m)
                    lst<f32x4>(smem + B_DMP + (((j + wid + 1) & 3) * 3 + j) * 8192 + (((n - 1) & 1) * 4 + m) * 1024 + lane * 16, acc[m][n - 1]);
            }
            SCHED();
        }
        if (i == 0) { TS(4); TC(60); }
        if (i == 7) TS(5);
        if (i == 14) { TS(6); TC(61); }
    }
    TS(1);
    const rsrc_t ro = mkrsrc(out, S_ * D_ * 4);
    LDS_WAIT();
    BAR();
    TS(2);
#pragma unroll
    for (int n = 0; n < 2; ++n) {
        const int row = 16 * (32 * wid + 16 * n + c) + s2;
        const float o2 = 2.0f * omg[n];
#pragma unroll
        for (int m = 0; m < 4; ++m) {
            f32x4 a = acc[m][6 + n];
#pragma unroll
            for (int j = 0; j < 3; ++j) a += lld<f32x4>(smem + B_DMP + (wid * 3 + j) * 8192 + (n * 4 + m) * 1024 + lane * 16);
            const f32x4 r0 = lld<f32x4>(smem + B_R0 + ((wid * 2 + n) * 4 + m) * 1024 + lane * 16);
            const f32x4 o = {fmaf(o2, a[0], r0[0]), fmaf(o2, a[1], r0[1]), fmaf(o2, a[2], r0[2]), fmaf(o2, a[3], r0[3])};
            st128<WTO>(__builtin_bit_cast(u32x4, o), ro, (row * D_ + 256 * m + 16 * da + 4 * q) * 4);
        }
    }
    TS(3);
    TSE(2, wid == 0);
}

#define PB_HI 3
#define PB_LO 0
#define PHI() __builtin_amdgcn_s_setprio(PB_HI)
#define PLO() __builtin_amdgcn_s_setprio(PB_LO)
#define LD128 __builtin_amdgcn_raw_buffer_load_b128
#define LD64 __builtin_amdgcn_raw_buffer_load_b64
#define LD32 __builtin_amdgcn_raw_buffer_load_b32
#define BLDS blds16
#define STT st128<SC1>
#define KSLP 8
DEV void kb_aux(const u16* __restrict__ Dskip, char* ws, lptr smem, int gb, int w, int lane) {
    int s2t, dat; tile_of(gb, s2t, dat);
    const int c = lane & 15, q = lane >> 4;
    const rsrc_t rs = mkrsrc(ws, WS_END), rd = mkrsrc(Dskip, D_ * 2);
    const unsigned cnt = ldsa(smem + B_CNT);
    TSD(); TS(0);
    if (w == 0) asm volatile("ds_write_b32 %0, %1\n\ts_waitcnt lgkmcnt(0)" :: "v"(cnt), "v"(0u) : "memory");
    BAR();
    u32x4 fc[2][2], fs[2][2];
#pragma unroll
    for (int n = 0; n < 2; ++n) {
        float dt = (float)(32 * w + 16 * n + c) * (1.0f / 128.0f); PIN(dt);
        float cc[8], ss[8];
        const float t0 = __builtin_amdgcn_fractf(dt * (float)(4 * q)), t1 = __builtin_amdgcn_fractf(dt * (float)(16 + 4 * q));
#pragma unroll
        for (int e = 0; e < 8; ++e) { const float f = fmaf((float)(e & 3), dt, e < 4 ? t0 : t1); cc[e] = __builtin_amdgcn_cosf(f); ss[e] = __builtin_amdgcn_sinf(f); }
        fc[n][0] = (u32x4){pkh(cc[0], cc[1]), pkh(cc[2], cc[3]), pkh(cc[4], cc[5]), pkh(cc[6], cc[7])};
        fs[n][0] = (u32x4){pkh(ss[0], ss[1]), pkh(ss[2], ss[3]), pkh(ss[4], ss[5]), pkh(ss[6], ss[7])};
        fc[n][1] = ((c & 1) ? fs[n][0] : fc[n][0]) ^ (((c + 1) & 2) ? 0x80008000u : 0u);
        fs[n][1] = ((c & 1) ? fc[n][0] : fs[n][0]) ^ ((c & 2) ? 0x80008000u : 0u);
        PIN(fc[n][0]); PIN(fs[n][0]); PIN(fc[n][1]); PIN(fs[n][1]);
    }
    TS(1);
    u32x2 gg[2], dk[4];
    u32x4 xv[4];
    PHI();
#pragma unroll
    for (int n = 0; n < 2; ++n) gg[n] = LD64(rs, c * 8 + n * 128, OFF_G + s2t * 1024 + w * 256, 0);
#pragma unroll
    for (int i = 0; i < 4; ++i) dk[i] = LD64(rd, q * 8 + i * 512, dat * 32, 0);
    int fe = LD32(rs, 0, OFF_E + 128, 0);
#pragma unroll
    for (int i = 0; i < 4; ++i) {
        __builtin_amdgcn_s_sleep(KSLP);
        xv[i] = LD128(rs, lane * 16 + i * 1024, OFF_X + (s2t * 16 + dat) * 16384 + w * 4096, 0);
        SCHED();
    }
#pragma unroll
    for (int i = 0; i < 4; ++i) {
        __builtin_amdgcn_s_sleep(KSLP);
        BLDS(rs, smem + B_UT + w * 4096 + i * 1024, (lane >> 1) * 64 + (lane & 1) * 16, OFF_T + (s2t * 16 + dat) * 16384 + w * 4096 + (i & 1) * 2048 + (i >> 1) * 32);
        SCHED();
    }
    PLO();
    TS(2);
    f32x4 r0[2][4];
#pragma unroll
    for (int i = 0; i < 4; ++i) PIN(dk[i]);
#pragma unroll
    for (int i = 0; i < 4; ++i) {
        PIN(xv[i]);
#pragma unroll
        for (int p = 0; p < 2; ++p) {
            const int m = 2 * (i & 1) + p;
            const unsigned x0 = xv[i][2 * p], x1 = xv[i][2 * p + 1];
            const unsigned p0 = pkb(lo2f(x0) * lo2f(dk[m].x), hi2f(x0) * hi2f(dk[m].x)), p1 = pkb(lo2f(x1) * lo2f(dk[m].y), hi2f(x1) * hi2f(dk[m].y));
            r0[i >> 1][m] = (f32x4){lo2f(p0), hi2f(p0), lo2f(p1), hi2f(p1)} * __uint_as_float(gg[i >> 1].y);
            PIN(r0[i >> 1][m]);
        }
    }
    TS(5);
    __builtin_amdgcn_s_waitcnt(0x0F70);
    TS(6);
    aux_sync(cnt, 4u, lane);
    TS(7);
    f32x4 P0[2], P1[2], P2[2], P3a[2], P3b[2];
#pragma unroll
    for (int n = 0; n < 2; ++n) { P0[n] = Z4; P1[n] = Z4; P2[n] = Z4; P3a[n] = Z4; P3b[n] = Z4; }
    lptr tt = smem + B_UT + lane * 8;
#pragma unroll
    for (int h = 0; h < 2; ++h) {
        const u32x4 a1 = trfrag(tt + 1 * 4096 + h * 1024, 512), b1 = trfrag(tt + 1 * 4096 + 2048 + h * 1024, 512);
        const u32x4 a3 = trfrag(tt + 3 * 4096 + h * 1024, 512), b3 = trfrag(tt + 3 * 4096 + 2048 + h * 1024, 512);
        const u32x4 a0 = trfrag(tt + h * 1024, 512), b0 = trfrag(tt + 2048 + h * 1024, 512);
        const u32x4 a2 = trfrag(tt + 2 * 4096 + h * 1024, 512), b2 = trfrag(tt + 2 * 4096 + 2048 + h * 1024, 512);
#pragma unroll
        for (int n = 0; n < 2; ++n) {
            u32x4 cw = fc[n][h], sw_ = fs[n][h];
            P1[n] = mfh(a1, cw, P1[n]); P3a[n] = mfh(a1, sw_, P3a[n]); P3b[n] = mfh(b1, cw, P3b[n]); P1[n] = mfh(b1, sw_, P1[n]);
            if (h == 0) cw.x = q == 0 ? (cw.x & 0xffff0000u) | ((c & 1) ? 0xbc00u : 0x3c00u) : cw.x;
            P3a[n] = mfh(b3, cw, P3a[n]); P3b[n] = mfh(a3, sw_, P3b[n]); P1[n] = mfh(a3, cw, P1[n]); P1[n] = mfh(b3, sw_, P1[n]);
            if (h == 0) { cw.x = q == 0 ? (cw.x & 0xffff0000u) | 0x3800u : cw.x; sw_.x = q == 0 ? (sw_.x & 0xffff0000u) | ((c & 1) ? 0x3800u : 0xb800u) : sw_.x; }
            P0[n] = mfh(a0, cw, P0[n]); P2[n] = mfh(a2, cw, P2[n]); P0[n] = mfh(b0, sw_, P0[n]); P2[n] = mfh(b2, sw_, P2[n]);
        }
    }
#pragma unroll
    for (int n = 0; n < 2; ++n) {
        const float g = __uint_as_float(gg[n].x);
        const f32x4 sp = P0[n] + P2[n], sm = P0[n] - P2[n], p3 = P3a[n] - P3b[n];
        const f32x4 av[4] = {sp + P1[n], sm - p3, sp - P1[n], sm + p3};
#pragma unroll
        for (int m = 0; m < 4; ++m) {
            const unsigned b0 = pkb(ldexpf(av[m][0], fe), ldexpf(av[m][1], fe)), b1 = pkb(ldexpf(av[m][2], fe), ldexpf(av[m][3], fe));
            const unsigned o0 = pkb(g * lo2f(b0), g * hi2f(b0)), o1 = pkb(g * lo2f(b1), g * hi2f(b1));
            lst<f32x4>(smem + B_R0 + ((w * 2 + n) * 4 + m) * 1024 + lane * 16, r0[n][m] + (f32x4){lo2f(o0), hi2f(o0), lo2f(o1), hi2f(o1)});
        }
    }
    LDS_WAIT();
    TS(8);
    TSE(3, w == 0);
    BAR();
}

NOPK __global__ __launch_bounds__(512, 1) void kb_kernel(const u16* __restrict__ Dskip, char* ws, float* __restrict__ out) {
    extern __shared__ __attribute__((aligned(16))) char smem_[];
    lptr smem = (lptr)smem_;
    const int wid = __builtin_amdgcn_readfirstlane(threadIdx.x >> 6), lane = threadIdx.x & 63;
    if (wid < 4) kb_mma(ws, out, smem, blockIdx.x, wid, lane);
    else kb_aux(Dskip, ws, smem, blockIdx.x, wid - 4, lane);
}

__global__ void k0_kernel(char* ws) { }
extern "C" void kernel_launch(void* const* d_in, const int* in_sizes, int n_in, void* d_out, int out_size, void* d_ws, size_t ws_size,
                              hipStream_t stream) {
    const u16* x = (const u16*)d_in[0];
    const u16* w_r = (const u16*)d_in[1];
    const u16* w_i = (const u16*)d_in[2];
    const u16* lam_re = (const u16*)d_in[4];
    const u16* lam_im = (const u16*)d_in[5];
    const u16* Bre = (const u16*)d_in[6];
    const u16* Bim = (const u16*)d_in[7];
    const u16* Cre = (const u16*)d_in[8];
    const u16* Cim = (const u16*)d_in[9];
    const u16* log_dt = (const u16*)d_in[10];
    const u16* Dskip = (const u16*)d_in[11];
    const u16* gate_w = (const u16*)d_in[12];
    char* ws = (char*)d_ws;
    (void)hipFuncSetAttribute((const void*)ka_kernel, hipFuncAttributeMaxDynamicSharedMemorySize, KA_SMEM);
    (void)hipFuncSetAttribute((const void*)kb_kernel, hipFuncAttributeMaxDynamicSharedMemorySize, KB_SMEM);
    ka_kernel<<<256, 768, KA_SMEM, stream>>>(x, w_r, w_i, Bre, Bim, Cre, Cim, lam_re, lam_im, log_dt, gate_w, ws);
    kb_kernel<<<256, 512, KB_SMEM, stream>>>(Dskip, ws, (float*)d_out);
    k0_kernel<<<1, 64, 0, stream>>>(ws);
    k0_kernel<<<1, 64, 0, stream>>>(ws);
}
```

```cpp
#include <hip/hip_runtime.h>
#include <stdint.h>

typedef __attribute__((ext_vector_type(8))) __bf16 bf16x8;
typedef __attribute__((ext_vector_type(2))) __bf16 bf16x2;
typedef __attribute__((ext_vector_type(8))) _Float16 f16x8;
typedef __attribute__((ext_vector_type(2))) _Float16 f16x2;
typedef __attribute__((ext_vector_type(4))) float f32x4;
typedef __attribute__((ext_vector_type(2))) float f32x2;
typedef __attribute__((ext_vector_type(4))) unsigned u32x4;
typedef __attribute__((ext_vector_type(2))) unsigned u32x2;
typedef __attribute__((ext_vector_type(4))) short s16x4;
typedef __attribute__((ext_vector_type(2))) unsigned short u16x2;
typedef unsigned short u16;
typedef unsigned long long u64;
#define GAS __attribute__((address_space(1)))
#define LAS __attribute__((address_space(3)))
#define NOPK __attribute__((target("no-packed-fp32-ops")))
#define RLX_AGENT __ATOMIC_RELAXED, __HIP_MEMORY_SCOPE_AGENT
#define DEV __device__ __forceinline__

constexpr int S_ = 2048, D_ = 1024, KF = 513;

constexpr unsigned OFF_H = 0;
constexpr unsigned OFF_U = OFF_H + (8u << 20);
constexpr unsigned OFF_T = OFF_U + (4u << 20);
constexpr unsigned OFF_W = OFF_T + (4u << 20);
constexpr unsigned OFF_G = OFF_W + 65u * 65536u;
constexpr unsigned OFF_MX = OFF_G + 16384u;
constexpr unsigned OFF_GRAN = OFF_MX + 32768u;
constexpr unsigned OFF_E = OFF_GRAN + 16u * 1024u * 16u;
constexpr unsigned OFF_X = OFF_E + 16u * 128u;
constexpr unsigned OFF_C = OFF_X + (4u << 20);
constexpr unsigned WS_END = OFF_C + (4u << 20);

#define TSD()
#define TS(i)
#define TC(i)
#define TSE(slot, on)

DEV float bf2f(u16 h) { return __uint_as_float((unsigned)h << 16); }
DEV unsigned pkb(float a, float b) { f32x2 v = {a, b}; return __builtin_bit_cast(unsigned, __builtin_convertvector(v, bf16x2)); }
DEV unsigned pkh(float a, float b) { f32x2 v = {a, b}; return __builtin_bit_cast(unsigned, __builtin_convertvector(v, f16x2)); }
DEV float lo2f(unsigned p) { return __uint_as_float(p << 16); }
DEV float hi2f(unsigned p) { return __uint_as_float(p & 0xffff0000u); }
DEV float bfr(float f) { return lo2f(pkb(f, f)); }
DEV float2 cmul(float2 a, float2 b) { return make_float2(a.x * b.x - a.y * b.y, a.x * b.y + a.y * b.x); }
DEV float2 cfma(float2 a, float2 b, float2 c) { return make_float2(fmaf(a.x, b.x, fmaf(-a.y, b.y, c.x)), fmaf(a.x, b.y, fmaf(a.y, b.x, c.y))); }
DEV f32x4 mfb(u32x4 a, u32x4 b, f32x4 c) { return __builtin_amdgcn_mfma_f32_16x16x32_bf16(__builtin_bit_cast(bf16x8, a), __builtin_bit_cast(bf16x8, b), c, 0, 0, 0); }
DEV f32x4 mfh(u32x4 a, u32x4 b, f32x4 c) { return __builtin_amdgcn_mfma_f32_16x16x32_f16(__builtin_bit_cast(f16x8, a), __builtin_bit_cast(f16x8, b), c, 0, 0, 0); }
DEV unsigned pkmax(unsigned a, unsigned b) { return __builtin_bit_cast(unsigned, __builtin_elementwise_max(__builtin_bit_cast(u16x2, a), __builtin_bit_cast(u16x2, b))); }
DEV float vcos(float r) { return __builtin_amdgcn_cosf(__builtin_amdgcn_fractf(r)); }
DEV float vsin(float r) { return __builtin_amdgcn_sinf(__builtin_amdgcn_fractf(r)); }
typedef LAS char* lptr;
DEV unsigned ldsa(lptr p) { return (unsigned)(uintptr_t)p; }
template <class T> DEV T lld(lptr p) { return *(LAS const T*)p; }
template <class T> DEV void lst(lptr p, T v) { *(LAS T*)p = v; }
DEV u32x2 trread(lptr p) { return __builtin_bit_cast(u32x2, __builtin_amdgcn_ds_read_tr16_b64_v4i16((LAS s16x4*)p)); }
DEV u32x4 trfrag(lptr p, int second) { const u32x2 a = trread(p), b = trread(p + second); return (u32x4){a.x, a.y, b.x, b.y}; }
DEV void glds16(const void* g, lptr l) { __builtin_amdgcn_global_load_lds((const GAS unsigned*)g, (LAS unsigned*)l, 16, 0, 0); }
typedef __amdgpu_buffer_rsrc_t rsrc_t;
template <int AUX = 0> DEV void blds16(rsrc_t rs, lptr l, unsigned vo, unsigned so) { __builtin_amdgcn_raw_ptr_buffer_load_lds(rs, (LAS void*)l, 16, vo, so, 0, AUX); }
DEV u32x4 ldfrag(lptr tile, int row, int chunk) { return lld<u32x4>(tile + row * 128 + ((chunk ^ (row & 7)) << 4)); }
template <int N> DEV void wait_vm() { asm volatile("s_waitcnt vmcnt(%0)" :: "i"(N) : "memory"); }
#define LDS_WAIT() asm volatile("s_waitcnt lgkmcnt(0)" ::: "memory")
#define SCHED() __builtin_amdgcn_sched_barrier(0)
#define BAR() do { __builtin_amdgcn_s_barrier(); asm volatile("" ::: "memory"); } while (0)
#define PIN(x) asm volatile("" : "+v"(x))
DEV rsrc_t mkrsrc(const void* p, unsigned n) { return __builtin_amdgcn_make_buffer_rsrc((void*)p, 0, n, 0x00020000); }
constexpr int SC1 = 16;
#define WT 16
#define WTO 16
template <int AUX> DEV void st128(u32x4 v, rsrc_t rs, unsigned off) { __builtin_amdgcn_raw_buffer_store_b128(v, rs, off, 0, AUX); }
DEV void lds_add(unsigned a) { asm volatile("ds_add_u32 %0, %1" :: "v"(a), "v"(1u) : "memory"); }
DEV unsigned lds_peek(unsigned a) {
    unsigned v;
    asm volatile("ds_read_b32 %0, %1\n\ts_waitcnt lgkmcnt(0)" : "=v"(v) : "v"(a) : "memory");
    return __builtin_amdgcn_readfirstlane(v);
}
DEV void tile_of(int gb, int& ta, int& tb) {
    const int xcd = gb & 7, loc = gb >> 3;
    ta = (xcd >> 1) * 4 + (loc >> 3);
    tb = (xcd & 1) * 8 + (loc & 7);
}
DEV void rows4(float a, float& a0, float& a1, float& a2, float& a3) {
    const unsigned u = __float_as_uint(a);
    const auto r = __builtin_amdgcn_permlane32_swap(u, u, false, false);
    const auto p = __builtin_amdgcn_permlane16_swap(r[0], r[0], false, false);
    const auto s = __builtin_amdgcn_permlane16_swap(r[1], r[1], false, false);
    a0 = __uint_as_float(p[0]); a1 = __uint_as_float(p[1]); a2 = __uint_as_float(s[0]); a3 = __uint_as_float(s[1]);
}
DEV float rowsum4(float a) {
    const unsigned u = __float_as_uint(a);
    const auto r = __builtin_amdgcn_permlane32_swap(u, u, false, false);
    const unsigned t = __float_as_uint(__uint_as_float(r[0]) + __uint_as_float(r[1]));
    const auto p = __builtin_amdgcn_permlane16_swap(t, t, false, false);
    return __uint_as_float(p[0]) + __uint_as_float(p[1]);
}
DEV float2 sel4(int q, float2 a, float2 b, float2 c, float2 d) {
    PIN(d.x); PIN(d.y);
    const float2 e = (q & 1) ? b : a, f = (q & 1) ? d : c;
    return (q & 2) ? f : e;
}
template <int R> DEV unsigned ror16(unsigned v) { return __builtin_amdgcn_update_dpp(0u, v, 0x120 + R, 0xf, 0xf, false); }
DEV unsigned wave_pkmax(unsigned m) {
    m = pkmax(m, ror16<8>(m)); m = pkmax(m, ror16<4>(m)); m = pkmax(m, ror16<2>(m)); m = pkmax(m, ror16<1>(m));
    const auto r = __builtin_amdgcn_permlane32_swap(m, m, false, false);
    m = pkmax(r[0], r[1]);
    const auto p = __builtin_amdgcn_permlane16_swap(m, m, false, false);
    return pkmax(p[0], p[1]);
}
DEV float half_sum(float v) {
    v += __uint_as_float(ror16<8>(__float_as_uint(v))); v += __uint_as_float(ror16<4>(__float_as_uint(v)));
    v += __uint_as_float(ror16<2>(__float_as_uint(v))); v += __uint_as_float(ror16<1>(__float_as_uint(v)));
    const auto p = __builtin_amdgcn_permlane16_swap(__float_as_uint(v), __float_as_uint(v), false, false);
    return __uint_as_float(p[0]) + __uint_as_float(p[1]);
}
DEV u32x4 to16(unsigned p0, unsigned p1, unsigned n0, unsigned n1) {
    const auto a = __builtin_amdgcn_permlane16_swap(p0, n0, false, false);
    const auto b = __builtin_amdgcn_permlane16_swap(p1, n1, false, false);
    return (u32x4){a[0], b[0], a[1], b[1]};
}

#define PRIOA 3
#define NREP 16
#define PRIOM 3
#define FLO 1
#define WSTEP 2
#define WSPL 1
#define XSTEP 4
#define PRIOT 2
#define RDPAT {0, 1, 2, -1, 3, 4, -1, 5, 6, -1, 7, -1, -1, -1, -1, -1}
#define Z4 ((f32x4){0.f, 0.f, 0.f, 0.f})
struct CM { u32x4 re, im; };
DEV CM cmat(float t0, float dt) {
    float cc[4], ss[4];
    t0 = __builtin_amdgcn_fractf(t0);
#pragma unroll
    for (int e = 0; e < 4; ++e) { const float f = fmaf((float)e, dt, t0); cc[e] = __builtin_amdgcn_cosf(f); ss[e] = __builtin_amdgcn_sinf(f); }
    const unsigned c0 = pkh(cc[0], cc[1]), c1 = pkh(cc[2], cc[3]), s0 = pkh(ss[0], ss[1]), s1 = pkh(ss[2], ss[3]);
    CM m; m.re = (u32x4){c0, c1, s0 ^ 0x80008000u, s1 ^ 0x80008000u}; m.im = (u32x4){s0, s1, c0, c1};
    PIN(m.re); PIN(m.im);
    return m;
}
DEV u32x4 pk8(f32x4 r, f32x4 i) { return (u32x4){pkh(r[0], r[1]), pkh(r[2], r[3]), pkh(i[0], i[1]), pkh(i[2], i[3])}; }
DEV u32x4 wmul(f32x4 r, f32x4 i, u32x4 w) {
    float yr[4], yi[4];
#pragma unroll
    for (int e = 0; e < 4; ++e) {
        const unsigned wu = w[e];
        const f16x2 h = __builtin_bit_cast(f16x2, wu);
        yr[e] = fmaf(r[e], (float)h[0], -i[e] * (float)h[1]); yi[e] = fmaf(r[e], (float)h[1], i[e] * (float)h[0]);
    }
    return (u32x4){pkh(yr[0], yr[1]), pkh(yr[2], yr[3]), pkh(yi[0], yi[1]), pkh(yi[2], yi[3])};
}
DEV u32x4 wcvt(u32x4 w, float sw) {
#pragma unroll
    for (int e = 0; e < 4; ++e) w[e] = pkh(lo2f(w[e]) * sw, hi2f(w[e]) * sw);
    PIN(w);
    return w;
}
DEV void aux_sync(unsigned cnt, unsigned target, int lane) {
    asm volatile("" ::: "memory");
    if (lane == 0) lds_add(cnt);
#pragma unroll 1
    for (int spin = 0; spin < (1 << 24); ++spin) { if (lds_peek(cnt) >= target) break; }
}

constexpr int A_STAGE = 32768, A_XT = 4 * A_STAGE, A_PRM = A_XT + 16384, A_AGG = A_PRM + 2048, A_FLG = A_AGG + 512, A_GW = A_FLG + 64,
              A_MAT = A_GW + 2048, KA_SMEM = A_MAT + 10240;
constexpr int A_UT = 16384, A_E1 = 2 * A_STAGE, E1P = 1040, A_EX = A_E1 + 18 * E1P + 32, A_E2 = A_XT;

DEV void ka_mma(char* ws, lptr smem, int gb, int wid, int lane) {
    int lt, pt; tile_of(gb, lt, pt);
    const int wr = wid >> 1, wc = wid & 1, c = lane & 15, q = lane >> 4;
    const rsrc_t rs = mkrsrc(ws, WS_END);
    TSD(); TS(0);
    u64 tag = __hip_atomic_load((GAS u64*)(ws + OFF_E), __ATOMIC_RELAXED, __HIP_MEMORY_SCOPE_WORKGROUP) + 0x9E3779B97F4A7C15ull;
    if (wid == 0 && lane < 8) lst<unsigned>(smem + A_FLG + lane * 4, 0u);
    f32x4 acc[4][4];
#pragma unroll
    for (int m = 0; m < 4; ++m)
#pragma unroll
        for (int n = 0; n < 4; ++n) acc[m][n] = Z4;
    u32x4 af[2][4], bf_[2][4];
#pragma unroll
    for (int m = 0; m < 4; ++m) { af[1][m] = (u32x4){0, 0, 0, 0}; bf_[1][m] = af[1][m]; }
    PIN(tag);
    if (wid == 0 && lane == 0) lst<u64>(smem + A_FLG + 32, tag);
    tag = (u64)(unsigned)__builtin_amdgcn_readfirstlane((unsigned)tag) | (u64)(unsigned)__builtin_amdgcn_readfirstlane((unsigned)(tag >> 32)) << 32;
    LDS_WAIT();
#pragma unroll 4
    for (int kt = 0; kt < 16; ++kt) {
        BAR();
        lptr tA = smem + (kt & 3) * A_STAGE;
        lptr tB = tA + 16384;
#pragma unroll
        for (int hf = 0; hf < 2; ++hf)
#pragma unroll
            for (int i = 0; i < 16; ++i) {
                acc[i >> 2][i & 3] = mfb(af[1 - hf][i >> 2], bf_[1 - hf][i & 3], acc[i >> 2][i & 3]);
                constexpr int RD[16] = RDPAT;
                const int r = RD[i];
                if (r == 0) af[hf][0] = ldfrag(tA, wr * 64 + c, 4 * hf + q);
                else if (r > 0 && r < 5) bf_[hf][r - 1] = ldfrag(tB, wc * 64 + (r - 1) * 16 + c, 4 * hf + q);
                else if (r >= 5) af[hf][r - 4] = ldfrag(tA, wr * 64 + (r - 4) * 16 + c, 4 * hf + q);
                SCHED();
            }
        LDS_WAIT();
    }
    __builtin_amdgcn_s_setprio(PRIOT);
    float2 lam[2], coef[2], l16[2], l128[2];
#pragma unroll
    for (int n = 0; n < 2; ++n) {
        const f32x4 a = lld<f32x4>(smem + A_PRM + (wc * 32 + 2 * c + n) * 32), b = lld<f32x4>(smem + A_PRM + (wc * 32 + 2 * c + n) * 32 + 16);
        lam[n] = make_float2(a[0], a[1]); coef[n] = make_float2(a[2], a[3]); l16[n] = make_float2(b[0], b[1]); l128[n] = make_float2(b[2], b[3]);
    }
#pragma unroll
    for (int m = 0; m < 4; ++m)
#pragma unroll
        for (int n = 0; n < 4; ++n) acc[m][n] = mfb(af[1][m], bf_[1][n], acc[m][n]);
    float2 l32[2], l64[2];
#pragma unroll
    for (int n = 0; n < 2; ++n) { l32[n] = cmul(l16[n], l16[n]); l64[n] = cmul(l32[n], l32[n]); }
    TS(1);
    float2 h[2][16];
#pragma unroll
    for (int n = 0; n < 2; ++n) h[n][0] = make_float2(acc[0][n][0], acc[0][2 + n][0]);
#pragma unroll
    for (int t = 1; t < 16; ++t)
#pragma unroll
        for (int n = 0; n < 2; ++n) h[n][t] = cfma(lam[n], h[n][t - 1], make_float2(acc[t >> 2][n][t & 3], acc[t >> 2][2 + n][t & 3]));
    float2 loc[2], wag[2], wq[2];
#pragma unroll
    for (int n = 0; n < 2; ++n) {
        float2 a0, a1, a2, a3;
        rows4(h[n][15].x, a0.x, a1.x, a2.x, a3.x);
        rows4(h[n][15].y, a0.y, a1.y, a2.y, a3.y);
        const float2 s2 = cfma(l16[n], a0, a1), s3 = cfma(l16[n], s2, a2);
        wag[n] = cfma(l16[n], s3, a3);
        loc[n] = sel4(q, make_float2(0.f, 0.f), a0, s2, s3);
    }
    lptr aggp = smem + A_AGG + (wc * 16 + c) * 16;
    const unsigned flg = ldsa(smem + A_FLG) + wc * 4;
    float2 W0[2] = {make_float2(0.f, 0.f), make_float2(0.f, 0.f)};
    if (wr == 0) {
        if (q == 0) lst<f32x4>(aggp, (f32x4){wag[0].x, wag[0].y, wag[1].x, wag[1].y});
        asm volatile("" ::: "memory");
        if (lane == 0) lds_add(flg);
    } else {
#pragma unroll 1
        for (int spin = 0; spin < (1 << 20); ++spin) { if (lds_peek(flg)) break; }
        const f32x4 w0 = lld<f32x4>(aggp);
        W0[0] = make_float2(w0[0], w0[1]); W0[1] = make_float2(w0[2], w0[3]);
        const float2 g0 = cfma(l64[0], W0[0], wag[0]), g1 = cfma(l64[1], W0[1], wag[1]);
        if (lt < 15 && q < 2) {
            const float2 gg = q == 0 ? g0 : g1;
            st128<SC1>((u32x4){__float_as_uint(gg.x), (unsigned)tag, __float_as_uint(gg.y), (unsigned)(tag >> 32)},
                       rs, (2 * c + q) * 16 + OFF_GRAN + ((15 - lt) * 1024 + pt * 64 + wc * 32) * 16);
        }
    }
    TS(2);
#pragma unroll
    for (int n = 0; n < 2; ++n) { PIN(lam[n].x); PIN(lam[n].y); PIN(coef[n].x); PIN(coef[n].y); PIN(l16[n].x); PIN(l16[n].y); PIN(l128[n].x); PIN(l128[n].y); }
    float2 pw[2][8], l8[2], lq[2], l512[2];
#pragma unroll
    for (int n = 0; n < 2; ++n) {
        wq[n] = sel4(q, make_float2(1.f, 0.f), l16[n], l32[n], cmul(l32[n], l16[n]));
        loc[n] = cfma(wq[n], W0[n], loc[n]);
        if (wr) wq[n] = cmul(wq[n], l64[n]);
        pw[n][0] = cmul(coef[n], lam[n]);
        l8[n] = cmul(lam[n], lam[n]); l8[n] = cmul(l8[n], l8[n]); l8[n] = cmul(l8[n], l8[n]);
        const float2 l256 = cmul(l128[n], l128[n]);
        lq[n] = sel4(q, make_float2(1.f, 0.f), l128[n], l256, cmul(l256, l128[n]));
        l512[n] = cmul(l256, l256);
    }
#pragma unroll
    for (int n = 0; n < 2; ++n) {
        PIN(wq[n].x); PIN(wq[n].y); PIN(loc[n].x); PIN(loc[n].y); PIN(l8[n].x); PIN(l8[n].y); PIN(lq[n].x); PIN(lq[n].y); PIN(l512[n].x); PIN(l512[n].y);
    }
#pragma unroll
    for (int t = 0; t < 8; ++t) {
#pragma unroll
        for (int n = 0; n < 2; ++n) {
            if (t) pw[n][t] = cmul(pw[n][t - 1], lam[n]);
            h[n][2 * t] = cmul(coef[n], h[n][2 * t]); h[n][2 * t + 1] = cmul(coef[n], h[n][2 * t + 1]);
        }
#pragma unroll
        for (int n = 0; n < 2; ++n) { PIN(pw[n][t].x); PIN(pw[n][t].y); PIN(h[n][2 * t].x); PIN(h[n][2 * t].y); PIN(h[n][2 * t + 1].x); PIN(h[n][2 * t + 1].y); }
        SCHED();
    }
    TS(3);
    if (lt > 0) {
        u32x4 gr[4][2];
        unsigned off = ((16 - lt + q) * 1024 + pt * 64 + wc * 32 + 2 * c) * 16;
#pragma unroll 1
        for (int spin = 0; spin < (1 << 20); ++spin) {
            bool ok = true;
            PIN(off);
#pragma unroll
            for (int j = 0; j < 4; ++j) {
                const rsrc_t rg = mkrsrc(ws + OFF_GRAN + j * 65536, (4 - j) * 65536);
                gr[j][0] = __builtin_amdgcn_raw_buffer_load_b128(rg, off, 0, SC1);
                gr[j][1] = __builtin_amdgcn_raw_buffer_load_b128(rg, off + 16, 0, SC1);
            }
#pragma unroll
            for (int j = 0; j < 4; ++j) {
                const bool v = lt - 1 - (4 * j + q) >= 0;
#pragma unroll
                for (int n = 0; n < 2; ++n) ok = ok & (!v | ((gr[j][n].y == (unsigned)tag) & (gr[j][n].w == (unsigned)(tag >> 32))));
            }
            if (__all(ok)) break;
            asm volatile("" ::: "memory");
        }
#pragma unroll
        for (int n = 0; n < 2; ++n) {
            float2 cs = make_float2(__uint_as_float(gr[3][n].x), __uint_as_float(gr[3][n].z));
#pragma unroll
            for (int j = 2; j >= 0; --j) cs = cfma(l512[n], cs, make_float2(__uint_as_float(gr[j][n].x), __uint_as_float(gr[j][n].z)));
            cs = cmul(cs, lq[n]);
            cs.x = rowsum4(cs.x); cs.y = rowsum4(cs.y);
            loc[n] = cfma(wq[n], cs, loc[n]);
        }
    }
    TS(4);
    lptr tb = smem + A_STAGE + wid * 8192;
    PIN(lane);
#pragma unroll
    for (int hf = 0; hf < 2; ++hf) {
        const float2 s0 = hf ? cmul(l8[0], loc[0]) : loc[0], s1 = hf ? cmul(l8[1], loc[1]) : loc[1];
#pragma unroll
        for (int t = 0; t < 8; ++t) {
            const float2 v0 = cfma(pw[0][t], s0, h[0][8 * hf + t]), v1 = cfma(pw[1][t], s1, h[1][8 * hf + t]);
            lst<unsigned>(tb + (8 * hf + t) * 256 + lane * 4, pkb(v0.x, v1.x));
            lst<unsigned>(tb + 4096 + (8 * hf + t) * 256 + lane * 4, pkb(-v0.y, -v1.y));
        }
        asm volatile("" ::: "memory");
        lptr rb = tb + (lane >> 4) * 256 + (lane & 3) * 64 + ((lane >> 2) & 3) * 16;
        const unsigned hoff = OFF_H + ((lane >> 4) * 64 + pt * 2 + wc) * 8192 + lt * 512 + wr * 256 + (lane & 15) * 16;
#pragma unroll
        for (int v = 2 * hf; v < 2 * hf + 2; ++v)
#pragma unroll
            for (int pl = 0; pl < 2; ++pl) st128<WT>(lld<u32x4>(rb + pl * 4096 + v * 1024), rs, hoff + (v * 256 + pl * 32) * 8192);
    }
    TS(5);
    TSE(0, wid == 0);
}

struct __attribute__((packed, aligned(2))) U64U { u32x2 v; };
DEV void w_load(const u16* __restrict__ w, int k1, int qw, int lw, int lane, u32x2 (&R)[4]) {
    const int kd0 = 4 * (lane & 15) + 64 * (4 * lw + (lane >> 4));
    const int col = lw < 2 ? kd0 : 1021 - kd0;
#pragma unroll
    for (int r = 0; r < 4; ++r) {
        const int ks = k1 + 128 * (4 * qw + r), row = lw < 2 ? ks : (S_ - ks) & (S_ - 1);
        R[r] = reinterpret_cast<const U64U*>(w + row * KF + col)->v;
    }
}
DEV unsigned w_part(const u16* __restrict__ w_r, const u16* __restrict__ w_i, int k1, int qw, int lw, int lane) {
    const int ks = k1 + 128 * (4 * qw + (lane & 3)), row = lw < 2 ? (S_ - ks) & (S_ - 1) : ks;
    return ((lane & 4) ? w_i : w_r)[row * KF + (lw < 2 ? 0 : 512)];
}
DEV void w_row(int r, int lw, int lane, u32x2 R, u32x2 I, unsigned sp, u32x4 (&o)[4], unsigned& mx) {
    const unsigned cj = lw < 2 ? 0u : 0x80008000u;
    const unsigned i0 = I.x ^ cj, i1 = I.y ^ cj;
    unsigned o0 = __builtin_amdgcn_perm(i0, R.x, 0x05040100u), o3 = __builtin_amdgcn_perm(i1, R.y, 0x07060302u);
    o[1][r] = __builtin_amdgcn_perm(i0, R.x, 0x07060302u); o[2][r] = __builtin_amdgcn_perm(i1, R.y, 0x05040100u);
    const float pr = lo2f(__builtin_amdgcn_readlane(sp, r)), pi = lo2f(__builtin_amdgcn_readlane(sp, 4 + r) ^ (lw < 2 ? 0x8000u : 0u));
    const unsigned t = lw < 2 ? o0 : o3;
    unsigned v = pkb(0.5f * (lo2f(t) + pr), 0.5f * (hi2f(t) + pi));
    PIN(v);
    o[0][r] = (lane == 0 && lw == 0) ? v : o0; o[3][r] = (lane == 0 && lw == 2) ? v : o3;
    mx = pkmax(mx, pkmax(pkmax(R.x & 0x7fff7fffu, R.y & 0x7fff7fffu), pkmax(I.x & 0x7fff7fffu, I.y & 0x7fff7fffu)));
}
DEV void w_store(rsrc_t rs, int k1, int qw, int lw, int lane, u32x4 (&o)[4], int e0, int e1) {
#pragma unroll
    for (int e = e0; e < e1; ++e)
        st128<SC1>(o[e], rs, (lane & 15) * 1024 + (lane >> 4) * 16 + OFF_W + (k1 * 4 + (lw < 2 ? e : 3 - e)) * 16384 + qw * 256 + lw * 64);
}

#define TSARG
#define TSPASS
DEV void ka_load(const u16* __restrict__ x, const u16* __restrict__ Bre, const u16* __restrict__ Bim, lptr smem, int gb, int lw, int lane TSARG) {
    int lt, pt; tile_of(gb, lt, pt);
    const int srow = lane >> 3, schunk = (lane & 7) ^ srow;
    const int rot = (lt + pt) & 15;
    const rsrc_t rx = mkrsrc(x, S_ * D_ * 2), rb = mkrsrc((lw & 1) ? Bim : Bre, D_ * D_ * 2);
    const unsigned vA = (16 * (srow >> 2) + (srow & 3)) * 2048 + schunk * 16, sA = (lt * 128 + 64 * (lw >> 1) + 8 * (lw & 1)) * 2048;
    const unsigned vB = 2 * srow * 2048 + schunk * 16, sB = (pt * 64 + 32 * (lw >> 1)) * 2048;
    auto stage = [&](int kk) {
        const int ko = ((kk + rot) & 15) * 128;
        lptr l = smem + (kk & 3) * A_STAGE + lw * 4096;
#pragma unroll
        for (int i = 0; i < 4; ++i) blds16(rx, l + i * 1024, vA, sA + (32 * (i & 1) + 4 * (i >> 1)) * 2048 + ko);
#pragma unroll
        for (int i = 0; i < 4; ++i) blds16(rb, l + 16384 + i * 1024, vB, sB + (16 * (i & 1) + (i >> 1)) * 2048 + ko);
    };
    TS(0);
    stage(0);
#pragma unroll
    for (int i = 0; i < 4; ++i) blds16(rx, smem + A_XT + lw * 4096 + i * 1024, (lane >> 1) * 32768 + (lane & 1) * 16, (i * 512 + lt) * 2048 + lw * 512 + pt * 32);
    stage(1); stage(2);
    TS(6);
#pragma unroll 1
    for (int kt = 0; kt < 13; ++kt) { wait_vm<16>(); TS(8 + kt); BAR(); TS(28 + kt); if (kt == 0) TC(60); stage(kt + 3); }
}

DEV void gate_step(float& gsum, unsigned xaddr, unsigned waddr) {
    unsigned xv, gw;
    asm volatile("ds_read_b32 %0, %2\n\tds_read_b32 %1, %3\n\ts_waitcnt lgkmcnt(0)" : "=&v"(xv), "=&v"(gw) : "v"(xaddr), "v"(waddr) : "memory");
    const unsigned pr = pkb(lo2f(xv) * lo2f(gw), hi2f(xv) * hi2f(gw));
    gsum += lo2f(pr); gsum += hi2f(pr); PIN(gsum);
}
DEV void gate_addr(lptr smem, int pt, int aw, int lane, int& gi, unsigned& gaddr, unsigned& gwaddr) {
    gi = 8 * pt + 2 * aw + (lane >> 5);
    gaddr = ldsa(smem) + gi * 128 + ((((lane & 31) >> 2) ^ (gi & 7)) << 4) + (lane & 3) * 4;
    gwaddr = ldsa(smem + A_GW) + (lane & 31) * 4;
}
DEV CM mat_sf(int k1, int c, int q) { const float dt = -(float)(k1 + 128 * c) * (1.0f / 2048.0f); return cmat(dt * (float)(4 * q), dt); }
DEV CM mat_si(int k1, int c, int q) { return cmat((float)((k1 + 512 * q) * c) * (1.0f / 2048.0f), (float)c * (1.0f / 16.0f)); }

DEV float ka_aux(const u16* __restrict__ w_r, const u16* __restrict__ w_i, const u16* __restrict__ lam_re, const u16* __restrict__ lam_im,
                 const u16* __restrict__ log_dt, const u16* __restrict__ gate_w, char* ws, lptr smem, int gb, int aw, int lane TSARG) {
    int lt, pt; tile_of(gb, lt, pt);
    const int c = lane & 15, q = lane >> 4;
    const int rot = (lt + pt) & 15;
    const rsrc_t rs = mkrsrc(ws, WS_END);
    TS(0);
    __builtin_amdgcn_s_setprio(PRIOA);
    const u32x2 gwv = *reinterpret_cast<const u32x2*>(gate_w + aw * 256 + lane * 4);
    unsigned lru = lam_re[pt * 64 + lane], liu = lam_im[pt * 64 + lane], ldu = log_dt[pt * 64 + lane];
    SCHED();
    int gi; unsigned gaddr, gwaddr; gate_addr(smem, pt, aw, lane, gi, gaddr, gwaddr);
    float gsum = 0.f;
    u32x4 fch, fcl, fsh, fsl;
    const bool m0 = 16 * aw + c == 0;
    {
        const float th = (float)(16 * aw + c) * (1.0f / 128.0f);
#pragma unroll
        for (int i = 0; i < 4; ++i) {
            const float t0 = __builtin_amdgcn_fractf(th * (float)(16 * (i >> 1) + 2 * (i & 1) + 4 * q)), t1 = t0 + th;
            const float c0 = __builtin_amdgcn_cosf(t0), c1 = __builtin_amdgcn_cosf(t1);
            const float s0 = m0 ? 1.f : -__builtin_amdgcn_sinf(t0), s1 = m0 ? -1.f : -__builtin_amdgcn_sinf(t1);
            const unsigned ch = pkb(c0, c1), sh = pkb(s0, s1);
            fch[i] = ch; fcl[i] = pkb(c0 - lo2f(ch), c1 - hi2f(ch));
            fsh[i] = sh; fsl[i] = pkb(s0 - lo2f(sh), s1 - hi2f(sh));
        }
        PIN(fch); PIN(fcl); PIN(fsh); PIN(fsl);
    }
    {
        const int mp = gb >> 2, j1 = gb & 3;
        const bool spc = mp == 0 && !(j1 & 1);
        const int k1 = mp ? mp : (j1 == 3 ? 64 : 0);
        lptr mt = smem + A_MAT + lane * 16;
        if (aw == 0) {
            const float dt = -(float)(j1 + 4 * (spc ? c & 7 : c)) * (1.0f / 64.0f), t0 = __builtin_amdgcn_fractf(dt * (float)(4 * q));
            float cc[4], ss[4];
#pragma unroll
            for (int e = 0; e < 4; ++e) { const float f = fmaf((float)e, dt, t0); cc[e] = __builtin_amdgcn_cosf(f); ss[e] = __builtin_amdgcn_sinf(f); }
            const unsigned c0 = pkb(cc[0], cc[1]), c1 = pkb(cc[2], cc[3]), s0 = pkb(ss[0], ss[1]), s1 = pkb(ss[2], ss[3]);
            const unsigned c0l = pkb(cc[0] - lo2f(c0), cc[1] - hi2f(c0)), c1l = pkb(cc[2] - lo2f(c1), cc[3] - hi2f(c1));
            const unsigned s0l = pkb(ss[0] - lo2f(s0), ss[1] - hi2f(s0)), s1l = pkb(ss[2] - lo2f(s1), ss[3] - hi2f(s1));
            u32x4 m[4] = {(u32x4){c0, c1, s0 ^ 0x80008000u, s1 ^ 0x80008000u}, (u32x4){s0, s1, c0, c1},
                          (u32x4){c0l, c1l, s0l ^ 0x80008000u, s1l ^ 0x80008000u}, (u32x4){s0l, s1l, c0l, c1l}};
            if (spc) {
                m[0] = c < 8 ? (u32x4){c0, c1, 0, 0} : (u32x4){0, 0, c0, c1}; m[1] = c < 8 ? (u32x4){s0, s1, 0, 0} : (u32x4){0, 0, s0, s1};
                m[2] = c < 8 ? (u32x4){c0l, c1l, 0, 0} : (u32x4){0, 0, c0l, c1l}; m[3] = c < 8 ? (u32x4){s0l, s1l, 0, 0} : (u32x4){0, 0, s0l, s1l};
            }
#pragma unroll
            for (int i = 0; i < 4; ++i) lst<u32x4>(mt + i * 1024, m[i]);
        } else if (aw == 1) {
            const int jq = spc ? (4 * q) & 7 : 4 * q;
            float cc[4], ss[4];
            const float t0 = __builtin_amdgcn_fractf((float)(c * (j1 + 4 * jq)) * (1.0f / 64.0f));
#pragma unroll
            for (int e = 0; e < 4; ++e) {
                const float f = fmaf((float)c, (float)e * (1.0f / 16.0f), t0);
                const float wg = !spc || (gb == 0 && jq + e == 0) ? 0.125f : 0.25f;
                cc[e] = __builtin_amdgcn_cosf(f) * wg; ss[e] = -__builtin_amdgcn_sinf(f) * wg;
            }
            const unsigned c0 = pkh(cc[0], cc[1]), c1 = pkh(cc[2], cc[3]), n0 = pkh(ss[0], ss[1]), n1 = pkh(ss[2], ss[3]);
            u32x4 mia = (u32x4){c0, n0, c1, n1}, mib = (u32x4){n0, c0 ^ 0x80008000u, n1, c1 ^ 0x80008000u};
            if (spc) { mib = q < 2 ? (u32x4){0, 0, 0, 0} : mia ^ 0x80008000u; if (q >= 2) mia = (u32x4){0, 0, 0, 0}; }
            lst<u32x4>(mt + 8 * 1024, mia); lst<u32x4>(mt + 9 * 1024, mib);
        } else {
            const CM m = aw == 2 ? mat_sf(k1, c, q) : mat_si(k1, c, q);
            lst<u32x4>(mt + 2 * aw * 1024, m.re); lst<u32x4>(mt + (2 * aw + 1) * 1024, m.im);
        }
    }
    const unsigned sg64 = (c & 1) ? 0x80008000u : 0u, sgA = (c & 2) ? 0x80008000u : 0u, sgB = ((c + 1) & 2) ? 0x80008000u : 0u;
    PIN(lru); PIN(liu); PIN(ldu);
    lst<u32x2>(smem + A_GW + aw * 512 + lane * 8, gwv);
    LDS_WAIT();
    u32x2 R[4], I[4], R2[4], I2[4]; unsigned sp = 0, sp2 = 0, mx = 0, um = 0;
#pragma unroll
    for (int r = 0; r < 4; ++r) { R2[r] = (u32x2){0, 0}; I2[r] = R2[r]; }
    u32x4 wo[4], uo[4];
    f32x4 vr[4], vi[4];
    float dt = 0.f, er = 0.f; float2 lam = make_float2(0.f, 0.f);
#pragma unroll
    for (int d1 = 0; d1 < 4; ++d1) { vr[d1] = Z4; vi[d1] = Z4; }
#pragma unroll
    for (int kt = 0; kt < 13; ++kt) {
        TS(8 + kt);
        BAR();
        u32x4 xa[2];
        if (kt < 8) {
            constexpr int KSO[4] = {0, 2, 3, 1};
#pragma unroll
            for (int i = 0; i < 2; ++i) xa[i] = trfrag(smem + A_XT + (2 * (kt & 1) + i) * 4096 + KSO[kt >> 1] * 1024 + lane * 8, 512);
        }
        u32x2 xc[4];
        if (kt == XSTEP || kt == XSTEP + 1) {
#pragma unroll
            for (int i = 0; i < 4; ++i) xc[i] = lld<u32x2>(smem + A_XT + i * 4096 + (32 * aw + 16 * (kt - XSTEP) + c) * 32 + q * 8);
        }
        gate_step(gsum, gaddr + (kt & 3) * A_STAGE, gwaddr + ((kt + rot) & 15) * 128);
        if (kt < 8) {
#pragma unroll
            for (int i = 0; i < 2; ++i) { const int d1 = 2 * (kt & 1) + i; vr[d1] = mfb(xa[i], fcl, vr[d1]); vi[d1] = mfb(xa[i], fsl, vi[d1]); }
#pragma unroll
            for (int i = 0; i < 2; ++i) { const int d1 = 2 * (kt & 1) + i; vr[d1] = mfb(xa[i], fch, vr[d1]); vi[d1] = mfb(xa[i], fsh, vi[d1]); PIN(vr[d1]); PIN(vi[d1]); }
        }
        if (kt == 1 || kt == 5) { fch ^= sg64; fsh ^= sg64; PIN(fch); PIN(fsh); if (FLO) { fcl ^= sg64; fsl ^= sg64; PIN(fcl); PIN(fsl); } }
        if (kt == 3) {
            const bool od = c & 1;
            const u32x4 a = (od ? fsh : fch) ^ sgA, b = (od ? fch : fsh) ^ sgB, al = (od ? fsl : fcl) ^ sgA, bl = (od ? fcl : fsl) ^ sgB;
            fch = a; fsh = b; PIN(fch); PIN(fsh);
            if (FLO) { fcl = al; fsl = bl; PIN(fcl); PIN(fsl); }
        }
        if (kt == WSTEP) {
            if (gb < 4) { w_load(w_r, 64, gb & 3, aw, lane, R2); w_load(w_i, 64, gb & 3, aw, lane, I2); sp2 = w_part(w_r, w_i, 64, gb & 3, aw, lane); }
            w_load(w_r, gb >> 2, gb & 3, aw, lane, R);
            sp = w_part(w_r, w_i, gb >> 2, gb & 3, aw, lane);
        }
        if (kt == WSTEP + WSPL) w_load(w_i, gb >> 2, gb & 3, aw, lane, I);
        if (kt == XSTEP || kt == XSTEP + 1) {
#pragma unroll
            for (int i = 0; i < 2; ++i)
                st128<WT>((u32x4){xc[2 * i].x, xc[2 * i].y, xc[2 * i + 1].x, xc[2 * i + 1].y}, rs,
                          OFF_X + (lt * 16 + pt) * 16384 + aw * 4096 + (2 * (kt - XSTEP) + i) * 1024 + lane * 16);
        }
        if (kt == 6 || kt == 7) {
            if (kt == 6) { PIN(sp); mx = sp & 0x7fffu; }
#pragma unroll
            for (int r = 2 * (kt - 6); r < 2 * (kt - 6) + 2; ++r) {
                PIN(R[r]); PIN(I[r]);
                w_row(r, aw, lane, R[r], I[r], sp, wo, mx);
                PIN(wo[0][r]); PIN(wo[1][r]); PIN(wo[2][r]); PIN(wo[3][r]); PIN(mx);
            }
        }
        if (kt == 8) {
            f32x4 ur[4], ui[4];
            const f32x4 t0r = vr[0] + vr[2], t1r = vr[0] - vr[2], t2r = vr[1] + vr[3], t3r = vr[1] - vr[3];
            const f32x4 t0i = vi[0] + vi[2], t1i = vi[0] - vi[2], t2i = vi[1] + vi[3], t3i = vi[1] - vi[3];
            ur[0] = t0r + t2r; ui[0] = t0i + t2i; ur[2] = t0r - t2r; ui[2] = t0i - t2i;
            ur[1] = t1r + t3i; ui[1] = t1i - t3r; ur[3] = t1r - t3i; ui[3] = t1i + t3r;
            if (m0) { ur[1] = t1r; ui[1] = -t3r; ur[3] = t1i; ui[3] = t3i; }
#pragma unroll
            for (int j1 = 0; j1 < 4; ++j1) {
                uo[j1] = (u32x4){pkb(ur[j1][0], ur[j1][1]), pkb(ur[j1][2], ur[j1][3]), pkb(ui[j1][0], ui[j1][1]), pkb(ui[j1][2], ui[j1][3])};
                PIN(uo[j1]);
            }
        }
        if (kt >= 8 && kt < 12) {
            const int j1 = kt - 8;
            PIN(uo[j1]);
            const u32x4 o = to16(uo[j1].x, uo[j1].y, uo[j1].z, uo[j1].w);
            um = pkmax(um, pkmax(pkmax(o.x & 0x7fff7fffu, o.y & 0x7fff7fffu), pkmax(o.z & 0x7fff7fffu, o.w & 0x7fff7fffu)));
            PIN(um);
            if (aw == 1) {
                const float lr = lo2f(lru), li = lo2f(liu);
                if (kt == 9) { dt = bfr(expf(lo2f(ldu))); er = expf(dt * lr); PIN(dt); PIN(er); }
                if (kt == 10) { float sn, cs; sincosf(dt * li, &sn, &cs); lam = make_float2(er * cs, er * sn); PIN(lam.x); PIN(lam.y); }
                if (kt == 11) {
                    const float nr = lam.x - 1.0f, ni = lam.y, den = lr * lr + li * li;
                    const float2 coef = make_float2((nr * lr + ni * li) / den, (ni * lr - nr * li) / den);
                    float2 l16 = lam;
#pragma unroll
                    for (int i = 0; i < 4; ++i) l16 = cmul(l16, l16);
                    float2 l128 = l16;
#pragma unroll
                    for (int i = 0; i < 3; ++i) l128 = cmul(l128, l128);
                    lst<f32x4>(smem + A_PRM + lane * 32, (f32x4){lam.x, lam.y, coef.x, coef.y});
                    lst<f32x4>(smem + A_PRM + lane * 32 + 16, (f32x4){l16.x, l16.y, l128.x, l128.y});
                }
            }
            st128<SC1>(o, rs, c * 65536 + (q & 1) * 32 + (q >> 1) * 16 + OFF_U + (aw * 64 + j1) * 16384 + lt * 1024 + pt * 64);
            w_store(rs, gb >> 2, gb & 3, aw, lane, wo, j1, j1 + 1);
        }
        if (kt == 12) {
            if (gb < 4) {
                mx = pkmax(mx, sp2 & 0x7fffu);
#pragma unroll
                for (int r = 0; r < 4; ++r) w_row(r, aw, lane, R2[r], I2[r], sp2, wo, mx);
                w_store(rs, 64, gb & 3, aw, lane, wo, 0, 4);
            }
            mx = wave_pkmax(__builtin_amdgcn_perm(pkmax(um, um >> 16), pkmax(mx, mx >> 16), 0x05040100u));
            if (lane == 0) lst<unsigned>(smem + A_FLG + 16 + aw * 4, mx);
            LDS_WAIT();
        }
        SCHED();
    }
    return gsum;
}

DEV void ka_mid(const u16* __restrict__ Cre, const u16* __restrict__ Cim, char* ws, lptr smem, int gb, int w, int lane, float gsum TSARG) {
    int lt, pt; tile_of(gb, lt, pt);
    const int c = lane & 15, q = lane >> 4, mp = gb >> 2, j1 = gb & 3, rot = (lt + pt) & 15;
    const bool spc = mp == 0 && !(j1 & 1), xtr = gb == 0, ld = w >= 4;
    const int k1 = mp ? mp : spc ? (w >> 2) * 64 : (j1 == 1 ? 0 : 64);
    const int ja0 = spc ? (2 * w) & 7 : 2 * w;
    const rsrc_t rs = mkrsrc(ws, WS_END);
    int gi; unsigned gaddr, gwaddr; gate_addr(smem, pt, w, lane, gi, gaddr, gwaddr);
    float cf = (float)c;
    CM gf[2], gi_[2];
    u32x2 tag;
#pragma unroll
    for (int kt = 13; kt < 16; ++kt) {
        if (ld) { if (kt == 13) wait_vm<16>(); else if (kt == 14) wait_vm<12>(); else wait_vm<4>(); }
        else if (kt == 14) wait_vm<0>();
        TS(8 + kt);
        BAR();
        TS(28 + kt);
        if (ld) {
            if (kt == 13) {
                const rsrc_t rc = mkrsrc((gb & 8) ? Cim : Cre, D_ * D_ * 2);
#pragma unroll
                for (int i = 0; i < 4; ++i)
                    blds16(rc, smem + (w - 4) * 4096 + i * 1024, q * 2048 + (c ^ (4 * q)) * 16, (256 * (w - 4) + (gb & ~15) + 4 * i) * 2048 + (gb & 7) * 256);
            }
        } else gate_step(gsum, gaddr + (kt & 3) * A_STAGE, gwaddr + ((kt + rot) & 15) * 128);
        if (kt == 13) asm volatile("ds_read_b64 %0, %1\n\ts_waitcnt lgkmcnt(0)" : "=v"(tag) : "v"(ldsa(smem + A_FLG + 32)) : "memory");
        if (kt == 14 && w == 0) {
            const u32x4 m4 = lld<u32x4>(smem + A_FLG + 16);
            const unsigned m = pkmax(pkmax(m4.x, m4.y), pkmax(m4.z, m4.w));
            if (lane < NREP)
                __builtin_amdgcn_raw_buffer_store_b64((u32x2){(tag.x & 0xffff0000u) | ((m >> 15) & 0xff00u) | ((m & 0xffffu) >> 7), tag.y}, rs, lane * 2048, OFF_MX + gb * 8, SC1);
        }
        if (kt < 15) {
            PIN(cf);
            const float dt = fmaf(cf, -1.0f / 16.0f, -(float)(j1 + 4 * (ja0 + kt - 13)) * (1.0f / 1024.0f));
            gf[kt - 13] = cmat(dt * (float)(4 * q), dt);
        }
        SCHED();
    }
    TC(61);
    const bool pol = w == 7;
    const unsigned rdy = ldsa(smem + A_FLG) + 12;
    unsigned em = 0;
    int spin = 0;
    auto mk_gi = [&]() {
        PIN(cf);
#pragma unroll
        for (int i = 0; i < 2; ++i) gi_[i] = cmat(cf * (float)(j1 + 4 * (ja0 + i) + 256 * q) * (1.0f / 1024.0f), cf * (1.0f / 16.0f));
    };
    if (pol) {
        __builtin_amdgcn_s_setprio(3);
        u32x4 fl[2];
        unsigned foff = lane * 16;
#pragma unroll 1
        for (; spin < (1 << 20); ++spin) {
            PIN(foff);
#pragma unroll
            for (int i = 0; i < 2; ++i) fl[i] = __builtin_amdgcn_raw_buffer_load_b128(rs, foff + i * 1024, OFF_MX + (gb & (NREP - 1)) * 2048, SC1);
            bool ok = true;
#pragma unroll
            for (int i = 0; i < 2; ++i) ok = ok & ((fl[i].x ^ tag.x) < 0x10000u) & (fl[i].y == tag.y) & ((fl[i].z ^ tag.x) < 0x10000u) & (fl[i].w == tag.y);
            if (__all(ok)) break;
        }
        constexpr unsigned SP = 0x0c010c00u;
        em = wave_pkmax(pkmax(pkmax(__builtin_amdgcn_perm(0u, fl[0].x, SP), __builtin_amdgcn_perm(0u, fl[0].z, SP)),
                              pkmax(__builtin_amdgcn_perm(0u, fl[1].x, SP), __builtin_amdgcn_perm(0u, fl[1].z, SP)))) | 0x8000u;
        asm volatile("ds_write_b32 %0, %1" :: "v"(rdy), "v"(em) : "memory");
        em = __builtin_amdgcn_readfirstlane(em);
    } else {
        __builtin_amdgcn_s_setprio(0);
        mk_gi();
        if (!ld) {
            gsum = half_sum(gsum);
            const float g = bfr(1.0f / (1.0f + expf(-bfr(gsum))));
            const int tau = (gi & 64) | ((gi & 12) << 2) | ((gi & 48) >> 2) | (gi & 3);
            if ((lane & 31) == 0)
                __builtin_amdgcn_raw_buffer_store_b64((u32x2){__float_as_uint(g), __float_as_uint(bfr(1.0f - g))}, rs, (tau & 15) * 1024 + (lt * 8 + (tau >> 4)) * 8, OFF_G, 0);
        }
        TS(1);
#pragma unroll 1
        for (; spin < (1 << 24); ++spin) { em = lds_peek(rdy); if (em) break; __builtin_amdgcn_s_sleep(1); }
    }
    asm volatile("" ::: "memory");
    __builtin_amdgcn_s_setprio(PRIOM);
    TS(2);
#pragma unroll
    for (int i = 0; i < 2; ++i) blds16<SC1>(rs, smem + A_UT + (2 * w + i) * 1024, lane * 16, OFF_U + gb * 16384 + (2 * w + i) * 1024);
    u32x4 wv[3];
#pragma unroll
    for (int i = 0; i < 2; ++i) wv[i] = __builtin_amdgcn_raw_buffer_load_b128(rs, lane * 16, OFF_W + (k1 * 4 + j1) * 16384 + (ja0 + i) * 1024, SC1);
    wv[2] = (u32x4){0, 0, 0, 0};
    if (xtr) wv[2] = __builtin_amdgcn_raw_buffer_load_b128(rs, lane * 16, OFF_W + (k1 * 4) * 16384 + 8 * 1024, SC1);
    SCHED();
    if (pol) mk_gi();
    u32x4 mt[10];
    asm volatile("ds_read_b128 %0, %10\n\tds_read_b128 %1, %10 offset:1024\n\tds_read_b128 %2, %10 offset:2048\n\tds_read_b128 %3, %10 offset:3072\n\t"
                 "ds_read_b128 %4, %10 offset:4096\n\tds_read_b128 %5, %10 offset:5120\n\tds_read_b128 %6, %10 offset:6144\n\tds_read_b128 %7, %10 offset:7168\n\t"
                 "ds_read_b128 %8, %10 offset:8192\n\tds_read_b128 %9, %10 offset:9216\n\ts_waitcnt lgkmcnt(0)"
                 : "=&v"(mt[0]), "=&v"(mt[1]), "=&v"(mt[2]), "=&v"(mt[3]), "=&v"(mt[4]), "=&v"(mt[5]), "=&v"(mt[6]), "=&v"(mt[7]), "=&v"(mt[8]), "=&v"(mt[9])
                 : "v"(ldsa(smem + A_MAT) + lane * 16) : "memory");
    if (spc && ld) { const CM a = mat_sf(64, c, q), b = mat_si(64, c, q); mt[4] = a.re; mt[5] = a.im; mt[6] = b.re; mt[7] = b.im; }
    const int eu = min(max((int)(em >> 16), 4), 254), ew = min((int)(em & 0xffu), 245);
    const float su = __uint_as_float((unsigned)(258 - eu) << 23), sw = __uint_as_float((unsigned)(246 - ew) << 23);
    PIN(wv[0]); PIN(wv[1]); PIN(wv[2]);
    TS(3);
    const unsigned cnt = ldsa(smem + A_FLG) + 8;
    {
        u32x2 ua[4];
        asm volatile("ds_read_b64_tr_b16 %0, %4\n\tds_read_b64_tr_b16 %1, %4 offset:32\n\tds_read_b64_tr_b16 %2, %4 offset:1024\n\tds_read_b64_tr_b16 %3, %4 offset:1056\n\t"
                     "s_waitcnt lgkmcnt(0)" : "=&v"(ua[0]), "=&v"(ua[1]), "=&v"(ua[2]), "=&v"(ua[3])
                     : "v"(ldsa(smem + A_UT) + 2 * w * 1024 + (lane >> 2) * 64 + (lane & 3) * 8) : "memory");
        const unsigned ea = ldsa(smem + A_E1) + c * E1P + q * 256 + 2 * w * 16;
#pragma unroll
        for (int i = 0; i < 2; ++i) {
            const u32x4 u = {ua[2 * i].x, ua[2 * i].y, ua[2 * i + 1].x, ua[2 * i + 1].y};
            f32x4 r = mfb(u, mt[2], Z4), m = mfb(u, mt[3], Z4);
            r = mfb(u, mt[0], r); m = mfb(u, mt[1], m);
            asm volatile("ds_write_b128 %0, %1 offset:%2" :: "v"(ea), "v"(pk8(r * su, m * su)), "i"(i * 16) : "memory");
            if (xtr) {
                const unsigned sg = 0xbf803f80u;
                const f32x4 e = mfb(u, c == 0 ? (u32x4){sg, sg, 0, 0} : (u32x4){0, 0, sg, sg}, Z4);
                if (c < 2) asm volatile("ds_write_b128 %0, %1 offset:%2" :: "v"(ea), "v"(pk8(e * su, Z4)), "i"(16 * E1P + i * 16) : "memory");
            }
        }
    }
    if (lane == 0) lds_add(cnt);
    if (ld) {
        u32x4 cv[4];
        const unsigned ca = ldsa(smem) + (w - 4) * 4096 + q * 1024 + (lane & 3) * 256 + ((lane >> 2) & 3) * 16;
        asm volatile("ds_read_b128 %0, %4\n\tds_read_b128 %1, %5\n\tds_read_b128 %2, %6\n\tds_read_b128 %3, %7\n\ts_waitcnt lgkmcnt(0)"
                     : "=&v"(cv[0]), "=&v"(cv[1]), "=&v"(cv[2]), "=&v"(cv[3])
                     : "v"(ca + (lane & 3) * 64), "v"(ca + ((lane & 3) ^ 1) * 64), "v"(ca + ((lane & 3) ^ 2) * 64), "v"(ca + ((lane & 3) ^ 3) * 64) : "memory");
#pragma unroll
        for (int j = 0; j < 4; ++j) st128<WT>(cv[j], rs, OFF_C + ((gb * 4 + j) * 4 + w - 4) * 1024 + lane * 16);
    }
    if (xtr && w == 0 && lane == 0) {
        __builtin_amdgcn_raw_buffer_store_b64(tag, rs, 0, OFF_E, SC1);
        __builtin_amdgcn_raw_buffer_store_b32(eu + ew - 267, rs, 0, OFF_E + 128, 0);
    }
    wv[0] = wcvt(wv[0], sw); wv[1] = wcvt(wv[1], sw);
    TS(4);
#pragma unroll 1
    for (int sp_ = 0; sp_ < (1 << 24); ++sp_) { if (lds_peek(cnt) >= 8u) break; }
    u32x4 dv[2];
#pragma unroll
    for (int i = 0; i < 2; ++i) dv[i] = lld<u32x4>(smem + A_E1 + (2 * w + i) * E1P + lane * 16);
#pragma unroll
    for (int i = 0; i < 2; ++i) dv[i] = pk8(mfh(dv[i], gf[i].re, Z4), mfh(dv[i], gf[i].im, Z4));
#pragma unroll
    for (int i = 0; i < 2; ++i) dv[i] = wmul(mfh(mt[4], dv[i], Z4), mfh(mt[5], dv[i], Z4), wv[i]);
#pragma unroll
    for (int i = 0; i < 2; ++i) dv[i] = pk8(mfh(dv[i], mt[6], Z4), mfh(dv[i], mt[7], Z4));
    {
        f32x4 r[2], m[2];
#pragma unroll
        for (int i = 0; i < 2; ++i) { r[i] = mfh(dv[i], gi_[i].re, Z4); m[i] = mfh(dv[i], gi_[i].im, Z4); }
#pragma unroll
        for (int e = 0; e < 4; ++e)
            lst<u32x2>(smem + A_E2 + (4 * q + e) * 1024 + (w >> 1) * 256 + c * 16 + (w & 1) * 8, (u32x2){pkh(r[0][e], r[1][e]), pkh(m[0][e], m[1][e])});
    }
    if (xtr && !(w & 3)) {
        const float dt = -(float)(32 + 64 * c) * (1.0f / 1024.0f);
        const CM f = cmat(dt * (float)(4 * q), dt), b = cmat(cf * (float)(32 + 256 * q) * (1.0f / 1024.0f), cf * (1.0f / 16.0f));
        u32x4 d = lld<u32x4>(smem + A_E1 + (16 + (w >> 2)) * E1P + lane * 16);
        d = pk8(mfh(d, f.re, Z4), mfh(d, f.im, Z4));
        d = wmul(mfh(mt[4], d, Z4), mfh(mt[5], d, Z4), wcvt(wv[2], sw));
        d = pk8(mfh(d, mt[6], Z4), mfh(d, mt[7], Z4));
        const f32x4 r = mfh(d, b.re, Z4);
#pragma unroll
        for (int e = 0; e < 4; ++e) lst<float>(smem + A_EX + ((w >> 2) * 256 + (4 * q + e) * 16 + c) * 4, r[e]);
    }
    TS(5);
    aux_sync(cnt, 16u, lane);
    {
        f32x4 xa[2], xb[2];
#pragma unroll
        for (int i = 0; i < 2; ++i) { xa[i] = Z4; xb[i] = Z4; }
        if (xtr) {
            const float sg = (c & 1) ? -0.125f : 0.125f;
#pragma unroll
            for (int i = 0; i < 2; ++i) {
                xa[i] = lld<f32x4>(smem + A_EX + (2 * w + i) * 64 + q * 16) * sg;
                xb[i] = lld<f32x4>(smem + A_EX + 1024 + (2 * w + i) * 64 + q * 16) * -sg;
            }
        }
#pragma unroll
        for (int i = 0; i < 2; ++i) {
            const int s2 = 2 * w + i;
            const u32x4 ea = lld<u32x4>(smem + A_E2 + s2 * 1024 + lane * 16);
            const f32x4 ta = mfh(ea, mt[8], xa[i]), tb = mfh(ea, mt[9], xb[i]);
            st128<WT>(to16(pkh(ta[0], ta[1]), pkh(ta[2], ta[3]), pkh(tb[0], tb[1]), pkh(tb[2], tb[3])), rs,
                      c * 16384 + (q & 1) * 32 + (q >> 1) * 16 + OFF_T + s2 * 262144 + j1 * 4096 + mp * 64);
        }
    }
    TS(7);
    TSE(w == 7 ? 4 : w ? 1 : 5, (w & 3) == 0 || w == 7);
}

NOPK __global__ __launch_bounds__(768, 1) void ka_kernel(const u16* __restrict__ x, const u16* __restrict__ w_r, const u16* __restrict__ w_i,
                                                         const u16* __restrict__ Bre, const u16* __restrict__ Bim, const u16* __restrict__ Cre,
                                                         const u16* __restrict__ Cim, const u16* __restrict__ lam_re,
                                                         const u16* __restrict__ lam_im, const u16* __restrict__ log_dt,
                                                         const u16* __restrict__ gate_w, char* ws) {
    extern __shared__ __attribute__((aligned(16))) char smem_[];
    lptr smem = (lptr)smem_;
    const int wid = __builtin_amdgcn_readfirstlane(threadIdx.x >> 6), lane = threadIdx.x & 63;
    if (wid < 4) ka_mma(ws, smem, blockIdx.x, wid, lane);
    else {
        TSD();
        float gsum = 0.f;
        if (wid < 8) ka_load(x, Bre, Bim, smem, blockIdx.x, wid - 4, lane TSPASS);
        else gsum = ka_aux(w_r, w_i, lam_re, lam_im, log_dt, gate_w, ws, smem, blockIdx.x, wid - 8, lane TSPASS);
        int w = wid & 7;
        asm volatile("" : "+s"(w));
        ka_mid(Cre, Cim, ws, smem, blockIdx.x, w, lane, gsum TSPASS);
    }
}

constexpr int B_UT = 0, B_R0 = B_UT + 16384, B_CNT = B_R0 + 32768, B_DMP = B_CNT + 64, KB_SMEM = B_DMP + 12 * 8192;

DEV void kb_mma(char* ws, float* __restrict__ out, lptr smem, int gb, int wid, int lane) {
    int s2, da; tile_of(gb, s2, da);
    const int c = lane & 15, q = lane >> 4;
    const rsrc_t rs = mkrsrc(ws, WS_END);
    TSD(); TS(0);
    const unsigned vo = (c >> 2) * 256 + q * 64 + (c & 3) * 16;
    const unsigned sC = OFF_C + (da * 64 + 16 * wid) * 4096, sH = OFF_H + (s2 * 64 + 16 * wid) * 8192;
    auto ldA = [&](int i, int m) { return __builtin_amdgcn_raw_buffer_load_b128(rs, vo + m * 1024, sC + i * 4096, 0); };
    auto ldB = [&](int i, int n) { return __builtin_amdgcn_raw_buffer_load_b128(rs, vo + (n & 1) * 1024, sH + i * 8192 + (((n >> 1) + wid + 1) & 3) * 2048, 0); };
    u32x4 A[3][4], B[2][8];
#pragma unroll
    for (int i = 0; i < 2; ++i) {
#pragma unroll
        for (int m = 0; m < 4; ++m) A[i][m] = ldA(i, m);
#pragma unroll
        for (int n = 0; n < 8; ++n) B[i][n] = ldB(i, n);
        SCHED();
        if (i == 0) BAR();
    }
    f32x4 acc[4][8];
#pragma unroll
    for (int m = 0; m < 4; ++m)
#pragma unroll
        for (int n = 0; n < 8; ++n) acc[m][n] = Z4;
    float omg[2];
#pragma unroll
    for (int i = 0; i < 16; ++i) {
        if (i + 2 < 16) {
#pragma unroll
            for (int m = 0; m < 4; ++m) A[(i + 2) % 3][m] = ldA(i + 2, m);
        }
        if (i == 14) {
#pragma unroll
            for (int n = 0; n < 2; ++n) omg[n] = __uint_as_float(__builtin_amdgcn_raw_buffer_load_b32(rs, c * 8 + n * 128 + 4, OFF_G + s2 * 1024 + wid * 256, 0));
        }
        SCHED();
#pragma unroll
        for (int n = 0; n < 8; ++n) {
#pragma unroll
            for (int m = 0; m < 4; ++m) acc[m][n] = mfb(A[i % 3][m], B[i & 1][n], acc[m][n]);
            if (i + 2 < 16) B[i & 1][n] = ldB(i + 2, n);
            if (i == 15 && n >= 1 && n < 7) {
                const int j = (n - 1) >> 1;
#pragma unroll
                for (int m = 0; m < 4; ++m)
                    lst<f32x4>(smem + B_DMP + (((j + wid + 1) & 3) * 3 + j) * 8192 + (((n - 1) & 1) * 4 + m) * 1024 + lane * 16, acc[m][n - 1]);
            }
            SCHED();
        }
        if (i == 0) { TS(4); TC(60); }
        if (i == 7) TS(5);
        if (i == 14) { TS(6); TC(61); }
    }
    TS(1);
    const rsrc_t ro = mkrsrc(out, S_ * D_ * 4);
    LDS_WAIT();
    BAR();
    TS(2);
#pragma unroll
    for (int n = 0; n < 2; ++n) {
        const int row = 16 * (32 * wid + 16 * n + c) + s2;
        const float o2 = 2.0f * omg[n];
#pragma unroll
        for (int m = 0; m < 4; ++m) {
            f32x4 a = acc[m][6 + n];
#pragma unroll
            for (int j = 0; j < 3; ++j) a += lld<f32x4>(smem + B_DMP + (wid * 3 + j) * 8192 + (n * 4 + m) * 1024 + lane * 16);
            const f32x4 r0 = lld<f32x4>(smem + B_R0 + ((wid * 2 + n) * 4 + m) * 1024 + lane * 16);
            const f32x4 o = {fmaf(o2, a[0], r0[0]), fmaf(o2, a[1], r0[1]), fmaf(o2, a[2], r0[2]), fmaf(o2, a[3], r0[3])};
            st128<WTO>(__builtin_bit_cast(u32x4, o), ro, (row * D_ + 256 * m + 16 * da + 4 * q) * 4);
        }
    }
    TS(3);
    TSE(2, wid == 0);
}

#define PB_HI 3
#define PB_LO 0
#define PHI() __builtin_amdgcn_s_setprio(PB_HI)
#define PLO() __builtin_amdgcn_s_setprio(PB_LO)
#define LD128 __builtin_amdgcn_raw_buffer_load_b128
#define LD64 __builtin_amdgcn_raw_buffer_load_b64
#define LD32 __builtin_amdgcn_raw_buffer_load_b32
#define BLDS blds16
#define STT st128<SC1>
#define KSLP 8
DEV void kb_aux(const u16* __restrict__ Dskip, char* ws, lptr smem, int gb, int w, int lane) {
    int s2t, dat; tile_of(gb, s2t, dat);
    const int c = lane & 15, q = lane >> 4;
    const rsrc_t rs = mkrsrc(ws, WS_END), rd = mkrsrc(Dskip, D_ * 2);
    const unsigned cnt = ldsa(smem + B_CNT);
    TSD(); TS(0);
    if (w == 0) asm volatile("ds_write_b32 %0, %1\n\ts_waitcnt lgkmcnt(0)" :: "v"(cnt), "v"(0u) : "memory");
    BAR();
    u32x4 fc[2][2], fs[2][2];
#pragma unroll
    for (int n = 0; n < 2; ++n) {
        float dt = (float)(32 * w + 16 * n + c) * (1.0f / 128.0f); PIN(dt);
        float cc[8], ss[8];
        const float t0 = __builtin_amdgcn_fractf(dt * (float)(4 * q)), t1 = __builtin_amdgcn_fractf(dt * (float)(16 + 4 * q));
#pragma unroll
        for (int e = 0; e < 8; ++e) { const float f = fmaf((float)(e & 3), dt, e < 4 ? t0 : t1); cc[e] = __builtin_amdgcn_cosf(f); ss[e] = __builtin_amdgcn_sinf(f); }
        fc[n][0] = (u32x4){pkh(cc[0], cc[1]), pkh(cc[2], cc[3]), pkh(cc[4], cc[5]), pkh(cc[6], cc[7])};
        fs[n][0] = (u32x4){pkh(ss[0], ss[1]), pkh(ss[2], ss[3]), pkh(ss[4], ss[5]), pkh(ss[6], ss[7])};
        fc[n][1] = ((c & 1) ? fs[n][0] : fc[n][0]) ^ (((c + 1) & 2) ? 0x80008000u : 0u);
        fs[n][1] = ((c & 1) ? fc[n][0] : fs[n][0]) ^ ((c & 2) ? 0x80008000u : 0u);
        PIN(fc[n][0]); PIN(fs[n][0]); PIN(fc[n][1]); PIN(fs[n][1]);
    }
    TS(1);
    u32x2 gg[2], dk[4];
    u32x4 xv[4];
    PHI();
#pragma unroll
    for (int n = 0; n < 2; ++n) gg[n] = LD64(rs, c * 8 + n * 128, OFF_G + s2t * 1024 + w * 256, 0);
#pragma unroll
    for (int i = 0; i < 4; ++i) dk[i] = LD64(rd, q * 8 + i * 512, dat * 32, 0);
    int fe = LD32(rs, 0, OFF_E + 128, 0);
#pragma unroll
    for (int i = 0; i < 4; ++i) {
        __builtin_amdgcn_s_sleep(KSLP);
        xv[i] = LD128(rs, lane * 16 + i * 1024, OFF_X + (s2t * 16 + dat) * 16384 + w * 4096, 0);
        SCHED();
    }
#pragma unroll
    for (int i = 0; i < 4; ++i) {
        __builtin_amdgcn_s_sleep(KSLP);
        BLDS(rs, smem + B_UT + w * 4096 + i * 1024, (lane >> 1) * 64 + (lane & 1) * 16, OFF_T + (s2t * 16 + dat) * 16384 + w * 4096 + (i & 1) * 2048 + (i >> 1) * 32);
        SCHED();
    }
    PLO();
    TS(2);
    f32x4 r0[2][4];
#pragma unroll
    for (int i = 0; i < 4; ++i) PIN(dk[i]);
#pragma unroll
    for (int i = 0; i < 4; ++i) {
        PIN(xv[i]);
#pragma unroll
        for (int p = 0; p < 2; ++p) {
            const int m = 2 * (i & 1) + p;
            const unsigned x0 = xv[i][2 * p], x1 = xv[i][2 * p + 1];
            const unsigned p0 = pkb(lo2f(x0) * lo2f(dk[m].x), hi2f(x0) * hi2f(dk[m].x)), p1 = pkb(lo2f(x1) * lo2f(dk[m].y), hi2f(x1) * hi2f(dk[m].y));
            r0[i >> 1][m] = (f32x4){lo2f(p0), hi2f(p0), lo2f(p1), hi2f(p1)} * __uint_as_float(gg[i >> 1].y);
            PIN(r0[i >> 1][m]);
        }
    }
    TS(5);
    __builtin_amdgcn_s_waitcnt(0x0F70);
    TS(6);
    aux_sync(cnt, 4u, lane);
    TS(7);
    f32x4 P0[2], P1[2], P2[2], P3a[2], P3b[2];
#pragma unroll
    for (int n = 0; n < 2; ++n) { P0[n] = Z4; P1[n] = Z4; P2[n] = Z4; P3a[n] = Z4; P3b[n] = Z4; }
    lptr tt = smem + B_UT + lane * 8;
#pragma unroll
    for (int h = 0; h < 2; ++h) {
        const u32x4 a1 = trfrag(tt + 1 * 4096 + h * 1024, 512), b1 = trfrag(tt + 1 * 4096 + 2048 + h * 1024, 512);
        const u32x4 a3 = trfrag(tt + 3 * 4096 + h * 1024, 512), b3 = trfrag(tt + 3 * 4096 + 2048 + h * 1024, 512);
        const u32x4 a0 = trfrag(tt + h * 1024, 512), b0 = trfrag(tt + 2048 + h * 1024, 512);
        const u32x4 a2 = trfrag(tt + 2 * 4096 + h * 1024, 512), b2 = trfrag(tt + 2 * 4096 + 2048 + h * 1024, 512);
#pragma unroll
        for (int n = 0; n < 2; ++n) {
            u32x4 cw = fc[n][h], sw_ = fs[n][h];
            P1[n] = mfh(a1, cw, P1[n]); P3a[n] = mfh(a1, sw_, P3a[n]); P3b[n] = mfh(b1, cw, P3b[n]); P1[n] = mfh(b1, sw_, P1[n]);
            if (h == 0) cw.x = q == 0 ? (cw.x & 0xffff0000u) | ((c & 1) ? 0xbc00u : 0x3c00u) : cw.x;
            P3a[n] = mfh(b3, cw, P3a[n]); P3b[n] = mfh(a3, sw_, P3b[n]); P1[n] = mfh(a3, cw, P1[n]); P1[n] = mfh(b3, sw_, P1[n]);
            if (h == 0) { cw.x = q == 0 ? (cw.x & 0xffff0000u) | 0x3800u : cw.x; sw_.x = q == 0 ? (sw_.x & 0xffff0000u) | ((c & 1) ? 0x3800u : 0xb800u) : sw_.x; }
            P0[n] = mfh(a0, cw, P0[n]); P2[n] = mfh(a2, cw, P2[n]); P0[n] = mfh(b0, sw_, P0[n]); P2[n] = mfh(b2, sw_, P2[n]);
        }
    }
#pragma unroll
    for (int n = 0; n < 2; ++n) {
        const float g = __uint_as_float(gg[n].x);
        const f32x4 sp = P0[n] + P2[n], sm = P0[n] - P2[n], p3 = P3a[n] - P3b[n];
        const f32x4 av[4] = {sp + P1[n], sm - p3, sp - P1[n], sm + p3};
#pragma unroll
        for (int m = 0; m < 4; ++m) {
            const unsigned b0 = pkb(ldexpf(av[m][0], fe), ldexpf(av[m][1], fe)), b1 = pkb(ldexpf(av[m][2], fe), ldexpf(av[m][3], fe));
            const unsigned o0 = pkb(g * lo2f(b0), g * hi2f(b0)), o1 = pkb(g * lo2f(b1), g * hi2f(b1));
            lst<f32x4>(smem + B_R0 + ((w * 2 + n) * 4 + m) * 1024 + lane * 16, r0[n][m] + (f32x4){lo2f(o0), hi2f(o0), lo2f(o1), hi2f(o1)});
        }
    }
    LDS_WAIT();
    TS(8);
    TSE(3, w == 0);
    BAR();
}

NOPK __global__ __launch_bounds__(512, 1) void kb_kernel(const u16* __restrict__ Dskip, char* ws, float* __restrict__ out) {
    extern __shared__ __attribute__((aligned(16))) char smem_[];
    lptr smem = (lptr)smem_;
    const int wid = __builtin_amdgcn_readfirstlane(threadIdx.x >> 6), lane = threadIdx.x & 63;
    if (wid < 4) kb_mma(ws, out, smem, blockIdx.x, wid, lane);
    else kb_aux(Dskip, ws, smem, blockIdx.x, wid - 4, lane);
}

extern "C" void kernel_launch(void* const* d_in, const int* in_sizes, int n_in, void* d_out, int out_size, void* d_ws, size_t ws_size,
                              hipStream_t stream) {
    const u16* x = (const u16*)d_in[0];
    const u16* w_r = (const u16*)d_in[1];
    const u16* w_i = (const u16*)d_in[2];
    const u16* lam_re = (const u16*)d_in[4];
    const u16* lam_im = (const u16*)d_in[5];
    const u16* Bre = (const u16*)d_in[6];
    const u16* Bim = (const u16*)d_in[7];
    const u16* Cre = (const u16*)d_in[8];
    const u16* Cim = (const u16*)d_in[9];
    const u16* log_dt = (const u16*)d_in[10];
    const u16* Dskip = (const u16*)d_in[11];
    const u16* gate_w = (const u16*)d_in[12];
    char* ws = (char*)d_ws;
    (void)hipFuncSetAttribute((const void*)ka_kernel, hipFuncAttributeMaxDynamicSharedMemorySize, KA_SMEM);
    (void)hipFuncSetAttribute((const void*)kb_kernel, hipFuncAttributeMaxDynamicSharedMemorySize, KB_SMEM);
    ka_kernel<<<256, 768, KA_SMEM, stream>>>(x, w_r, w_i, Bre, Bim, Cre, Cim, lam_re, lam_im, log_dt, gate_w, ws);
    kb_kernel<<<256, 512, KB_SMEM, stream>>>(Dskip, ws, (float*)d_out);
}
```

```cpp
#include <hip/hip_runtime.h>
#include <stdint.h>

typedef __attribute__((ext_vector_type(8))) __bf16 bf16x8;
typedef __attribute__((ext_vector_type(2))) __bf16 bf16x2;
typedef __attribute__((ext_vector_type(8))) _Float16 f16x8;
typedef __attribute__((ext_vector_type(2))) _Float16 f16x2;
typedef __attribute__((ext_vector_type(4))) float f32x4;
typedef __attribute__((ext_vector_type(16))) float f32x16;
typedef __attribute__((ext_vector_type(2))) float f32x2;
typedef __attribute__((ext_vector_type(4))) unsigned u32x4;
typedef __attribute__((ext_vector_type(2))) unsigned u32x2;
typedef __attribute__((ext_vector_type(4))) short s16x4;
typedef __attribute__((ext_vector_type(2))) unsigned short u16x2;
typedef unsigned short u16;
typedef unsigned long long u64;
#define GAS __attribute__((address_space(1)))
#define LAS __attribute__((address_space(3)))
#define NOPK __attribute__((target("no-packed-fp32-ops")))
#define DEV __device__ __forceinline__

constexpr int S_ = 2048, D_ = 1024, KF = 513;

constexpr unsigned OFF_H = 0;
constexpr unsigned OFF_U = OFF_H + (8u << 20);
constexpr unsigned OFF_T = OFF_U + (4u << 20);
constexpr unsigned OFF_G = OFF_T + (4u << 20);
constexpr unsigned OFF_FU = OFF_G + 16384u;
constexpr unsigned OFF_FT = OFF_FU + 32768u;
constexpr unsigned OFF_FH = OFF_FT + 32768u;
constexpr unsigned OFF_GRAN = OFF_FH + 32768u;
constexpr unsigned OFF_E = OFF_GRAN + 16u * 1024u * 16u;
constexpr unsigned WS_END = OFF_E + 128u;

#define TL(i)
#define TSD()
#define TS(i)
#define TC(i)
#define TSE(slot, on)

DEV unsigned pkb(float a, float b) { f32x2 v = {a, b}; return __builtin_bit_cast(unsigned, __builtin_convertvector(v, bf16x2)); }
DEV unsigned pkh(float a, float b) { f32x2 v = {a, b}; return __builtin_bit_cast(unsigned, __builtin_convertvector(v, f16x2)); }
DEV float lo2f(unsigned p) { return __uint_as_float(p << 16); }
DEV float hi2f(unsigned p) { return __uint_as_float(p & 0xffff0000u); }
DEV float bfr(float f) { return lo2f(pkb(f, f)); }
DEV float2 cmul(float2 a, float2 b) { return make_float2(a.x * b.x - a.y * b.y, a.x * b.y + a.y * b.x); }
DEV float2 cfma(float2 a, float2 b, float2 c) { return make_float2(fmaf(a.x, b.x, fmaf(-a.y, b.y, c.x)), fmaf(a.x, b.y, fmaf(a.y, b.x, c.y))); }
DEV f32x4 mfb(u32x4 a, u32x4 b, f32x4 c) { return __builtin_amdgcn_mfma_f32_16x16x32_bf16(__builtin_bit_cast(bf16x8, a), __builtin_bit_cast(bf16x8, b), c, 0, 0, 0); }
DEV f32x16 mf32(u32x4 a, u32x4 b, f32x16 c) { return __builtin_amdgcn_mfma_f32_32x32x16_bf16(__builtin_bit_cast(bf16x8, a), __builtin_bit_cast(bf16x8, b), c, 0, 0, 0); }
DEV f32x4 mfh(u32x4 a, u32x4 b, f32x4 c) { return __builtin_amdgcn_mfma_f32_16x16x32_f16(__builtin_bit_cast(f16x8, a), __builtin_bit_cast(f16x8, b), c, 0, 0, 0); }
DEV unsigned pkmax(unsigned a, unsigned b) { return __builtin_bit_cast(unsigned, __builtin_elementwise_max(__builtin_bit_cast(u16x2, a), __builtin_bit_cast(u16x2, b))); }
typedef LAS char* lptr;
DEV unsigned ldsa(lptr p) { return (unsigned)(uintptr_t)p; }
template <class T> DEV T lld(lptr p) { return *(LAS const T*)p; }
template <class T> DEV void lst(lptr p, T v) { *(LAS T*)p = v; }
DEV u32x2 trread(lptr p) { return __builtin_bit_cast(u32x2, __builtin_amdgcn_ds_read_tr16_b64_v4i16((LAS s16x4*)p)); }
DEV u32x4 trfrag(lptr p, int second) { const u32x2 a = trread(p), b = trread(p + second); return (u32x4){a.x, a.y, b.x, b.y}; }
typedef __amdgpu_buffer_rsrc_t rsrc_t;
DEV u32x4 ldfrag(lptr tile, int row, int chunk) { return lld<u32x4>(tile + row * 128 + ((chunk ^ (row & 7)) << 4)); }
template <int N> DEV void wait_vm() { asm volatile("s_waitcnt vmcnt(%0)" :: "i"(N) : "memory"); }
#define LDS_WAIT() asm volatile("s_waitcnt lgkmcnt(0)" ::: "memory")
#define SCHED() __builtin_amdgcn_sched_barrier(0)
#define BAR() do { __builtin_amdgcn_s_barrier(); asm volatile("" ::: "memory"); } while (0)
#define PIN(x) asm volatile("" : "+v"(x))
template <int I> struct IC { static constexpr int v = I; };
template <int I, int N, class F> DEV void sfor(F&& f) { if constexpr (I < N) { f(IC<I>{}); sfor<I + 1, N>(f); } }
#define INL __attribute__((always_inline))
DEV rsrc_t mkrsrc(const void* p, unsigned n) { return __builtin_amdgcn_make_buffer_rsrc((void*)p, 0, n, 0x00020000); }
constexpr int SC1 = 16;
#define WTO 16
template <int AUX> DEV void st128(u32x4 v, rsrc_t rs, unsigned off) { __builtin_amdgcn_raw_buffer_store_b128(v, rs, off, 0, AUX); }
DEV void lds_add(unsigned a) { asm volatile("ds_add_u32 %0, %1" :: "v"(a), "v"(1u) : "memory"); }
DEV unsigned lds_peek(unsigned a) {
    unsigned v;
    asm volatile("ds_read_b32 %0, %1\n\ts_waitcnt lgkmcnt(0)" : "=v"(v) : "v"(a) : "memory");
    return __builtin_amdgcn_readfirstlane(v);
}
DEV void tile_of(int gb, int& ta, int& tb) {
    const int xcd = gb & 7, loc = gb >> 3;
    ta = (xcd >> 1) * 4 + (loc >> 3);
    tb = (xcd & 1) * 8 + (loc & 7);
}
DEV void rows4(float a, float& a0, float& a1, float& a2, float& a3) {
    const unsigned u = __float_as_uint(a);
    const auto r = __builtin_amdgcn_permlane32_swap(u, u, false, false);
    const auto p = __builtin_amdgcn_permlane16_swap(r[0], r[0], false, false);
    const auto s = __builtin_amdgcn_permlane16_swap(r[1], r[1], false, false);
    a0 = __uint_as_float(p[0]); a1 = __uint_as_float(p[1]); a2 = __uint_as_float(s[0]); a3 = __uint_as_float(s[1]);
}
DEV float rowsum4(float a) {
    const unsigned u = __float_as_uint(a);
    const auto r = __builtin_amdgcn_permlane32_swap(u, u, false, false);
    const unsigned t = __float_as_uint(__uint_as_float(r[0]) + __uint_as_float(r[1]));
    const auto p = __builtin_amdgcn_permlane16_swap(t, t, false, false);
    return __uint_as_float(p[0]) + __uint_as_float(p[1]);
}
DEV float2 sel4(int q, float2 a, float2 b, float2 c, float2 d) {
    PIN(d.x); PIN(d.y);
    const float2 e = (q & 1) ? b : a, f = (q & 1) ? d : c;
    return (q & 2) ? f : e;
}
template <int R> DEV unsigned ror16(unsigned v) { return __builtin_amdgcn_update_dpp(0u, v, 0x120 + R, 0xf, 0xf, false); }
DEV unsigned wave_pkmax(unsigned m) {
    m = pkmax(m, ror16<8>(m)); m = pkmax(m, ror16<4>(m)); m = pkmax(m, ror16<2>(m)); m = pkmax(m, ror16<1>(m));
    const auto r = __builtin_amdgcn_permlane32_swap(m, m, false, false);
    m = pkmax(r[0], r[1]);
    const auto p = __builtin_amdgcn_permlane16_swap(m, m, false, false);
    return pkmax(p[0], p[1]);
}
DEV float half_sum(float v) {
    v += __uint_as_float(ror16<8>(__float_as_uint(v))); v += __uint_as_float(ror16<4>(__float_as_uint(v)));
    v += __uint_as_float(ror16<2>(__float_as_uint(v))); v += __uint_as_float(ror16<1>(__float_as_uint(v)));
    const auto p = __builtin_amdgcn_permlane16_swap(__float_as_uint(v), __float_as_uint(v), false, false);
    return __uint_as_float(p[0]) + __uint_as_float(p[1]);
}
DEV u32x4 quads(lptr buf, int lane, u32x2 p, u32x2 n) {
    lptr wp = buf + (lane & 15) * 64 + ((lane >> 4) & 1) * 8;
    const int v = (lane >> 5) ^ ((lane >> 2) & 3);
    lst<u32x2>(wp + (v << 4), p); lst<u32x2>(wp + ((v ^ 2) << 4), n);
    asm volatile("" ::: "memory");
    return lld<u32x4>(buf + ((lane ^ (lane >> 4)) << 4));
}

#define PRIOA 2
#define NREP 8
#define PRIOM 1
#define PRIOT 2
#define PRIOG 2
#define FLO 0
#define LAG 0
#define AD 6
#define BD 7
#define WROW {0, 0, 0, 0, 6, 11, 16}
#define ROT 0
#define HAUX SC1
#define PRIOL 3
#define XM 0
#define C0S 15
#define C1S 15
#define UFS 9
#define PFS 99
#define PRIOR 0
#define PRS -1
#define WXS 13
#define ROT2 0
#define RDPAT {0, 1, 2, -1, 3, 4, -1, 5, 6, -1, 7, -1, -1, -1, -1, -1}
#define Z4 ((f32x4){0.f, 0.f, 0.f, 0.f})
constexpr unsigned SG = 0x80008000u;
struct CM { u32x4 re, im; };
DEV u32x4 cmat1(float t0, float dt) {
    float cc[4], ss[4];
    t0 = __builtin_amdgcn_fractf(t0);
#pragma unroll
    for (int e = 0; e < 4; ++e) { const float f = fmaf((float)e, dt, t0); cc[e] = __builtin_amdgcn_cosf(f); ss[e] = __builtin_amdgcn_sinf(f); }
    return (u32x4){pkh(ss[0], ss[1]), pkh(ss[2], ss[3]), pkh(cc[0], cc[1]), pkh(cc[2], cc[3])};
}
DEV CM cexp(u32x4 v) { CM m; m.re = (u32x4){v.z, v.w, v.x ^ SG, v.y ^ SG}; m.im = v; PIN(m.re); PIN(m.im); return m; }
DEV u32x4 pk8(f32x4 r, f32x4 i) { return (u32x4){pkh(r[0], r[1]), pkh(r[2], r[3]), pkh(i[0], i[1]), pkh(i[2], i[3])}; }
DEV u32x4 wmul(f32x4 r, f32x4 i, u32x4 w) {
    float yr[4], yi[4];
#pragma unroll
    for (int e = 0; e < 4; ++e) {
        const unsigned wu = w[e];
        const f16x2 h = __builtin_bit_cast(f16x2, wu);
        yr[e] = fmaf(r[e], (float)h[0], -i[e] * (float)h[1]); yi[e] = fmaf(r[e], (float)h[1], i[e] * (float)h[0]);
    }
    return (u32x4){pkh(yr[0], yr[1]), pkh(yr[2], yr[3]), pkh(yi[0], yi[1]), pkh(yi[2], yi[3])};
}
DEV u32x4 wcvt(u32x4 w, float sr, float si) {
#pragma unroll
    for (int e = 0; e < 4; ++e) w[e] = pkh(lo2f(w[e]) * sr, hi2f(w[e]) * si);
    PIN(w);
    return w;
}
DEV u32x4 mkdesc(const void* p, unsigned n) { const u64 a = (u64)p; return (u32x4){(unsigned)a, (unsigned)(a >> 32) & 0xffffu, n, 0x00020000u}; }
DEV void dma16(u32x4 d, unsigned l, unsigned vo, unsigned so) {
    asm volatile("s_nop 0\n\tbuffer_load_dwordx4 %0, %1, %2 offen lds" :: "v"(vo), "s"(d), "s"(so), "{m0}"(l) : "memory");
}
DEV void sig(unsigned a, unsigned v) { asm volatile("s_mov_b64 exec, 1\n\tds_add_u32 %0, %1\n\ts_mov_b64 exec, -1" :: "v"(a), "v"(v) : "memory"); }
DEV unsigned sig_rtn(unsigned a) {
    unsigned v;
    asm volatile("s_mov_b64 exec, 1\n\tds_add_rtn_u32 %0, %1, %2\n\ts_mov_b64 exec, -1\n\ts_waitcnt lgkmcnt(0)" : "=&v"(v) : "v"(a), "v"(1u) : "memory");
    return __builtin_amdgcn_readfirstlane(v);
}
template <int SLP> DEV unsigned await(unsigned a, unsigned mask) {
    unsigned v, n, r, t;
    asm volatile("s_mov_b32 %1, 0x400000\n"
                 "1:\tds_read_b32 %0, %4\n\ts_waitcnt lgkmcnt(0)\n\tv_readfirstlane_b32 %2, %0\n\ts_and_b32 %3, %2, %5\n\ts_cbranch_scc1 2f\n\t"
                 "s_sleep %6\n\ts_sub_u32 %1, %1, 1\n\ts_cbranch_scc0 1b\n"
                 "2:" : "=&v"(v), "=&s"(n), "=&s"(r), "=&s"(t) : "v"(a), "s"(mask), "i"(SLP) : "memory", "scc");
    return r;
}
DEV void flag_put(rsrc_t rs, unsigned off, int gb, u32x2 v, int lane) {
    if (lane < NREP) __builtin_amdgcn_raw_buffer_store_b64(v, rs, lane * 2048, off + gb * 8, SC1);
}
DEV void flag_get(rsrc_t rs, unsigned off, int gb, int lane, u32x4 (&fl)[2]) {
    unsigned o = lane * 16; PIN(o);
#pragma unroll
    for (int i = 0; i < 2; ++i) fl[i] = __builtin_amdgcn_raw_buffer_load_b128(rs, o + i * 1024, off + (gb & (NREP - 1)) * 2048, SC1);
}
DEV bool flag_ok(const u32x4 (&fl)[2], u32x2 tag) {
    bool ok = true;
#pragma unroll
    for (int i = 0; i < 2; ++i) ok = ok & ((fl[i].x ^ tag.x) < 0x10000u) & (fl[i].y == tag.y) & ((fl[i].z ^ tag.x) < 0x10000u) & (fl[i].w == tag.y);
    return __all(ok);
}
DEV void flag_wait(rsrc_t rs, unsigned off, int gb, u32x2 tag, int lane, unsigned rdy) {
    u32x4 fl[2];
#pragma unroll 1
    for (int spin = 0; spin < (1 << 20); ++spin) { flag_get(rs, off, gb, lane, fl); if (flag_ok(fl, tag)) break; }
    sig(rdy, 1u);
}

constexpr int A_STAGE = 32768, A_XT = 4 * A_STAGE, A_PRM = A_XT + 16384, A_AGG = A_PRM + 2048, A_FLG = A_AGG + 512, A_GW = A_FLG + 128,
              A_MAT = A_GW + 2048, SMEM = A_MAT + 11264;
constexpr int E1P = 1040, A_EX = A_GW, A_W = 2 * A_STAGE, A_E1 = A_W + 19456, A_HS = 3 * A_STAGE + 16384, A_R0 = 0;
constexpr int F_MID = 8, F_EM = 12, F_MAX = 16, F_TAG = 32, F_FULL = 40, F_FREE = 44, F_HC = 48, F_HR = 52, F_TC = 56, F_TR = 60, F_TT = 64, F_R0 = 68, F_WM = 80;

constexpr int XG = 0xC6;
DEV void slot_of(int gb, int& mp, int& j1) { gb ^= XG; mp = (gb >> 5) * 8 + (gb & 7); j1 = (gb >> 3) & 3; }
DEV u32x4 mat_sf(int k1, int c, int q) { const float dt = -(float)(k1 + 128 * c) * (1.0f / 2048.0f); return cmat1(dt * (float)(4 * q), dt); }
DEV u32x4 mat_si(int k1, int c, int q) { return cmat1((float)((k1 + 512 * q) * c) * (1.0f / 2048.0f), (float)c * (1.0f / 16.0f)); }

DEV void mk_tables(lptr smem, int gb, int wid, int lane) {
    int mp, j1; slot_of(gb, mp, j1);
    const int c = lane & 15, q = lane >> 4;
    const bool spc = mp == 0 && !(j1 & 1);
    const int k1 = mp ? mp : (j1 == 3 ? 64 : 0);
    lptr mt = smem + A_MAT + lane * 16;
    if (wid == 0) {
        const float dt = -(float)(j1 + 4 * (spc ? c & 7 : c)) * (1.0f / 64.0f), t0 = __builtin_amdgcn_fractf(dt * (float)(4 * q));
        float cc[4], ss[4];
#pragma unroll
        for (int e = 0; e < 4; ++e) { const float f = fmaf((float)e, dt, t0); cc[e] = __builtin_amdgcn_cosf(f); ss[e] = __builtin_amdgcn_sinf(f); }
        const unsigned c0 = pkb(cc[0], cc[1]), c1 = pkb(cc[2], cc[3]), s0 = pkb(ss[0], ss[1]), s1 = pkb(ss[2], ss[3]);
        const unsigned c0l = pkb(cc[0] - lo2f(c0), cc[1] - hi2f(c0)), c1l = pkb(cc[2] - lo2f(c1), cc[3] - hi2f(c1));
        const unsigned s0l = pkb(ss[0] - lo2f(s0), ss[1] - hi2f(s0)), s1l = pkb(ss[2] - lo2f(s1), ss[3] - hi2f(s1));
        u32x4 m[4] = {(u32x4){c0, c1, s0 ^ SG, s1 ^ SG}, (u32x4){s0, s1, c0, c1}, (u32x4){c0l, c1l, s0l ^ SG, s1l ^ SG}, (u32x4){s0l, s1l, c0l, c1l}};
        if (spc) {
            m[0] = c < 8 ? (u32x4){c0, c1, 0, 0} : (u32x4){0, 0, c0, c1}; m[1] = c < 8 ? (u32x4){s0, s1, 0, 0} : (u32x4){0, 0, s0, s1};
            m[2] = c < 8 ? (u32x4){c0l, c1l, 0, 0} : (u32x4){0, 0, c0l, c1l}; m[3] = c < 8 ? (u32x4){s0l, s1l, 0, 0} : (u32x4){0, 0, s0l, s1l};
        }
#pragma unroll
        for (int i = 0; i < 4; ++i) lst<u32x4>(mt + i * 1024, m[i]);
    } else if (wid == 1) {
        const int jq = spc ? (4 * q) & 7 : 4 * q;
        float cc[4], ss[4];
        const float t0 = __builtin_amdgcn_fractf((float)(c * (j1 + 4 * jq)) * (1.0f / 64.0f));
#pragma unroll
        for (int e = 0; e < 4; ++e) {
            const float f = fmaf((float)c, (float)e * (1.0f / 16.0f), t0);
            const float wg = !spc || (gb == XG && jq + e == 0) ? 0.125f : 0.25f;
            cc[e] = __builtin_amdgcn_cosf(f) * wg; ss[e] = -__builtin_amdgcn_sinf(f) * wg;
        }
        const unsigned c0 = pkh(cc[0], cc[1]), c1 = pkh(cc[2], cc[3]), n0 = pkh(ss[0], ss[1]), n1 = pkh(ss[2], ss[3]);
        u32x4 mia = (u32x4){c0, n0, c1, n1}, mib = (u32x4){n0, c0 ^ SG, n1, c1 ^ SG};
        if (spc) { mib = q < 2 ? (u32x4){0, 0, 0, 0} : mia ^ SG; if (q >= 2) mia = (u32x4){0, 0, 0, 0}; }
        lst<u32x4>(mt + 6 * 1024, mia); lst<u32x4>(mt + 7 * 1024, mib);
        lst<u32x4>(mt + 8 * 1024, cmat1((float)(c * q) * -0.25f, (float)c * (-1.0f / 16.0f)));
    } else {
        lst<u32x4>(mt + (2 + wid) * 1024, wid == 2 ? mat_sf(k1, c, q) : mat_si(k1, c, q));
        const int ja = (spc ? 0 : 8 * (wid - 2)) + (lane >> 3);
        const float dt = -(float)(j1 + 4 * ja) * (1.0f / 1024.0f), t0 = dt * (float)(2 * (lane & 7)), t1 = t0 + dt;
        lst<f32x4>(mt + (7 + wid) * 1024, (f32x4){__builtin_amdgcn_cosf(t0), __builtin_amdgcn_sinf(t0), __builtin_amdgcn_cosf(t1), __builtin_amdgcn_sinf(t1)});
    }
}

DEV void role_mma(char* ws, float* __restrict__ out, lptr smem, int gb, int wid, int lane) {
    int lt, pt; tile_of(gb, lt, pt);
    const int wr = wid >> 1, wc = wid & 1, c = lane & 15, q = lane >> 4;
    const rsrc_t rs = mkrsrc(ws, WS_END);
    const unsigned fb = ldsa(smem + A_FLG);
    TSD(); TS(0);
    u64 tag = __hip_atomic_load((GAS u64*)(ws + OFF_E), __ATOMIC_RELAXED, __HIP_MEMORY_SCOPE_WORKGROUP) + 0x9E3779B97F4A7C15ull;
    SCHED();
    if (wid == 0 && lane < 32) lst<unsigned>(smem + A_FLG + lane * 4, 0u);
    mk_tables(smem, gb, wid, lane);
    f32x4 acc[4][4];
#pragma unroll
    for (int m = 0; m < 4; ++m)
#pragma unroll
        for (int n = 0; n < 4; ++n) acc[m][n] = Z4;
    u32x4 af[2][4], bf_[2][4];
#pragma unroll
    for (int m = 0; m < 4; ++m) { af[1][m] = (u32x4){0, 0, 0, 0}; bf_[1][m] = af[1][m]; }
    PIN(tag);
    if (wid == 0 && lane == 0) lst<u64>(smem + A_FLG + F_TAG, tag);
    tag = (u64)(unsigned)__builtin_amdgcn_readfirstlane((unsigned)tag) | (u64)(unsigned)__builtin_amdgcn_readfirstlane((unsigned)(tag >> 32)) << 32;
    __builtin_amdgcn_s_waitcnt(0xC07F);
    TS(13);
#pragma unroll 4
    for (int kt = 0; kt < 16; ++kt) {
        BAR();
        lptr tA = smem + (kt & 3) * A_STAGE;
        lptr tB = tA + 16384;
#pragma unroll
        for (int hf = 0; hf < 2; ++hf)
#pragma unroll
            for (int i = 0; i < 16; ++i) {
                acc[i >> 2][i & 3] = mfb(af[1 - hf][i >> 2], bf_[1 - hf][i & 3], acc[i >> 2][i & 3]);
                constexpr int RD[16] = RDPAT;
                const int r = RD[i];
                if (r == 0) af[hf][0] = ldfrag(tA, wr * 64 + c, 4 * hf + q);
                else if (r > 0 && r < 5) bf_[hf][r - 1] = ldfrag(tB, wc * 64 + (r - 1) * 16 + c, 4 * hf + q);
                else if (r >= 5) af[hf][r - 4] = ldfrag(tA, wr * 64 + (r - 4) * 16 + c, 4 * hf + q);
                SCHED();
            }
        LDS_WAIT();
    }
    __builtin_amdgcn_s_setprio(PRIOT);
    sig(fb + F_FREE, 1u << 20);
    float2 lam[2], coef[2], l16[2], l128[2];
#pragma unroll
    for (int n = 0; n < 2; ++n) {
        const f32x4 a = lld<f32x4>(smem + A_PRM + (wc * 32 + 2 * c + n) * 32), b = lld<f32x4>(smem + A_PRM + (wc * 32 + 2 * c + n) * 32 + 16);
        lam[n] = make_float2(a[0], a[1]); coef[n] = make_float2(a[2], a[3]); l16[n] = make_float2(b[0], b[1]); l128[n] = make_float2(b[2], b[3]);
    }
#pragma unroll
    for (int m = 0; m < 4; ++m)
#pragma unroll
        for (int n = 0; n < 4; ++n) acc[m][n] = mfb(af[1][m], bf_[1][n], acc[m][n]);
    float2 l32[2], l64[2];
#pragma unroll
    for (int n = 0; n < 2; ++n) { l32[n] = cmul(l16[n], l16[n]); l64[n] = cmul(l32[n], l32[n]); }
    TS(1);
    float2 h[2][16];
#pragma unroll
    for (int n = 0; n < 2; ++n) h[n][0] = make_float2(acc[0][n][0], acc[0][2 + n][0]);
#pragma unroll
    for (int t = 1; t < 16; ++t)
#pragma unroll
        for (int n = 0; n < 2; ++n) h[n][t] = cfma(lam[n], h[n][t - 1], make_float2(acc[t >> 2][n][t & 3], acc[t >> 2][2 + n][t & 3]));
    float2 loc[2], wag[2], wq[2];
#pragma unroll
    for (int n = 0; n < 2; ++n) {
        float2 a0, a1, a2, a3;
        rows4(h[n][15].x, a0.x, a1.x, a2.x, a3.x);
        rows4(h[n][15].y, a0.y, a1.y, a2.y, a3.y);
        const float2 s2 = cfma(l16[n], a0, a1), s3 = cfma(l16[n], s2, a2);
        wag[n] = cfma(l16[n], s3, a3);
        loc[n] = sel4(q, make_float2(0.f, 0.f), a0, s2, s3);
    }
    lptr aggp = smem + A_AGG + (wc * 16 + c) * 16;
    const unsigned flg = ldsa(smem + A_FLG) + wc * 4;
    float2 W0[2] = {make_float2(0.f, 0.f), make_float2(0.f, 0.f)};
    if (wr == 0) {
        if (q == 0) lst<f32x4>(aggp, (f32x4){wag[0].x, wag[0].y, wag[1].x, wag[1].y});
        asm volatile("" ::: "memory");
        if (lane == 0) lds_add(flg);
    } else {
#pragma unroll 1
        for (int spin = 0; spin < (1 << 20); ++spin) { if (lds_peek(flg)) break; }
        const f32x4 w0 = lld<f32x4>(aggp);
        W0[0] = make_float2(w0[0], w0[1]); W0[1] = make_float2(w0[2], w0[3]);
        const float2 g0 = cfma(l64[0], W0[0], wag[0]), g1 = cfma(l64[1], W0[1], wag[1]);
        if (lt < 15 && q < 2) {
            const float2 gg = q == 0 ? g0 : g1;
            st128<SC1>((u32x4){__float_as_uint(gg.x), (unsigned)tag, __float_as_uint(gg.y), (unsigned)(tag >> 32)},
                       rs, (2 * c + q) * 16 + OFF_GRAN + ((15 - lt) * 1024 + pt * 64 + wc * 32) * 16);
        }
    }
    TS(2);
#pragma unroll
    for (int n = 0; n < 2; ++n) { PIN(lam[n].x); PIN(lam[n].y); PIN(coef[n].x); PIN(coef[n].y); PIN(l16[n].x); PIN(l16[n].y); PIN(l128[n].x); PIN(l128[n].y); }
    float2 pw[2][8], l8[2], lq[2], l512[2];
#pragma unroll
    for (int n = 0; n < 2; ++n) {
        wq[n] = sel4(q, make_float2(1.f, 0.f), l16[n], l32[n], cmul(l32[n], l16[n]));
        loc[n] = cfma(wq[n], W0[n], loc[n]);
        if (wr) wq[n] = cmul(wq[n], l64[n]);
        pw[n][0] = cmul(coef[n], lam[n]);
        l8[n] = cmul(lam[n], lam[n]); l8[n] = cmul(l8[n], l8[n]); l8[n] = cmul(l8[n], l8[n]);
        const float2 l256 = cmul(l128[n], l128[n]);
        lq[n] = sel4(q, make_float2(1.f, 0.f), l128[n], l256, cmul(l256, l128[n]));
        l512[n] = cmul(l256, l256);
    }
#pragma unroll
    for (int n = 0; n < 2; ++n) {
        PIN(wq[n].x); PIN(wq[n].y); PIN(loc[n].x); PIN(loc[n].y); PIN(l8[n].x); PIN(l8[n].y); PIN(lq[n].x); PIN(lq[n].y); PIN(l512[n].x); PIN(l512[n].y);
    }
#pragma unroll
    for (int t = 0; t < 8; ++t) {
#pragma unroll
        for (int n = 0; n < 2; ++n) {
            if (t) pw[n][t] = cmul(pw[n][t - 1], lam[n]);
            h[n][2 * t] = cmul(coef[n], h[n][2 * t]); h[n][2 * t + 1] = cmul(coef[n], h[n][2 * t + 1]);
        }
#pragma unroll
        for (int n = 0; n < 2; ++n) { PIN(pw[n][t].x); PIN(pw[n][t].y); PIN(h[n][2 * t].x); PIN(h[n][2 * t].y); PIN(h[n][2 * t + 1].x); PIN(h[n][2 * t + 1].y); }
        SCHED();
    }
    await<0>(fb + F_FREE, 8u << 20);
    TS(3);
    if (lt > 0) {
        u32x4 gr[4][2];
        unsigned off = ((16 - lt + q) * 1024 + pt * 64 + wc * 32 + 2 * c) * 16;
#pragma unroll 1
        for (int spin = 0; spin < (1 << 20); ++spin) {
            bool ok = true;
            PIN(off);
#pragma unroll
            for (int j = 0; j < 4; ++j) {
                const rsrc_t rg = mkrsrc(ws + OFF_GRAN + j * 65536, (4 - j) * 65536);
                gr[j][0] = __builtin_amdgcn_raw_buffer_load_b128(rg, off, 0, SC1);
                gr[j][1] = __builtin_amdgcn_raw_buffer_load_b128(rg, off + 16, 0, SC1);
            }
#pragma unroll
            for (int j = 0; j < 4; ++j) {
                const bool v = lt - 1 - (4 * j + q) >= 0;
#pragma unroll
                for (int n = 0; n < 2; ++n) ok = ok & (!v | ((gr[j][n].y == (unsigned)tag) & (gr[j][n].w == (unsigned)(tag >> 32))));
            }
            if (__all(ok)) break;
            asm volatile("" ::: "memory");
        }
#pragma unroll
        for (int n = 0; n < 2; ++n) {
            float2 cs = make_float2(__uint_as_float(gr[3][n].x), __uint_as_float(gr[3][n].z));
#pragma unroll
            for (int j = 2; j >= 0; --j) cs = cfma(l512[n], cs, make_float2(__uint_as_float(gr[j][n].x), __uint_as_float(gr[j][n].z)));
            cs = cmul(cs, lq[n]);
            cs.x = rowsum4(cs.x); cs.y = rowsum4(cs.y);
            loc[n] = cfma(wq[n], cs, loc[n]);
        }
    }
    TS(4);
    lptr tb = smem + A_HS + wid * 4096;
    PIN(lane);
#pragma unroll
    for (int hf = 0; hf < 2; ++hf) {
        const float2 s0 = hf ? cmul(l8[0], loc[0]) : loc[0], s1 = hf ? cmul(l8[1], loc[1]) : loc[1];
#pragma unroll
        for (int t = 0; t < 8; ++t) {
            const float2 v0 = cfma(pw[0][t], s0, h[0][8 * hf + t]), v1 = cfma(pw[1][t], s1, h[1][8 * hf + t]);
            lst<unsigned>(tb + t * 256 + lane * 4, pkb(v0.x, v1.x));
            lst<unsigned>(tb + 2048 + t * 256 + lane * 4, pkb(-v0.y, -v1.y));
        }
        asm volatile("" ::: "memory");
        lptr rb = tb + (lane >> 4) * 256 + (lane & 3) * 64 + ((lane >> 2) & 3) * 16;
        const unsigned hoff = OFF_H + ((lane >> 4) * 64 + pt * 2 + wc) * 8192 + lt * 512 + wr * 256 + (lane & 15) * 16;
#pragma unroll
        for (int v = 0; v < 2; ++v)
#pragma unroll
            for (int pl = 0; pl < 2; ++pl) st128<SC1>(lld<u32x4>(rb + pl * 2048 + v * 1024), rs, hoff + ((2 * hf + v) * 256 + pl * 32) * 8192);
        asm volatile("" ::: "memory");
    }
    TS(5);
    wait_vm<0>();
    TS(6);
    const bool pub = sig_rtn(fb + F_HC) == 3u;
    if (pub) {
        const u32x2 tg = {(unsigned)tag, (unsigned)(tag >> 32)};
        flag_put(rs, OFF_FH, gb, tg, lane);
        flag_wait(rs, OFF_FH, gb, tg, lane, fb + F_HR);
    } else await<1>(fb + F_HR, 1u);
    __builtin_amdgcn_s_setprio(PRIOG);
    TS(7);
    PIN(lane);
    const unsigned vo = ((lane & 31) >> 2) * 256 + (lane >> 5) * 64 + (lane & 3) * 16, sH = OFF_H + lt * 524288 + wid * 2048;
    const int rot2 = ROT2;
    auto ldB = [&](int i, int n) { return __builtin_amdgcn_raw_buffer_load_b128(rs, vo + n * 128, sH + (((i >> 3) + rot2) & 7) * 65536 + (i & 7) * 8192, HAUX); };
    lptr ab[4];
#pragma unroll
    for (int i = 0; i < 4; ++i) { ab[i] = smem + (lane & 31) * 128 + (((2 * i + (lane >> 5)) ^ (lane & 7)) << 4); PIN(ab[i]); }
    auto ldA = [&](int f) {
        const int a = (f >> 1) & 3;
        if ((f & 63) < 8 && f > 7 && !(f & 1)) { ab[a] += (f & 64) ? 2 * A_STAGE : -2 * A_STAGE; PIN(ab[a]); }
        return lld<u32x4>(ab[a] + ((f >> 3) & 7) * 8192 + (f & 1) * 4096);
    };
    u32x4 A[AD], B[BD][2];
#pragma unroll
    for (int i = 0; i < BD; ++i) { B[i][0] = ldB(i, 0); B[i][1] = ldB(i, 1); }
    SCHED();
    unsigned fv = fb; PIN(fv);
    await<0>(fv + F_FULL, 4u);
#pragma unroll
    for (int f = 0; f < AD; ++f) A[f] = ldA(f);
    f32x16 ac[2];
#pragma unroll
    for (int e = 0; e < 16; ++e) { ac[0][e] = 0.f; ac[1][e] = 0.f; }
    SCHED();
    sfor<0, 64>([&](auto ic) INL {
        constexpr int i = decltype(ic)::v;
#pragma unroll
        for (int k = 0; k < 4; ++k) {
            const int f = 4 * i + k;
            ac[k & 1] = mf32(A[f % AD], B[i % BD][k >> 1], ac[k & 1]);
            SCHED();
            if ((k & 1) && i + BD < 64) B[i % BD][k >> 1] = ldB(i + BD, k >> 1);
            if (f + AD < 256) {
                if (((f + AD) & 31) == 0) await<0>(fv + F_FULL, 4u << (4 * ((f + AD) >> 5)));
                A[f % AD] = ldA(f + AD);
            }
            SCHED();
        }
        if ((i & 7) == 7 && i < 40) sig(fv + F_FREE, 1u << (4 * (i >> 3)));
        if (i == 0) { TS(8); TC(60); }
        if (i == 31) TS(9);
        if (i == 60) { TS(14); TC(61); }
    });
    TS(10);
    await<0>(fv + F_R0, 4u);
    TS(11);
    const rsrc_t ro = mkrsrc(out, S_ * D_ * 4);
    PIN(lane);
    const int qo = ((lane >> 3) & 2) | (lane >> 5);
    float o2[2];
#pragma unroll
    for (int n = 0; n < 2; ++n) o2[n] = lld<float>(smem + A_AGG + (32 * wid + 16 * n + (lane & 15)) * 4);
#pragma unroll
    for (int m = 0; m < 4; ++m) {
        f32x4 a[2];
#pragma unroll
        for (int e = 0; e < 4; ++e) {
            const float xv = ac[m >> 1][8 * (m & 1) + e], yv = ac[m >> 1][8 * (m & 1) + 4 + e];
            const auto p = __builtin_amdgcn_permlane16_swap(__float_as_uint(xv), __float_as_uint(yv), false, false);
            a[0][e] = __uint_as_float(p[0]); a[1][e] = __uint_as_float(p[1]);
        }
#pragma unroll
        for (int n = 0; n < 2; ++n) {
            const int row = 16 * (32 * wid + 16 * n + (lane & 15)) + lt;
            const f32x4 r0 = lld<f32x4>(smem + A_R0 + ((wid * 2 + n) * 4 + m) * 1024 + (qo * 16 + (lane & 15)) * 16);
            const f32x4 o = {fmaf(o2[n], a[n][0], r0[0]), fmaf(o2[n], a[n][1], r0[1]), fmaf(o2[n], a[n][2], r0[2]), fmaf(o2[n], a[n][3], r0[3])};
            st128<WTO>(__builtin_bit_cast(u32x4, o), ro, (row * D_ + 256 * m + 16 * pt + 4 * qo) * 4);
        }
    }
    TS(12);
    TSE(0, wid == 0);
    TSE(2, pub);
}

#define TSARG
#define TSPASS
struct Gate {
    unsigned xa, wa, xv, gw; float sum; int rot;
    DEV void rd(int kt) { if (XM & 8) return; asm volatile("ds_read_b32 %0, %2\n\tds_read_b32 %1, %3" : "=&v"(xv), "=&v"(gw) : "v"(xa + (kt & 3) * A_STAGE), "v"(wa + ((kt + rot) & 15) * 128) : "memory"); }
    DEV void acc() {
        if (XM & 8) return;
        asm volatile("s_waitcnt lgkmcnt(0)" : "+v"(xv), "+v"(gw) :: "memory");
        const unsigned pr = pkb(lo2f(xv) * lo2f(gw), hi2f(xv) * hi2f(gw));
        sum += lo2f(pr); sum += hi2f(pr); PIN(sum);
    }
};
DEV void c_chunk(const u16* __restrict__ Cre, const u16* __restrict__ Cim, unsigned lb, int pt, int lw, int lane, int j) {
    const int jp = (j + ROT2) & 7;
    const u32x4 rc = mkdesc(jp < 4 ? Cre : Cim, D_ * D_ * 2);
    const unsigned vo = (lane >> 3) * 2048 + (((lane & 7) ^ (lane >> 3)) << 4);
#pragma unroll
    for (int i = 0; i < 8; ++i)
        dma16(rc, lb + (j & 3) * A_STAGE + lw * 8192 + i * 1024, vo, (256 * (i >> 1) + 16 * pt + 8 * (i & 1)) * 2048 + (jp & 3) * 512 + lw * 128);
}

DEV unsigned u_load(char* ws, lptr smem, int gb, int w, int lane, u32x4 (&ut)[2]) {
    int mp, j1; slot_of(gb, mp, j1);
    __builtin_amdgcn_s_setprio(PRIOM);
    const unsigned em = await<1>(ldsa(smem + A_FLG + F_EM), 0x8000u);
    asm volatile("buffer_load_dwordx4 %0, %2, %3, %4 offen sc1\n\tbuffer_load_dwordx4 %1, %2, %3, %4 offen offset:1024 sc1"
                 : "=&v"(ut[0]), "=&v"(ut[1]) : "v"(lane * 16), "s"(mkdesc(ws, WS_END)), "s"(OFF_U + (mp * 4 + j1) * 16384 + w * 2048) : "memory");
    return em;
}

DEV float role_load(const u16* __restrict__ x, const u16* __restrict__ Bre, const u16* __restrict__ Bim,
                    const u16* __restrict__ gate_w, char* ws, lptr smem, int gb, int lw, int lane TSARG) {
    __builtin_amdgcn_s_waitcnt(0x0F70);
    Gate g;
    int lt, pt; tile_of(gb, lt, pt);
    const int srow = lane >> 3, schunk = (lane & 7) ^ srow;
    const int rot = ROT;
    const u32x4 rx = mkdesc(x, S_ * D_ * 2), rb = mkdesc((lw & 1) ? Bim : Bre, D_ * D_ * 2);
    const unsigned lb = ldsa(smem);
    const unsigned vA = (16 * (srow >> 2) + (srow & 3)) * 2048 + schunk * 16, sA = (lt * 128 + 64 * (lw >> 1) + 8 * (lw & 1)) * 2048;
    const unsigned vB = 2 * srow * 2048 + schunk * 16, sB = (pt * 64 + 32 * (lw >> 1)) * 2048;
    auto stA = [&](int kk) {
#pragma unroll
        for (int i = 0; i < 4; ++i) dma16(rx, lb + (kk & 3) * A_STAGE + lw * 4096 + i * 1024, vA, sA + (32 * (i & 1) + 4 * (i >> 1)) * 2048 + ((kk + rot) & 15) * 128);
    };
    auto stB = [&](int kk) {
#pragma unroll
        for (int i = 0; i < 4; ++i) dma16(rb, lb + (kk & 3) * A_STAGE + 16384 + lw * 4096 + i * 1024, vB, sB + (16 * (i & 1) + (i >> 1)) * 2048 + ((kk + rot) & 15) * 128);
    };
    TS(0);
    __builtin_amdgcn_s_setprio(PRIOL);
    stA(0); stB(0);
    dma16(mkdesc(gate_w, D_ * 2), lb + A_GW + (lw & 1) * 1024, lane * 16, (lw & 1) * 1024);
#pragma unroll
    for (int i = 0; i < 4; ++i) dma16(rx, lb + A_XT + lw * 4096 + i * 1024, (lane >> 1) * 32768 + (lane & 1) * 16, (i * 512 + lt) * 2048 + lw * 512 + pt * 32);
    const int gi = 8 * pt + 2 * lw + (lane >> 5);
    g.xa = lb + gi * 128 + ((((lane & 31) >> 2) ^ (gi & 7)) << 4) + (lane & 3) * 4;
    g.wa = lb + A_GW + (lane & 31) * 4; g.sum = 0.f; g.rot = rot;
    stA(1); stB(1); stA(2); stB(2);
    TS(6);
    constexpr int K0 = 0;
#pragma unroll 1
    for (int kt = K0; kt < 13; ++kt) { wait_vm<16>(); TL(8 + kt); BAR(); TL(28 + kt); if (kt == 0) { TS(28); TC(60); } g.rd(kt); stA(kt + 3); stB(kt + 3); g.acc(); }
    if (lw == 3) {
        const rsrc_t rs = mkrsrc(ws, WS_END);
        u32x4 fl[2];
        u32x2 tag = lld<u32x2>(smem + A_FLG + F_TAG); PIN(tag);
        auto publish = [&]() { sig(lb + A_FLG + F_EM, (wave_pkmax(pkmax(pkmax(fl[0].x, fl[0].z), pkmax(fl[1].x, fl[1].z))) & 0xffu) | 0x8000u); };
        wait_vm<16>(); BAR();
        asm volatile("buffer_load_dwordx4 %0, %2, %3, %4 offen sc1\n\tbuffer_load_dwordx4 %1, %2, %3, %4 offen offset:1024 sc1"
                     : "=&v"(fl[0]), "=&v"(fl[1]) : "v"(lane * 16), "s"(mkdesc(ws, WS_END)), "s"(OFF_FU + (gb & (NREP - 1)) * 2048) : "memory");
        g.rd(13); g.acc();
        wait_vm<10>(); BAR();
        asm volatile("s_waitcnt vmcnt(0)" : "+v"(fl[0]), "+v"(fl[1]) :: "memory");
        const bool ok = flag_ok(fl, tag);
        if (ok) publish();
        g.rd(14); g.acc();
        BAR(); TS(43);
        if (!ok) {
#pragma unroll 1
            for (int spin = 0; spin < (1 << 20); ++spin) { flag_get(rs, OFF_FU, gb, lane, fl); if (flag_ok(fl, tag)) break; }
            publish();
        }
    } else {
        wait_vm<16>(); TL(21); BAR(); TL(41); g.rd(13); g.acc();
        wait_vm<8>(); TL(22); BAR(); TL(42); g.rd(14); g.acc();
        wait_vm<0>(); TL(23); BAR(); TS(43); TC(61);
    }
    g.rd(15); g.acc();
    sig(lb + A_FLG + F_FREE, 1u << 20);
    return g.sum;
}

DEV unsigned ldw(rsrc_t r, unsigned vo, unsigned so) { return __builtin_amdgcn_raw_buffer_load_b32(r, vo, so, 0); }
DEV u32x4 w_pack(u32x4 re, u32x4 im, unsigned sel) {
    PIN(re); PIN(im);
#pragma unroll
    for (int r = 0; r < 4; ++r) re[r] = __builtin_amdgcn_perm(im[r], re[r], sel);
    return re;
}
DEV unsigned amax4(u32x4 p) { return pkmax(pkmax(p.x & 0x7fff7fffu, p.y & 0x7fff7fffu), pkmax(p.z & 0x7fff7fffu, p.w & 0x7fff7fffu)); }

DEV unsigned role_aux(const u16* __restrict__ Cre, const u16* __restrict__ Cim, const u16* __restrict__ w_r, const u16* __restrict__ w_i, const u16* __restrict__ lam_re, const u16* __restrict__ lam_im,
                  const u16* __restrict__ log_dt, const u16* __restrict__ Dskip, char* ws, lptr smem, int gb, int aw, int lane, u32x2 (&xc)[8],
                  u32x4 (&ut)[2] TSARG) {
    int lt, pt; tile_of(gb, lt, pt);
    int mp, j1; slot_of(gb, mp, j1);
    const int c = lane & 15, q = lane >> 4;
    const bool spc = mp == 0 && !(j1 & 1);
    const rsrc_t rs = mkrsrc(ws, WS_END), rr = mkrsrc(w_r, S_ * KF * 2), ri = mkrsrc(w_i, S_ * KF * 2);
    TS(0);
    __builtin_amdgcn_s_setprio(PRIOA);
    u32x4 fch, fcl, fsh, fsl;
    const bool m0 = 16 * aw + c == 0;
    {
        const float th = (float)(16 * aw + c) * (1.0f / 128.0f);
#pragma unroll
        for (int i = 0; i < 4; ++i) {
            const float t0 = __builtin_amdgcn_fractf(th * (float)(16 * (i >> 1) + 2 * (i & 1) + 4 * q)), t1 = t0 + th;
            const float c0 = __builtin_amdgcn_cosf(t0), c1 = __builtin_amdgcn_cosf(t1);
            const float s0 = m0 ? 1.f : -__builtin_amdgcn_sinf(t0), s1 = m0 ? -1.f : -__builtin_amdgcn_sinf(t1);
            const unsigned ch = pkb(c0, c1), sh = pkb(s0, s1);
            fch[i] = ch; fcl[i] = pkb(c0 - lo2f(ch), c1 - hi2f(ch));
            fsh[i] = sh; fsl[i] = pkb(s0 - lo2f(sh), s1 - hi2f(sh));
        }
        PIN(fch); PIN(fsh);
        if (FLO) { PIN(fcl); PIN(fsl); }
    }
    unsigned sg64 = (c & 1) ? SG : 0u, sgA = (c & 2) ? SG : 0u, sgB = ((c + 1) & 2) ? SG : 0u;
    constexpr unsigned WR = 128 * KF * 2;
    const int k1 = mp ? mp : spc ? (c >> 3) * 64 : (j1 == 1 ? 0 : 64);
    const int kd = j1 + 4 * (spc ? c & 7 : c) + 64 * (4 * aw + q);
    unsigned wo = aw < 2 ? (k1 * KF + kd) * 2 : ((128 - k1) * KF + 1023 - kd) * 2;
    unsigned wo0 = aw < 2 ? wo : k1 ? wo + 15 * WR : (1023 - kd) * 2;
    unsigned uoff = (lane >> 2) * 65536 + (lane & 3) * 16 + OFF_U + aw * 64 * 16384 + lt * 1024 + pt * 64;
    PIN(sg64); PIN(sgA); PIN(sgB); PIN(wo); PIN(wo0); PIN(uoff);
    unsigned lru, liu, ldu, um = 0, em, pf = 0;
    u32x2 tag, dk[4];
    u32x4 wre[5], wim[5];
    wre[4] = (u32x4){0, 0, 0, 0}; wim[4] = wre[4];
    if (XM & 1) for (int i = 0; i < 4; ++i) { wre[i] = fch; wim[i] = fsh; }
    f32x4 vr[4], vi[4];
    float dt = 0.f, er = 0.f; float2 lam = make_float2(0.f, 0.f);
#pragma unroll
    for (int d1 = 0; d1 < 4; ++d1) { vr[d1] = Z4; vi[d1] = Z4; }
    auto wx = [&]() INL {
        if (j1 == 0 && (aw == 3 || (gb == XG && aw < 2))) {
            u32x4 o;
            if (aw == 3) {
                const int ks = (spc ? (lane >> 5) * 64 : mp) + 128 * c, ng = (S_ - ks) & (S_ - 1);
                o = (u32x4){(q & 1) ? (ng * KF + 511) * 2u : ks * KF * 2u, (q & 1) ? (ks * KF + 511) * 2u : ng * KF * 2u, S_ * KF * 2u, S_ * KF * 2u};
            } else {
                const int sg = c < 8 ? 1 : -1;
#pragma unroll
                for (int r = 0; r < 4; ++r) o[r] = ((sg * (64 * aw + 128 * (4 * q + r)) & (S_ - 1)) * KF + (sg * (32 + 64 * c) & 1023)) * 2;
            }
            PIN(o);
#pragma unroll
            for (int r = 0; r < 4; ++r) { wre[4][r] = ldw(rr, o[r], 0); wim[4][r] = ldw(ri, o[r], 0); }
        }
    };
    auto pfx = [&]() INL {
        const u16* p = aw == 0 ? Cre : aw == 1 ? Cim : aw == 2 ? w_r : w_i;
        pf = ldw(mkrsrc(p, aw < 2 ? D_ * D_ * 2 : S_ * KF * 2), lane * 128, gb * (aw < 2 ? 8192 : 8208));
    };
    auto prm = [&]() INL {
        lru = ldw(mkrsrc(lam_re, 2048), (lane >> 1) * 4, pt * 128); liu = ldw(mkrsrc(lam_im, 2048), (lane >> 1) * 4, pt * 128);
        ldu = ldw(mkrsrc(log_dt, 2048), (lane >> 1) * 4, pt * 128);
#pragma unroll
        for (int i = 0; i < 4; ++i) dk[i] = __builtin_amdgcn_raw_buffer_load_b64(mkrsrc(Dskip, D_ * 2), q * 8 + i * 512, pt * 32, 0);
    };
    if (PRS < 0) prm();
    if (WXS < 0) wx();
    if (PFS < 0) pfx();
    sfor<0, 16>([&](auto ic) INL {
        constexpr int kt = decltype(ic)::v;
        if (kt == UFS) wait_vm<0>();
        TL(8 + kt);
        BAR();
        if (kt == 15) { em = u_load(ws, smem, gb, aw, lane, ut); TS(2); }
        if (kt == PFS) pfx();
        if (kt == UFS) PIN(pf);
        u32x4 xa[4];
        if (kt < 4) {
            constexpr int KSO[4] = {0, 2, 3, 1};
#pragma unroll
            for (int i = 0; i < 4; ++i) xa[i] = trfrag(smem + A_XT + i * 4096 + KSO[kt & 3] * 1024 + lane * 8, 512);
        }
        if (kt == 4 || kt == 5) {
#pragma unroll
            for (int i = 0; i < 4; ++i) xc[4 * (kt & 1) + i] = lld<u32x2>(smem + A_XT + i * 4096 + (32 * aw + 16 * (kt & 1) + c) * 32 + q * 8);
        }
        if (kt >= 10 && !(XM & 1)) {
            constexpr int WRW[7] = WROW;
#pragma unroll
            for (int r = 0; r < 16; ++r) {
                if (r < WRW[kt < 10 ? 0 : kt - 10] || r >= WRW[kt < 10 ? 0 : kt - 9]) continue;
                const unsigned so = r ? (aw < 2 ? r : 15 - r) * WR : 0u;
                wre[r >> 2][r & 3] = ldw(rr, r ? wo : wo0, so); wim[r >> 2][r & 3] = ldw(ri, r ? wo : wo0, so);
            }
        }
        if (kt == PRS) prm();
        if (kt == WXS) wx();
        SCHED();
        if (kt < 4 && !(XM & 4)) {
#pragma unroll
            for (int i = 0; i < 4; ++i) { vr[i] = mfb(xa[i], fch, vr[i]); vi[i] = mfb(xa[i], fsh, vi[i]); PIN(vr[i]); PIN(vi[i]); }
        }
        if (kt == 0 || kt == 2) { fch ^= sg64; fsh ^= sg64; PIN(fch); PIN(fsh); if (FLO) { fcl ^= sg64; fsl ^= sg64; PIN(fcl); PIN(fsl); } }
        if (kt == 1) {
            const bool od = c & 1;
            const u32x4 a = (od ? fsh : fch) ^ sgA, b = (od ? fch : fsh) ^ sgB, al = (od ? fsl : fcl) ^ sgA, bl = (od ? fcl : fsl) ^ sgB;
            fch = a; fsh = b; PIN(fch); PIN(fsh);
            if (FLO) { fcl = al; fsl = bl; PIN(fcl); PIN(fsl); }
        }
        if (kt == 4) {
            const f32x4 t0r = vr[0] + vr[2], t1r = vr[0] - vr[2], t2r = vr[1] + vr[3], t3r = vr[1] - vr[3];
            const f32x4 t0i = vi[0] + vi[2], t1i = vi[0] - vi[2], t2i = vi[1] + vi[3], t3i = vi[1] - vi[3];
            vr[0] = t0r; vr[1] = t1r; vr[2] = t2r; vr[3] = t3r; vi[0] = t0i; vi[1] = t1i; vi[2] = t2i; vi[3] = t3i;
        }
        if (kt == 5) {
            const f32x4 t0r = vr[0], t1r = vr[1], t2r = vr[2], t3r = vr[3], t0i = vi[0], t1i = vi[1], t2i = vi[2], t3i = vi[3];
            vr[0] = t0r + t2r; vi[0] = t0i + t2i; vr[2] = t0r - t2r; vi[2] = t0i - t2i;
            vr[1] = t1r + t3i; vi[1] = t1i - t3r; vr[3] = t1r - t3i; vi[3] = t1i + t3r;
            if (aw == 0) { if (m0) { vr[1] = t1r; vi[1] = -t3r; vr[3] = t1i; vi[3] = t3i; } }
        }
        if (kt == 4 || kt == 5) {
#pragma unroll
            for (int i = 0; i < 4; ++i) { PIN(vr[i]); PIN(vi[i]); PIN(xc[4 * (kt & 1) + i]); }
        }
        if (kt == 6 || kt == 7) {
#pragma unroll
            for (int j = 2 * (kt & 1); j < 2 * (kt & 1) + 2; ++j) {
                PIN(vr[j]); PIN(vi[j]);
                const u32x4 o = quads(smem + A_XT + aw * 4096 + j * 1024, lane, (u32x2){pkb(vr[j][0], vr[j][1]), pkb(vr[j][2], vr[j][3])},
                                      (u32x2){pkb(vi[j][0], vi[j][1]), pkb(vi[j][2], vi[j][3])});
                if (!(XM & 2)) st128<SC1>(o, rs, uoff + j * 16384);
                um = pkmax(um, amax4(o));
                PIN(um);
            }
        }
        if (kt == 8) {
            um = wave_pkmax(pkmax(um, um >> 16));
            if (lane == 0) lst<unsigned>(smem + A_FLG + F_MAX + aw * 4, um);
        }
        if (kt == 8 || kt == 9) {
#pragma unroll
            for (int i = 4 * (kt & 1); i < 4 * (kt & 1) + 4; ++i) {
                const u32x2 d = dk[i & 3];
                xc[i] = (u32x2){pkb(lo2f(xc[i].x) * lo2f(d.x), hi2f(xc[i].x) * hi2f(d.x)), pkb(lo2f(xc[i].y) * lo2f(d.y), hi2f(xc[i].y) * hi2f(d.y))};
                PIN(xc[i]);
            }
        }
        if (kt == 8) { tag = lld<u32x2>(smem + A_FLG + F_TAG); PIN(tag); }
        if (kt == UFS && aw == 0) {
            const u32x4 m4 = lld<u32x4>(smem + A_FLG + F_MAX);
            const unsigned m = pkmax(pkmax(m4.x, m4.y), pkmax(m4.z, m4.w));
            flag_put(rs, OFF_FU, gb, (u32x2){(tag.x & 0xffff0000u) | ((m & 0xffffu) >> 7), tag.y}, lane);
        }
        if (kt == 9) {
            PIN(lru); PIN(liu); PIN(ldu);
            if (lane & 1) { lru >>= 16; liu >>= 16; ldu >>= 16; }
            PIN(lru); PIN(liu); PIN(ldu);
        }
        if (kt >= 10 && kt < 13 && aw == 1) {
            const float lr = lo2f(lru), li = lo2f(liu);
            if (kt == 10) { dt = bfr(expf(lo2f(ldu))); er = expf(dt * lr); PIN(dt); PIN(er); }
            if (kt == 11) { float sn, cs; sincosf(dt * li, &sn, &cs); lam = make_float2(er * cs, er * sn); PIN(lam.x); PIN(lam.y); }
            if (kt == 12) {
                const float nr = lam.x - 1.0f, ni = lam.y, den = lr * lr + li * li;
                const float2 coef = make_float2((nr * lr + ni * li) / den, (ni * lr - nr * li) / den);
                float2 l16 = lam;
#pragma unroll
                for (int i = 0; i < 4; ++i) l16 = cmul(l16, l16);
                float2 l128 = l16;
#pragma unroll
                for (int i = 0; i < 3; ++i) l128 = cmul(l128, l128);
                lst<f32x4>(smem + A_PRM + lane * 32, (f32x4){lam.x, lam.y, coef.x, coef.y});
                lst<f32x4>(smem + A_PRM + lane * 32 + 16, (f32x4){l16.x, l16.y, l128.x, l128.y});
            }
        }
        LDS_WAIT();
        SCHED();
    });
    unsigned mx = 0;
    const bool skip = j1 == 0 && !(aw & 1) && (lane & (spc ? 55 : 63)) == 0;
#pragma unroll
    for (int i = 0; i < 4; ++i) {
        const u32x4 p = w_pack(wre[i], wim[i], aw < 2 ? 0x05040100u : 0x07060302u);
        if (!skip) lst<u32x4>(smem + A_W + c * 1024 + i * 256 + (((4 * aw + q) ^ c) << 4), p);
        mx = pkmax(mx, amax4(p));
    }
    if (j1 == 0) {
        const u32x4 p = w_pack(wre[4], wim[4], aw == 3 && (q & 1) ? 0x07060302u : 0x05040100u);
        mx = pkmax(mx, amax4(p));
        const int it = spc ? (lane >> 5) * 8 : 0;
        if (aw == 3) lst<unsigned>(smem + A_W + it * 1024 + (c >> 2) * 256 + ((((q & 1) * 8) ^ it) << 4) + (c & 3) * 4,
                                  pkb(0.5f * (lo2f(p.x) + lo2f(p.y)), 0.5f * (hi2f(p.x) - hi2f(p.y))));
        else lst<u32x4>(smem + A_W + 16384 + aw * 1024 + lane * 16, p);
    }
    mx = wave_pkmax(pkmax(mx, mx >> 16));
    if (lane == 0) lst<unsigned>(smem + A_FLG + F_WM + aw * 4, mx);
    wait_vm<0>();
    SCHED();
    return em;
}

DEV int role_mid(const u16* __restrict__ Cre, const u16* __restrict__ Cim, char* ws, lptr smem,
                 int gb, int w, int lane, float gsum, u32x2& tag, u32x4 (&ut)[2], unsigned em TSARG) {
    int lt, pt; tile_of(gb, lt, pt);
    int mp, j1; slot_of(gb, mp, j1);
    const int c = lane & 15, q = lane >> 4;
    const bool spc = mp == 0 && !(j1 & 1), xtr = gb == XG, ld = w >= 4;
    const rsrc_t rs = mkrsrc(ws, WS_END);
    const unsigned lb = ldsa(smem), fb = lb + A_FLG;
    u32x4 wv[2], mt[6];
    f32x4 tf[2]; f32x2 ti[2];
    tag = lld<u32x2>(smem + A_FLG + F_TAG);
#pragma unroll
    for (int i = 0; i < 2; ++i) {
        tf[i] = lld<f32x4>(smem + A_MAT + 9216 + c * 128 + q * 32 + i * 16);
        ti[i] = lld<f32x2>(smem + A_MAT + 9216 + (2 * w + i) * 128 + c * 8);
    }
    lptr mb = smem + A_MAT + lane * 16;
#pragma unroll
    for (int i = 0; i < 6; ++i) mt[i] = lld<u32x4>(mb + (i < 4 ? i : i + 2) * 1024);
    u32x4 sf0 = lld<u32x4>(mb + 4096), si0 = lld<u32x4>(mb + 5120);
    const u32x4 f0 = lld<u32x4>(mb + 8192);
    SCHED();
    if (spc && ld) { sf0 = mat_sf(64, c, q); si0 = mat_si(64, c, q); }
    const CM sf = cexp(sf0), si = cexp(si0), ff = cexp(f0), fi = cexp((u32x4){f0.x ^ SG, f0.y ^ SG, f0.z, f0.w});
#pragma unroll
    for (int i = 0; i < 6; ++i) PIN(mt[i]);
    PIN(ti[0]); PIN(ti[1]); PIN(tag);
    const int eu = min(max((int)(em & 0xffu), 4), 254);
    const float su = __uint_as_float((unsigned)(258 - eu) << 23);
    tf[0] *= su; tf[1] *= su; PIN(tf[0]); PIN(tf[1]);
    u32x2 gv = {0, 0};
    if (ld) {
        const float gg = bfr(1.0f / (1.0f + expf(-bfr(half_sum(gsum)))));
        gv = (u32x2){__float_as_uint(gg), __float_as_uint(bfr(1.0f - gg))};
        PIN(gv);
    }
    await<0>(fb + F_FREE, 8u << 20);
    asm volatile("s_waitcnt vmcnt(%2)" : "+v"(ut[0]), "+v"(ut[1]) : "i"((C0S == 15 ? 8 : 0) + (C1S == 15 ? 8 : 0)) : "memory");
    lptr ua = smem + A_XT + w * 2048;
#pragma unroll
    for (int i = 0; i < 2; ++i) lst<u32x4>(ua + i * 1024 + lane * 16, ut[i]);
    TS(3);
    asm volatile("" ::: "memory");
    {
        lptr ea = smem + A_E1 + c * E1P + q * 256 + 2 * w * 16;
#pragma unroll
        for (int i = 0; i < 2; ++i) ut[i] = trfrag(ua + i * 1024 + (lane >> 2) * 64 + (lane & 3) * 8, 32);
#pragma unroll
        for (int i = 0; i < 2; ++i) {
            f32x4 r = mfb(ut[i], mt[2], Z4), m = mfb(ut[i], mt[3], Z4);
            r = mfb(ut[i], mt[0], r); m = mfb(ut[i], mt[1], m);
            f32x4 yr, yi;
#pragma unroll
            for (int e = 0; e < 4; ++e) {
                const float tc = tf[e >> 1][2 * (e & 1)], ts = tf[e >> 1][2 * (e & 1) + 1];
                yr[e] = fmaf(r[e], tc, -m[e] * ts); yi[e] = fmaf(r[e], ts, m[e] * tc);
            }
            lst<u32x4>(ea + i * 16, pk8(yr, yi));
            if (xtr) {
                const unsigned sg = 0xbf803f80u;
                const f32x4 e = mfb(ut[i], c == 0 ? (u32x4){sg, sg, 0, 0} : (u32x4){0, 0, sg, sg}, Z4);
                if (c < 2) lst<u32x4>(ea + 16 * E1P + i * 16, pk8(e * su, Z4));
            }
        }
    }
    sig(fb + F_MID, 1u);
    if (ld) {
        const int gi = 8 * pt + 2 * (w - 4) + (lane >> 5), tau = (gi & 64) | ((gi & 12) << 2) | ((gi & 48) >> 2) | (gi & 3);
        if ((lane & 31) == 0) __builtin_amdgcn_raw_buffer_store_b64(gv, rs, (tau & 15) * 1024 + (lt * 8 + (tau >> 4)) * 8, OFF_G, SC1);
    }
    if (xtr && w == 0 && lane == 0) __builtin_amdgcn_raw_buffer_store_b64(tag, rs, 0, OFF_E, SC1);
    TS(4);
    await<0>(fb + F_MID, 24u);
    if (ld && C0S == 16) c_chunk(Cre, Cim, lb, pt, w - 4, lane, 0);
    if (ld && C1S == 16) c_chunk(Cre, Cim, lb, pt, w - 4, lane, 1);
    u32x4 dv[2];
#pragma unroll
    for (int i = 0; i < 2; ++i) dv[i] = lld<u32x4>(smem + A_E1 + (2 * w + i) * E1P + lane * 16);
    const u32x4 m4 = lld<u32x4>(smem + A_FLG + F_WM);
#pragma unroll
    for (int i = 0; i < 2; ++i) wv[i] = lld<u32x4>(smem + A_W + (2 * w + i) * 1024 + ((lane ^ (2 * w + i)) << 4));
#pragma unroll
    for (int i = 0; i < 2; ++i) dv[i] = pk8(mfh(dv[i], ff.re, Z4), mfh(dv[i], ff.im, Z4));
    const unsigned ew = min((pkmax(pkmax(m4.x, m4.y), pkmax(m4.z, m4.w)) & 0xffffu) >> 7, 245u);
    const float sw = __uint_as_float((246u - ew) << 23), swi = c < 8 ? sw : -sw;
    wv[0] = wcvt(wv[0], sw, swi); wv[1] = wcvt(wv[1], sw, swi);
#pragma unroll
    for (int i = 0; i < 2; ++i) dv[i] = wmul(mfh(sf.re, dv[i], Z4), mfh(sf.im, dv[i], Z4), wv[i]);
#pragma unroll
    for (int i = 0; i < 2; ++i) dv[i] = pk8(mfh(dv[i], si.re, Z4), mfh(dv[i], si.im, Z4));
    {
        f32x4 r[2], m[2];
#pragma unroll
        for (int i = 0; i < 2; ++i) {
            const f32x4 a = mfh(dv[i], fi.re, Z4), b = mfh(dv[i], fi.im, Z4);
#pragma unroll
            for (int e = 0; e < 4; ++e) { r[i][e] = fmaf(a[e], ti[i][0], b[e] * ti[i][1]); m[i][e] = fmaf(b[e], ti[i][0], -a[e] * ti[i][1]); }
        }
#pragma unroll
        for (int e = 0; e < 4; ++e)
            lst<u32x2>(smem + A_XT + (4 * q + e) * 1024 + (w >> 1) * 256 + c * 16 + (w & 1) * 8, (u32x2){pkh(r[0][e], r[1][e]), pkh(m[0][e], m[1][e])});
    }
    if (xtr && !(w & 3)) {
        const float dt = -(float)(32 + 64 * c) * (1.0f / 1024.0f);
        const CM f = cexp(cmat1(dt * (float)(4 * q), dt)), b = cexp(cmat1((float)(c * (32 + 256 * q)) * (1.0f / 1024.0f), (float)c * (1.0f / 16.0f)));
        u32x4 d = lld<u32x4>(smem + A_E1 + (16 + (w >> 2)) * E1P + lane * 16);
        d = pk8(mfh(d, f.re, Z4), mfh(d, f.im, Z4));
        d = wmul(mfh(sf.re, d, Z4), mfh(sf.im, d, Z4), wcvt(lld<u32x4>(smem + A_W + 16384 + w * 256 + lane * 16), sw, swi));
        d = pk8(mfh(d, si.re, Z4), mfh(d, si.im, Z4));
        const f32x4 r = mfh(d, b.re, Z4);
#pragma unroll
        for (int e = 0; e < 4; ++e) lst<float>(smem + A_EX + ((w >> 2) * 256 + (4 * q + e) * 16 + c) * 4, r[e]);
    }
    TS(5);
    sig(fb + F_MID, 1u);
    await<0>(fb + F_MID, 16u);
    {
        f32x4 xa[2], xb[2];
#pragma unroll
        for (int i = 0; i < 2; ++i) { xa[i] = Z4; xb[i] = Z4; }
        if (xtr) {
            const float sg = (c & 1) ? -0.125f : 0.125f;
#pragma unroll
            for (int i = 0; i < 2; ++i) {
                xa[i] = lld<f32x4>(smem + A_EX + (2 * w + i) * 64 + q * 16) * sg;
                xb[i] = lld<f32x4>(smem + A_EX + 1024 + (2 * w + i) * 64 + q * 16) * -sg;
            }
        }
#pragma unroll
        for (int i = 0; i < 2; ++i) {
            const int s2 = 2 * w + i;
            const u32x4 ea = lld<u32x4>(smem + A_XT + s2 * 1024 + lane * 16);
            const f32x4 ta = mfh(ea, mt[4], xa[i]), tb = mfh(ea, mt[5], xb[i]);
            st128<SC1>(quads(smem + A_XT + s2 * 1024, lane, (u32x2){pkh(ta[0], ta[1]), pkh(ta[2], ta[3])}, (u32x2){pkh(tb[0], tb[1]), pkh(tb[2], tb[3])}), rs,
                       (lane >> 2) * 16384 + (lane & 3) * 16 + OFF_T + s2 * 262144 + j1 * 4096 + mp * 64);
        }
    }
    TS(7);
    tag.x = (tag.x & 0xffff0000u) | ew;
    return eu - 267;
}

DEV void t_done(rsrc_t rs, unsigned fb, int gb, u32x2 tag, int lane) { if (sig_rtn(fb + F_TC) == 7u) flag_put(rs, OFF_FT, gb ^ XG, tag, lane); }

DEV void role_cload(const u16* __restrict__ Cre, const u16* __restrict__ Cim, char* ws, lptr smem, int gb, int lw, int lane, u32x2 tag TSARG) {
    int lt, pt; tile_of(gb, lt, pt);
    const unsigned lb = ldsa(smem), fb = lb + A_FLG;
    __builtin_amdgcn_s_setprio(3);
    const bool early = lds_peek(fb + F_HC) & 4u;
    if (!early) {
        wait_vm<0>();
        t_done(mkrsrc(ws, WS_END), fb, gb, tag, lane);
        await<2>(fb + F_HC, 4u);
    }
    c_chunk(Cre, Cim, lb, pt, lw, lane, 2); c_chunk(Cre, Cim, lb, pt, lw, lane, 3);
    if (early) {
        wait_vm<16>();
        t_done(mkrsrc(ws, WS_END), fb, gb, tag, lane);
    }
    sig(fb + F_FULL, 0x11u);
    TS(44);
    wait_vm<0>();
    sig(fb + F_FULL, 0x1100u);
    TS(47);
#pragma unroll 1
    for (int j = 4; j < 8; ++j) {
        await<2>(fb + F_FREE, 4u << (4 * (j - 4)));
        c_chunk(Cre, Cim, lb, pt, lw, lane, j);
        wait_vm<8>();
        if (j > 4) sig(fb + F_FULL, 1u << (4 * (j - 1)));
        TS(44 + j);
    }
    wait_vm<0>();
    sig(fb + F_FULL, 1u << 28);
    TSE(1, lw == 0);
}

DEV void role_last(char* ws, lptr smem, int gb, int w, int lane, u32x2 tag, int fe, u32x2 (&xc)[8] TSARG) {
    int s2t, dat; tile_of(gb, s2t, dat);
    const int c = lane & 15, q = lane >> 4;
    const rsrc_t rs = mkrsrc(ws, WS_END);
    const unsigned fb = ldsa(smem + A_FLG);
    SCHED();
    u32x4 fc[2][2], fs[2][2];
#pragma unroll
    for (int n = 0; n < 2; ++n) {
        float dt = (float)(32 * w + 16 * n + c) * (1.0f / 128.0f); PIN(dt);
        float cc[8], ss[8];
        const float t0 = __builtin_amdgcn_fractf(dt * (float)(4 * q)), t1 = __builtin_amdgcn_fractf(dt * (float)(16 + 4 * q));
#pragma unroll
        for (int e = 0; e < 8; ++e) { const float f = fmaf((float)(e & 3), dt, e < 4 ? t0 : t1); cc[e] = __builtin_amdgcn_cosf(f); ss[e] = __builtin_amdgcn_sinf(f); }
        fc[n][0] = (u32x4){pkh(cc[0], cc[1]), pkh(cc[2], cc[3]), pkh(cc[4], cc[5]), pkh(cc[6], cc[7])};
        fs[n][0] = (u32x4){pkh(ss[0], ss[1]), pkh(ss[2], ss[3]), pkh(ss[4], ss[5]), pkh(ss[6], ss[7])};
        fc[n][1] = ((c & 1) ? fs[n][0] : fc[n][0]) ^ (((c + 1) & 2) ? SG : 0u);
        fs[n][1] = ((c & 1) ? fc[n][0] : fs[n][0]) ^ ((c & 2) ? SG : 0u);
        PIN(fc[n][0]); PIN(fs[n][0]); PIN(fc[n][1]); PIN(fs[n][1]);
        if (n == 0) { wait_vm<0>(); t_done(rs, fb, gb, tag, lane); TS(44); }
    }
    TS(45);
    __builtin_amdgcn_s_setprio(3);
    if (w == 0) {
        u32x4 fl[2];
        await<2>(fb + F_HR, 1u);
#pragma unroll 1
        for (int spin = 0; spin < (1 << 20); ++spin) { flag_get(rs, OFF_FT, gb, lane, fl); if (flag_ok(fl, tag)) break; }
        const unsigned em = wave_pkmax(pkmax(pkmax(fl[0].x, fl[0].z), pkmax(fl[1].x, fl[1].z))) & 0xffu;
#pragma unroll
        for (int i = 0; i < 2; ++i) {
            const float a = ldexpf(1.f, (int)(fl[i].x & 0xffu) - (int)em), b = ldexpf(1.f, (int)(fl[i].z & 0xffu) - (int)em);
            lst<u32x2>(smem + A_MAT + i * 512 + lane * 8, (u32x2){pkh(a, a), pkh(b, b)});
        }
        sig(fb + F_TR, em | 0x8000u);
    }
    fe += await<1>(fb + F_TR, 0x8000u) & 0xffu;
    TS(46);
    u32x4 tv[4]; u32x2 gg[2];
#pragma unroll
    for (int i = 0; i < 4; ++i) tv[i] = __builtin_amdgcn_raw_buffer_load_b128(rs, lane * 16 + i * 1024, OFF_T + (s2t * 16 + dat) * 16384 + w * 4096, SC1);
#pragma unroll
    for (int n = 0; n < 2; ++n) gg[n] = __builtin_amdgcn_raw_buffer_load_b64(rs, c * 8 + n * 128, OFF_G + s2t * 1024 + w * 256, SC1);
    __builtin_amdgcn_s_setprio(PRIOR);
    unsigned ts[4];
#pragma unroll
    for (int i = 0; i < 4; ++i) ts[i] = lld<unsigned>(smem + A_MAT + i * 256 + (lane >> 5) * 128 + w * 32 + ((lane >> 2) & 7) * 4);
#pragma unroll
    for (int i = 0; i < 4; ++i) {
        tv[i] = __builtin_bit_cast(u32x4, __builtin_bit_cast(f16x8, tv[i]) * __builtin_bit_cast(f16x8, (u32x4){ts[i], ts[i], ts[i], ts[i]}));
        lst<u32x4>(smem + A_XT + w * 4096 + ((lane >> 1) & 1) * 2048 + i * 512 + (lane >> 2) * 32 + (lane & 1) * 16, tv[i]);
    }
    TS(47);
    sig(fb + F_TT, 1u);
    await<0>(fb + F_TT, 4u);
    f32x4 P0[2], P1[2], P2[2], P3a[2], P3b[2];
#pragma unroll
    for (int n = 0; n < 2; ++n) { P0[n] = Z4; P1[n] = Z4; P2[n] = Z4; P3a[n] = Z4; P3b[n] = Z4; }
    lptr tt = smem + A_XT + lane * 8;
#pragma unroll
    for (int h = 0; h < 2; ++h) {
        const u32x4 a1 = trfrag(tt + 1 * 4096 + h * 1024, 512), b1 = trfrag(tt + 1 * 4096 + 2048 + h * 1024, 512);
        const u32x4 a3 = trfrag(tt + 3 * 4096 + h * 1024, 512), b3 = trfrag(tt + 3 * 4096 + 2048 + h * 1024, 512);
        const u32x4 a0 = trfrag(tt + h * 1024, 512), b0 = trfrag(tt + 2048 + h * 1024, 512);
        const u32x4 a2 = trfrag(tt + 2 * 4096 + h * 1024, 512), b2 = trfrag(tt + 2 * 4096 + 2048 + h * 1024, 512);
#pragma unroll
        for (int n = 0; n < 2; ++n) {
            u32x4 cw = fc[n][h], sw_ = fs[n][h];
            P1[n] = mfh(a1, cw, P1[n]); P3a[n] = mfh(a1, sw_, P3a[n]); P3b[n] = mfh(b1, cw, P3b[n]); P1[n] = mfh(b1, sw_, P1[n]);
            if (h == 0) cw.x = q == 0 ? (cw.x & 0xffff0000u) | ((c & 1) ? 0xbc00u : 0x3c00u) : cw.x;
            P3a[n] = mfh(b3, cw, P3a[n]); P3b[n] = mfh(a3, sw_, P3b[n]); P1[n] = mfh(a3, cw, P1[n]); P1[n] = mfh(b3, sw_, P1[n]);
            if (h == 0) { cw.x = q == 0 ? (cw.x & 0xffff0000u) | 0x3800u : cw.x; sw_.x = q == 0 ? (sw_.x & 0xffff0000u) | ((c & 1) ? 0x3800u : 0xb800u) : sw_.x; }
            P0[n] = mfh(a0, cw, P0[n]); P2[n] = mfh(a2, cw, P2[n]); P0[n] = mfh(b0, sw_, P0[n]); P2[n] = mfh(b2, sw_, P2[n]);
        }
    }
    f32x4 r0[2][4];
#pragma unroll
    for (int n = 0; n < 2; ++n) {
        const float g = __uint_as_float(gg[n].x), og = __uint_as_float(gg[n].y);
        const f32x4 sp = P0[n] + P2[n], sm = P0[n] - P2[n], p3 = P3a[n] - P3b[n];
        const f32x4 av[4] = {sp + P1[n], sm - p3, sp - P1[n], sm + p3};
#pragma unroll
        for (int m = 0; m < 4; ++m) {
            const unsigned b0 = pkb(ldexpf(av[m][0], fe), ldexpf(av[m][1], fe)), b1 = pkb(ldexpf(av[m][2], fe), ldexpf(av[m][3], fe));
            const unsigned o0 = pkb(g * lo2f(b0), g * hi2f(b0)), o1 = pkb(g * lo2f(b1), g * hi2f(b1));
            const u32x2 p = xc[4 * n + m];
            r0[n][m] = (f32x4){fmaf(og, lo2f(p.x), lo2f(o0)), fmaf(og, hi2f(p.x), hi2f(o0)), fmaf(og, lo2f(p.y), lo2f(o1)), fmaf(og, hi2f(p.y), hi2f(o1))};
            PIN(r0[n][m]);
        }
    }
    TS(48);
    await<2>(fb + F_FREE, 4u << 16);
#pragma unroll
    for (int n = 0; n < 2; ++n) {
#pragma unroll
        for (int m = 0; m < 4; ++m) lst<f32x4>(smem + A_R0 + ((w * 2 + n) * 4 + m) * 1024 + lane * 16, r0[n][m]);
        if (q == 0) lst<float>(smem + A_AGG + (32 * w + 16 * n + c) * 4, 2.0f * __uint_as_float(gg[n].y));
    }
    sig(fb + F_R0, 1u);
    TS(49);
    TSE(w ? 4 : 5, w == 0 || w == 2);
}

NOPK __global__ __launch_bounds__(768, 3) void gm_kernel(const u16* __restrict__ x, const u16* __restrict__ w_r, const u16* __restrict__ w_i,
                                                         const u16* __restrict__ Bre, const u16* __restrict__ Bim, const u16* __restrict__ Cre,
                                                         const u16* __restrict__ Cim, const u16* __restrict__ lam_re,
                                                         const u16* __restrict__ lam_im, const u16* __restrict__ log_dt,
                                                         const u16* __restrict__ Dskip, const u16* __restrict__ gate_w, char* ws, float* __restrict__ out) {
    extern __shared__ __attribute__((aligned(16))) char smem_[];
    lptr smem = (lptr)smem_;
    const int wid = __builtin_amdgcn_readfirstlane(threadIdx.x >> 6), lane = threadIdx.x & 63;
    int w = wid & 7, gb = blockIdx.x;
    asm volatile("" : "+s"(w), "+s"(gb));
    if (wid < 4) role_mma(ws, out, smem, blockIdx.x, wid, lane);
    else {
        TSD();
        float gsum = 0.f;
        u32x2 xc[8], tag;
        u32x4 ut[2];
        unsigned em;
        if (wid < 8) {
            gsum = role_load(x, Bre, Bim, gate_w, ws, smem, blockIdx.x, wid - 4, lane TSPASS);
            em = u_load(ws, smem, blockIdx.x, wid, lane, ut);
            TS(2);
            int lt, pt; tile_of(blockIdx.x, lt, pt);
            if (C0S == 15) c_chunk(Cre, Cim, ldsa(smem), pt, wid - 4, lane, 0);
            if (C1S == 15) c_chunk(Cre, Cim, ldsa(smem), pt, wid - 4, lane, 1);
#pragma unroll
            for (int i = 0; i < 8; ++i) asm volatile("" : "=v"(xc[i]));
        } else em = role_aux(Cre, Cim, w_r, w_i, lam_re, lam_im, log_dt, Dskip, ws, smem, blockIdx.x, wid - 8, lane, xc, ut TSPASS);
        const int fe = role_mid(Cre, Cim, ws, smem, gb, w, lane, gsum, tag, ut, em TSPASS);
        if (w >= 4) role_cload(Cre, Cim, ws, smem, gb, w - 4, lane, tag TSPASS);
        else role_last(ws, smem, gb, w, lane, tag, fe, xc TSPASS);
    }
}

extern "C" void kernel_launch(void* const* d_in, const int* in_sizes, int n_in, void* d_out, int out_size, void* d_ws, size_t ws_size,
                              hipStream_t stream) {
    (void)hipFuncSetAttribute((const void*)gm_kernel, hipFuncAttributeMaxDynamicSharedMemorySize, SMEM);
    gm_kernel<<<256, 768, SMEM, stream>>>((const u16*)d_in[0], (const u16*)d_in[1], (const u16*)d_in[2], (const u16*)d_in[6], (const u16*)d_in[7],
                                          (const u16*)d_in[8], (const u16*)d_in[9], (const u16*)d_in[4], (const u16*)d_in[5], (const u16*)d_in[10],
                                          (const u16*)d_in[11], (const u16*)d_in[12], (char*)d_ws, (float*)d_out);
}
```

```cpp
#define PRS 4
#define PDL 48
#define PD0 32
#include <hip/hip_runtime.h>
#include <stdint.h>

typedef __attribute__((ext_vector_type(8))) __bf16 bf16x8;
typedef __attribute__((ext_vector_type(2))) __bf16 bf16x2;
typedef __attribute__((ext_vector_type(8))) _Float16 f16x8;
typedef __attribute__((ext_vector_type(2))) _Float16 f16x2;
typedef __attribute__((ext_vector_type(4))) float f32x4;
typedef __attribute__((ext_vector_type(16))) float f32x16;
typedef __attribute__((ext_vector_type(2))) float f32x2;
typedef __attribute__((ext_vector_type(4))) unsigned u32x4;
typedef __attribute__((ext_vector_type(2))) unsigned u32x2;
typedef __attribute__((ext_vector_type(4))) short s16x4;
typedef __attribute__((ext_vector_type(2))) unsigned short u16x2;
typedef unsigned short u16;
typedef unsigned long long u64;
#define GAS __attribute__((address_space(1)))
#define LAS __attribute__((address_space(3)))
#define NOPK __attribute__((target("no-packed-fp32-ops")))
#define DEV __device__ __forceinline__

constexpr int S_ = 2048, D_ = 1024, KF = 513;

constexpr unsigned OFF_H = 0;
constexpr unsigned OFF_U = OFF_H + (8u << 20);
constexpr unsigned OFF_T = OFF_U + (4u << 20);
constexpr unsigned OFF_G = OFF_T + (4u << 20);
constexpr unsigned OFF_FU = OFF_G + 16384u;
constexpr unsigned OFF_FT = OFF_FU + 32768u;
constexpr unsigned OFF_FH = OFF_FT + 32768u;
constexpr unsigned OFF_GRAN = OFF_FH + 1048576u;
constexpr unsigned OFF_E = OFF_GRAN + 16u * 1024u * 16u;
constexpr unsigned WS_END = OFF_E + 128u;

#define TL(i)
#define TSD()
#define TS(i)
#define TC(i)
#define TSE(slot, on)

DEV unsigned pkb(float a, float b) { f32x2 v = {a, b}; return __builtin_bit_cast(unsigned, __builtin_convertvector(v, bf16x2)); }
DEV unsigned pkh(float a, float b) { f32x2 v = {a, b}; return __builtin_bit_cast(unsigned, __builtin_convertvector(v, f16x2)); }
DEV float lo2f(unsigned p) { return __uint_as_float(p << 16); }
DEV float hi2f(unsigned p) { return __uint_as_float(p & 0xffff0000u); }
DEV float bfr(float f) { return lo2f(pkb(f, f)); }
DEV float2 cmul(float2 a, float2 b) { return make_float2(a.x * b.x - a.y * b.y, a.x * b.y + a.y * b.x); }
DEV float2 cfma(float2 a, float2 b, float2 c) { return make_float2(fmaf(a.x, b.x, fmaf(-a.y, b.y, c.x)), fmaf(a.x, b.y, fmaf(a.y, b.x, c.y))); }
DEV f32x4 mfb(u32x4 a, u32x4 b, f32x4 c) { return __builtin_amdgcn_mfma_f32_16x16x32_bf16(__builtin_bit_cast(bf16x8, a), __builtin_bit_cast(bf16x8, b), c, 0, 0, 0); }
DEV f32x16 mf32(u32x4 a, u32x4 b, f32x16 c) { return __builtin_amdgcn_mfma_f32_32x32x16_bf16(__builtin_bit_cast(bf16x8, a), __builtin_bit_cast(bf16x8, b), c, 0, 0, 0); }
DEV f32x4 mfh(u32x4 a, u32x4 b, f32x4 c) { return __builtin_amdgcn_mfma_f32_16x16x32_f16(__builtin_bit_cast(f16x8, a), __builtin_bit_cast(f16x8, b), c, 0, 0, 0); }
DEV unsigned pkmax(unsigned a, unsigned b) { return __builtin_bit_cast(unsigned, __builtin_elementwise_max(__builtin_bit_cast(u16x2, a), __builtin_bit_cast(u16x2, b))); }
typedef LAS char* lptr;
DEV unsigned ldsa(lptr p) { return (unsigned)(uintptr_t)p; }
template <class T> DEV T lld(lptr p) { return *(LAS const T*)p; }
template <class T> DEV void lst(lptr p, T v) { *(LAS T*)p = v; }
DEV u32x2 trread(lptr p) { return __builtin_bit_cast(u32x2, __builtin_amdgcn_ds_read_tr16_b64_v4i16((LAS s16x4*)p)); }
DEV u32x4 trfrag(lptr p, int second) { const u32x2 a = trread(p), b = trread(p + second); return (u32x4){a.x, a.y, b.x, b.y}; }
typedef __amdgpu_buffer_rsrc_t rsrc_t;
DEV u32x4 ldfrag(lptr tile, int row, int chunk) { return lld<u32x4>(tile + row * 128 + ((chunk ^ (row & 7)) << 4)); }
template <int N> DEV void wait_vm() { asm volatile("s_waitcnt vmcnt(%0)" :: "i"(N) : "memory"); }
#define LDS_WAIT() asm volatile("s_waitcnt lgkmcnt(0)" ::: "memory")
#define SCHED() __builtin_amdgcn_sched_barrier(0)
#define BAR() do { __builtin_amdgcn_s_barrier(); asm volatile("" ::: "memory"); } while (0)
#define PIN(x) asm volatile("" : "+v"(x))
template <int I> struct IC { static constexpr int v = I; };
template <int I, int N, class F> DEV void sfor(F&& f) { if constexpr (I < N) { f(IC<I>{}); sfor<I + 1, N>(f); } }
#define INL __attribute__((always_inline))
DEV rsrc_t mkrsrc(const void* p, unsigned n) { return __builtin_amdgcn_make_buffer_rsrc((void*)p, 0, n, 0x00020000); }
constexpr int SC1 = 16;
#define WTO 16
template <int AUX> DEV void st128(u32x4 v, rsrc_t rs, unsigned off) { __builtin_amdgcn_raw_buffer_store_b128(v, rs, off, 0, AUX); }
DEV void lds_add(unsigned a) { asm volatile("ds_add_u32 %0, %1" :: "v"(a), "v"(1u) : "memory"); }
DEV unsigned lds_peek(unsigned a) {
    unsigned v;
    asm volatile("ds_read_b32 %0, %1\n\ts_waitcnt lgkmcnt(0)" : "=v"(v) : "v"(a) : "memory");
    return __builtin_amdgcn_readfirstlane(v);
}
DEV void tile_of(int gb, int& ta, int& tb) {
    const int xcd = gb & 7, loc = gb >> 3;
    ta = (xcd >> 1) * 4 + (loc >> 3);
    tb = (xcd & 1) * 8 + (loc & 7);
}
DEV void rows4(float a, float& a0, float& a1, float& a2, float& a3) {
    const unsigned u = __float_as_uint(a);
    const auto r = __builtin_amdgcn_permlane32_swap(u, u, false, false);
    const auto p = __builtin_amdgcn_permlane16_swap(r[0], r[0], false, false);
    const auto s = __builtin_amdgcn_permlane16_swap(r[1], r[1], false, false);
    a0 = __uint_as_float(p[0]); a1 = __uint_as_float(p[1]); a2 = __uint_as_float(s[0]); a3 = __uint_as_float(s[1]);
}
DEV float rowsum4(float a) {
    const unsigned u = __float_as_uint(a);
    const auto r = __builtin_amdgcn_permlane32_swap(u, u, false, false);
    const unsigned t = __float_as_uint(__uint_as_float(r[0]) + __uint_as_float(r[1]));
    const auto p = __builtin_amdgcn_permlane16_swap(t, t, false, false);
    return __uint_as_float(p[0]) + __uint_as_float(p[1]);
}
DEV float2 sel4(int q, float2 a, float2 b, float2 c, float2 d) {
    PIN(d.x); PIN(d.y);
    const float2 e = (q & 1) ? b : a, f = (q & 1) ? d : c;
    return (q & 2) ? f : e;
}
template <int R> DEV unsigned ror16(unsigned v) { return __builtin_amdgcn_update_dpp(0u, v, 0x120 + R, 0xf, 0xf, false); }
DEV unsigned wave_pkmax(unsigned m) {
    m = pkmax(m, ror16<8>(m)); m = pkmax(m, ror16<4>(m)); m = pkmax(m, ror16<2>(m)); m = pkmax(m, ror16<1>(m));
    const auto r = __builtin_amdgcn_permlane32_swap(m, m, false, false);
    m = pkmax(r[0], r[1]);
    const auto p = __builtin_amdgcn_permlane16_swap(m, m, false, false);
    return pkmax(p[0], p[1]);
}
DEV float half_sum(float v) {
    v += __uint_as_float(ror16<8>(__float_as_uint(v))); v += __uint_as_float(ror16<4>(__float_as_uint(v)));
    v += __uint_as_float(ror16<2>(__float_as_uint(v))); v += __uint_as_float(ror16<1>(__float_as_uint(v)));
    const auto p = __builtin_amdgcn_permlane16_swap(__float_as_uint(v), __float_as_uint(v), false, false);
    return __uint_as_float(p[0]) + __uint_as_float(p[1]);
}
DEV u32x4 quads(lptr buf, int lane, u32x2 p, u32x2 n) {
    lptr wp = buf + (lane & 15) * 64 + ((lane >> 4) & 1) * 8;
    const int v = (lane >> 5) ^ ((lane >> 2) & 3);
    lst<u32x2>(wp + (v << 4), p); lst<u32x2>(wp + ((v ^ 2) << 4), n);
    asm volatile("" ::: "memory");
    return lld<u32x4>(buf + ((lane ^ (lane >> 4)) << 4));
}

#define PRIOA 2
#define NREP 8
#define PRIOM 1
#define PRIOT 2
#define PRIOG 2
#define FLO 0
#define LAG 0
#define AD 6
#define BD 7
#define WROW {0, 0, 0, 0, 6, 11, 16}
#define ROT 0
#define HAUX SC1
#define PRIOL 3
#define XM 0
#define C0S 15
#define C1S 15
#define UFS 9
#define PFS 99
#define PRIOR 0
#define WXS 13
#define CPF 1
#define TDL 0
#define CDL 0
#define FST 8
#define HREP 8
#define NP1 1
#define HFIRST 8u
#define RDPAT {0, 1, 2, -1, 3, 4, -1, 5, 6, -1, 7, -1, -1, -1, -1, -1}
#define Z4 ((f32x4){0.f, 0.f, 0.f, 0.f})
constexpr unsigned SG = 0x80008000u;
struct CM { u32x4 re, im; };
DEV u32x4 cmat1(float t0, float dt) {
    float cc[4], ss[4];
    t0 = __builtin_amdgcn_fractf(t0);
#pragma unroll
    for (int e = 0; e < 4; ++e) { const float f = fmaf((float)e, dt, t0); cc[e] = __builtin_amdgcn_cosf(f); ss[e] = __builtin_amdgcn_sinf(f); }
    return (u32x4){pkh(ss[0], ss[1]), pkh(ss[2], ss[3]), pkh(cc[0], cc[1]), pkh(cc[2], cc[3])};
}
DEV CM cexp(u32x4 v) { CM m; m.re = (u32x4){v.z, v.w, v.x ^ SG, v.y ^ SG}; m.im = v; PIN(m.re); PIN(m.im); return m; }
DEV u32x4 pk8(f32x4 r, f32x4 i) { return (u32x4){pkh(r[0], r[1]), pkh(r[2], r[3]), pkh(i[0], i[1]), pkh(i[2], i[3])}; }
DEV u32x4 wmul(f32x4 r, f32x4 i, u32x4 w) {
    float yr[4], yi[4];
#pragma unroll
    for (int e = 0; e < 4; ++e) {
        const unsigned wu = w[e];
        const f16x2 h = __builtin_bit_cast(f16x2, wu);
        yr[e] = fmaf(r[e], (float)h[0], -i[e] * (float)h[1]); yi[e] = fmaf(r[e], (float)h[1], i[e] * (float)h[0]);
    }
    return (u32x4){pkh(yr[0], yr[1]), pkh(yr[2], yr[3]), pkh(yi[0], yi[1]), pkh(yi[2], yi[3])};
}
DEV u32x4 wcvt(u32x4 w, float sr, float si) {
#pragma unroll
    for (int e = 0; e < 4; ++e) w[e] = pkh(lo2f(w[e]) * sr, hi2f(w[e]) * si);
    PIN(w);
    return w;
}
DEV u32x4 mkdesc(const void* p, unsigned n) { const u64 a = (u64)p; return (u32x4){(unsigned)a, (unsigned)(a >> 32) & 0xffffu, n, 0x00020000u}; }
DEV void dma16(u32x4 d, unsigned l, unsigned vo, unsigned so) {
    asm volatile("s_nop 0\n\tbuffer_load_dwordx4 %0, %1, %2 offen lds" :: "v"(vo), "s"(d), "s"(so), "{m0}"(l) : "memory");
}
DEV void sig(unsigned a, unsigned v) { asm volatile("s_mov_b64 exec, 1\n\tds_add_u32 %0, %1\n\ts_mov_b64 exec, -1" :: "v"(a), "v"(v) : "memory"); }
DEV unsigned sig_rtn(unsigned a) {
    unsigned v;
    asm volatile("s_mov_b64 exec, 1\n\tds_add_rtn_u32 %0, %1, %2\n\ts_mov_b64 exec, -1\n\ts_waitcnt lgkmcnt(0)" : "=&v"(v) : "v"(a), "v"(1u) : "memory");
    return __builtin_amdgcn_readfirstlane(v);
}
template <int SLP> DEV unsigned await(unsigned a, unsigned mask) {
    unsigned v, n, r, t;
    asm volatile("s_mov_b32 %1, 0x400000\n"
                 "1:\tds_read_b32 %0, %4\n\ts_waitcnt lgkmcnt(0)\n\tv_readfirstlane_b32 %2, %0\n\ts_and_b32 %3, %2, %5\n\ts_cbranch_scc1 2f\n\t"
                 "s_sleep %6\n\ts_sub_u32 %1, %1, 1\n\ts_cbranch_scc0 1b\n"
                 "2:" : "=&v"(v), "=&s"(n), "=&s"(r), "=&s"(t) : "v"(a), "s"(mask), "i"(SLP) : "memory", "scc");
    return r;
}
DEV void flag_put(rsrc_t rs, unsigned off, int gb, u32x2 v, int lane) {
    if (lane < NREP) __builtin_amdgcn_raw_buffer_store_b64(v, rs, lane * 2048, off + gb * 8, SC1);
}
DEV void flag_get(rsrc_t rs, unsigned off, int gb, int lane, u32x4 (&fl)[2]) {
    unsigned o = lane * 16; PIN(o);
#pragma unroll
    for (int i = 0; i < 2; ++i) fl[i] = __builtin_amdgcn_raw_buffer_load_b128(rs, o + i * 1024, off + (gb & (NREP - 1)) * 2048, SC1);
}
DEV bool flag_ok(const u32x4 (&fl)[2], u32x2 tag) {
    bool ok = true;
#pragma unroll
    for (int i = 0; i < 2; ++i) ok = ok & ((fl[i].x ^ tag.x) < 0x10000u) & (fl[i].y == tag.y) & ((fl[i].z ^ tag.x) < 0x10000u) & (fl[i].w == tag.y);
    return __all(ok);
}
template <int N> DEV unsigned h_poll(rsrc_t rs, int gb, int lane, u32x2 tag) {
    unsigned o = lane * FST, m = 0; PIN(o);
    u32x2 fl[N];
#pragma unroll
    for (int i = 0; i < N; ++i) fl[i] = __builtin_amdgcn_raw_buffer_load_b64(rs, o, OFF_FH + (gb & (HREP - 1)) * 256 * FST + i * 64 * FST, SC1);
#pragma unroll
    for (int i = 0; i < N; ++i) {
        const u64 b = __ballot(((fl[i].x ^ tag.x) < 0x10000u) & (fl[i].y == tag.y));
        m |= ((unsigned)b == ~0u ? 8u << (8 * i) : 0u) | ((unsigned)(b >> 32) == ~0u ? 128u << (8 * i) : 0u);
    }
    return m;
}
DEV void sig_or(unsigned a, unsigned v) { asm volatile("s_mov_b64 exec, 1\n\tds_or_b32 %0, %1\n\ts_mov_b64 exec, -1" :: "v"(a), "v"(v) : "memory"); }

constexpr int A_STAGE = 32768, A_XT = 4 * A_STAGE, A_PRM = A_XT + 16384, A_AGG = A_PRM + 2048, A_FLG = A_AGG + 512, A_GW = A_FLG + 128,
              A_MAT = A_GW + 2048, SMEM = A_MAT + 11264;
constexpr int E1P = 1040, A_EX = A_GW, A_W = 2 * A_STAGE, A_E1 = A_W + 19456, A_HS = 3 * A_STAGE + 16384, A_R0 = 0;
constexpr int F_MID = 8, F_EM = 12, F_MAX = 16, F_TAG = 32, F_FULL = 40, F_FREE = 44, F_HC = 48, F_HR = 52, F_TC = 56, F_TR = 60, F_TT = 64, F_R0 = 68, F_WM = 80;

constexpr int XG = 0xFF;
DEV void slot_of(int gb, int& mp, int& j1) { gb ^= XG; mp = (gb >> 5) * 8 + (gb & 7); j1 = (gb >> 3) & 3; }
DEV u32x4 mat_sf(int k1, int c, int q) { const float dt = -(float)(k1 + 128 * c) * (1.0f / 2048.0f); return cmat1(dt * (float)(4 * q), dt); }
DEV u32x4 mat_si(int k1, int c, int q) { return cmat1((float)((k1 + 512 * q) * c) * (1.0f / 2048.0f), (float)c * (1.0f / 16.0f)); }

DEV void mk_tables(lptr smem, int gb, int wid, int lane) {
    int mp, j1; slot_of(gb, mp, j1);
    const int c = lane & 15, q = lane >> 4;
    const bool spc = mp == 0 && !(j1 & 1);
    const int k1 = mp ? mp : (j1 == 3 ? 64 : 0);
    lptr mt = smem + A_MAT + lane * 16;
    if (wid == 0) {
        const float dt = -(float)(j1 + 4 * (spc ? c & 7 : c)) * (1.0f / 64.0f), t0 = __builtin_amdgcn_fractf(dt * (float)(4 * q));
        float cc[4], ss[4];
#pragma unroll
        for (int e = 0; e < 4; ++e) { const float f = fmaf((float)e, dt, t0); cc[e] = __builtin_amdgcn_cosf(f); ss[e] = __builtin_amdgcn_sinf(f); }
        const unsigned c0 = pkb(cc[0], cc[1]), c1 = pkb(cc[2], cc[3]), s0 = pkb(ss[0], ss[1]), s1 = pkb(ss[2], ss[3]);
        const unsigned c0l = pkb(cc[0] - lo2f(c0), cc[1] - hi2f(c0)), c1l = pkb(cc[2] - lo2f(c1), cc[3] - hi2f(c1));
        const unsigned s0l = pkb(ss[0] - lo2f(s0), ss[1] - hi2f(s0)), s1l = pkb(ss[2] - lo2f(s1), ss[3] - hi2f(s1));
        u32x4 m[4] = {(u32x4){c0, c1, s0 ^ SG, s1 ^ SG}, (u32x4){s0, s1, c0, c1}, (u32x4){c0l, c1l, s0l ^ SG, s1l ^ SG}, (u32x4){s0l, s1l, c0l, c1l}};
        if (spc) {
            m[0] = c < 8 ? (u32x4){c0, c1, 0, 0} : (u32x4){0, 0, c0, c1}; m[1] = c < 8 ? (u32x4){s0, s1, 0, 0} : (u32x4){0, 0, s0, s1};
            m[2] = c < 8 ? (u32x4){c0l, c1l, 0, 0} : (u32x4){0, 0, c0l, c1l}; m[3] = c < 8 ? (u32x4){s0l, s1l, 0, 0} : (u32x4){0, 0, s0l, s1l};
        }
#pragma unroll
        for (int i = 0; i < 4; ++i) lst<u32x4>(mt + i * 1024, m[i]);
    } else if (wid == 1) {
        const int jq = spc ? (4 * q) & 7 : 4 * q;
        float cc[4], ss[4];
        const float t0 = __builtin_amdgcn_fractf((float)(c * (j1 + 4 * jq)) * (1.0f / 64.0f));
#pragma unroll
        for (int e = 0; e < 4; ++e) {
            const float f = fmaf((float)c, (float)e * (1.0f / 16.0f), t0);
            const float wg = !spc || (gb == XG && jq + e == 0) ? 0.125f : 0.25f;
            cc[e] = __builtin_amdgcn_cosf(f) * wg; ss[e] = -__builtin_amdgcn_sinf(f) * wg;
        }
        const unsigned c0 = pkh(cc[0], cc[1]), c1 = pkh(cc[2], cc[3]), n0 = pkh(ss[0], ss[1]), n1 = pkh(ss[2], ss[3]);
        u32x4 mia = (u32x4){c0, n0, c1, n1}, mib = (u32x4){n0, c0 ^ SG, n1, c1 ^ SG};
        if (spc) { mib = q < 2 ? (u32x4){0, 0, 0, 0} : mia ^ SG; if (q >= 2) mia = (u32x4){0, 0, 0, 0}; }
        lst<u32x4>(mt + 6 * 1024, mia); lst<u32x4>(mt + 7 * 1024, mib);
        lst<u32x4>(mt + 8 * 1024, cmat1((float)(c * q) * -0.25f, (float)c * (-1.0f / 16.0f)));
    } else {
        lst<u32x4>(mt + (2 + wid) * 1024, wid == 2 ? mat_sf(k1, c, q) : mat_si(k1, c, q));
        const int ja = (spc ? 0 : 8 * (wid - 2)) + (lane >> 3);
        const float dt = -(float)(j1 + 4 * ja) * (1.0f / 1024.0f), t0 = dt * (float)(2 * (lane & 7)), t1 = t0 + dt;
        lst<f32x4>(mt + (7 + wid) * 1024, (f32x4){__builtin_amdgcn_cosf(t0), __builtin_amdgcn_sinf(t0), __builtin_amdgcn_cosf(t1), __builtin_amdgcn_sinf(t1)});
    }
}

DEV void role_mma(char* ws, float* __restrict__ out, lptr smem, int gb, int wid, int lane, u64 pc, int len) {
    int lt, pt; tile_of(gb, lt, pt);
    const int wr = wid >> 1, wc = wid & 1, c = lane & 15, q = lane >> 4;
    const rsrc_t rs = mkrsrc(ws, WS_END);
    const unsigned fb = ldsa(smem + A_FLG);
    TSD(); TS(0);
    u64 tag = __hip_atomic_load((GAS u64*)(ws + OFF_E), __ATOMIC_RELAXED, __HIP_MEMORY_SCOPE_WORKGROUP) + 0x9E3779B97F4A7C15ull;
    unsigned cpf = 0;
    if (CPF == 1) { if (wid == 3) cpf = __builtin_amdgcn_raw_buffer_load_b32(mkrsrc((const void*)pc, max(len, 0)), lane * 128, ((gb >> 3) % 6) * 8192, 0); }
    if (CPF == 2) cpf = __builtin_amdgcn_raw_buffer_load_b32(mkrsrc((const void*)pc, max(len, 0)), (lane & 15) * 128 + wid * 2048, ((gb >> 3) % 6) * 8192, 0);
    if (CPF == 3) { if (lane < 3) cpf = __builtin_amdgcn_raw_buffer_load_b32(mkrsrc((const void*)pc, max(len, 0)), lane * 128 + wid * 384, (gb >> 3) * 1536, 0); }
    SCHED();
    if (wid == 0 && lane < 32) lst<unsigned>(smem + A_FLG + lane * 4, 0u);
    mk_tables(smem, gb, wid, lane);
    f32x4 acc[4][4];
#pragma unroll
    for (int m = 0; m < 4; ++m)
#pragma unroll
        for (int n = 0; n < 4; ++n) acc[m][n] = Z4;
    u32x4 af[2][4], bf_[2][4];
#pragma unroll
    for (int m = 0; m < 4; ++m) { af[1][m] = (u32x4){0, 0, 0, 0}; bf_[1][m] = af[1][m]; }
    PIN(tag); PIN(cpf);
    if (wid == 0 && lane == 0) lst<u64>(smem + A_FLG + F_TAG, tag);
    tag = (u64)(unsigned)__builtin_amdgcn_readfirstlane((unsigned)tag) | (u64)(unsigned)__builtin_amdgcn_readfirstlane((unsigned)(tag >> 32)) << 32;
    __builtin_amdgcn_s_waitcnt(0xC07F);
    TS(13);
#pragma unroll 4
    for (int kt = 0; kt < 16; ++kt) {
        BAR();
        lptr tA = smem + (kt & 3) * A_STAGE;
        lptr tB = tA + 16384;
#pragma unroll
        for (int hf = 0; hf < 2; ++hf)
#pragma unroll
            for (int i = 0; i < 16; ++i) {
                acc[i >> 2][i & 3] = mfb(af[1 - hf][i >> 2], bf_[1 - hf][i & 3], acc[i >> 2][i & 3]);
                constexpr int RD[16] = RDPAT;
                const int r = RD[i];
                if (r == 0) af[hf][0] = ldfrag(tA, wr * 64 + c, 4 * hf + q);
                else if (r > 0 && r < 5) bf_[hf][r - 1] = ldfrag(tB, wc * 64 + (r - 1) * 16 + c, 4 * hf + q);
                else if (r >= 5) af[hf][r - 4] = ldfrag(tA, wr * 64 + (r - 4) * 16 + c, 4 * hf + q);
                SCHED();
            }
        LDS_WAIT();
    }
    __builtin_amdgcn_s_setprio(PRIOT);
    sig(fb + F_FREE, 1u << 20);
    float2 lam[2], coef[2], l16[2], l128[2];
#pragma unroll
    for (int n = 0; n < 2; ++n) {
        const f32x4 a = lld<f32x4>(smem + A_PRM + (wc * 32 + 2 * c + n) * 32), b = lld<f32x4>(smem + A_PRM + (wc * 32 + 2 * c + n) * 32 + 16);
        lam[n] = make_float2(a[0], a[1]); coef[n] = make_float2(a[2], a[3]); l16[n] = make_float2(b[0], b[1]); l128[n] = make_float2(b[2], b[3]);
    }
#pragma unroll
    for (int m = 0; m < 4; ++m)
#pragma unroll
        for (int n = 0; n < 4; ++n) acc[m][n] = mfb(af[1][m], bf_[1][n], acc[m][n]);
    float2 l32[2], l64[2];
#pragma unroll
    for (int n = 0; n < 2; ++n) { l32[n] = cmul(l16[n], l16[n]); l64[n] = cmul(l32[n], l32[n]); }
    TS(1);
    float2 h[2][16];
#pragma unroll
    for (int n = 0; n < 2; ++n) h[n][0] = make_float2(acc[0][n][0], acc[0][2 + n][0]);
#pragma unroll
    for (int t = 1; t < 16; ++t)
#pragma unroll
        for (int n = 0; n < 2; ++n) h[n][t] = cfma(lam[n], h[n][t - 1], make_float2(acc[t >> 2][n][t & 3], acc[t >> 2][2 + n][t & 3]));
    float2 loc[2], wag[2], wq[2];
#pragma unroll
    for (int n = 0; n < 2; ++n) {
        float2 a0, a1, a2, a3;
        rows4(h[n][15].x, a0.x, a1.x, a2.x, a3.x);
        rows4(h[n][15].y, a0.y, a1.y, a2.y, a3.y);
        const float2 s2 = cfma(l16[n], a0, a1), s3 = cfma(l16[n], s2, a2);
        wag[n] = cfma(l16[n], s3, a3);
        loc[n] = sel4(q, make_float2(0.f, 0.f), a0, s2, s3);
    }
    lptr aggp = smem + A_AGG + (wc * 16 + c) * 16;
    const unsigned flg = ldsa(smem + A_FLG) + wc * 4;
    float2 W0[2] = {make_float2(0.f, 0.f), make_float2(0.f, 0.f)};
    if (wr == 0) {
        if (q == 0) lst<f32x4>(aggp, (f32x4){wag[0].x, wag[0].y, wag[1].x, wag[1].y});
        asm volatile("" ::: "memory");
        if (lane == 0) lds_add(flg);
    } else {
#pragma unroll 1
        for (int spin = 0; spin < (1 << 20); ++spin) { if (lds_peek(flg)) break; }
        const f32x4 w0 = lld<f32x4>(aggp);
        W0[0] = make_float2(w0[0], w0[1]); W0[1] = make_float2(w0[2], w0[3]);
        const float2 g0 = cfma(l64[0], W0[0], wag[0]), g1 = cfma(l64[1], W0[1], wag[1]);
        if (lt < 15 && q < 2) {
            const float2 gg = q == 0 ? g0 : g1;
            st128<SC1>((u32x4){__float_as_uint(gg.x), (unsigned)tag, __float_as_uint(gg.y), (unsigned)(tag >> 32)},
                       rs, (2 * c + q) * 16 + OFF_GRAN + ((15 - lt) * 1024 + pt * 64 + wc * 32) * 16);
        }
    }
    TS(2);
#pragma unroll
    for (int n = 0; n < 2; ++n) { PIN(lam[n].x); PIN(lam[n].y); PIN(coef[n].x); PIN(coef[n].y); PIN(l16[n].x); PIN(l16[n].y); PIN(l128[n].x); PIN(l128[n].y); }
    float2 pw[2][8], l8[2], lq[2], l512[2];
#pragma unroll
    for (int n = 0; n < 2; ++n) {
        wq[n] = sel4(q, make_float2(1.f, 0.f), l16[n], l32[n], cmul(l32[n], l16[n]));
        loc[n] = cfma(wq[n], W0[n], loc[n]);
        if (wr) wq[n] = cmul(wq[n], l64[n]);
        pw[n][0] = cmul(coef[n], lam[n]);
        l8[n] = cmul(lam[n], lam[n]); l8[n] = cmul(l8[n], l8[n]); l8[n] = cmul(l8[n], l8[n]);
        const float2 l256 = cmul(l128[n], l128[n]);
        lq[n] = sel4(q, make_float2(1.f, 0.f), l128[n], l256, cmul(l256, l128[n]));
        l512[n] = cmul(l256, l256);
    }
#pragma unroll
    for (int n = 0; n < 2; ++n) {
        PIN(wq[n].x); PIN(wq[n].y); PIN(loc[n].x); PIN(loc[n].y); PIN(l8[n].x); PIN(l8[n].y); PIN(lq[n].x); PIN(lq[n].y); PIN(l512[n].x); PIN(l512[n].y);
    }
#pragma unroll
    for (int t = 0; t < 8; ++t) {
#pragma unroll
        for (int n = 0; n < 2; ++n) {
            if (t) pw[n][t] = cmul(pw[n][t - 1], lam[n]);
            h[n][2 * t] = cmul(coef[n], h[n][2 * t]); h[n][2 * t + 1] = cmul(coef[n], h[n][2 * t + 1]);
        }
#pragma unroll
        for (int n = 0; n < 2; ++n) { PIN(pw[n][t].x); PIN(pw[n][t].y); PIN(h[n][2 * t].x); PIN(h[n][2 * t].y); PIN(h[n][2 * t + 1].x); PIN(h[n][2 * t + 1].y); }
        SCHED();
    }
    await<0>(fb + F_FREE, 8u << 20);
    TS(3);
    if (lt > 0) {
        u32x4 gr[4][2];
        unsigned off = ((16 - lt + q) * 1024 + pt * 64 + wc * 32 + 2 * c) * 16;
#pragma unroll 1
        for (int spin = 0; spin < (1 << 20); ++spin) {
            bool ok = true;
            PIN(off);
#pragma unroll
            for (int j = 0; j < 4; ++j) {
                const rsrc_t rg = mkrsrc(ws + OFF_GRAN + j * 65536, (4 - j) * 65536);
                gr[j][0] = __builtin_amdgcn_raw_buffer_load_b128(rg, off, 0, SC1);
                gr[j][1] = __builtin_amdgcn_raw_buffer_load_b128(rg, off + 16, 0, SC1);
            }
#pragma unroll
            for (int j = 0; j < 4; ++j) {
                const bool v = lt - 1 - (4 * j + q) >= 0;
#pragma unroll
                for (int n = 0; n < 2; ++n) ok = ok & (!v | ((gr[j][n].y == (unsigned)tag) & (gr[j][n].w == (unsigned)(tag >> 32))));
            }
            if (__all(ok)) break;
            asm volatile("" ::: "memory");
        }
#pragma unroll
        for (int n = 0; n < 2; ++n) {
            float2 cs = make_float2(__uint_as_float(gr[3][n].x), __uint_as_float(gr[3][n].z));
#pragma unroll
            for (int j = 2; j >= 0; --j) cs = cfma(l512[n], cs, make_float2(__uint_as_float(gr[j][n].x), __uint_as_float(gr[j][n].z)));
            cs = cmul(cs, lq[n]);
            cs.x = rowsum4(cs.x); cs.y = rowsum4(cs.y);
            loc[n] = cfma(wq[n], cs, loc[n]);
        }
    }
    TS(4);
    lptr tb = smem + A_HS + wid * 4096;
    PIN(lane);
#pragma unroll
    for (int hf = 0; hf < 2; ++hf) {
        const float2 s0 = hf ? cmul(l8[0], loc[0]) : loc[0], s1 = hf ? cmul(l8[1], loc[1]) : loc[1];
#pragma unroll
        for (int t = 0; t < 8; ++t) {
            const float2 v0 = cfma(pw[0][t], s0, h[0][8 * hf + t]), v1 = cfma(pw[1][t], s1, h[1][8 * hf + t]);
            lst<unsigned>(tb + t * 256 + lane * 4, pkb(v0.x, v1.x));
            lst<unsigned>(tb + 2048 + t * 256 + lane * 4, pkb(-v0.y, -v1.y));
        }
        asm volatile("" ::: "memory");
        lptr rb = tb + (lane >> 4) * 256 + (lane & 3) * 64 + ((lane >> 2) & 3) * 16;
        const unsigned hoff = OFF_H + ((lane >> 4) * 64 + pt * 2 + wc) * 8192 + lt * 512 + wr * 256 + (lane & 15) * 16;
#pragma unroll
        for (int v = 0; v < 2; ++v)
#pragma unroll
            for (int pl = 0; pl < 2; ++pl) st128<SC1>(lld<u32x4>(rb + pl * 2048 + v * 1024), rs, hoff + ((2 * hf + v) * 256 + pl * 32) * 8192);
        asm volatile("" ::: "memory");
    }
    TS(5);
    wait_vm<0>();
    TS(6);
    const bool pub = sig_rtn(fb + F_HC) == 3u;
    if (pub) {
        const u32x2 tg = {(unsigned)tag, (unsigned)(tag >> 32)};
        if (lane < HREP) __builtin_amdgcn_raw_buffer_store_b64(tg, rs, lane * 256 * FST, OFF_FH + (pt * 16 + lt) * FST, SC1);
        TS(15);
        int spin; unsigned m = 0;
        if (PDL) __builtin_amdgcn_s_sleep(PDL);
        if (PD0 && lt == 0) __builtin_amdgcn_s_sleep(PD0);
        TS(19);
#pragma unroll 1
        for (spin = 0; spin < (1 << 20); ++spin) {
            m = h_poll<NP1>(rs, gb, lane, tg);
            if (spin == 0) TS(16);
            if (spin == 1) TS(17);
            if (spin == 2) TS(18);
            if ((m & HFIRST) == HFIRST) break;
        }
        sig_or(fb + F_FULL, m);
    } else await<1>(fb + F_FULL, 8u);
    __builtin_amdgcn_s_setprio(PRIOG);
    TS(7);
    PIN(lane);
    const unsigned vo = ((lane & 31) >> 2) * 256 + (lane >> 5) * 64 + (lane & 3) * 16, sH = OFF_H + lt * 524288 + wid * 2048;
    auto ldB = [&](int i, int n) { return __builtin_amdgcn_raw_buffer_load_b128(rs, vo + n * 128, sH + ((i >> 3) * 4 + (i & 3) + (i & 4) * 8) * 8192, HAUX); };
    lptr ab[4];
#pragma unroll
    for (int i = 0; i < 4; ++i) { ab[i] = smem + (lane & 31) * 128 + (((2 * i + (lane >> 5)) ^ (lane & 7)) << 4); PIN(ab[i]); }
    auto ldA = [&](int f) {
        const int a = (f >> 1) & 3;
        if ((f & 63) < 8 && f > 7 && !(f & 1)) { ab[a] += (f & 64) ? 2 * A_STAGE : -2 * A_STAGE; PIN(ab[a]); }
        return lld<u32x4>(ab[a] + ((f >> 3) & 7) * 8192 + (f & 1) * 4096);
    };
    u32x4 A[AD], B[BD][2];
#pragma unroll
    for (int i = 0; i < BD; ++i) { B[i][0] = ldB(i, 0); B[i][1] = ldB(i, 1); }
    SCHED();
    unsigned fv = fb; PIN(fv);
    await<0>(fv + F_FULL, 4u);
#pragma unroll
    for (int f = 0; f < AD; ++f) A[f] = ldA(f);
    f32x16 ac[2];
    unsigned fe = 0, fs = 0;
#pragma unroll
    for (int e = 0; e < 16; ++e) { ac[0][e] = 0.f; ac[1][e] = 0.f; }
    SCHED();
    sfor<0, 64>([&](auto ic) INL {
        constexpr int i = decltype(ic)::v;
#pragma unroll
        for (int k = 0; k < 4; ++k) {
            const int f = 4 * i + k;
            ac[k & 1] = mf32(A[f % AD], B[i % BD][k >> 1], ac[k & 1]);
            SCHED();
            if (k == 0 && ((i + BD + 1) & 7) == 0 && i + BD + 1 < 64) fe = *(volatile LAS unsigned*)(smem + A_FLG + F_FULL);
            if (k == 1 && ((i + BD) & 7) == 0 && i + BD < 64) {
                fs = __builtin_amdgcn_readfirstlane(fe);
                if (!(fs & (8u << (4 * (((i + BD) >> 3) & 7))))) fs = await<0>(fv + F_FULL, 8u << (4 * (((i + BD) >> 3) & 7)));
                asm volatile("" ::: "memory");
                TS(20 + ((i + BD) >> 3));
            }
            if ((k & 1) && i + BD < 64) B[i % BD][k >> 1] = ldB(i + BD, k >> 1);
            if (f + AD < 256) {
                if (((f + AD) & 31) == 0) {
                    if (!(fs & (4u << (4 * ((f + AD) >> 5))))) await<0>(fv + F_FULL, 4u << (4 * ((f + AD) >> 5)));
                    asm volatile("" ::: "memory");
                }
                A[f % AD] = ldA(f + AD);
            }
            SCHED();
        }
        if ((i & 7) == 7 && i < 40) sig(fv + F_FREE, 1u << (4 * (i >> 3)));
        if (i == 0) { TS(8); TC(60); }
        if (i == 31) TS(9);
        if (i == 60) { TS(14); TC(61); }
    });
    TS(10);
    await<0>(fv + F_R0, 4u);
    TS(11);
    const rsrc_t ro = mkrsrc(out, S_ * D_ * 4);
    PIN(lane);
    const int qo = ((lane >> 3) & 2) | (lane >> 5);
    float o2[2];
#pragma unroll
    for (int n = 0; n < 2; ++n) o2[n] = lld<float>(smem + A_AGG + (32 * wid + 16 * n + (lane & 15)) * 4);
#pragma unroll
    for (int m = 0; m < 4; ++m) {
        f32x4 a[2];
#pragma unroll
        for (int e = 0; e < 4; ++e) {
            const float xv = ac[m >> 1][8 * (m & 1) + e], yv = ac[m >> 1][8 * (m & 1) + 4 + e];
            const auto p = __builtin_amdgcn_permlane16_swap(__float_as_uint(xv), __float_as_uint(yv), false, false);
            a[0][e] = __uint_as_float(p[0]); a[1][e] = __uint_as_float(p[1]);
        }
#pragma unroll
        for (int n = 0; n < 2; ++n) {
            const int row = 16 * (32 * wid + 16 * n + (lane & 15)) + lt;
            const f32x4 r0 = lld<f32x4>(smem + A_R0 + ((wid * 2 + n) * 4 + m) * 1024 + (qo * 16 + (lane & 15)) * 16);
            const f32x4 o = {fmaf(o2[n], a[n][0], r0[0]), fmaf(o2[n], a[n][1], r0[1]), fmaf(o2[n], a[n][2], r0[2]), fmaf(o2[n], a[n][3], r0[3])};
            st128<WTO>(__builtin_bit_cast(u32x4, o), ro, (row * D_ + 256 * m + 16 * pt + 4 * qo) * 4);
        }
    }
    asm volatile("gm_code_end:");
    TS(12);
    TSE(0, wid == 0);
    TSE(2, pub);
}

#define TSARG
#define TSPASS
struct Gate {
    unsigned xa, wa, xv, gw; float sum; int rot;
    DEV void rd(int kt) { if (XM & 8) return; asm volatile("ds_read_b32 %0, %2\n\tds_read_b32 %1, %3" : "=&v"(xv), "=&v"(gw) : "v"(xa + (kt & 3) * A_STAGE), "v"(wa + ((kt + rot) & 15) * 128) : "memory"); }
    DEV void acc() {
        if (XM & 8) return;
        asm volatile("s_waitcnt lgkmcnt(0)" : "+v"(xv), "+v"(gw) :: "memory");
        const unsigned pr = pkb(lo2f(xv) * lo2f(gw), hi2f(xv) * hi2f(gw));
        sum += lo2f(pr); sum += hi2f(pr); PIN(sum);
    }
};
DEV void c_chunk(const u16* __restrict__ Cre, const u16* __restrict__ Cim, unsigned lb, int pt, int lw, int lane, int j) {
    const u32x4 rc = mkdesc(lw < 2 ? Cre : Cim, D_ * D_ * 2);
    const unsigned vo = (lane >> 3) * 2048 + (((lane & 7) ^ (lane >> 3)) << 4);
#pragma unroll
    for (int i = 0; i < 8; ++i)
        dma16(rc, lb + (j & 3) * A_STAGE + lw * 8192 + i * 1024, vo, (256 * (i >> 1) + 16 * pt + 8 * (i & 1)) * 2048 + j * 256 + (lw & 1) * 128);
}

DEV unsigned u_load(char* ws, lptr smem, int gb, int w, int lane, u32x4 (&ut)[2]) {
    int mp, j1; slot_of(gb, mp, j1);
    __builtin_amdgcn_s_setprio(PRIOM);
    const unsigned em = await<1>(ldsa(smem + A_FLG + F_EM), 0x8000u);
    asm volatile("buffer_load_dwordx4 %0, %2, %3, %4 offen sc1\n\tbuffer_load_dwordx4 %1, %2, %3, %4 offen offset:1024 sc1"
                 : "=&v"(ut[0]), "=&v"(ut[1]) : "v"(lane * 16), "s"(mkdesc(ws, WS_END)), "s"(OFF_U + (mp * 4 + j1) * 16384 + w * 2048) : "memory");
    return em;
}

DEV float role_load(const u16* __restrict__ x, const u16* __restrict__ Bre, const u16* __restrict__ Bim,
                    const u16* __restrict__ gate_w, char* ws, lptr smem, int gb, int lw, int lane TSARG) {
    __builtin_amdgcn_s_waitcnt(0x0F70);
    Gate g;
    int lt, pt; tile_of(gb, lt, pt);
    const int srow = lane >> 3, schunk = (lane & 7) ^ srow;
    const int rot = ROT;
    const u32x4 rx = mkdesc(x, S_ * D_ * 2), rb = mkdesc((lw & 1) ? Bim : Bre, D_ * D_ * 2);
    const unsigned lb = ldsa(smem);
    const unsigned vA = (16 * (srow >> 2) + (srow & 3)) * 2048 + schunk * 16, sA = (lt * 128 + 64 * (lw >> 1) + 8 * (lw & 1)) * 2048;
    const unsigned vB = 2 * srow * 2048 + schunk * 16, sB = (pt * 64 + 32 * (lw >> 1)) * 2048;
    auto stA = [&](int kk) {
#pragma unroll
        for (int i = 0; i < 4; ++i) dma16(rx, lb + (kk & 3) * A_STAGE + lw * 4096 + i * 1024, vA, sA + (32 * (i & 1) + 4 * (i >> 1)) * 2048 + ((kk + rot) & 15) * 128);
    };
    auto stB = [&](int kk) {
#pragma unroll
        for (int i = 0; i < 4; ++i) dma16(rb, lb + (kk & 3) * A_STAGE + 16384 + lw * 4096 + i * 1024, vB, sB + (16 * (i & 1) + (i >> 1)) * 2048 + ((kk + rot) & 15) * 128);
    };
    TS(0);
    __builtin_amdgcn_s_setprio(PRIOL);
    stA(0); stB(0);
    dma16(mkdesc(gate_w, D_ * 2), lb + A_GW + (lw & 1) * 1024, lane * 16, (lw & 1) * 1024);
#pragma unroll
    for (int i = 0; i < 4; ++i) dma16(rx, lb + A_XT + lw * 4096 + i * 1024, (lane >> 1) * 32768 + (lane & 1) * 16, (i * 512 + lt) * 2048 + lw * 512 + pt * 32);
    const int gi = 8 * pt + 2 * lw + (lane >> 5);
    g.xa = lb + gi * 128 + ((((lane & 31) >> 2) ^ (gi & 7)) << 4) + (lane & 3) * 4;
    g.wa = lb + A_GW + (lane & 31) * 4; g.sum = 0.f; g.rot = rot;
    stA(1); stB(1); stA(2); stB(2);
    TS(6);
    constexpr int K0 = 0;
#pragma unroll 1
    for (int kt = K0; kt < 13; ++kt) { wait_vm<16>(); TL(8 + kt); BAR(); TL(28 + kt); if (kt == 0) { TS(28); TC(60); } g.rd(kt); stA(kt + 3); stB(kt + 3); g.acc(); }
    if (lw == 3) {
        const rsrc_t rs = mkrsrc(ws, WS_END);
        u32x4 fl[2];
        u32x2 tag = lld<u32x2>(smem + A_FLG + F_TAG); PIN(tag);
        auto publish = [&]() { sig(lb + A_FLG + F_EM, (wave_pkmax(pkmax(pkmax(fl[0].x, fl[0].z), pkmax(fl[1].x, fl[1].z))) & 0xffu) | 0x8000u); };
        wait_vm<16>(); BAR();
        asm volatile("buffer_load_dwordx4 %0, %2, %3, %4 offen sc1\n\tbuffer_load_dwordx4 %1, %2, %3, %4 offen offset:1024 sc1"
                     : "=&v"(fl[0]), "=&v"(fl[1]) : "v"(lane * 16), "s"(mkdesc(ws, WS_END)), "s"(OFF_FU + (gb & (NREP - 1)) * 2048) : "memory");
        g.rd(13); g.acc();
        wait_vm<10>(); BAR();
        asm volatile("s_waitcnt vmcnt(0)" : "+v"(fl[0]), "+v"(fl[1]) :: "memory");
        const bool ok = flag_ok(fl, tag);
        if (ok) publish();
        g.rd(14); g.acc();
        BAR(); TS(43);
        if (!ok) {
#pragma unroll 1
            for (int spin = 0; spin < (1 << 20); ++spin) { flag_get(rs, OFF_FU, gb, lane, fl); if (flag_ok(fl, tag)) break; }
            publish();
        }
    } else {
        wait_vm<16>(); TL(21); BAR(); TL(41); g.rd(13); g.acc();
        wait_vm<8>(); TL(22); BAR(); TL(42); g.rd(14); g.acc();
        wait_vm<0>(); TL(23); BAR(); TS(43); TC(61);
    }
    g.rd(15); g.acc();
    sig(lb + A_FLG + F_FREE, 1u << 20);
    return g.sum;
}

DEV unsigned ldw(rsrc_t r, unsigned vo, unsigned so) { return __builtin_amdgcn_raw_buffer_load_b32(r, vo, so, 0); }
DEV u32x4 w_pack(u32x4 re, u32x4 im, unsigned sel) {
    PIN(re); PIN(im);
#pragma unroll
    for (int r = 0; r < 4; ++r) re[r] = __builtin_amdgcn_perm(im[r], re[r], sel);
    return re;
}
DEV unsigned amax4(u32x4 p) { return pkmax(pkmax(p.x & 0x7fff7fffu, p.y & 0x7fff7fffu), pkmax(p.z & 0x7fff7fffu, p.w & 0x7fff7fffu)); }

DEV unsigned role_aux(const u16* __restrict__ Cre, const u16* __restrict__ Cim, const u16* __restrict__ w_r, const u16* __restrict__ w_i, const u16* __restrict__ lam_re, const u16* __restrict__ lam_im,
                  const u16* __restrict__ log_dt, const u16* __restrict__ Dskip, char* ws, lptr smem, int gb, int aw, int lane, u32x2 (&xc)[8],
                  u32x4 (&ut)[2] TSARG) {
    int lt, pt; tile_of(gb, lt, pt);
    int mp, j1; slot_of(gb, mp, j1);
    const int c = lane & 15, q = lane >> 4;
    const bool spc = mp == 0 && !(j1 & 1);
    const rsrc_t rs = mkrsrc(ws, WS_END), rr = mkrsrc(w_r, S_ * KF * 2), ri = mkrsrc(w_i, S_ * KF * 2);
    TS(0);
    __builtin_amdgcn_s_setprio(PRIOA);
    u32x4 fch, fcl, fsh, fsl;
    const bool m0 = 16 * aw + c == 0;
    {
        const float th = (float)(16 * aw + c) * (1.0f / 128.0f);
#pragma unroll
        for (int i = 0; i < 4; ++i) {
            const float t0 = __builtin_amdgcn_fractf(th * (float)(16 * (i >> 1) + 2 * (i & 1) + 4 * q)), t1 = t0 + th;
            const float c0 = __builtin_amdgcn_cosf(t0), c1 = __builtin_amdgcn_cosf(t1);
            const float s0 = m0 ? 1.f : -__builtin_amdgcn_sinf(t0), s1 = m0 ? -1.f : -__builtin_amdgcn_sinf(t1);
            const unsigned ch = pkb(c0, c1), sh = pkb(s0, s1);
            fch[i] = ch; fcl[i] = pkb(c0 - lo2f(ch), c1 - hi2f(ch));
            fsh[i] = sh; fsl[i] = pkb(s0 - lo2f(sh), s1 - hi2f(sh));
        }
        PIN(fch); PIN(fsh);
        if (FLO) { PIN(fcl); PIN(fsl); }
    }
    unsigned sg64 = (c & 1) ? SG : 0u, sgA = (c & 2) ? SG : 0u, sgB = ((c + 1) & 2) ? SG : 0u;
    constexpr unsigned WR = 128 * KF * 2;
    const int k1 = mp ? mp : spc ? (c >> 3) * 64 : (j1 == 1 ? 0 : 64);
    const int kd = j1 + 4 * (spc ? c & 7 : c) + 64 * (4 * aw + q);
    unsigned wo = aw < 2 ? (k1 * KF + kd) * 2 : ((128 - k1) * KF + 1023 - kd) * 2;
    unsigned wo0 = aw < 2 ? wo : k1 ? wo + 15 * WR : (1023 - kd) * 2;
    unsigned uoff = (lane >> 2) * 65536 + (lane & 3) * 16 + OFF_U + aw * 64 * 16384 + lt * 1024 + pt * 64;
    PIN(sg64); PIN(sgA); PIN(sgB); PIN(wo); PIN(wo0); PIN(uoff);
    unsigned lru, liu, ldu, um = 0, em, pf = 0;
    u32x2 tag, dk[4];
    u32x4 wre[5], wim[5];
    wre[4] = (u32x4){0, 0, 0, 0}; wim[4] = wre[4];
    if (XM & 1) for (int i = 0; i < 4; ++i) { wre[i] = fch; wim[i] = fsh; }
    f32x4 vr[4], vi[4];
    float dt = 0.f, er = 0.f; float2 lam = make_float2(0.f, 0.f);
#pragma unroll
    for (int d1 = 0; d1 < 4; ++d1) { vr[d1] = Z4; vi[d1] = Z4; }
    auto wx = [&]() INL {
        if (j1 == 0 && (aw == 3 || (gb == XG && aw < 2))) {
            u32x4 o;
            if (aw == 3) {
                const int ks = (spc ? (lane >> 5) * 64 : mp) + 128 * c, ng = (S_ - ks) & (S_ - 1);
                o = (u32x4){(q & 1) ? (ng * KF + 511) * 2u : ks * KF * 2u, (q & 1) ? (ks * KF + 511) * 2u : ng * KF * 2u, S_ * KF * 2u, S_ * KF * 2u};
            } else {
                const int sg = c < 8 ? 1 : -1;
#pragma unroll
                for (int r = 0; r < 4; ++r) o[r] = ((sg * (64 * aw + 128 * (4 * q + r)) & (S_ - 1)) * KF + (sg * (32 + 64 * c) & 1023)) * 2;
            }
            PIN(o);
#pragma unroll
            for (int r = 0; r < 4; ++r) { wre[4][r] = ldw(rr, o[r], 0); wim[4][r] = ldw(ri, o[r], 0); }
        }
    };
    auto pfx = [&]() INL {
        const u16* p = aw == 0 ? Cre : aw == 1 ? Cim : aw == 2 ? w_r : w_i;
        pf = ldw(mkrsrc(p, aw < 2 ? D_ * D_ * 2 : S_ * KF * 2), lane * 128, gb * (aw < 2 ? 8192 : 8208));
    };
    auto prm = [&]() INL {
        lru = ldw(mkrsrc(lam_re, 2048), (lane >> 1) * 4, pt * 128); liu = ldw(mkrsrc(lam_im, 2048), (lane >> 1) * 4, pt * 128);
        ldu = ldw(mkrsrc(log_dt, 2048), (lane >> 1) * 4, pt * 128);
#pragma unroll
        for (int i = 0; i < 4; ++i) dk[i] = __builtin_amdgcn_raw_buffer_load_b64(mkrsrc(Dskip, D_ * 2), q * 8 + i * 512, pt * 32, 0);
    };
    if (PRS < 0) prm();
    if (WXS < 0) wx();
    if (PFS < 0) pfx();
    sfor<0, 16>([&](auto ic) INL {
        constexpr int kt = decltype(ic)::v;
        if (kt == UFS) wait_vm<0>();
        TL(8 + kt);
        BAR();
        if (kt == 15) { em = u_load(ws, smem, gb, aw, lane, ut); TS(2); }
        if (kt == PFS) pfx();
        if (kt == UFS) PIN(pf);
        u32x4 xa[4];
        if (kt < 4) {
            constexpr int KSO[4] = {0, 2, 3, 1};
#pragma unroll
            for (int i = 0; i < 4; ++i) xa[i] = trfrag(smem + A_XT + i * 4096 + KSO[kt & 3] * 1024 + lane * 8, 512);
        }
        if (kt == 4 || kt == 5) {
#pragma unroll
            for (int i = 0; i < 4; ++i) xc[4 * (kt & 1) + i] = lld<u32x2>(smem + A_XT + i * 4096 + (32 * aw + 16 * (kt & 1) + c) * 32 + q * 8);
        }
        if (kt >= 10 && !(XM & 1)) {
            constexpr int WRW[7] = WROW;
#pragma unroll
            for (int r = 0; r < 16; ++r) {
                if (r < WRW[kt < 10 ? 0 : kt - 10] || r >= WRW[kt < 10 ? 0 : kt - 9]) continue;
                const unsigned so = r ? (aw < 2 ? r : 15 - r) * WR : 0u;
                wre[r >> 2][r & 3] = ldw(rr, r ? wo : wo0, so); wim[r >> 2][r & 3] = ldw(ri, r ? wo : wo0, so);
            }
        }
        if (kt == PRS) prm();
        if (kt == WXS) wx();
        SCHED();
        if (kt < 4 && !(XM & 4)) {
#pragma unroll
            for (int i = 0; i < 4; ++i) { vr[i] = mfb(xa[i], fch, vr[i]); vi[i] = mfb(xa[i], fsh, vi[i]); PIN(vr[i]); PIN(vi[i]); }
        }
        if (kt == 0 || kt == 2) { fch ^= sg64; fsh ^= sg64; PIN(fch); PIN(fsh); if (FLO) { fcl ^= sg64; fsl ^= sg64; PIN(fcl); PIN(fsl); } }
        if (kt == 1) {
            const bool od = c & 1;
            const u32x4 a = (od ? fsh : fch) ^ sgA, b = (od ? fch : fsh) ^ sgB, al = (od ? fsl : fcl) ^ sgA, bl = (od ? fcl : fsl) ^ sgB;
            fch = a; fsh = b; PIN(fch); PIN(fsh);
            if (FLO) { fcl = al; fsl = bl; PIN(fcl); PIN(fsl); }
        }
        if (kt == 4) {
            const f32x4 t0r = vr[0] + vr[2], t1r = vr[0] - vr[2], t2r = vr[1] + vr[3], t3r = vr[1] - vr[3];
            const f32x4 t0i = vi[0] + vi[2], t1i = vi[0] - vi[2], t2i = vi[1] + vi[3], t3i = vi[1] - vi[3];
            vr[0] = t0r; vr[1] = t1r; vr[2] = t2r; vr[3] = t3r; vi[0] = t0i; vi[1] = t1i; vi[2] = t2i; vi[3] = t3i;
        }
        if (kt == 5) {
            const f32x4 t0r = vr[0], t1r = vr[1], t2r = vr[2], t3r = vr[3], t0i = vi[0], t1i = vi[1], t2i = vi[2], t3i = vi[3];
            vr[0] = t0r + t2r; vi[0] = t0i + t2i; vr[2] = t0r - t2r; vi[2] = t0i - t2i;
            vr[1] = t1r + t3i; vi[1] = t1i - t3r; vr[3] = t1r - t3i; vi[3] = t1i + t3r;
            if (aw == 0) { if (m0) { vr[1] = t1r; vi[1] = -t3r; vr[3] = t1i; vi[3] = t3i; } }
        }
        if (kt == 4 || kt == 5) {
#pragma unroll
            for (int i = 0; i < 4; ++i) { PIN(vr[i]); PIN(vi[i]); PIN(xc[4 * (kt & 1) + i]); }
        }
        if (kt == 6 || kt == 7) {
#pragma unroll
            for (int j = 2 * (kt & 1); j < 2 * (kt & 1) + 2; ++j) {
                PIN(vr[j]); PIN(vi[j]);
                const u32x4 o = quads(smem + A_XT + aw * 4096 + j * 1024, lane, (u32x2){pkb(vr[j][0], vr[j][1]), pkb(vr[j][2], vr[j][3])},
                                      (u32x2){pkb(vi[j][0], vi[j][1]), pkb(vi[j][2], vi[j][3])});
                if (!(XM & 2)) st128<SC1>(o, rs, uoff + j * 16384);
                um = pkmax(um, amax4(o));
                PIN(um);
            }
        }
        if (kt == 8) {
            um = wave_pkmax(pkmax(um, um >> 16));
            if (lane == 0) lst<unsigned>(smem + A_FLG + F_MAX + aw * 4, um);
        }
        if (kt == 8 || kt == 9) {
#pragma unroll
            for (int i = 4 * (kt & 1); i < 4 * (kt & 1) + 4; ++i) {
                const u32x2 d = dk[i & 3];
                xc[i] = (u32x2){pkb(lo2f(xc[i].x) * lo2f(d.x), hi2f(xc[i].x) * hi2f(d.x)), pkb(lo2f(xc[i].y) * lo2f(d.y), hi2f(xc[i].y) * hi2f(d.y))};
                PIN(xc[i]);
            }
        }
        if (kt == 8) { tag = lld<u32x2>(smem + A_FLG + F_TAG); PIN(tag); }
        if (kt == UFS && aw == 0) {
            const u32x4 m4 = lld<u32x4>(smem + A_FLG + F_MAX);
            const unsigned m = pkmax(pkmax(m4.x, m4.y), pkmax(m4.z, m4.w));
            flag_put(rs, OFF_FU, gb, (u32x2){(tag.x & 0xffff0000u) | ((m & 0xffffu) >> 7), tag.y}, lane);
        }
        if (kt == 9) {
            PIN(lru); PIN(liu); PIN(ldu);
            if (lane & 1) { lru >>= 16; liu >>= 16; ldu >>= 16; }
            PIN(lru); PIN(liu); PIN(ldu);
        }
        if (kt >= 10 && kt < 13 && aw == 1) {
            const float lr = lo2f(lru), li = lo2f(liu);
            if (kt == 10) { dt = bfr(expf(lo2f(ldu))); er = expf(dt * lr); PIN(dt); PIN(er); }
            if (kt == 11) { float sn, cs; sincosf(dt * li, &sn, &cs); lam = make_float2(er * cs, er * sn); PIN(lam.x); PIN(lam.y); }
            if (kt == 12) {
                const float nr = lam.x - 1.0f, ni = lam.y, den = lr * lr + li * li;
                const float2 coef = make_float2((nr * lr + ni * li) / den, (ni * lr - nr * li) / den);
                float2 l16 = lam;
#pragma unroll
                for (int i = 0; i < 4; ++i) l16 = cmul(l16, l16);
                float2 l128 = l16;
#pragma unroll
                for (int i = 0; i < 3; ++i) l128 = cmul(l128, l128);
                lst<f32x4>(smem + A_PRM + lane * 32, (f32x4){lam.x, lam.y, coef.x, coef.y});
                lst<f32x4>(smem + A_PRM + lane * 32 + 16, (f32x4){l16.x, l16.y, l128.x, l128.y});
            }
        }
        LDS_WAIT();
        SCHED();
    });
    unsigned mx = 0;
    const bool skip = j1 == 0 && !(aw & 1) && (lane & (spc ? 55 : 63)) == 0;
#pragma unroll
    for (int i = 0; i < 4; ++i) {
        const u32x4 p = w_pack(wre[i], wim[i], aw < 2 ? 0x05040100u : 0x07060302u);
        if (!skip) lst<u32x4>(smem + A_W + c * 1024 + i * 256 + (((4 * aw + q) ^ c) << 4), p);
        mx = pkmax(mx, amax4(p));
    }
    if (j1 == 0) {
        const u32x4 p = w_pack(wre[4], wim[4], aw == 3 && (q & 1) ? 0x07060302u : 0x05040100u);
        mx = pkmax(mx, amax4(p));
        const int it = spc ? (lane >> 5) * 8 : 0;
        if (aw == 3) lst<unsigned>(smem + A_W + it * 1024 + (c >> 2) * 256 + ((((q & 1) * 8) ^ it) << 4) + (c & 3) * 4,
                                  pkb(0.5f * (lo2f(p.x) + lo2f(p.y)), 0.5f * (hi2f(p.x) - hi2f(p.y))));
        else lst<u32x4>(smem + A_W + 16384 + aw * 1024 + lane * 16, p);
    }
    mx = wave_pkmax(pkmax(mx, mx >> 16));
    if (lane == 0) lst<unsigned>(smem + A_FLG + F_WM + aw * 4, mx);
    wait_vm<0>();
    SCHED();
    return em;
}

DEV int role_mid(const u16* __restrict__ Cre, const u16* __restrict__ Cim, char* ws, lptr smem,
                 int gb, int w, int lane, float gsum, u32x2& tag, u32x4 (&ut)[2], unsigned em TSARG) {
    int lt, pt; tile_of(gb, lt, pt);
    int mp, j1; slot_of(gb, mp, j1);
    const int c = lane & 15, q = lane >> 4;
    const bool spc = mp == 0 && !(j1 & 1), xtr = gb == XG, ld = w >= 4;
    const rsrc_t rs = mkrsrc(ws, WS_END);
    const unsigned lb = ldsa(smem), fb = lb + A_FLG;
    u32x4 wv[2], mt[6];
    f32x4 tf[2]; f32x2 ti[2];
    tag = lld<u32x2>(smem + A_FLG + F_TAG);
#pragma unroll
    for (int i = 0; i < 2; ++i) {
        tf[i] = lld<f32x4>(smem + A_MAT + 9216 + c * 128 + q * 32 + i * 16);
        ti[i] = lld<f32x2>(smem + A_MAT + 9216 + (2 * w + i) * 128 + c * 8);
    }
    lptr mb = smem + A_MAT + lane * 16;
#pragma unroll
    for (int i = 0; i < 6; ++i) mt[i] = lld<u32x4>(mb + (i < 4 ? i : i + 2) * 1024);
    u32x4 sf0 = lld<u32x4>(mb + 4096), si0 = lld<u32x4>(mb + 5120);
    const u32x4 f0 = lld<u32x4>(mb + 8192);
    SCHED();
    if (spc && ld) { sf0 = mat_sf(64, c, q); si0 = mat_si(64, c, q); }
    const CM sf = cexp(sf0), si = cexp(si0), ff = cexp(f0), fi = cexp((u32x4){f0.x ^ SG, f0.y ^ SG, f0.z, f0.w});
#pragma unroll
    for (int i = 0; i < 6; ++i) PIN(mt[i]);
    PIN(ti[0]); PIN(ti[1]); PIN(tag);
    const int eu = min(max((int)(em & 0xffu), 4), 254);
    const float su = __uint_as_float((unsigned)(258 - eu) << 23);
    tf[0] *= su; tf[1] *= su; PIN(tf[0]); PIN(tf[1]);
    u32x2 gv = {0, 0};
    if (ld) {
        const float gg = bfr(1.0f / (1.0f + expf(-bfr(half_sum(gsum)))));
        gv = (u32x2){__float_as_uint(gg), __float_as_uint(bfr(1.0f - gg))};
        PIN(gv);
    }
    await<0>(fb + F_FREE, 8u << 20);
    asm volatile("s_waitcnt vmcnt(%2)" : "+v"(ut[0]), "+v"(ut[1]) : "i"((C0S == 15 ? 8 : 0) + (C1S == 15 ? 8 : 0)) : "memory");
    lptr ua = smem + A_XT + w * 2048;
#pragma unroll
    for (int i = 0; i < 2; ++i) lst<u32x4>(ua + i * 1024 + lane * 16, ut[i]);
    TS(3);
    asm volatile("" ::: "memory");
    {
        lptr ea = smem + A_E1 + c * E1P + q * 256 + 2 * w * 16;
#pragma unroll
        for (int i = 0; i < 2; ++i) ut[i] = trfrag(ua + i * 1024 + (lane >> 2) * 64 + (lane & 3) * 8, 32);
#pragma unroll
        for (int i = 0; i < 2; ++i) {
            f32x4 r = mfb(ut[i], mt[2], Z4), m = mfb(ut[i], mt[3], Z4);
            r = mfb(ut[i], mt[0], r); m = mfb(ut[i], mt[1], m);
            f32x4 yr, yi;
#pragma unroll
            for (int e = 0; e < 4; ++e) {
                const float tc = tf[e >> 1][2 * (e & 1)], ts = tf[e >> 1][2 * (e & 1) + 1];
                yr[e] = fmaf(r[e], tc, -m[e] * ts); yi[e] = fmaf(r[e], ts, m[e] * tc);
            }
            lst<u32x4>(ea + i * 16, pk8(yr, yi));
            if (xtr) {
                const unsigned sg = 0xbf803f80u;
                const f32x4 e = mfb(ut[i], c == 0 ? (u32x4){sg, sg, 0, 0} : (u32x4){0, 0, sg, sg}, Z4);
                if (c < 2) lst<u32x4>(ea + 16 * E1P + i * 16, pk8(e * su, Z4));
            }
        }
    }
    sig(fb + F_MID, 1u);
    if (ld) {
        const int gi = 8 * pt + 2 * (w - 4) + (lane >> 5), tau = (gi & 64) | ((gi & 12) << 2) | ((gi & 48) >> 2) | (gi & 3);
        if ((lane & 31) == 0) __builtin_amdgcn_raw_buffer_store_b64(gv, rs, (tau & 15) * 1024 + (lt * 8 + (tau >> 4)) * 8, OFF_G, SC1);
    }
    if (xtr && w == 0 && lane == 0) __builtin_amdgcn_raw_buffer_store_b64(tag, rs, 0, OFF_E, SC1);
    TS(4);
    await<0>(fb + F_MID, 24u);
    if (ld && C0S == 16) c_chunk(Cre, Cim, lb, pt, w - 4, lane, 0);
    if (ld && C1S == 16) c_chunk(Cre, Cim, lb, pt, w - 4, lane, 1);
    u32x4 dv[2];
#pragma unroll
    for (int i = 0; i < 2; ++i) dv[i] = lld<u32x4>(smem + A_E1 + (2 * w + i) * E1P + lane * 16);
    const u32x4 m4 = lld<u32x4>(smem + A_FLG + F_WM);
#pragma unroll
    for (int i = 0; i < 2; ++i) wv[i] = lld<u32x4>(smem + A_W + (2 * w + i) * 1024 + ((lane ^ (2 * w + i)) << 4));
#pragma unroll
    for (int i = 0; i < 2; ++i) dv[i] = pk8(mfh(dv[i], ff.re, Z4), mfh(dv[i], ff.im, Z4));
    const unsigned ew = min((pkmax(pkmax(m4.x, m4.y), pkmax(m4.z, m4.w)) & 0xffffu) >> 7, 245u);
    const float sw = __uint_as_float((246u - ew) << 23), swi = c < 8 ? sw : -sw;
    wv[0] = wcvt(wv[0], sw, swi); wv[1] = wcvt(wv[1], sw, swi);
#pragma unroll
    for (int i = 0; i < 2; ++i) dv[i] = wmul(mfh(sf.re, dv[i], Z4), mfh(sf.im, dv[i], Z4), wv[i]);
#pragma unroll
    for (int i = 0; i < 2; ++i) dv[i] = pk8(mfh(dv[i], si.re, Z4), mfh(dv[i], si.im, Z4));
    {
        f32x4 r[2], m[2];
#pragma unroll
        for (int i = 0; i < 2; ++i) {
            const f32x4 a = mfh(dv[i], fi.re, Z4), b = mfh(dv[i], fi.im, Z4);
#pragma unroll
            for (int e = 0; e < 4; ++e) { r[i][e] = fmaf(a[e], ti[i][0], b[e] * ti[i][1]); m[i][e] = fmaf(b[e], ti[i][0], -a[e] * ti[i][1]); }
        }
#pragma unroll
        for (int e = 0; e < 4; ++e)
            lst<u32x2>(smem + A_XT + (4 * q + e) * 1024 + (w >> 1) * 256 + c * 16 + (w & 1) * 8, (u32x2){pkh(r[0][e], r[1][e]), pkh(m[0][e], m[1][e])});
    }
    if (xtr && !(w & 3)) {
        const float dt = -(float)(32 + 64 * c) * (1.0f / 1024.0f);
        const CM f = cexp(cmat1(dt * (float)(4 * q), dt)), b = cexp(cmat1((float)(c * (32 + 256 * q)) * (1.0f / 1024.0f), (float)c * (1.0f / 16.0f)));
        u32x4 d = lld<u32x4>(smem + A_E1 + (16 + (w >> 2)) * E1P + lane * 16);
        d = pk8(mfh(d, f.re, Z4), mfh(d, f.im, Z4));
        d = wmul(mfh(sf.re, d, Z4), mfh(sf.im, d, Z4), wcvt(lld<u32x4>(smem + A_W + 16384 + w * 256 + lane * 16), sw, swi));
        d = pk8(mfh(d, si.re, Z4), mfh(d, si.im, Z4));
        const f32x4 r = mfh(d, b.re, Z4);
#pragma unroll
        for (int e = 0; e < 4; ++e) lst<float>(smem + A_EX + ((w >> 2) * 256 + (4 * q + e) * 16 + c) * 4, r[e]);
    }
    TS(5);
    if (ld) { wait_vm<1>(); sig(fb + F_FULL, 0x11u); }
    sig(fb + F_MID, 1u);
    await<0>(fb + F_MID, 16u);
    {
        f32x4 xa[2], xb[2];
#pragma unroll
        for (int i = 0; i < 2; ++i) { xa[i] = Z4; xb[i] = Z4; }
        if (xtr) {
            const float sg = (c & 1) ? -0.125f : 0.125f;
#pragma unroll
            for (int i = 0; i < 2; ++i) {
                xa[i] = lld<f32x4>(smem + A_EX + (2 * w + i) * 64 + q * 16) * sg;
                xb[i] = lld<f32x4>(smem + A_EX + 1024 + (2 * w + i) * 64 + q * 16) * -sg;
            }
        }
#pragma unroll
        for (int i = 0; i < 2; ++i) {
            const int s2 = 2 * w + i;
            const u32x4 ea = lld<u32x4>(smem + A_XT + s2 * 1024 + lane * 16);
            const f32x4 ta = mfh(ea, mt[4], xa[i]), tb = mfh(ea, mt[5], xb[i]);
            u32x4 tq = quads(smem + A_XT + s2 * 1024, lane, (u32x2){pkh(ta[0], ta[1]), pkh(ta[2], ta[3])}, (u32x2){pkh(tb[0], tb[1]), pkh(tb[2], tb[3])});
            PIN(tq);
            if (TDL && i == 0) await<1>(fb + F_FULL, 8u);
            if (!(XM & 32)) st128<SC1>(tq, rs, (lane >> 2) * 16384 + (lane & 3) * 16 + OFF_T + s2 * 262144 + j1 * 4096 + mp * 64);
        }
    }
    TS(7);
    tag.x = (tag.x & 0xffff0000u) | ew;
    return eu - 267;
}

DEV void t_done(rsrc_t rs, unsigned fb, int gb, u32x2 tag, int lane) { if (sig_rtn(fb + F_TC) == 7u) flag_put(rs, OFF_FT, gb ^ XG, tag, lane); }

DEV void role_cload(const u16* __restrict__ Cre, const u16* __restrict__ Cim, char* ws, lptr smem, int gb, int lw, int lane, u32x2 tag TSARG) {
    int lt, pt; tile_of(gb, lt, pt);
    const unsigned lb = ldsa(smem), fb = lb + A_FLG;
    __builtin_amdgcn_s_setprio(3);
    const bool early = CDL ? lds_peek(fb + F_FULL) & 8u : lds_peek(fb + F_HC) & 4u;
    if (!early) {
        wait_vm<0>();
        t_done(mkrsrc(ws, WS_END), fb, gb, tag, lane);
        if (CDL) await<1>(fb + F_FULL, 8u); else await<2>(fb + F_HC, 4u);
    }
    c_chunk(Cre, Cim, lb, pt, lw, lane, 2); c_chunk(Cre, Cim, lb, pt, lw, lane, 3);
    if (early) {
        wait_vm<16>();
        t_done(mkrsrc(ws, WS_END), fb, gb, tag, lane);
    }
    TS(44);
    wait_vm<0>();
    sig(fb + F_FULL, 0x1100u);
    TS(47);
#pragma unroll 1
    for (int j = 4; j < 8; ++j) {
        await<2>(fb + F_FREE, 4u << (4 * (j - 4)));
        if (!(XM & 16)) c_chunk(Cre, Cim, lb, pt, lw, lane, j);
        wait_vm<8>();
        if (j > 4) sig(fb + F_FULL, 1u << (4 * (j - 1)));
        TS(44 + j);
    }
    wait_vm<0>();
    sig(fb + F_FULL, 1u << 28);
    TSE(1, lw == 0);
}

DEV void role_last(char* ws, lptr smem, int gb, int w, int lane, u32x2 tag, int fe, u32x2 (&xc)[8] TSARG) {
    int s2t, dat; tile_of(gb, s2t, dat);
    const int c = lane & 15, q = lane >> 4;
    const rsrc_t rs = mkrsrc(ws, WS_END);
    const unsigned fb = ldsa(smem + A_FLG);
    SCHED();
    u32x4 fc[2][2], fs[2][2];
#pragma unroll
    for (int n = 0; n < 2; ++n) {
        float dt = (float)(32 * w + 16 * n + c) * (1.0f / 128.0f); PIN(dt);
        float cc[8], ss[8];
        const float t0 = __builtin_amdgcn_fractf(dt * (float)(4 * q)), t1 = __builtin_amdgcn_fractf(dt * (float)(16 + 4 * q));
#pragma unroll
        for (int e = 0; e < 8; ++e) { const float f = fmaf((float)(e & 3), dt, e < 4 ? t0 : t1); cc[e] = __builtin_amdgcn_cosf(f); ss[e] = __builtin_amdgcn_sinf(f); }
        fc[n][0] = (u32x4){pkh(cc[0], cc[1]), pkh(cc[2], cc[3]), pkh(cc[4], cc[5]), pkh(cc[6], cc[7])};
        fs[n][0] = (u32x4){pkh(ss[0], ss[1]), pkh(ss[2], ss[3]), pkh(ss[4], ss[5]), pkh(ss[6], ss[7])};
        fc[n][1] = ((c & 1) ? fs[n][0] : fc[n][0]) ^ (((c + 1) & 2) ? SG : 0u);
        fs[n][1] = ((c & 1) ? fc[n][0] : fs[n][0]) ^ ((c & 2) ? SG : 0u);
        PIN(fc[n][0]); PIN(fs[n][0]); PIN(fc[n][1]); PIN(fs[n][1]);
        if (n == 0) { wait_vm<0>(); t_done(rs, fb, gb, tag, lane); TS(44); }
    }
    TS(45);
    __builtin_amdgcn_s_setprio(3);
    if (w == 0) {
        u32x4 fl[2];
        unsigned m = await<2>(fb + F_FULL, 8u) & 0x88888888u;
#pragma unroll 1
        for (int spin = 0; spin < (1 << 20) && m != 0x88888888u; ++spin) {
            m = h_poll<4>(rs, gb, lane, tag);
            sig_or(fb + F_FULL, m);
        }
        TS(50);
#pragma unroll 1
        for (int spin = 0; spin < (1 << 20); ++spin) { flag_get(rs, OFF_FT, gb, lane, fl); if (flag_ok(fl, tag)) break; }
        const unsigned em = wave_pkmax(pkmax(pkmax(fl[0].x, fl[0].z), pkmax(fl[1].x, fl[1].z))) & 0xffu;
#pragma unroll
        for (int i = 0; i < 2; ++i) {
            const float a = ldexpf(1.f, (int)(fl[i].x & 0xffu) - (int)em), b = ldexpf(1.f, (int)(fl[i].z & 0xffu) - (int)em);
            lst<u32x2>(smem + A_MAT + i * 512 + lane * 8, (u32x2){pkh(a, a), pkh(b, b)});
        }
        sig(fb + F_TR, em | 0x8000u);
    }
    fe += await<1>(fb + F_TR, 0x8000u) & 0xffu;
    TS(46);
    u32x4 tv[4]; u32x2 gg[2];
#pragma unroll
    for (int i = 0; i < 4; ++i) tv[i] = __builtin_amdgcn_raw_buffer_load_b128(rs, lane * 16 + i * 1024, OFF_T + (s2t * 16 + dat) * 16384 + w * 4096, SC1);
#pragma unroll
    for (int n = 0; n < 2; ++n) gg[n] = __builtin_amdgcn_raw_buffer_load_b64(rs, c * 8 + n * 128, OFF_G + s2t * 1024 + w * 256, SC1);
    __builtin_amdgcn_s_setprio(PRIOR);
    unsigned ts[4];
#pragma unroll
    for (int i = 0; i < 4; ++i) ts[i] = lld<unsigned>(smem + A_MAT + i * 256 + (lane >> 5) * 128 + w * 32 + ((lane >> 2) & 7) * 4);
#pragma unroll
    for (int i = 0; i < 4; ++i) {
        tv[i] = __builtin_bit_cast(u32x4, __builtin_bit_cast(f16x8, tv[i]) * __builtin_bit_cast(f16x8, (u32x4){ts[i], ts[i], ts[i], ts[i]}));
        lst<u32x4>(smem + A_XT + w * 4096 + ((lane >> 1) & 1) * 2048 + i * 512 + (lane >> 2) * 32 + (lane & 1) * 16, tv[i]);
    }
    TS(47);
    sig(fb + F_TT, 1u);
    await<0>(fb + F_TT, 4u);
    f32x4 P0[2], P1[2], P2[2], P3a[2], P3b[2];
#pragma unroll
    for (int n = 0; n < 2; ++n) { P0[n] = Z4; P1[n] = Z4; P2[n] = Z4; P3a[n] = Z4; P3b[n] = Z4; }
    lptr tt = smem + A_XT + lane * 8;
#pragma unroll
    for (int h = 0; h < 2; ++h) {
        const u32x4 a1 = trfrag(tt + 1 * 4096 + h * 1024, 512), b1 = trfrag(tt + 1 * 4096 + 2048 + h * 1024, 512);
        const u32x4 a3 = trfrag(tt + 3 * 4096 + h * 1024, 512), b3 = trfrag(tt + 3 * 4096 + 2048 + h * 1024, 512);
        const u32x4 a0 = trfrag(tt + h * 1024, 512), b0 = trfrag(tt + 2048 + h * 1024, 512);
        const u32x4 a2 = trfrag(tt + 2 * 4096 + h * 1024, 512), b2 = trfrag(tt + 2 * 4096 + 2048 + h * 1024, 512);
#pragma unroll
        for (int n = 0; n < 2; ++n) {
            u32x4 cw = fc[n][h], sw_ = fs[n][h];
            P1[n] = mfh(a1, cw, P1[n]); P3a[n] = mfh(a1, sw_, P3a[n]); P3b[n] = mfh(b1, cw, P3b[n]); P1[n] = mfh(b1, sw_, P1[n]);
            if (h == 0) cw.x = q == 0 ? (cw.x & 0xffff0000u) | ((c & 1) ? 0xbc00u : 0x3c00u) : cw.x;
            P3a[n] = mfh(b3, cw, P3a[n]); P3b[n] = mfh(a3, sw_, P3b[n]); P1[n] = mfh(a3, cw, P1[n]); P1[n] = mfh(b3, sw_, P1[n]);
            if (h == 0) { cw.x = q == 0 ? (cw.x & 0xffff0000u) | 0x3800u : cw.x; sw_.x = q == 0 ? (sw_.x & 0xffff0000u) | ((c & 1) ? 0x3800u : 0xb800u) : sw_.x; }
            P0[n] = mfh(a0, cw, P0[n]); P2[n] = mfh(a2, cw, P2[n]); P0[n] = mfh(b0, sw_, P0[n]); P2[n] = mfh(b2, sw_, P2[n]);
        }
    }
    f32x4 r0[2][4];
#pragma unroll
    for (int n = 0; n < 2; ++n) {
        const float g = __uint_as_float(gg[n].x), og = __uint_as_float(gg[n].y);
        const f32x4 sp = P0[n] + P2[n], sm = P0[n] - P2[n], p3 = P3a[n] - P3b[n];
        const f32x4 av[4] = {sp + P1[n], sm - p3, sp - P1[n], sm + p3};
#pragma unroll
        for (int m = 0; m < 4; ++m) {
            const unsigned b0 = pkb(ldexpf(av[m][0], fe), ldexpf(av[m][1], fe)), b1 = pkb(ldexpf(av[m][2], fe), ldexpf(av[m][3], fe));
            const unsigned o0 = pkb(g * lo2f(b0), g * hi2f(b0)), o1 = pkb(g * lo2f(b1), g * hi2f(b1));
            const u32x2 p = xc[4 * n + m];
            r0[n][m] = (f32x4){fmaf(og, lo2f(p.x), lo2f(o0)), fmaf(og, hi2f(p.x), hi2f(o0)), fmaf(og, lo2f(p.y), lo2f(o1)), fmaf(og, hi2f(p.y), hi2f(o1))};
            PIN(r0[n][m]);
        }
    }
    TS(48);
    await<2>(fb + F_FREE, 4u << 16);
#pragma unroll
    for (int n = 0; n < 2; ++n) {
#pragma unroll
        for (int m = 0; m < 4; ++m) lst<f32x4>(smem + A_R0 + ((w * 2 + n) * 4 + m) * 1024 + lane * 16, r0[n][m]);
        if (q == 0) lst<float>(smem + A_AGG + (32 * w + 16 * n + c) * 4, 2.0f * __uint_as_float(gg[n].y));
    }
    sig(fb + F_R0, 1u);
    TS(49);
    TSE(w ? 4 : 5, w == 0 || w == 2);
}

NOPK __global__ __launch_bounds__(768, 3) void gm_kernel(const u16* __restrict__ x, const u16* __restrict__ w_r, const u16* __restrict__ w_i,
                                                         const u16* __restrict__ Bre, const u16* __restrict__ Bim, const u16* __restrict__ Cre,
                                                         const u16* __restrict__ Cim, const u16* __restrict__ lam_re,
                                                         const u16* __restrict__ lam_im, const u16* __restrict__ log_dt,
                                                         const u16* __restrict__ Dskip, const u16* __restrict__ gate_w, char* ws, float* __restrict__ out) {
    extern __shared__ __attribute__((aligned(16))) char smem_[];
    lptr smem = (lptr)smem_;
    u64 pc; int len;
    asm volatile("s_getpc_b64 %0\ngm_code_beg:\n\ts_mov_b32 %1, gm_code_end - gm_code_beg" : "=s"(pc), "=s"(len));
    const int wid = __builtin_amdgcn_readfirstlane(threadIdx.x >> 6), lane = threadIdx.x & 63;
    int w = wid & 7, gb = blockIdx.x;
    asm volatile("" : "+s"(w), "+s"(gb));
    if (wid < 4) role_mma(ws, out, smem, blockIdx.x, wid, lane, pc, len);
    else {
        TSD();
        float gsum = 0.f;
        u32x2 xc[8], tag;
        u32x4 ut[2];
        unsigned em;
        if (wid < 8) {
            gsum = role_load(x, Bre, Bim, gate_w, ws, smem, blockIdx.x, wid - 4, lane TSPASS);
            em = u_load(ws, smem, blockIdx.x, wid, lane, ut);
            TS(2);
            int lt, pt; tile_of(blockIdx.x, lt, pt);
            if (C0S == 15) c_chunk(Cre, Cim, ldsa(smem), pt, wid - 4, lane, 0);
            if (C1S == 15) c_chunk(Cre, Cim, ldsa(smem), pt, wid - 4, lane, 1);
#pragma unroll
            for (int i = 0; i < 8; ++i) asm volatile("" : "=v"(xc[i]));
        } else em = role_aux(Cre, Cim, w_r, w_i, lam_re, lam_im, log_dt, Dskip, ws, smem, blockIdx.x, wid - 8, lane, xc, ut TSPASS);
        const int fe = role_mid(Cre, Cim, ws, smem, gb, w, lane, gsum, tag, ut, em TSPASS);
        if (w >= 4) role_cload(Cre, Cim, ws, smem, gb, w - 4, lane, tag TSPASS);
        else role_last(ws, smem, gb, w, lane, tag, fe, xc TSPASS);
    }
}

extern "C" void kernel_launch(void* const* d_in, const int* in_sizes, int n_in, void* d_out, int out_size, void* d_ws, size_t ws_size,
                              hipStream_t stream) {
    (void)hipFuncSetAttribute((const void*)gm_kernel, hipFuncAttributeMaxDynamicSharedMemorySize, SMEM);
    gm_kernel<<<256, 768, SMEM, stream>>>((const u16*)d_in[0], (const u16*)d_in[1], (const u16*)d_in[2], (const u16*)d_in[6], (const u16*)d_in[7],
                                          (const u16*)d_in[8], (const u16*)d_in[9], (const u16*)d_in[4], (const u16*)d_in[5], (const u16*)d_in[10],
                                          (const u16*)d_in[11], (const u16*)d_in[12], (char*)d_ws, (float*)d_out);
}
```

```cpp
#include <hip/hip_runtime.h>
#include <stdint.h>

typedef __attribute__((ext_vector_type(8))) __bf16 bf16x8;
typedef __attribute__((ext_vector_type(2))) __bf16 bf16x2;
typedef __attribute__((ext_vector_type(8))) _Float16 f16x8;
typedef __attribute__((ext_vector_type(2))) _Float16 f16x2;
typedef __attribute__((ext_vector_type(4))) float f32x4;
typedef __attribute__((ext_vector_type(16))) float f32x16;
typedef __attribute__((ext_vector_type(2))) float f32x2;
typedef __attribute__((ext_vector_type(4))) unsigned u32x4;
typedef __attribute__((ext_vector_type(2))) unsigned u32x2;
typedef __attribute__((ext_vector_type(4))) short s16x4;
typedef __attribute__((ext_vector_type(2))) unsigned short u16x2;
typedef unsigned short u16;
typedef unsigned long long u64;
#define GAS __attribute__((address_space(1)))
#define LAS __attribute__((address_space(3)))
#define NOPK __attribute__((target("no-packed-fp32-ops")))
#define DEV __device__ __forceinline__

constexpr int S_ = 2048, D_ = 1024, KF = 513;

constexpr unsigned OFF_H = 0;
constexpr unsigned OFF_U = OFF_H + (8u << 20);
constexpr unsigned OFF_T = OFF_U + (4u << 20);
constexpr unsigned OFF_G = OFF_T + (4u << 20);
constexpr unsigned OFF_FU = OFF_G + 16384u;
constexpr unsigned OFF_FT = OFF_FU + 32768u;
constexpr unsigned OFF_FH = OFF_FT + 32768u;
constexpr unsigned OFF_GRAN = OFF_FH + 1048576u;
constexpr unsigned OFF_E = OFF_GRAN + 16u * 1024u * 16u;
constexpr unsigned WS_END = OFF_E + 128u;

#define TL(i)
#define TSD()
#define TS(i)
#define TC(i)
#define TSE(slot, on)

DEV unsigned pkb(float a, float b) { f32x2 v = {a, b}; return __builtin_bit_cast(unsigned, __builtin_convertvector(v, bf16x2)); }
DEV unsigned pkh(float a, float b) { f32x2 v = {a, b}; return __builtin_bit_cast(unsigned, __builtin_convertvector(v, f16x2)); }
DEV float lo2f(unsigned p) { return __uint_as_float(p << 16); }
DEV float hi2f(unsigned p) { return __uint_as_float(p & 0xffff0000u); }
DEV float bfr(float f) { return lo2f(pkb(f, f)); }
DEV float2 cmul(float2 a, float2 b) { return make_float2(a.x * b.x - a.y * b.y, a.x * b.y + a.y * b.x); }
DEV float2 cfma(float2 a, float2 b, float2 c) { return make_float2(fmaf(a.x, b.x, fmaf(-a.y, b.y, c.x)), fmaf(a.x, b.y, fmaf(a.y, b.x, c.y))); }
DEV f32x4 mfb(u32x4 a, u32x4 b, f32x4 c) { return __builtin_amdgcn_mfma_f32_16x16x32_bf16(__builtin_bit_cast(bf16x8, a), __builtin_bit_cast(bf16x8, b), c, 0, 0, 0); }
DEV f32x16 mf32(u32x4 a, u32x4 b, f32x16 c) { return __builtin_amdgcn_mfma_f32_32x32x16_bf16(__builtin_bit_cast(bf16x8, a), __builtin_bit_cast(bf16x8, b), c, 0, 0, 0); }
DEV f32x4 mfh(u32x4 a, u32x4 b, f32x4 c) { return __builtin_amdgcn_mfma_f32_16x16x32_f16(__builtin_bit_cast(f16x8, a), __builtin_bit_cast(f16x8, b), c, 0, 0, 0); }
DEV unsigned pkmax(unsigned a, unsigned b) { return __builtin_bit_cast(unsigned, __builtin_elementwise_max(__builtin_bit_cast(u16x2, a), __builtin_bit_cast(u16x2, b))); }
typedef LAS char* lptr;
DEV unsigned ldsa(lptr p) { return (unsigned)(uintptr_t)p; }
template <class T> DEV T lld(lptr p) { return *(LAS const T*)p; }
template <class T> DEV void lst(lptr p, T v) { *(LAS T*)p = v; }
DEV u32x2 trread(lptr p) { return __builtin_bit_cast(u32x2, __builtin_amdgcn_ds_read_tr16_b64_v4i16((LAS s16x4*)p)); }
DEV u32x4 trfrag(lptr p, int second) { const u32x2 a = trread(p), b = trread(p + second); return (u32x4){a.x, a.y, b.x, b.y}; }
typedef __amdgpu_buffer_rsrc_t rsrc_t;
DEV u32x4 ldfrag(lptr tile, int row, int chunk) { return lld<u32x4>(tile + row * 128 + ((chunk ^ (row & 7)) << 4)); }
template <int N> DEV void wait_vm() { asm volatile("s_waitcnt vmcnt(%0)" :: "i"(N) : "memory"); }
#define LDS_WAIT() asm volatile("s_waitcnt lgkmcnt(0)" ::: "memory")
#define SCHED() __builtin_amdgcn_sched_barrier(0)
#define BAR() do { __builtin_amdgcn_s_barrier(); asm volatile("" ::: "memory"); } while (0)
#define PIN(x) asm volatile("" : "+v"(x))
template <int I> struct IC { static constexpr int v = I; };
template <int I, int N, class F> DEV void sfor(F&& f) { if constexpr (I < N) { f(IC<I>{}); sfor<I + 1, N>(f); } }
#define INL __attribute__((always_inline))
DEV rsrc_t mkrsrc(const void* p, unsigned n) { return __builtin_amdgcn_make_buffer_rsrc((void*)p, 0, n, 0x00020000); }
constexpr int SC1 = 16;
#define WTO 16
template <int AUX> DEV void st128(u32x4 v, rsrc_t rs, unsigned off) { __builtin_amdgcn_raw_buffer_store_b128(v, rs, off, 0, AUX); }
DEV void lds_add(unsigned a) { asm volatile("ds_add_u32 %0, %1" :: "v"(a), "v"(1u) : "memory"); }
DEV unsigned lds_peek(unsigned a) {
    unsigned v;
    asm volatile("ds_read_b32 %0, %1\n\ts_waitcnt lgkmcnt(0)" : "=v"(v) : "v"(a) : "memory");
    return __builtin_amdgcn_readfirstlane(v);
}
DEV void tile_of(int gb, int& ta, int& tb) {
    const int xcd = gb & 7, loc = gb >> 3;
    ta = (xcd >> 1) * 4 + (loc >> 3);
    tb = (xcd & 1) * 8 + (loc & 7);
}
DEV void rows4(float a, float& a0, float& a1, float& a2, float& a3) {
    const unsigned u = __float_as_uint(a);
    const auto r = __builtin_amdgcn_permlane32_swap(u, u, false, false);
    const auto p = __builtin_amdgcn_permlane16_swap(r[0], r[0], false, false);
    const auto s = __builtin_amdgcn_permlane16_swap(r[1], r[1], false, false);
    a0 = __uint_as_float(p[0]); a1 = __uint_as_float(p[1]); a2 = __uint_as_float(s[0]); a3 = __uint_as_float(s[1]);
}
DEV float rowsum4(float a) {
    const unsigned u = __float_as_uint(a);
    const auto r = __builtin_amdgcn_permlane32_swap(u, u, false, false);
    const unsigned t = __float_as_uint(__uint_as_float(r[0]) + __uint_as_float(r[1]));
    const auto p = __builtin_amdgcn_permlane16_swap(t, t, false, false);
    return __uint_as_float(p[0]) + __uint_as_float(p[1]);
}
DEV float2 sel4(int q, float2 a, float2 b, float2 c, float2 d) {
    PIN(d.x); PIN(d.y);
    const float2 e = (q & 1) ? b : a, f = (q & 1) ? d : c;
    return (q & 2) ? f : e;
}
template <int R> DEV unsigned ror16(unsigned v) { return __builtin_amdgcn_update_dpp(0u, v, 0x120 + R, 0xf, 0xf, false); }
DEV unsigned wave_pkmax(unsigned m) {
    m = pkmax(m, ror16<8>(m)); m = pkmax(m, ror16<4>(m)); m = pkmax(m, ror16<2>(m)); m = pkmax(m, ror16<1>(m));
    const auto r = __builtin_amdgcn_permlane32_swap(m, m, false, false);
    m = pkmax(r[0], r[1]);
    const auto p = __builtin_amdgcn_permlane16_swap(m, m, false, false);
    return pkmax(p[0], p[1]);
}
DEV float half_sum(float v) {
    v += __uint_as_float(ror16<8>(__float_as_uint(v))); v += __uint_as_float(ror16<4>(__float_as_uint(v)));
    v += __uint_as_float(ror16<2>(__float_as_uint(v))); v += __uint_as_float(ror16<1>(__float_as_uint(v)));
    const auto p = __builtin_amdgcn_permlane16_swap(__float_as_uint(v), __float_as_uint(v), false, false);
    return __uint_as_float(p[0]) + __uint_as_float(p[1]);
}
DEV u32x4 quads(lptr buf, int lane, u32x2 p, u32x2 n) {
    lptr wp = buf + (lane & 15) * 64 + ((lane >> 4) & 1) * 8;
    const int v = (lane >> 5) ^ ((lane >> 2) & 3);
    lst<u32x2>(wp + (v << 4), p); lst<u32x2>(wp + ((v ^ 2) << 4), n);
    asm volatile("" ::: "memory");
    return lld<u32x4>(buf + ((lane ^ (lane >> 4)) << 4));
}

#define PRIOA 2
#define NREP 8
#define PRIOM 1
#define PRIOT 2
#define PRIOG 2
#define FLO 0
#define LAG 0
#define AD 6
#define BD 7
#define WROW {0, 0, 0, 0, 6, 11, 16}
#define ROT 0
#define HAUX SC1
#define PRIOL 3
#define XM 0
#define C0S 15
#define C1S 15
#define UFS 9
#define PFS 99
#define PRIOR 0
#define PRS 4
#define WXS 13
#define CPF 1
#define TDL 0
#define CDL 0
#define FST 8
#define HREP 8
#define NP1 1
#define PDL 48
#define PD0 32
#define M16 1
#define BK1 1
#define PFH 0
#define CRG 0
#define CE 0
#define TLBW 0
#define XO 0
#define UL 1
#define TL2 1
#define OQ 1
#define HFIRST 8u
#define RDPAT {0, 1, 2, -1, 3, 4, -1, 5, 6, -1, 7, -1, -1, -1, -1, -1}
#define Z4 ((f32x4){0.f, 0.f, 0.f, 0.f})
constexpr unsigned SG = 0x80008000u;
struct CM { u32x4 re, im; };
DEV u32x4 cmat1(float t0, float dt) {
    float cc[4], ss[4];
    t0 = __builtin_amdgcn_fractf(t0);
#pragma unroll
    for (int e = 0; e < 4; ++e) { const float f = fmaf((float)e, dt, t0); cc[e] = __builtin_amdgcn_cosf(f); ss[e] = __builtin_amdgcn_sinf(f); }
    return (u32x4){pkh(ss[0], ss[1]), pkh(ss[2], ss[3]), pkh(cc[0], cc[1]), pkh(cc[2], cc[3])};
}
DEV CM cexp(u32x4 v) { CM m; m.re = (u32x4){v.z, v.w, v.x ^ SG, v.y ^ SG}; m.im = v; PIN(m.re); PIN(m.im); return m; }
DEV u32x4 pk8(f32x4 r, f32x4 i) { return (u32x4){pkh(r[0], r[1]), pkh(r[2], r[3]), pkh(i[0], i[1]), pkh(i[2], i[3])}; }
DEV u32x4 wmul(f32x4 r, f32x4 i, u32x4 w) {
    float yr[4], yi[4];
#pragma unroll
    for (int e = 0; e < 4; ++e) {
        const unsigned wu = w[e];
        const f16x2 h = __builtin_bit_cast(f16x2, wu);
        yr[e] = fmaf(r[e], (float)h[0], -i[e] * (float)h[1]); yi[e] = fmaf(r[e], (float)h[1], i[e] * (float)h[0]);
    }
    return (u32x4){pkh(yr[0], yr[1]), pkh(yr[2], yr[3]), pkh(yi[0], yi[1]), pkh(yi[2], yi[3])};
}
DEV u32x4 wcvt(u32x4 w, float sr, float si) {
#pragma unroll
    for (int e = 0; e < 4; ++e) w[e] = pkh(lo2f(w[e]) * sr, hi2f(w[e]) * si);
    PIN(w);
    return w;
}
DEV u32x4 mkdesc(const void* p, unsigned n) { const u64 a = (u64)p; return (u32x4){(unsigned)a, (unsigned)(a >> 32) & 0xffffu, n, 0x00020000u}; }
DEV void dma16(u32x4 d, unsigned l, unsigned vo, unsigned so) {
    asm volatile("s_nop 0\n\tbuffer_load_dwordx4 %0, %1, %2 offen lds" :: "v"(vo), "s"(d), "s"(so), "{m0}"(l) : "memory");
}
DEV void sig(unsigned a, unsigned v) { asm volatile("s_mov_b64 exec, 1\n\tds_add_u32 %0, %1\n\ts_mov_b64 exec, -1" :: "v"(a), "v"(v) : "memory"); }
DEV unsigned sig_rtn(unsigned a) {
    unsigned v;
    asm volatile("s_mov_b64 exec, 1\n\tds_add_rtn_u32 %0, %1, %2\n\ts_mov_b64 exec, -1\n\ts_waitcnt lgkmcnt(0)" : "=&v"(v) : "v"(a), "v"(1u) : "memory");
    return __builtin_amdgcn_readfirstlane(v);
}
template <int SLP> DEV unsigned await(unsigned a, unsigned mask) {
    unsigned v, n, r, t;
    asm volatile("s_mov_b32 %1, 0x400000\n"
                 "1:\tds_read_b32 %0, %4\n\ts_waitcnt lgkmcnt(0)\n\tv_readfirstlane_b32 %2, %0\n\ts_and_b32 %3, %2, %5\n\ts_cbranch_scc1 2f\n\t"
                 "s_sleep %6\n\ts_sub_u32 %1, %1, 1\n\ts_cbranch_scc0 1b\n"
                 "2:" : "=&v"(v), "=&s"(n), "=&s"(r), "=&s"(t) : "v"(a), "s"(mask), "i"(SLP) : "memory", "scc");
    return r;
}
DEV void flag_put(rsrc_t rs, unsigned off, int gb, u32x2 v, int lane) {
    if (lane < NREP) __builtin_amdgcn_raw_buffer_store_b64(v, rs, lane * 2048, off + gb * 8, SC1);
}
DEV void flag_get(rsrc_t rs, unsigned off, int gb, int lane, u32x4 (&fl)[2]) {
    unsigned o = lane * 16; PIN(o);
#pragma unroll
    for (int i = 0; i < 2; ++i) fl[i] = __builtin_amdgcn_raw_buffer_load_b128(rs, o + i * 1024, off + (gb & (NREP - 1)) * 2048, SC1);
}
DEV bool flag_ok(const u32x4 (&fl)[2], u32x2 tag) {
    bool ok = true;
#pragma unroll
    for (int i = 0; i < 2; ++i) ok = ok & ((fl[i].x ^ tag.x) < 0x10000u) & (fl[i].y == tag.y) & ((fl[i].z ^ tag.x) < 0x10000u) & (fl[i].w == tag.y);
    return __all(ok);
}
template <int N> DEV unsigned h_poll(rsrc_t rs, int gb, int lane, u32x2 tag) {
    unsigned o = lane * FST, m = 0; PIN(o);
    u32x2 fl[N];
#pragma unroll
    for (int i = 0; i < N; ++i) fl[i] = __builtin_amdgcn_raw_buffer_load_b64(rs, o, OFF_FH + (gb & (HREP - 1)) * 256 * FST + i * 64 * FST, SC1);
#pragma unroll
    for (int i = 0; i < N; ++i) {
        const u64 b = __ballot(((fl[i].x ^ tag.x) < 0x10000u) & (fl[i].y == tag.y));
        m |= ((unsigned)b == ~0u ? 8u << (8 * i) : 0u) | ((unsigned)(b >> 32) == ~0u ? 128u << (8 * i) : 0u);
    }
    return m;
}
DEV void sig_or(unsigned a, unsigned v) { asm volatile("s_mov_b64 exec, 1\n\tds_or_b32 %0, %1\n\ts_mov_b64 exec, -1" :: "v"(a), "v"(v) : "memory"); }

constexpr int A_STAGE = 32768, A_XT = 4 * A_STAGE, A_PRM = A_XT + 16384, A_AGG = A_PRM + 2048, A_FLG = A_AGG + 512, A_GW = A_FLG + 128,
              A_MAT = A_GW + 2048, SMEM = A_MAT + 11264;
constexpr int E1P = 1040, A_EX = A_GW, A_W = 2 * A_STAGE, A_E1 = A_W + 19456, A_HS = 3 * A_STAGE + 16384, A_R0 = 0;
constexpr int F_MID = 8, F_EM = 12, F_MAX = 16, F_TAG = 32, F_FULL = 40, F_FREE = 44, F_HC = 48, F_HR = 52, F_TC = 56, F_TR = 60, F_TT = 64, F_R0 = 68, F_WM = 80;

constexpr int XG = 0xFF;
DEV void slot_of(int gb, int& mp, int& j1) { gb ^= XG; mp = UL ? (gb & 7) * 8 + (gb >> 5) : (gb >> 5) * 8 + (gb & 7); j1 = (gb >> 3) & 3; }
DEV u32x4 mat_sf(int k1, int c, int q) { const float dt = -(float)(k1 + 128 * c) * (1.0f / 2048.0f); return cmat1(dt * (float)(4 * q), dt); }
DEV u32x4 mat_si(int k1, int c, int q) { return cmat1((float)((k1 + 512 * q) * c) * (1.0f / 2048.0f), (float)c * (1.0f / 16.0f)); }

DEV void mk_tables(lptr smem, int gb, int wid, int lane) {
    int mp, j1; slot_of(gb, mp, j1);
    const int c = lane & 15, q = lane >> 4;
    const bool spc = mp == 0 && !(j1 & 1);
    const int k1 = mp ? mp : (j1 == 3 ? 64 : 0);
    lptr mt = smem + A_MAT + lane * 16;
    if (wid == 0) {
        const float dt = -(float)(j1 + 4 * (spc ? c & 7 : c)) * (1.0f / 64.0f), t0 = __builtin_amdgcn_fractf(dt * (float)(4 * q));
        float cc[4], ss[4];
#pragma unroll
        for (int e = 0; e < 4; ++e) { const float f = fmaf((float)e, dt, t0); cc[e] = __builtin_amdgcn_cosf(f); ss[e] = __builtin_amdgcn_sinf(f); }
        const unsigned c0 = pkb(cc[0], cc[1]), c1 = pkb(cc[2], cc[3]), s0 = pkb(ss[0], ss[1]), s1 = pkb(ss[2], ss[3]);
        const unsigned c0l = pkb(cc[0] - lo2f(c0), cc[1] - hi2f(c0)), c1l = pkb(cc[2] - lo2f(c1), cc[3] - hi2f(c1));
        const unsigned s0l = pkb(ss[0] - lo2f(s0), ss[1] - hi2f(s0)), s1l = pkb(ss[2] - lo2f(s1), ss[3] - hi2f(s1));
        u32x4 m[4] = {(u32x4){c0, c1, s0 ^ SG, s1 ^ SG}, (u32x4){s0, s1, c0, c1}, (u32x4){c0l, c1l, s0l ^ SG, s1l ^ SG}, (u32x4){s0l, s1l, c0l, c1l}};
        if (spc) {
            m[0] = c < 8 ? (u32x4){c0, c1, 0, 0} : (u32x4){0, 0, c0, c1}; m[1] = c < 8 ? (u32x4){s0, s1, 0, 0} : (u32x4){0, 0, s0, s1};
            m[2] = c < 8 ? (u32x4){c0l, c1l, 0, 0} : (u32x4){0, 0, c0l, c1l}; m[3] = c < 8 ? (u32x4){s0l, s1l, 0, 0} : (u32x4){0, 0, s0l, s1l};
        }
#pragma unroll
        for (int i = 0; i < 4; ++i) lst<u32x4>(mt + i * 1024, m[i]);
    } else if (wid == 1) {
        const int jq = spc ? (4 * q) & 7 : 4 * q;
        float cc[4], ss[4];
        const float t0 = __builtin_amdgcn_fractf((float)(c * (j1 + 4 * jq)) * (1.0f / 64.0f));
#pragma unroll
        for (int e = 0; e < 4; ++e) {
            const float f = fmaf((float)c, (float)e * (1.0f / 16.0f), t0);
            const float wg = !spc || (gb == XG && jq + e == 0) ? 0.125f : 0.25f;
            cc[e] = __builtin_amdgcn_cosf(f) * wg; ss[e] = -__builtin_amdgcn_sinf(f) * wg;
        }
        const unsigned c0 = pkh(cc[0], cc[1]), c1 = pkh(cc[2], cc[3]), n0 = pkh(ss[0], ss[1]), n1 = pkh(ss[2], ss[3]);
        u32x4 mia = (u32x4){c0, n0, c1, n1}, mib = (u32x4){n0, c0 ^ SG, n1, c1 ^ SG};
        if (spc) { mib = q < 2 ? (u32x4){0, 0, 0, 0} : mia ^ SG; if (q >= 2) mia = (u32x4){0, 0, 0, 0}; }
        lst<u32x4>(mt + 6 * 1024, mia); lst<u32x4>(mt + 7 * 1024, mib);
        lst<u32x4>(mt + 8 * 1024, cmat1((float)(c * q) * -0.25f, (float)c * (-1.0f / 16.0f)));
    } else {
        lst<u32x4>(mt + (2 + wid) * 1024, wid == 2 ? mat_sf(k1, c, q) : mat_si(k1, c, q));
        const int ja = (spc ? 0 : 8 * (wid - 2)) + (lane >> 3);
        const float dt = -(float)(j1 + 4 * ja) * (1.0f / 1024.0f), t0 = dt * (float)(2 * (lane & 7)), t1 = t0 + dt;
        lst<f32x4>(mt + (7 + wid) * 1024, (f32x4){__builtin_amdgcn_cosf(t0), __builtin_amdgcn_sinf(t0), __builtin_amdgcn_cosf(t1), __builtin_amdgcn_sinf(t1)});
    }
}

DEV void c_chunk(const u16* __restrict__ Cre, const u16* __restrict__ Cim, unsigned lb, int pt, int lw, int lane, int j) {
    const u32x4 rc = mkdesc(lw < 2 ? Cre : Cim, D_ * D_ * 2);
    const unsigned vo = ((lane >> 5) * 256 + ((lane >> 3) & 3) * 4) * 2048 + (((lane & 7) ^ (lane >> 3)) << 4);
#pragma unroll
    for (int i = 0; i < 8; ++i)
        dma16(rc, lb + (j & 3) * A_STAGE + lw * 8192 + i * 1024, vo, (512 * (i & 1) + 16 * pt + (i >> 1)) * 2048 + j * 256 + (lw & 1) * 128);
}

DEV void role_mma(const u16* __restrict__ Cre, const u16* __restrict__ Cim, char* ws, float* __restrict__ out, lptr smem, int gb, int wid, int lane, u64 pc, int len) {
    int lt, pt; tile_of(gb, lt, pt);
    const int wr = wid >> 1, wc = wid & 1, c = lane & 15, q = lane >> 4;
    const rsrc_t rs = mkrsrc(ws, WS_END);
    const unsigned fb = ldsa(smem + A_FLG);
    TSD(); TS(0);
    u64 tag = __hip_atomic_load((GAS u64*)(ws + OFF_E), __ATOMIC_RELAXED, __HIP_MEMORY_SCOPE_WORKGROUP) + 0x9E3779B97F4A7C15ull;
    unsigned cpf = 0;
    if (CPF == 1) { if (wid == 3) cpf = __builtin_amdgcn_raw_buffer_load_b32(mkrsrc((const void*)pc, max(len, 0)), lane * 128, ((gb >> 3) % 6) * 8192, 0); }
    if (CPF == 2) cpf = __builtin_amdgcn_raw_buffer_load_b32(mkrsrc((const void*)pc, max(len, 0)), (lane & 15) * 128 + wid * 2048, ((gb >> 3) % 6) * 8192, 0);
    if (CPF == 3) { if (lane < 3) cpf = __builtin_amdgcn_raw_buffer_load_b32(mkrsrc((const void*)pc, max(len, 0)), lane * 128 + wid * 384, (gb >> 3) * 1536, 0); }
    SCHED();
    if (wid == 0 && lane < 32) lst<unsigned>(smem + A_FLG + lane * 4, 0u);
    mk_tables(smem, gb, wid, lane);
    f32x4 acc[4][4];
#pragma unroll
    for (int m = 0; m < 4; ++m)
#pragma unroll
        for (int n = 0; n < 4; ++n) acc[m][n] = Z4;
    u32x4 af[2][4], bf_[2][4];
#pragma unroll
    for (int m = 0; m < 4; ++m) { af[1][m] = (u32x4){0, 0, 0, 0}; bf_[1][m] = af[1][m]; }
    PIN(tag); PIN(cpf);
    if (wid == 0 && lane == 0) lst<u64>(smem + A_FLG + F_TAG, tag);
    tag = (u64)(unsigned)__builtin_amdgcn_readfirstlane((unsigned)tag) | (u64)(unsigned)__builtin_amdgcn_readfirstlane((unsigned)(tag >> 32)) << 32;
    __builtin_amdgcn_s_waitcnt(0xC07F);
    TS(13);
#pragma unroll 4
    for (int kt = 0; kt < 16; ++kt) {
        BAR();
        lptr tA = smem + (kt & 3) * A_STAGE;
        lptr tB = tA + 16384;
#pragma unroll
        for (int hf = 0; hf < 2; ++hf)
#pragma unroll
            for (int i = 0; i < 16; ++i) {
                acc[i >> 2][i & 3] = mfb(af[1 - hf][i >> 2], bf_[1 - hf][i & 3], acc[i >> 2][i & 3]);
                constexpr int RD[16] = RDPAT;
                const int r = RD[i];
                if (r == 0) af[hf][0] = ldfrag(tA, wr * 64 + c, 4 * hf + q);
                else if (r > 0 && r < 5) bf_[hf][r - 1] = ldfrag(tB, wc * 64 + (r - 1) * 16 + c, 4 * hf + q);
                else if (r >= 5) af[hf][r - 4] = ldfrag(tA, wr * 64 + (r - 4) * 16 + c, 4 * hf + q);
                SCHED();
            }
        LDS_WAIT();
    }
    __builtin_amdgcn_s_setprio(PRIOT);
    sig(fb + F_FREE, 1u << 20);
    float2 lam[2], coef[2], l16[2], l128[2];
#pragma unroll
    for (int n = 0; n < 2; ++n) {
        const f32x4 a = lld<f32x4>(smem + A_PRM + (wc * 32 + 2 * c + n) * 32), b = lld<f32x4>(smem + A_PRM + (wc * 32 + 2 * c + n) * 32 + 16);
        lam[n] = make_float2(a[0], a[1]); coef[n] = make_float2(a[2], a[3]); l16[n] = make_float2(b[0], b[1]); l128[n] = make_float2(b[2], b[3]);
    }
#pragma unroll
    for (int m = 0; m < 4; ++m)
#pragma unroll
        for (int n = 0; n < 4; ++n) acc[m][n] = mfb(af[1][m], bf_[1][n], acc[m][n]);
    float2 l32[2], l64[2];
#pragma unroll
    for (int n = 0; n < 2; ++n) { l32[n] = cmul(l16[n], l16[n]); l64[n] = cmul(l32[n], l32[n]); }
    TS(1);
    float2 h[2][16];
#pragma unroll
    for (int n = 0; n < 2; ++n) h[n][0] = make_float2(acc[0][n][0], acc[0][2 + n][0]);
#pragma unroll
    for (int t = 1; t < 16; ++t)
#pragma unroll
        for (int n = 0; n < 2; ++n) h[n][t] = cfma(lam[n], h[n][t - 1], make_float2(acc[t >> 2][n][t & 3], acc[t >> 2][2 + n][t & 3]));
    float2 loc[2], wag[2], wq[2];
#pragma unroll
    for (int n = 0; n < 2; ++n) {
        float2 a0, a1, a2, a3;
        rows4(h[n][15].x, a0.x, a1.x, a2.x, a3.x);
        rows4(h[n][15].y, a0.y, a1.y, a2.y, a3.y);
        const float2 s2 = cfma(l16[n], a0, a1), s3 = cfma(l16[n], s2, a2);
        wag[n] = cfma(l16[n], s3, a3);
        loc[n] = sel4(q, make_float2(0.f, 0.f), a0, s2, s3);
    }
    lptr aggp = smem + A_AGG + (wc * 16 + c) * 16;
    const unsigned flg = ldsa(smem + A_FLG) + wc * 4;
    float2 W0[2] = {make_float2(0.f, 0.f), make_float2(0.f, 0.f)};
    if (wr == 0) {
        if (q == 0) lst<f32x4>(aggp, (f32x4){wag[0].x, wag[0].y, wag[1].x, wag[1].y});
        asm volatile("" ::: "memory");
        if (lane == 0) lds_add(flg);
    } else {
#pragma unroll 1
        for (int spin = 0; spin < (1 << 20); ++spin) { if (lds_peek(flg)) break; }
        const f32x4 w0 = lld<f32x4>(aggp);
        W0[0] = make_float2(w0[0], w0[1]); W0[1] = make_float2(w0[2], w0[3]);
        const float2 g0 = cfma(l64[0], W0[0], wag[0]), g1 = cfma(l64[1], W0[1], wag[1]);
        if (lt < 15 && q < 2) {
            const float2 gg = q == 0 ? g0 : g1;
            st128<SC1>((u32x4){__float_as_uint(gg.x), (unsigned)tag, __float_as_uint(gg.y), (unsigned)(tag >> 32)},
                       rs, (2 * c + q) * 16 + OFF_GRAN + ((15 - lt) * 1024 + pt * 64 + wc * 32) * 16);
        }
    }
    TS(2);
#pragma unroll
    for (int n = 0; n < 2; ++n) { PIN(lam[n].x); PIN(lam[n].y); PIN(coef[n].x); PIN(coef[n].y); PIN(l16[n].x); PIN(l16[n].y); PIN(l128[n].x); PIN(l128[n].y); }
    float2 pw[2][8], l8[2], lq[2], l512[2];
#pragma unroll
    for (int n = 0; n < 2; ++n) {
        wq[n] = sel4(q, make_float2(1.f, 0.f), l16[n], l32[n], cmul(l32[n], l16[n]));
        loc[n] = cfma(wq[n], W0[n], loc[n]);
        if (wr) wq[n] = cmul(wq[n], l64[n]);
        pw[n][0] = cmul(coef[n], lam[n]);
        l8[n] = cmul(lam[n], lam[n]); l8[n] = cmul(l8[n], l8[n]); l8[n] = cmul(l8[n], l8[n]);
        const float2 l256 = cmul(l128[n], l128[n]);
        lq[n] = sel4(q, make_float2(1.f, 0.f), l128[n], l256, cmul(l256, l128[n]));
        l512[n] = cmul(l256, l256);
    }
#pragma unroll
    for (int n = 0; n < 2; ++n) {
        PIN(wq[n].x); PIN(wq[n].y); PIN(loc[n].x); PIN(loc[n].y); PIN(l8[n].x); PIN(l8[n].y); PIN(lq[n].x); PIN(lq[n].y); PIN(l512[n].x); PIN(l512[n].y);
    }
#pragma unroll
    for (int t = 0; t < 8; ++t) {
#pragma unroll
        for (int n = 0; n < 2; ++n) {
            if (t) pw[n][t] = cmul(pw[n][t - 1], lam[n]);
            h[n][2 * t] = cmul(coef[n], h[n][2 * t]); h[n][2 * t + 1] = cmul(coef[n], h[n][2 * t + 1]);
        }
#pragma unroll
        for (int n = 0; n < 2; ++n) { PIN(pw[n][t].x); PIN(pw[n][t].y); PIN(h[n][2 * t].x); PIN(h[n][2 * t].y); PIN(h[n][2 * t + 1].x); PIN(h[n][2 * t + 1].y); }
        SCHED();
    }
    await<0>(fb + F_FREE, 8u << 20);
    TS(3);
    if (lt > 0) {
        u32x4 gr[4][2];
        unsigned off = ((16 - lt + q) * 1024 + pt * 64 + wc * 32 + 2 * c) * 16;
#pragma unroll 1
        for (int spin = 0; spin < (1 << 20); ++spin) {
            bool ok = true;
            PIN(off);
#pragma unroll
            for (int j = 0; j < 4; ++j) {
                const rsrc_t rg = mkrsrc(ws + OFF_GRAN + j * 65536, (4 - j) * 65536);
                gr[j][0] = __builtin_amdgcn_raw_buffer_load_b128(rg, off, 0, SC1);
                gr[j][1] = __builtin_amdgcn_raw_buffer_load_b128(rg, off + 16, 0, SC1);
            }
#pragma unroll
            for (int j = 0; j < 4; ++j) {
                const bool v = lt - 1 - (4 * j + q) >= 0;
#pragma unroll
                for (int n = 0; n < 2; ++n) ok = ok & (!v | ((gr[j][n].y == (unsigned)tag) & (gr[j][n].w == (unsigned)(tag >> 32))));
            }
            if (__all(ok)) break;
            asm volatile("" ::: "memory");
        }
#pragma unroll
        for (int n = 0; n < 2; ++n) {
            float2 cs = make_float2(__uint_as_float(gr[3][n].x), __uint_as_float(gr[3][n].z));
#pragma unroll
            for (int j = 2; j >= 0; --j) cs = cfma(l512[n], cs, make_float2(__uint_as_float(gr[j][n].x), __uint_as_float(gr[j][n].z)));
            cs = cmul(cs, lq[n]);
            cs.x = rowsum4(cs.x); cs.y = rowsum4(cs.y);
            loc[n] = cfma(wq[n], cs, loc[n]);
        }
    }
    TS(4);
    lptr tb = smem + A_HS + wid * 4096;
    PIN(lane);
#pragma unroll
    for (int hf = 0; hf < 2; ++hf) {
        const float2 s0 = hf ? cmul(l8[0], loc[0]) : loc[0], s1 = hf ? cmul(l8[1], loc[1]) : loc[1];
#pragma unroll
        for (int t = 0; t < 8; ++t) {
            const float2 v0 = cfma(pw[0][t], s0, h[0][8 * hf + t]), v1 = cfma(pw[1][t], s1, h[1][8 * hf + t]);
            lst<unsigned>(tb + t * 256 + lane * 4, pkb(v0.x, v1.x));
            lst<unsigned>(tb + 2048 + t * 256 + lane * 4, pkb(-v0.y, -v1.y));
        }
        asm volatile("" ::: "memory");
        lptr rb = tb + (lane >> 4) * 256 + (lane & 3) * 64 + ((lane >> 2) & 3) * 16;
        const unsigned hoff = OFF_H + ((lane >> 4) * 64 + pt * 2 + wc) * 8192 + lt * 512 + wr * 256 + (lane & 15) * 16;
#pragma unroll
        for (int v = 0; v < 2; ++v)
#pragma unroll
            for (int pl = 0; pl < 2; ++pl) st128<SC1>(lld<u32x4>(rb + pl * 2048 + v * 1024), rs, hoff + ((2 * hf + v) * 256 + pl * 32) * 8192);
        asm volatile("" ::: "memory");
    }
    TS(5);
    wait_vm<0>();
    TS(6);
    const bool pub = sig_rtn(fb + F_HC) == 3u;
    if (pub) {
        const u32x2 tg = {(unsigned)tag, (unsigned)(tag >> 32)};
        if (lane < HREP) __builtin_amdgcn_raw_buffer_store_b64(tg, rs, lane * 256 * FST, OFF_FH + (pt * 16 + lt) * FST, SC1);
        TS(15);
        int spin; unsigned m = 0;
        if (PDL) __builtin_amdgcn_s_sleep(PDL);
        if (PD0 && lt == 0) __builtin_amdgcn_s_sleep(PD0);
        TS(19);
#pragma unroll 1
        for (spin = 0; spin < (1 << 20); ++spin) {
            m = h_poll<NP1>(rs, gb, lane, tg);
            if (spin == 0) TS(16);
            if (spin == 1) TS(17);
            if (spin == 2) TS(18);
            if ((m & HFIRST) == HFIRST) break;
        }
        sig_or(fb + F_FULL, m);
    } else await<1>(fb + F_FULL, 8u);
    __builtin_amdgcn_s_setprio(PRIOG);
    TS(7);
    PIN(lane);
    const unsigned vo = ((lane & 15) >> 2) * 256 + (lane >> 4) * 64 + (lane & 3) * 16, sH = OFF_H + lt * 524288 + wid * 2048;
    auto ldB = [&](int i, int n) { return __builtin_amdgcn_raw_buffer_load_b128(rs, vo + n * 1024, sH + ((i >> 3) * 4 + (i & 3) + (i & 4) * 8) * 8192, HAUX); };
    lptr ab[2];
#pragma unroll
    for (int i = 0; i < 2; ++i) { ab[i] = smem + (lane & 15) * 128 + (((4 * i + (lane >> 4)) ^ (lane & 7)) << 4); PIN(ab[i]); }
    auto ldA = [&](int f) {
        const int a = (f >> 2) & 1;
        if ((f & 63) < 8 && f > 7 && !(f & 3)) { ab[a] += (f & 64) ? 2 * A_STAGE : -2 * A_STAGE; PIN(ab[a]); }
        return lld<u32x4>(ab[a] + ((f >> 3) & 7) * 8192 + (f & 3) * 2048);
    };
    u32x4 A[AD], B[BD][2];
#pragma unroll
    for (int i = 0; i < BD; ++i) { B[i][0] = ldB(i, 0); B[i][1] = ldB(i, 1); }
    SCHED();
    unsigned fv = fb; PIN(fv);
    await<0>(fv + F_FULL, 4u);
#pragma unroll
    for (int f = 0; f < AD; ++f) A[f] = ldA(f);
    f32x4 ac[4][2];
    unsigned fe = 0, fs = 0;
#pragma unroll
    for (int e = 0; e < 8; ++e) ac[e >> 1][e & 1] = Z4;
    SCHED();
    sfor<0, 64>([&](auto ic) INL {
        constexpr int i = decltype(ic)::v;
#pragma unroll
        for (int k = 0; k < 4; ++k) {
            const int f = 4 * i + k;
            ac[k][0] = OQ ? mfb(B[i % BD][0], A[f % AD], ac[k][0]) : mfb(A[f % AD], B[i % BD][0], ac[k][0]);
            SCHED();
            if (CE && f == 0) { wait_vm<2 * BD - 1>(); sig(fv + F_FULL, 0x1100u); }
            if (k == 2 && ((i + BD + 1) & 7) == 0 && i + BD + 1 < 64) fe = *(volatile LAS unsigned*)(smem + A_FLG + F_FULL);
            if (k == 3 && ((i + BD) & 7) == 0 && i + BD < 64) {
                fs = __builtin_amdgcn_readfirstlane(fe);
                if (!(fs & (8u << (4 * (((i + BD) >> 3) & 7))))) fs = await<0>(fv + F_FULL, 8u << (4 * (((i + BD) >> 3) & 7)));
                asm volatile("" ::: "memory");
                TS(20 + ((i + BD) >> 3));
            }
            if (k == 3 && i + BD < 64) B[i % BD][0] = ldB(i + BD, 0);
            if (k == BK1 && i > 0 && i - 1 + BD < 64) B[(i + BD - 1) % BD][1] = ldB(i - 1 + BD, 1);
            SCHED();
            ac[k][1] = OQ ? mfb(B[i % BD][1], A[f % AD], ac[k][1]) : mfb(A[f % AD], B[i % BD][1], ac[k][1]);
            SCHED();
            if (f + AD < 256) {
                if (((f + AD) & 31) == 0) {
                    if (!(fs & (4u << (4 * ((f + AD) >> 5))))) await<0>(fv + F_FULL, 4u << (4 * ((f + AD) >> 5)));
                    asm volatile("" ::: "memory");
                }
                A[f % AD] = ldA(f + AD);
            }
            SCHED();
        }
        if ((i & 7) == 7 && i < 40) sig(fv + F_FREE, 1u << (4 * (i >> 3)));
        if (i == 0) { TS(8); TC(60); }
        if (i == 31) TS(9);
        if (i == 60) { TS(14); TC(61); }
    });
    TS(10);
    await<0>(fv + F_R0, 4u);
    TS(11);
    const rsrc_t ro = mkrsrc(out, S_ * D_ * 4);
    PIN(lane);
    const int c2 = lane & 15, q2 = lane >> 4;
    const unsigned oo = ((64 * q2 + 512 * wid + lt) * D_ + 256 * (c2 >> 2) + 16 * pt + 4 * (c2 & 3)) * 4;
#pragma unroll
    for (int n = 0; n < 2; ++n) {
        const f32x4 o2 = lld<f32x4>(smem + A_AGG + (32 * wid + 16 * n + 4 * q2) * 4);
#pragma unroll
        for (int e = 0; e < 4; ++e) {
            const f32x4 r0 = lld<f32x4>(smem + A_R0 + (wid * 2 + n) * 4096 + c2 * 256 + (((4 * q2 + e) ^ c2) << 4));
            const f32x4 o = {fmaf(o2[e], ac[0][n][e], r0[0]), fmaf(o2[e], ac[1][n][e], r0[1]), fmaf(o2[e], ac[2][n][e], r0[2]), fmaf(o2[e], ac[3][n][e], r0[3])};
            st128<WTO>(__builtin_bit_cast(u32x4, o), ro, oo + (256 * n + 16 * e) * D_ * 4);
        }
    }
    asm volatile("gm_code_end:");
    TS(12);
    TSE(0, wid == 0);
    TSE(2, pub);
}

#define TSARG
#define TSPASS
struct Gate {
    unsigned xa, wa, xv, gw; float sum; int rot;
    DEV void rd(int kt) { if (XM & 8) return; asm volatile("ds_read_b32 %0, %2\n\tds_read_b32 %1, %3" : "=&v"(xv), "=&v"(gw) : "v"(xa + (kt & 3) * A_STAGE), "v"(wa + ((kt + rot) & 15) * 128) : "memory"); }
    DEV void acc() {
        if (XM & 8) return;
        asm volatile("s_waitcnt lgkmcnt(0)" : "+v"(xv), "+v"(gw) :: "memory");
        const unsigned pr = pkb(lo2f(xv) * lo2f(gw), hi2f(xv) * hi2f(gw));
        sum += lo2f(pr); sum += hi2f(pr); PIN(sum);
    }
};
DEV unsigned u_load(char* ws, lptr smem, int gb, int w, int lane, u32x4 (&ut)[2]) {
    int mp, j1; slot_of(gb, mp, j1);
    __builtin_amdgcn_s_setprio(PRIOM);
    const unsigned em = await<1>(ldsa(smem + A_FLG + F_EM), 0x8000u);
    asm volatile("buffer_load_dwordx4 %0, %2, %3, %4 offen sc1\n\tbuffer_load_dwordx4 %1, %2, %3, %4 offen offset:2048 sc1"
                 : "=&v"(ut[0]), "=&v"(ut[1]) : "v"((lane >> 2) * 128 + (lane & 3) * 16), "s"(mkdesc(ws, WS_END)),
                   "s"(OFF_U + ((mp >> 1) * 4 + j1) * 32768 + (mp & 1) * 64 + w * 4096) : "memory");
    return em;
}

DEV float role_load(const u16* __restrict__ x, const u16* __restrict__ Bre, const u16* __restrict__ Bim,
                    const u16* __restrict__ gate_w, char* ws, lptr smem, int gb, int lw, int lane TSARG) {
    __builtin_amdgcn_s_waitcnt(0x0F70);
    Gate g;
    int lt, pt; tile_of(gb, lt, pt);
    const int srow = lane >> 3, schunk = (lane & 7) ^ srow;
    const int rot = ROT;
    const u32x4 rx = mkdesc(x, S_ * D_ * 2), rb = mkdesc((lw & 1) ? Bim : Bre, D_ * D_ * 2);
    const unsigned lb = ldsa(smem);
    const unsigned vA = (16 * (srow >> 2) + (srow & 3)) * 2048 + schunk * 16, sA = (lt * 128 + 64 * (lw >> 1) + 8 * (lw & 1)) * 2048;
    const unsigned vB = 2 * srow * 2048 + schunk * 16, sB = (pt * 64 + 32 * (lw >> 1)) * 2048;
    auto stA = [&](int kk) {
#pragma unroll
        for (int i = 0; i < 4; ++i) dma16(rx, lb + (kk & 3) * A_STAGE + lw * 4096 + i * 1024, vA, sA + (32 * (i & 1) + 4 * (i >> 1)) * 2048 + ((kk + rot) & 15) * 128);
    };
    auto stB = [&](int kk) {
#pragma unroll
        for (int i = 0; i < 4; ++i) dma16(rb, lb + (kk & 3) * A_STAGE + 16384 + lw * 4096 + i * 1024, vB, sB + (16 * (i & 1) + (i >> 1)) * 2048 + ((kk + rot) & 15) * 128);
    };
    TS(0);
    __builtin_amdgcn_s_setprio(PRIOL);
    stA(0); TL(53); stB(0); TL(54);
    dma16(mkdesc(gate_w, D_ * 2), lb + A_GW + (lw & 1) * 1024, lane * 16, (lw & 1) * 1024);
#pragma unroll
    for (int i = 0; i < 4; ++i) dma16(rx, lb + A_XT + lw * 4096 + i * 1024, (lane >> 1) * 32768 + (lane & 1) * 16, (i * 512 + lt) * 2048 + lw * 512 + pt * 32);
    const int gi = 8 * pt + 2 * lw + (lane >> 5);
    g.xa = lb + gi * 128 + ((((lane & 31) >> 2) ^ (gi & 7)) << 4) + (lane & 3) * 4;
    g.wa = lb + A_GW + (lane & 31) * 4; g.sum = 0.f; g.rot = rot;
    TL(55); stA(1); stB(1); TL(56); stA(2); stB(2);
    TS(6);
    constexpr int K0 = 0;
#pragma unroll 1
    for (int kt = K0; kt < 13; ++kt) { wait_vm<16>(); TL(8 + kt); BAR(); TL(28 + kt); if (kt == 0) { TS(28); TC(60); } g.rd(kt); stA(kt + 3); stB(kt + 3); g.acc(); }
    if (lw == 3) {
        const rsrc_t rs = mkrsrc(ws, WS_END);
        u32x4 fl[2];
        u32x2 tag = lld<u32x2>(smem + A_FLG + F_TAG); PIN(tag);
        auto publish = [&]() { sig(lb + A_FLG + F_EM, (wave_pkmax(pkmax(pkmax(fl[0].x, fl[0].z), pkmax(fl[1].x, fl[1].z))) & 0xffu) | 0x8000u); };
        wait_vm<16>(); BAR();
        asm volatile("buffer_load_dwordx4 %0, %2, %3, %4 offen sc1\n\tbuffer_load_dwordx4 %1, %2, %3, %4 offen offset:1024 sc1"
                     : "=&v"(fl[0]), "=&v"(fl[1]) : "v"(lane * 16), "s"(mkdesc(ws, WS_END)), "s"(OFF_FU + (gb & (NREP - 1)) * 2048) : "memory");
        g.rd(13); g.acc();
        wait_vm<10>(); BAR();
        asm volatile("s_waitcnt vmcnt(0)" : "+v"(fl[0]), "+v"(fl[1]) :: "memory");
        const bool ok = flag_ok(fl, tag);
        if (ok) publish();
        g.rd(14); g.acc();
        BAR(); TS(43);
        if (!ok) {
#pragma unroll 1
            for (int spin = 0; spin < (1 << 20); ++spin) { flag_get(rs, OFF_FU, gb, lane, fl); if (flag_ok(fl, tag)) break; }
            publish();
        }
    } else {
        wait_vm<16>(); TL(21); BAR(); TL(41); g.rd(13); g.acc();
        wait_vm<8>(); TL(22); BAR(); TL(42); g.rd(14); g.acc();
        wait_vm<0>(); TL(23); BAR(); TS(43); TC(61);
    }
    g.rd(15); g.acc();
    sig(lb + A_FLG + F_FREE, 1u << 20);
    return g.sum;
}

DEV unsigned ldw(rsrc_t r, unsigned vo, unsigned so) { return __builtin_amdgcn_raw_buffer_load_b32(r, vo, so, 0); }
DEV u32x4 w_pack(u32x4 re, u32x4 im, unsigned sel) {
    PIN(re); PIN(im);
#pragma unroll
    for (int r = 0; r < 4; ++r) re[r] = __builtin_amdgcn_perm(im[r], re[r], sel);
    return re;
}
DEV unsigned amax4(u32x4 p) { return pkmax(pkmax(p.x & 0x7fff7fffu, p.y & 0x7fff7fffu), pkmax(p.z & 0x7fff7fffu, p.w & 0x7fff7fffu)); }

DEV unsigned role_aux(const u16* __restrict__ Cre, const u16* __restrict__ Cim, const u16* __restrict__ w_r, const u16* __restrict__ w_i, const u16* __restrict__ lam_re, const u16* __restrict__ lam_im,
                  const u16* __restrict__ log_dt, const u16* __restrict__ Dskip, char* ws, lptr smem, int gb, int aw, int lane, u32x2 (&xc)[8],
                  u32x4 (&ut)[2] TSARG) {
    int lt, pt; tile_of(gb, lt, pt);
    int mp, j1; slot_of(gb, mp, j1);
    const int c = lane & 15, q = lane >> 4;
    const bool spc = mp == 0 && !(j1 & 1);
    const rsrc_t rs = mkrsrc(ws, WS_END), rr = mkrsrc(w_r, S_ * KF * 2), ri = mkrsrc(w_i, S_ * KF * 2);
    TS(0);
    __builtin_amdgcn_s_setprio(PRIOA);
    u32x4 fch, fcl, fsh, fsl;
    const bool m0 = 16 * aw + c == 0;
    {
        const float th = (float)(16 * aw + c) * (1.0f / 128.0f);
#pragma unroll
        for (int i = 0; i < 4; ++i) {
            const float t0 = __builtin_amdgcn_fractf(th * (float)(16 * (i >> 1) + 2 * (i & 1) + 4 * q)), t1 = t0 + th;
            const float c0 = __builtin_amdgcn_cosf(t0), c1 = __builtin_amdgcn_cosf(t1);
            const float s0 = m0 ? 1.f : -__builtin_amdgcn_sinf(t0), s1 = m0 ? -1.f : -__builtin_amdgcn_sinf(t1);
            const unsigned ch = pkb(c0, c1), sh = pkb(s0, s1);
            fch[i] = ch; fcl[i] = pkb(c0 - lo2f(ch), c1 - hi2f(ch));
            fsh[i] = sh; fsl[i] = pkb(s0 - lo2f(sh), s1 - hi2f(sh));
        }
        PIN(fch); PIN(fsh);
        if (FLO) { PIN(fcl); PIN(fsl); }
    }
    unsigned sg64 = (c & 1) ? SG : 0u, sgA = (c & 2) ? SG : 0u, sgB = ((c + 1) & 2) ? SG : 0u;
    constexpr unsigned WR = 128 * KF * 2;
    const int k1 = mp ? mp : spc ? (c >> 3) * 64 : (j1 == 1 ? 0 : 64);
    const int kd = j1 + 4 * (spc ? c & 7 : c) + 64 * (4 * aw + q);
    unsigned wo = aw < 2 ? (k1 * KF + kd) * 2 : ((128 - k1) * KF + 1023 - kd) * 2;
    unsigned wo0 = aw < 2 ? wo : k1 ? wo + 15 * WR : (1023 - kd) * 2;
    unsigned uoff = UL ? (lane >> 3) * 131072 + (lane & 7) * 16 + OFF_U + aw * 64 * 16384 + lt * 2048 + pt * 128
                       : (lane >> 2) * 65536 + (lane & 3) * 16 + OFF_U + aw * 64 * 16384 + lt * 1024 + pt * 64;
    PIN(sg64); PIN(sgA); PIN(sgB); PIN(wo); PIN(wo0); PIN(uoff);
    unsigned lru, liu, ldu, um = 0, em, pf = 0, pf1 = 0;
    u32x2 tag, dk[4];
    u32x4 wre[5], wim[5];
    wre[4] = (u32x4){0, 0, 0, 0}; wim[4] = wre[4];
    if (XM & 1) for (int i = 0; i < 4; ++i) { wre[i] = fch; wim[i] = fsh; }
    f32x4 vr[4], vi[4];
    float dt = 0.f, er = 0.f; float2 lam = make_float2(0.f, 0.f);
#pragma unroll
    for (int d1 = 0; d1 < 4; ++d1) { vr[d1] = Z4; vi[d1] = Z4; }
    auto wx = [&]() INL {
        if (j1 == 0 && (aw == 3 || (gb == XG && aw < 2))) {
            u32x4 o;
            if (aw == 3) {
                const int ks = (spc ? (lane >> 5) * 64 : mp) + 128 * c, ng = (S_ - ks) & (S_ - 1);
                o = (u32x4){(q & 1) ? (ng * KF + 511) * 2u : ks * KF * 2u, (q & 1) ? (ks * KF + 511) * 2u : ng * KF * 2u, S_ * KF * 2u, S_ * KF * 2u};
            } else {
                const int sg = c < 8 ? 1 : -1;
#pragma unroll
                for (int r = 0; r < 4; ++r) o[r] = ((sg * (64 * aw + 128 * (4 * q + r)) & (S_ - 1)) * KF + (sg * (32 + 64 * c) & 1023)) * 2;
            }
            PIN(o);
#pragma unroll
            for (int r = 0; r < 4; ++r) { wre[4][r] = ldw(rr, o[r], 0); wim[4][r] = ldw(ri, o[r], 0); }
        }
    };
    auto pfx = [&]() INL {
        const rsrc_t rc = mkrsrc((aw & 1) ? Cim : Cre, D_ * D_ * 2);
        const unsigned so = (256 * (lt & 3) + 16 * pt + 8 * (aw >> 1)) * 2048;
        if (PFH) pf = ldw(rc, (lane >> 3) * 2048 + (lane & 7) * 128, so + (PFH - 1) * 1024);
        else { pf = ldw(rc, lane * 128, so); pf1 = ldw(rc, lane * 128, so + 8192); }
    };
    auto prm = [&]() INL {
        lru = ldw(mkrsrc(lam_re, 2048), (lane >> 1) * 4, pt * 128); liu = ldw(mkrsrc(lam_im, 2048), (lane >> 1) * 4, pt * 128);
        ldu = ldw(mkrsrc(log_dt, 2048), (lane >> 1) * 4, pt * 128);
#pragma unroll
        for (int i = 0; i < 4; ++i) dk[i] = __builtin_amdgcn_raw_buffer_load_b64(mkrsrc(Dskip, D_ * 2), q * 8 + i * 512, pt * 32, 0);
    };
    if (PRS < 0) prm();
    if (WXS < 0) wx();
    if (PFS < 0) pfx();
    sfor<0, 16>([&](auto ic) INL {
        constexpr int kt = decltype(ic)::v;
        if (kt == UFS) wait_vm<0>();
        TL(8 + kt);
        BAR();
        if (kt == 15) { em = u_load(ws, smem, gb, aw, lane, ut); TS(2); }
        if (kt == PFS) pfx();
        if (kt == UFS && PFS < UFS) { PIN(pf); PIN(pf1); }
        u32x4 xa[4];
        if (kt < 4) {
            constexpr int KSO[4] = {0, 2, 3, 1};
#pragma unroll
            for (int i = 0; i < 4; ++i) xa[i] = trfrag(smem + A_XT + i * 4096 + KSO[kt & 3] * 1024 + lane * 8, 512);
        }
        if (kt == 4 || kt == 5) {
#pragma unroll
            for (int i = 0; i < 4; ++i) xc[4 * (kt & 1) + i] = lld<u32x2>(smem + A_XT + i * 4096 + (32 * aw + 16 * (kt & 1) + c) * 32 + q * 8);
        }
        if (kt >= 10 && !(XM & 1)) {
            constexpr int WRW[7] = WROW;
#pragma unroll
            for (int r = 0; r < 16; ++r) {
                if (r < WRW[kt < 10 ? 0 : kt - 10] || r >= WRW[kt < 10 ? 0 : kt - 9]) continue;
                const unsigned so = r ? (aw < 2 ? r : 15 - r) * WR : 0u;
                wre[r >> 2][r & 3] = ldw(rr, r ? wo : wo0, so); wim[r >> 2][r & 3] = ldw(ri, r ? wo : wo0, so);
            }
        }
        if (kt == PRS) prm();
        if (kt == WXS) wx();
        SCHED();
        if (kt < 4 && !(XM & 4)) {
#pragma unroll
            for (int i = 0; i < 4; ++i) { vr[i] = mfb(xa[i], fch, vr[i]); vi[i] = mfb(xa[i], fsh, vi[i]); PIN(vr[i]); PIN(vi[i]); }
        }
        if (kt == 0 || kt == 2) { fch ^= sg64; fsh ^= sg64; PIN(fch); PIN(fsh); if (FLO) { fcl ^= sg64; fsl ^= sg64; PIN(fcl); PIN(fsl); } }
        if (kt == 1) {
            const bool od = c & 1;
            const u32x4 a = (od ? fsh : fch) ^ sgA, b = (od ? fch : fsh) ^ sgB, al = (od ? fsl : fcl) ^ sgA, bl = (od ? fcl : fsl) ^ sgB;
            fch = a; fsh = b; PIN(fch); PIN(fsh);
            if (FLO) { fcl = al; fsl = bl; PIN(fcl); PIN(fsl); }
        }
        if (kt == 4) {
            const f32x4 t0r = vr[0] + vr[2], t1r = vr[0] - vr[2], t2r = vr[1] + vr[3], t3r = vr[1] - vr[3];
            const f32x4 t0i = vi[0] + vi[2], t1i = vi[0] - vi[2], t2i = vi[1] + vi[3], t3i = vi[1] - vi[3];
            vr[0] = t0r; vr[1] = t1r; vr[2] = t2r; vr[3] = t3r; vi[0] = t0i; vi[1] = t1i; vi[2] = t2i; vi[3] = t3i;
        }
        if (kt == 5) {
            const f32x4 t0r = vr[0], t1r = vr[1], t2r = vr[2], t3r = vr[3], t0i = vi[0], t1i = vi[1], t2i = vi[2], t3i = vi[3];
            vr[0] = t0r + t2r; vi[0] = t0i + t2i; vr[2] = t0r - t2r; vi[2] = t0i - t2i;
            vr[1] = t1r + t3i; vi[1] = t1i - t3r; vr[3] = t1r - t3i; vi[3] = t1i + t3r;
            if (aw == 0) { if (m0) { vr[1] = t1r; vi[1] = -t3r; vr[3] = t1i; vi[3] = t3i; } }
        }
        if (kt == 4 || kt == 5) {
#pragma unroll
            for (int i = 0; i < 4; ++i) { PIN(vr[i]); PIN(vi[i]); PIN(xc[4 * (kt & 1) + i]); }
        }
        if (kt == 6 || kt == 7) {
#pragma unroll
            for (int j = 2 * (kt & 1); j < 2 * (kt & 1) + 2; ++j) {
                PIN(vr[j]); PIN(vi[j]);
                const u32x4 o = quads(smem + A_XT + aw * 4096 + j * 1024, lane, (u32x2){pkb(vr[j][0], vr[j][1]), pkb(vr[j][2], vr[j][3])},
                                      (u32x2){pkb(vi[j][0], vi[j][1]), pkb(vi[j][2], vi[j][3])});
                if (!(XM & 2)) st128<SC1>(o, rs, uoff + j * (UL ? 32768 : 16384));
                um = pkmax(um, amax4(o));
                PIN(um);
            }
        }
        if (kt == 8) {
            um = wave_pkmax(pkmax(um, um >> 16));
            if (lane == 0) lst<unsigned>(smem + A_FLG + F_MAX + aw * 4, um);
        }
        if (kt == 8 || kt == 9) {
#pragma unroll
            for (int i = 4 * (kt & 1); i < 4 * (kt & 1) + 4; ++i) {
                const u32x2 d = dk[i & 3];
                xc[i] = (u32x2){pkb(lo2f(xc[i].x) * lo2f(d.x), hi2f(xc[i].x) * hi2f(d.x)), pkb(lo2f(xc[i].y) * lo2f(d.y), hi2f(xc[i].y) * hi2f(d.y))};
                PIN(xc[i]);
            }
        }
        if (kt == 8) { tag = lld<u32x2>(smem + A_FLG + F_TAG); PIN(tag); }
        if (kt == UFS && aw == 0) {
            const u32x4 m4 = lld<u32x4>(smem + A_FLG + F_MAX);
            const unsigned m = pkmax(pkmax(m4.x, m4.y), pkmax(m4.z, m4.w));
            flag_put(rs, OFF_FU, gb, (u32x2){(tag.x & 0xffff0000u) | ((m & 0xffffu) >> 7), tag.y}, lane);
        }
        if (kt == 9) {
            PIN(lru); PIN(liu); PIN(ldu);
            if (lane & 1) { lru >>= 16; liu >>= 16; ldu >>= 16; }
            PIN(lru); PIN(liu); PIN(ldu);
        }
        if (kt >= 10 && kt < 13 && aw == 1) {
            const float lr = lo2f(lru), li = lo2f(liu);
            if (kt == 10) { dt = bfr(expf(lo2f(ldu))); er = expf(dt * lr); PIN(dt); PIN(er); }
            if (kt == 11) { float sn, cs; sincosf(dt * li, &sn, &cs); lam = make_float2(er * cs, er * sn); PIN(lam.x); PIN(lam.y); }
            if (kt == 12) {
                const float nr = lam.x - 1.0f, ni = lam.y, den = lr * lr + li * li;
                const float2 coef = make_float2((nr * lr + ni * li) / den, (ni * lr - nr * li) / den);
                float2 l16 = lam;
#pragma unroll
                for (int i = 0; i < 4; ++i) l16 = cmul(l16, l16);
                float2 l128 = l16;
#pragma unroll
                for (int i = 0; i < 3; ++i) l128 = cmul(l128, l128);
                lst<f32x4>(smem + A_PRM + lane * 32, (f32x4){lam.x, lam.y, coef.x, coef.y});
                lst<f32x4>(smem + A_PRM + lane * 32 + 16, (f32x4){l16.x, l16.y, l128.x, l128.y});
            }
        }
        LDS_WAIT();
        SCHED();
    });
    unsigned mx = 0;
    const bool skip = j1 == 0 && !(aw & 1) && (lane & (spc ? 55 : 63)) == 0;
#pragma unroll
    for (int i = 0; i < 4; ++i) {
        const u32x4 p = w_pack(wre[i], wim[i], aw < 2 ? 0x05040100u : 0x07060302u);
        if (!skip) lst<u32x4>(smem + A_W + c * 1024 + i * 256 + (((4 * aw + q) ^ c) << 4), p);
        mx = pkmax(mx, amax4(p));
    }
    if (j1 == 0) {
        const u32x4 p = w_pack(wre[4], wim[4], aw == 3 && (q & 1) ? 0x07060302u : 0x05040100u);
        mx = pkmax(mx, amax4(p));
        const int it = spc ? (lane >> 5) * 8 : 0;
        if (aw == 3) lst<unsigned>(smem + A_W + it * 1024 + (c >> 2) * 256 + ((((q & 1) * 8) ^ it) << 4) + (c & 3) * 4,
                                  pkb(0.5f * (lo2f(p.x) + lo2f(p.y)), 0.5f * (hi2f(p.x) - hi2f(p.y))));
        else lst<u32x4>(smem + A_W + 16384 + aw * 1024 + lane * 16, p);
    }
    mx = wave_pkmax(pkmax(mx, mx >> 16));
    if (lane == 0) lst<unsigned>(smem + A_FLG + F_WM + aw * 4, mx);
    wait_vm<0>();
    PIN(pf); PIN(pf1);
    SCHED();
    return em;
}

DEV int role_mid(const u16* __restrict__ Cre, const u16* __restrict__ Cim, char* ws, lptr smem,
                 int gb, int w, int lane, float gsum, u32x2& tag, u32x4 (&ut)[2], unsigned em TSARG) {
    int lt, pt; tile_of(gb, lt, pt);
    int mp, j1; slot_of(gb, mp, j1);
    const int c = lane & 15, q = lane >> 4;
    const bool spc = mp == 0 && !(j1 & 1), xtr = gb == XG, ld = w >= 4;
    const rsrc_t rs = mkrsrc(ws, WS_END);
    const unsigned lb = ldsa(smem), fb = lb + A_FLG;
    u32x4 wv[2], mt[6];
    f32x4 tf[2]; f32x2 ti[2];
    tag = lld<u32x2>(smem + A_FLG + F_TAG);
#pragma unroll
    for (int i = 0; i < 2; ++i) {
        tf[i] = lld<f32x4>(smem + A_MAT + 9216 + c * 128 + q * 32 + i * 16);
        ti[i] = lld<f32x2>(smem + A_MAT + 9216 + (2 * w + i) * 128 + c * 8);
    }
    lptr mb = smem + A_MAT + lane * 16;
#pragma unroll
    for (int i = 0; i < 6; ++i) mt[i] = lld<u32x4>(mb + (i < 4 ? i : i + 2) * 1024);
    u32x4 sf0 = lld<u32x4>(mb + 4096), si0 = lld<u32x4>(mb + 5120);
    const u32x4 f0 = lld<u32x4>(mb + 8192);
    SCHED();
    if (spc && ld) { sf0 = mat_sf(64, c, q); si0 = mat_si(64, c, q); }
    const CM sf = cexp(sf0), si = cexp(si0), ff = cexp(f0), fi = cexp((u32x4){f0.x ^ SG, f0.y ^ SG, f0.z, f0.w});
#pragma unroll
    for (int i = 0; i < 6; ++i) PIN(mt[i]);
    PIN(ti[0]); PIN(ti[1]); PIN(tag);
    const int eu = min(max((int)(em & 0xffu), 4), 254);
    const float su = __uint_as_float((unsigned)(258 - eu) << 23);
    tf[0] *= su; tf[1] *= su; PIN(tf[0]); PIN(tf[1]);
    u32x2 gv = {0, 0};
    if (ld) {
        const float gg = bfr(1.0f / (1.0f + expf(-bfr(half_sum(gsum)))));
        gv = (u32x2){__float_as_uint(gg), __float_as_uint(bfr(1.0f - gg))};
        PIN(gv);
    }
    await<0>(fb + F_FREE, 8u << 20);
    asm volatile("s_waitcnt vmcnt(%2)" : "+v"(ut[0]), "+v"(ut[1]) : "i"((C0S == 15 ? 8 : 0) + (C1S == 15 ? 8 : 0)) : "memory");
    lptr ua = smem + A_XT + w * 2048;
#pragma unroll
    for (int i = 0; i < 2; ++i) lst<u32x4>(ua + i * 1024 + lane * 16, ut[i]);
    TS(3);
    asm volatile("" ::: "memory");
    {
        lptr ea = smem + A_E1 + c * E1P + q * 256 + 2 * w * 16;
#pragma unroll
        for (int i = 0; i < 2; ++i) ut[i] = trfrag(ua + i * 1024 + (lane >> 2) * 64 + (lane & 3) * 8, 32);
#pragma unroll
        for (int i = 0; i < 2; ++i) {
            f32x4 r = mfb(ut[i], mt[2], Z4), m = mfb(ut[i], mt[3], Z4);
            r = mfb(ut[i], mt[0], r); m = mfb(ut[i], mt[1], m);
            f32x4 yr, yi;
#pragma unroll
            for (int e = 0; e < 4; ++e) {
                const float tc = tf[e >> 1][2 * (e & 1)], ts = tf[e >> 1][2 * (e & 1) + 1];
                yr[e] = fmaf(r[e], tc, -m[e] * ts); yi[e] = fmaf(r[e], ts, m[e] * tc);
            }
            lst<u32x4>(ea + i * 16, pk8(yr, yi));
            if (xtr) {
                const unsigned sg = 0xbf803f80u;
                const f32x4 e = mfb(ut[i], c == 0 ? (u32x4){sg, sg, 0, 0} : (u32x4){0, 0, sg, sg}, Z4);
                if (c < 2) lst<u32x4>(ea + 16 * E1P + i * 16, pk8(e * su, Z4));
            }
        }
    }
    sig(fb + F_MID, 1u);
    if (ld) {
        const int gi = 8 * pt + 2 * (w - 4) + (lane >> 5), tau = (gi & 64) | ((gi & 12) << 2) | ((gi & 48) >> 2) | (gi & 3);
        if ((lane & 31) == 0) __builtin_amdgcn_raw_buffer_store_b64(gv, rs, (tau & 15) * 1024 + (lt * 8 + (tau >> 4)) * 8, OFF_G, SC1);
    }
    if (xtr && w == 0 && lane == 0) __builtin_amdgcn_raw_buffer_store_b64(tag, rs, 0, OFF_E, SC1);
    TS(4);
    await<0>(fb + F_MID, 24u);
    if (ld && C0S == 16) c_chunk(Cre, Cim, lb, pt, w - 4, lane, 0);
    if (ld && C1S == 16) c_chunk(Cre, Cim, lb, pt, w - 4, lane, 1);
    u32x4 dv[2];
#pragma unroll
    for (int i = 0; i < 2; ++i) dv[i] = lld<u32x4>(smem + A_E1 + (2 * w + i) * E1P + lane * 16);
    const u32x4 m4 = lld<u32x4>(smem + A_FLG + F_WM);
#pragma unroll
    for (int i = 0; i < 2; ++i) wv[i] = lld<u32x4>(smem + A_W + (2 * w + i) * 1024 + ((lane ^ (2 * w + i)) << 4));
#pragma unroll
    for (int i = 0; i < 2; ++i) dv[i] = pk8(mfh(dv[i], ff.re, Z4), mfh(dv[i], ff.im, Z4));
    const unsigned ew = min((pkmax(pkmax(m4.x, m4.y), pkmax(m4.z, m4.w)) & 0xffffu) >> 7, 245u);
    const float sw = __uint_as_float((246u - ew) << 23), swi = c < 8 ? sw : -sw;
    wv[0] = wcvt(wv[0], sw, swi); wv[1] = wcvt(wv[1], sw, swi);
#pragma unroll
    for (int i = 0; i < 2; ++i) dv[i] = wmul(mfh(sf.re, dv[i], Z4), mfh(sf.im, dv[i], Z4), wv[i]);
#pragma unroll
    for (int i = 0; i < 2; ++i) dv[i] = pk8(mfh(dv[i], si.re, Z4), mfh(dv[i], si.im, Z4));
    {
        f32x4 r[2], m[2];
#pragma unroll
        for (int i = 0; i < 2; ++i) {
            const f32x4 a = mfh(dv[i], fi.re, Z4), b = mfh(dv[i], fi.im, Z4);
#pragma unroll
            for (int e = 0; e < 4; ++e) { r[i][e] = fmaf(a[e], ti[i][0], b[e] * ti[i][1]); m[i][e] = fmaf(b[e], ti[i][0], -a[e] * ti[i][1]); }
        }
#pragma unroll
        for (int e = 0; e < 4; ++e)
            lst<u32x2>(smem + A_XT + (4 * q + e) * 1024 + (w >> 1) * 256 + c * 16 + (w & 1) * 8, (u32x2){pkh(r[0][e], r[1][e]), pkh(m[0][e], m[1][e])});
    }
    if (xtr && !(w & 3)) {
        const float dt = -(float)(32 + 64 * c) * (1.0f / 1024.0f);
        const CM f = cexp(cmat1(dt * (float)(4 * q), dt)), b = cexp(cmat1((float)(c * (32 + 256 * q)) * (1.0f / 1024.0f), (float)c * (1.0f / 16.0f)));
        u32x4 d = lld<u32x4>(smem + A_E1 + (16 + (w >> 2)) * E1P + lane * 16);
        d = pk8(mfh(d, f.re, Z4), mfh(d, f.im, Z4));
        d = wmul(mfh(sf.re, d, Z4), mfh(sf.im, d, Z4), wcvt(lld<u32x4>(smem + A_W + 16384 + w * 256 + lane * 16), sw, swi));
        d = pk8(mfh(d, si.re, Z4), mfh(d, si.im, Z4));
        const f32x4 r = mfh(d, b.re, Z4);
#pragma unroll
        for (int e = 0; e < 4; ++e) lst<float>(smem + A_EX + ((w >> 2) * 256 + (4 * q + e) * 16 + c) * 4, r[e]);
    }
    TS(5);
    if (!CRG && ld) { wait_vm<1>(); sig(fb + F_FULL, 0x11u); }
    sig(fb + F_MID, 1u);
    await<0>(fb + F_MID, 16u);
    {
        f32x4 xa[2], xb[2];
#pragma unroll
        for (int i = 0; i < 2; ++i) { xa[i] = Z4; xb[i] = Z4; }
        if (xtr) {
            const float sg = (c & 1) ? -0.125f : 0.125f;
#pragma unroll
            for (int i = 0; i < 2; ++i) {
                xa[i] = lld<f32x4>(smem + A_EX + (2 * w + i) * 64 + q * 16) * sg;
                xb[i] = lld<f32x4>(smem + A_EX + 1024 + (2 * w + i) * 64 + q * 16) * -sg;
            }
        }
#pragma unroll
        for (int i = 0; i < 2; ++i) {
            const int s2 = 2 * w + i;
            const u32x4 ea = lld<u32x4>(smem + A_XT + s2 * 1024 + lane * 16);
            const f32x4 ta = mfh(ea, mt[4], xa[i]), tb = mfh(ea, mt[5], xb[i]);
            u32x4 tq = quads(smem + A_XT + s2 * 1024, lane, (u32x2){pkh(ta[0], ta[1]), pkh(ta[2], ta[3])}, (u32x2){pkh(tb[0], tb[1]), pkh(tb[2], tb[3])});
            PIN(tq);
            if (TDL && i == 0) await<1>(fb + F_FULL, 8u);
            if (TL2) st128<SC1>(tq, rs, (lane >> 3) * 32768 + (lane & 7) * 16 + OFF_T + s2 * 262144 + j1 * 8192 + mp * 128);
            else st128<SC1>(tq, rs, (lane >> 2) * 16384 + (lane & 3) * 16 + OFF_T + s2 * 262144 + j1 * 4096 + mp * 64);
        }
    }
    TS(7);
    tag.x = (tag.x & 0xffff0000u) | ew;
    return eu - 267;
}

DEV void t_done(rsrc_t rs, unsigned fb, int gb, u32x2 tag, int lane) {
    int mp, j1; slot_of(gb, mp, j1);
    if (sig_rtn(fb + F_TC) == 7u) flag_put(rs, OFF_FT, (mp >> 3) * 32 + j1 * 8 + (mp & 7), tag, lane);
}

DEV void role_cload(const u16* __restrict__ Cre, const u16* __restrict__ Cim, char* ws, lptr smem, int gb, int lw, int lane, u32x2 tag TSARG) {
    int lt, pt; tile_of(gb, lt, pt);
    const unsigned lb = ldsa(smem), fb = lb + A_FLG;
    __builtin_amdgcn_s_setprio(3);
    const bool early = CDL ? lds_peek(fb + F_FULL) & 8u : lds_peek(fb + F_HC) & 4u;
    if (!early) {
        wait_vm<0>();
        t_done(mkrsrc(ws, WS_END), fb, gb, tag, lane);
        if (CDL) await<1>(fb + F_FULL, 8u); else await<2>(fb + F_HC, 4u);
    }
    c_chunk(Cre, Cim, lb, pt, lw, lane, 2); c_chunk(Cre, Cim, lb, pt, lw, lane, 3);
    if (early) {
        wait_vm<16>();
        t_done(mkrsrc(ws, WS_END), fb, gb, tag, lane);
    }
    TS(44);
    wait_vm<0>();
    sig(fb + F_FULL, 0x1100u);
    TS(47);
#pragma unroll 1
    for (int j = 4; j < 8; ++j) {
        await<2>(fb + F_FREE, 4u << (4 * (j - 4)));
        if (!(XM & 16)) c_chunk(Cre, Cim, lb, pt, lw, lane, j);
        wait_vm<8>();
        if (j > 4) sig(fb + F_FULL, 1u << (4 * (j - 1)));
        TS(44 + j);
    }
    wait_vm<0>();
    sig(fb + F_FULL, 1u << 28);
    TSE(1, lw == 0);
}

DEV void role_last(char* ws, lptr smem, int gb, int w, int lane, u32x2 tag, int fe, u32x2 (&xc)[8] TSARG) {
    int s2t, dat; tile_of(gb, s2t, dat);
    const int c = lane & 15, q = lane >> 4;
    const rsrc_t rs = mkrsrc(ws, WS_END);
    const unsigned fb = ldsa(smem + A_FLG);
    SCHED();
    u32x4 fc[2][2], fs[2][2];
#pragma unroll
    for (int n = 0; n < 2; ++n) {
        float dt = (float)(32 * w + 16 * n + c) * (1.0f / 128.0f); PIN(dt);
        float cc[8], ss[8];
        const float t0 = __builtin_amdgcn_fractf(dt * (float)(4 * q)), t1 = __builtin_amdgcn_fractf(dt * (float)(16 + 4 * q));
#pragma unroll
        for (int e = 0; e < 8; ++e) { const float f = fmaf((float)(e & 3), dt, e < 4 ? t0 : t1); cc[e] = __builtin_amdgcn_cosf(f); ss[e] = __builtin_amdgcn_sinf(f); }
        fc[n][0] = (u32x4){pkh(cc[0], cc[1]), pkh(cc[2], cc[3]), pkh(cc[4], cc[5]), pkh(cc[6], cc[7])};
        fs[n][0] = (u32x4){pkh(ss[0], ss[1]), pkh(ss[2], ss[3]), pkh(ss[4], ss[5]), pkh(ss[6], ss[7])};
        fc[n][1] = ((c & 1) ? fs[n][0] : fc[n][0]) ^ (((c + 1) & 2) ? SG : 0u);
        fs[n][1] = ((c & 1) ? fc[n][0] : fs[n][0]) ^ ((c & 2) ? SG : 0u);
        PIN(fc[n][0]); PIN(fs[n][0]); PIN(fc[n][1]); PIN(fs[n][1]);
        if (n == 0) { wait_vm<0>(); t_done(rs, fb, gb, tag, lane); TS(44); }
    }
    TS(45);
    __builtin_amdgcn_s_setprio(3);
    if (w == 0) {
        u32x4 fl[2];
        unsigned m = await<2>(fb + F_FULL, 8u) & 0x88888888u;
#pragma unroll 1
        for (int spin = 0; spin < (1 << 20) && m != 0x88888888u; ++spin) {
            m = h_poll<4>(rs, gb, lane, tag);
            sig_or(fb + F_FULL, m);
        }
        TS(50);
#pragma unroll 1
        for (int spin = 0; spin < (1 << 20); ++spin) { flag_get(rs, OFF_FT, gb, lane, fl); if (flag_ok(fl, tag)) break; }
        const unsigned em = wave_pkmax(pkmax(pkmax(fl[0].x, fl[0].z), pkmax(fl[1].x, fl[1].z))) & 0xffu;
#pragma unroll
        for (int i = 0; i < 2; ++i) {
            const float a = ldexpf(1.f, (int)(fl[i].x & 0xffu) - (int)em), b = ldexpf(1.f, (int)(fl[i].z & 0xffu) - (int)em);
            lst<u32x2>(smem + A_MAT + i * 512 + lane * 8, (u32x2){pkh(a, a), pkh(b, b)});
        }
        sig(fb + F_TR, em | 0x8000u);
    }
    fe += await<1>(fb + F_TR, 0x8000u) & 0xffu;
    TS(46);
    u32x4 tv[4]; u32x2 gg[2];
#pragma unroll
    for (int i = 0; i < 4; ++i)
        tv[i] = TL2 ? __builtin_amdgcn_raw_buffer_load_b128(rs, (lane >> 2) * 128 + (lane & 3) * 16 + (i & 1) * 2048, OFF_T + (s2t * 8 + (dat >> 1)) * 32768 + w * 8192 + (dat & 1) * 64 + (i >> 1) * 4096, SC1)
                    : __builtin_amdgcn_raw_buffer_load_b128(rs, lane * 16 + i * 1024, OFF_T + (s2t * 16 + dat) * 16384 + w * 4096, SC1);
#pragma unroll
    for (int n = 0; n < 2; ++n) gg[n] = __builtin_amdgcn_raw_buffer_load_b64(rs, c * 8 + n * 128, OFF_G + s2t * 1024 + w * 256, SC1);
    __builtin_amdgcn_s_setprio(PRIOR);
    unsigned ts[4];
#pragma unroll
    for (int i = 0; i < 4; ++i) ts[i] = lld<unsigned>(smem + A_MAT + i * 256 + (lane >> 5) * 128 + w * 32 + ((lane >> 2) & 7) * 4);
#pragma unroll
    for (int i = 0; i < 4; ++i) {
        tv[i] = __builtin_bit_cast(u32x4, __builtin_bit_cast(f16x8, tv[i]) * __builtin_bit_cast(f16x8, (u32x4){ts[i], ts[i], ts[i], ts[i]}));
        lst<u32x4>(smem + A_XT + w * 4096 + ((lane >> 1) & 1) * 2048 + i * 512 + (lane >> 2) * 32 + (lane & 1) * 16, tv[i]);
    }
    TS(47);
    sig(fb + F_TT, 1u);
    await<0>(fb + F_TT, 4u);
    f32x4 P0[2], P1[2], P2[2], P3a[2], P3b[2];
#pragma unroll
    for (int n = 0; n < 2; ++n) { P0[n] = Z4; P1[n] = Z4; P2[n] = Z4; P3a[n] = Z4; P3b[n] = Z4; }
    lptr tt = smem + A_XT + lane * 8;
#pragma unroll
    for (int h = 0; h < 2; ++h) {
        const u32x4 a1 = trfrag(tt + 1 * 4096 + h * 1024, 512), b1 = trfrag(tt + 1 * 4096 + 2048 + h * 1024, 512);
        const u32x4 a3 = trfrag(tt + 3 * 4096 + h * 1024, 512), b3 = trfrag(tt + 3 * 4096 + 2048 + h * 1024, 512);
        const u32x4 a0 = trfrag(tt + h * 1024, 512), b0 = trfrag(tt + 2048 + h * 1024, 512);
        const u32x4 a2 = trfrag(tt + 2 * 4096 + h * 1024, 512), b2 = trfrag(tt + 2 * 4096 + 2048 + h * 1024, 512);
#pragma unroll
        for (int n = 0; n < 2; ++n) {
            u32x4 cw = fc[n][h], sw_ = fs[n][h];
            P1[n] = mfh(a1, cw, P1[n]); P3a[n] = mfh(a1, sw_, P3a[n]); P3b[n] = mfh(b1, cw, P3b[n]); P1[n] = mfh(b1, sw_, P1[n]);
            if (h == 0) cw.x = q == 0 ? (cw.x & 0xffff0000u) | ((c & 1) ? 0xbc00u : 0x3c00u) : cw.x;
            P3a[n] = mfh(b3, cw, P3a[n]); P3b[n] = mfh(a3, sw_, P3b[n]); P1[n] = mfh(a3, cw, P1[n]); P1[n] = mfh(b3, sw_, P1[n]);
            if (h == 0) { cw.x = q == 0 ? (cw.x & 0xffff0000u) | 0x3800u : cw.x; sw_.x = q == 0 ? (sw_.x & 0xffff0000u) | ((c & 1) ? 0x3800u : 0xb800u) : sw_.x; }
            P0[n] = mfh(a0, cw, P0[n]); P2[n] = mfh(a2, cw, P2[n]); P0[n] = mfh(b0, sw_, P0[n]); P2[n] = mfh(b2, sw_, P2[n]);
        }
    }
    f32x4 r0[2][4];
#pragma unroll
    for (int n = 0; n < 2; ++n) {
        const float g = __uint_as_float(gg[n].x), og = __uint_as_float(gg[n].y);
        const f32x4 sp = P0[n] + P2[n], sm = P0[n] - P2[n], p3 = P3a[n] - P3b[n];
        const f32x4 av[4] = {sp + P1[n], sm - p3, sp - P1[n], sm + p3};
#pragma unroll
        for (int m = 0; m < 4; ++m) {
            const unsigned b0 = pkb(ldexpf(av[m][0], fe), ldexpf(av[m][1], fe)), b1 = pkb(ldexpf(av[m][2], fe), ldexpf(av[m][3], fe));
            const unsigned o0 = pkb(g * lo2f(b0), g * hi2f(b0)), o1 = pkb(g * lo2f(b1), g * hi2f(b1));
            const u32x2 p = xc[4 * n + m];
            r0[n][m] = (f32x4){fmaf(og, lo2f(p.x), lo2f(o0)), fmaf(og, hi2f(p.x), hi2f(o0)), fmaf(og, lo2f(p.y), lo2f(o1)), fmaf(og, hi2f(p.y), hi2f(o1))};
            PIN(r0[n][m]);
        }
    }
    TS(48);
    await<2>(fb + F_FREE, 4u << 16);
#pragma unroll
    for (int n = 0; n < 2; ++n) {
#pragma unroll
        for (int m = 0; m < 4; ++m) lst<f32x4>(smem + A_R0 + ((w * 2 + n) * 4 + m) * 1024 + (OQ ? q * 256 + ((c ^ (4 * m + q)) << 4) : lane * 16), r0[n][m]);
        if (q == 0) lst<float>(smem + A_AGG + (32 * w + 16 * n + c) * 4, 2.0f * __uint_as_float(gg[n].y));
    }
    sig(fb + F_R0, 1u);
    TS(49);
    TSE(w ? 4 : 5, w == 0 || w == 2);
}

NOPK __global__ __launch_bounds__(768, 3) void gm_kernel(const u16* __restrict__ x, const u16* __restrict__ w_r, const u16* __restrict__ w_i,
                                                         const u16* __restrict__ Bre, const u16* __restrict__ Bim, const u16* __restrict__ Cre,
                                                         const u16* __restrict__ Cim, const u16* __restrict__ lam_re,
                                                         const u16* __restrict__ lam_im, const u16* __restrict__ log_dt,
                                                         const u16* __restrict__ Dskip, const u16* __restrict__ gate_w, char* ws, float* __restrict__ out) {
    extern __shared__ __attribute__((aligned(16))) char smem_[];
    lptr smem = (lptr)smem_;
    u64 pc; int len;
    asm volatile("s_getpc_b64 %0\ngm_code_beg:\n\ts_mov_b32 %1, gm_code_end - gm_code_beg" : "=s"(pc), "=s"(len));
    const int wid = __builtin_amdgcn_readfirstlane(threadIdx.x >> 6), lane = threadIdx.x & 63;
    int w = wid & 7, gb = blockIdx.x;
    asm volatile("" : "+s"(w), "+s"(gb));
    if (wid < 4) role_mma(Cre, Cim, ws, out, smem, blockIdx.x, wid, lane, pc, len);
    else {
        TSD();
        float gsum = 0.f;
        u32x2 xc[8], tag;
        u32x4 ut[2];
        unsigned em;
        if (wid < 8) {
            gsum = role_load(x, Bre, Bim, gate_w, ws, smem, blockIdx.x, wid - 4, lane TSPASS);
            em = u_load(ws, smem, blockIdx.x, wid, lane, ut);
            TS(2);
            int lt, pt; tile_of(blockIdx.x, lt, pt);
            if (C0S == 15) c_chunk(Cre, Cim, ldsa(smem), pt, wid - 4, lane, 0);
            if (C1S == 15) c_chunk(Cre, Cim, ldsa(smem), pt, wid - 4, lane, 1);
#pragma unroll
            for (int i = 0; i < 8; ++i) asm volatile("" : "=v"(xc[i]));
        } else em = role_aux(Cre, Cim, w_r, w_i, lam_re, lam_im, log_dt, Dskip, ws, smem, blockIdx.x, wid - 8, lane, xc, ut TSPASS);
        const int fe = role_mid(Cre, Cim, ws, smem, gb, w, lane, gsum, tag, ut, em TSPASS);
        if (w >= 4) role_cload(Cre, Cim, ws, smem, gb, w - 4, lane, tag TSPASS);
        else role_last(ws, smem, gb, w, lane, tag, fe, xc TSPASS);
    }
}

extern "C" void kernel_launch(void* const* d_in, const int* in_sizes, int n_in, void* d_out, int out_size, void* d_ws, size_t ws_size,
                              hipStream_t stream) {
    (void)hipFuncSetAttribute((const void*)gm_kernel, hipFuncAttributeMaxDynamicSharedMemorySize, SMEM);
    gm_kernel<<<256, 768, SMEM, stream>>>((const u16*)d_in[0], (const u16*)d_in[1], (const u16*)d_in[2], (const u16*)d_in[6], (const u16*)d_in[7],
                                          (const u16*)d_in[8], (const u16*)d_in[9], (const u16*)d_in[4], (const u16*)d_in[5], (const u16*)d_in[10],
                                          (const u16*)d_in[11], (const u16*)d_in[12], (char*)d_ws, (float*)d_out);
}
```

```cpp
#include <hip/hip_runtime.h>
#include <stdint.h>

typedef __attribute__((ext_vector_type(8))) __bf16 bf16x8;
typedef __attribute__((ext_vector_type(2))) __bf16 bf16x2;
typedef __attribute__((ext_vector_type(8))) _Float16 f16x8;
typedef __attribute__((ext_vector_type(2))) _Float16 f16x2;
typedef __attribute__((ext_vector_type(4))) float f32x4;
typedef __attribute__((ext_vector_type(2))) float f32x2;
typedef __attribute__((ext_vector_type(4))) unsigned u32x4;
typedef __attribute__((ext_vector_type(2))) unsigned u32x2;
typedef __attribute__((ext_vector_type(4))) short s16x4;
typedef __attribute__((ext_vector_type(2))) unsigned short u16x2;
typedef unsigned short u16;
typedef unsigned long long u64;
#define GAS __attribute__((address_space(1)))
#define LAS __attribute__((address_space(3)))
#define NOPK __attribute__((target("no-packed-fp32-ops")))
#define DEV __device__ __forceinline__

constexpr int S_ = 2048, D_ = 1024, KF = 513;

constexpr unsigned OFF_H = 0;
constexpr unsigned OFF_U = OFF_H + (8u << 20);
constexpr unsigned OFF_T = OFF_U + (4u << 20);
constexpr unsigned OFF_G = OFF_T + (4u << 20);
constexpr unsigned OFF_FU = OFF_G + 16384u;
constexpr unsigned OFF_FT = OFF_FU + 32768u;
constexpr unsigned OFF_FH = OFF_FT + 32768u;
constexpr unsigned OFF_GRAN = OFF_FH + 1048576u;
constexpr unsigned OFF_E = OFF_GRAN + 16u * 1024u * 16u;
constexpr unsigned WS_END = OFF_E + 128u;

#define TL(i)
#define TSD()
#define TS(i)
#define TC(i)
#define TSE(slot, on)

DEV unsigned pkb(float a, float b) { f32x2 v = {a, b}; return __builtin_bit_cast(unsigned, __builtin_convertvector(v, bf16x2)); }
DEV unsigned pkh(float a, float b) { f32x2 v = {a, b}; return __builtin_bit_cast(unsigned, __builtin_convertvector(v, f16x2)); }
DEV float lo2f(unsigned p) { return __uint_as_float(p << 16); }
DEV float hi2f(unsigned p) { return __uint_as_float(p & 0xffff0000u); }
DEV float bfr(float f) { return lo2f(pkb(f, f)); }
DEV float2 cmul(float2 a, float2 b) { return make_float2(a.x * b.x - a.y * b.y, a.x * b.y + a.y * b.x); }
DEV float2 cfma(float2 a, float2 b, float2 c) { return make_float2(fmaf(a.x, b.x, fmaf(-a.y, b.y, c.x)), fmaf(a.x, b.y, fmaf(a.y, b.x, c.y))); }
DEV f32x4 mfb(u32x4 a, u32x4 b, f32x4 c) { return __builtin_amdgcn_mfma_f32_16x16x32_bf16(__builtin_bit_cast(bf16x8, a), __builtin_bit_cast(bf16x8, b), c, 0, 0, 0); }
DEV f32x4 mfh(u32x4 a, u32x4 b, f32x4 c) { return __builtin_amdgcn_mfma_f32_16x16x32_f16(__builtin_bit_cast(f16x8, a), __builtin_bit_cast(f16x8, b), c, 0, 0, 0); }
DEV unsigned pkmax(unsigned a, unsigned b) { return __builtin_bit_cast(unsigned, __builtin_elementwise_max(__builtin_bit_cast(u16x2, a), __builtin_bit_cast(u16x2, b))); }
typedef LAS char* lptr;
DEV unsigned ldsa(lptr p) { return (unsigned)(uintptr_t)p; }
template <class T> DEV T lld(lptr p) { return *(LAS const T*)p; }
template <class T> DEV void lst(lptr p, T v) { *(LAS T*)p = v; }
DEV u32x2 trread(lptr p) { return __builtin_bit_cast(u32x2, __builtin_amdgcn_ds_read_tr16_b64_v4i16((LAS s16x4*)p)); }
DEV u32x4 trfrag(lptr p, int second) { const u32x2 a = trread(p), b = trread(p + second); return (u32x4){a.x, a.y, b.x, b.y}; }
typedef __amdgpu_buffer_rsrc_t rsrc_t;
DEV u32x4 ldfrag(lptr tile, int row, int chunk) { return lld<u32x4>(tile + row * 128 + ((chunk ^ (row & 7)) << 4)); }
template <int N> DEV void wait_vm() { asm volatile("s_waitcnt vmcnt(%0)" :: "i"(N) : "memory"); }
#define LDS_WAIT() asm volatile("s_waitcnt lgkmcnt(0)" ::: "memory")
#define SCHED() __builtin_amdgcn_sched_barrier(0)
#define BAR() do { __builtin_amdgcn_s_barrier(); asm volatile("" ::: "memory"); } while (0)
#define PIN(x) asm volatile("" : "+v"(x))
template <int I> struct IC { static constexpr int v = I; };
template <int I, int N, class F> DEV void sfor(F&& f) { if constexpr (I < N) { f(IC<I>{}); sfor<I + 1, N>(f); } }
#define INL __attribute__((always_inline))
DEV rsrc_t mkrsrc(const void* p, unsigned n) { return __builtin_amdgcn_make_buffer_rsrc((void*)p, 0, n, 0x00020000); }
constexpr int SC1 = 16;
#define WTO 16
template <int AUX> DEV void st128(u32x4 v, rsrc_t rs, unsigned off) { __builtin_amdgcn_raw_buffer_store_b128(v, rs, off, 0, AUX); }
DEV void lds_add(unsigned a) { asm volatile("ds_add_u32 %0, %1" :: "v"(a), "v"(1u) : "memory"); }
DEV unsigned lds_peek(unsigned a) {
    unsigned v;
    asm volatile("ds_read_b32 %0, %1\n\ts_waitcnt lgkmcnt(0)" : "=v"(v) : "v"(a) : "memory");
    return __builtin_amdgcn_readfirstlane(v);
}
DEV void tile_of(int gb, int& ta, int& tb) {
    const int xcd = gb & 7, loc = gb >> 3;
    ta = (xcd >> 1) * 4 + (loc >> 3);
    tb = (xcd & 1) * 8 + (loc & 7);
}
DEV void rows4(float a, float& a0, float& a1, float& a2, float& a3) {
    const unsigned u = __float_as_uint(a);
    const auto r = __builtin_amdgcn_permlane32_swap(u, u, false, false);
    const auto p = __builtin_amdgcn_permlane16_swap(r[0], r[0], false, false);
    const auto s = __builtin_amdgcn_permlane16_swap(r[1], r[1], false, false);
    a0 = __uint_as_float(p[0]); a1 = __uint_as_float(p[1]); a2 = __uint_as_float(s[0]); a3 = __uint_as_float(s[1]);
}
DEV float rowsum4(float a) {
    const unsigned u = __float_as_uint(a);
    const auto r = __builtin_amdgcn_permlane32_swap(u, u, false, false);
    const unsigned t = __float_as_uint(__uint_as_float(r[0]) + __uint_as_float(r[1]));
    const auto p = __builtin_amdgcn_permlane16_swap(t, t, false, false);
    return __uint_as_float(p[0]) + __uint_as_float(p[1]);
}
DEV float2 sel4(int q, float2 a, float2 b, float2 c, float2 d) {
    PIN(d.x); PIN(d.y);
    const float2 e = (q & 1) ? b : a, f = (q & 1) ? d : c;
    return (q & 2) ? f : e;
}
template <int R> DEV unsigned ror16(unsigned v) { return __builtin_amdgcn_update_dpp(0u, v, 0x120 + R, 0xf, 0xf, false); }
DEV unsigned wave_pkmax(unsigned m) {
    m = pkmax(m, ror16<8>(m)); m = pkmax(m, ror16<4>(m)); m = pkmax(m, ror16<2>(m)); m = pkmax(m, ror16<1>(m));
    const auto r = __builtin_amdgcn_permlane32_swap(m, m, false, false);
    m = pkmax(r[0], r[1]);
    const auto p = __builtin_amdgcn_permlane16_swap(m, m, false, false);
    return pkmax(p[0], p[1]);
}
DEV float half_sum(float v) {
    v += __uint_as_float(ror16<8>(__float_as_uint(v))); v += __uint_as_float(ror16<4>(__float_as_uint(v)));
    v += __uint_as_float(ror16<2>(__float_as_uint(v))); v += __uint_as_float(ror16<1>(__float_as_uint(v)));
    const auto p = __builtin_amdgcn_permlane16_swap(__float_as_uint(v), __float_as_uint(v), false, false);
    return __uint_as_float(p[0]) + __uint_as_float(p[1]);
}
DEV u32x4 quads(lptr buf, int lane, u32x2 p, u32x2 n) {
    lptr wp = buf + (lane & 15) * 64 + ((lane >> 4) & 1) * 8;
    const int v = (lane >> 5) ^ ((lane >> 2) & 3);
    lst<u32x2>(wp + (v << 4), p); lst<u32x2>(wp + ((v ^ 2) << 4), n);
    asm volatile("" ::: "memory");
    return lld<u32x4>(buf + ((lane ^ (lane >> 4)) << 4));
}

#define PRIOA 2
#define NREP 8
#define PRIOM 1
#define PRIOT 2
#define PRIOG 2
#define AD 6
#define BD 7
#define WROW {0, 0, 0, 0, 6, 11, 16}
#define ROT (lt & 12)
#define PRIOL 3
#define XM 0
#define UFS 8
#define PRIOR 0
#define PRS 2
#define WXS 13
#define CPF 1
#define FST 8
#define HREP 8
#define NP1 1
#define PDL 48
#define PD0 32
#define CXM 6
#define CX(g) ((g) & CXM)
#define GSTEP {0, 0, 1, 1}
#define XS 2
#define B1S 2
#define US 3
#define UPS 1
#define DS 8
#define P0S 9
#define VL 1
#define PN -1
#define PG -1
#define SNK 1
#define HFIRST 8u
#define RDPAT {0, 1, 2, -1, 3, 4, -1, 5, 6, -1, 7, -1, -1, -1, -1, -1}
#define Z4 ((f32x4){0.f, 0.f, 0.f, 0.f})
constexpr unsigned SG = 0x80008000u;
struct CM { u32x4 re, im; };
DEV u32x4 cmat1(float t0, float dt) {
    float cc[4], ss[4];
    t0 = __builtin_amdgcn_fractf(t0);
#pragma unroll
    for (int e = 0; e < 4; ++e) { const float f = fmaf((float)e, dt, t0); cc[e] = __builtin_amdgcn_cosf(f); ss[e] = __builtin_amdgcn_sinf(f); }
    return (u32x4){pkh(ss[0], ss[1]), pkh(ss[2], ss[3]), pkh(cc[0], cc[1]), pkh(cc[2], cc[3])};
}
DEV CM cexp(u32x4 v) { CM m; m.re = (u32x4){v.z, v.w, v.x ^ SG, v.y ^ SG}; m.im = v; PIN(m.re); PIN(m.im); return m; }
DEV u32x4 pk8(f32x4 r, f32x4 i) { return (u32x4){pkh(r[0], r[1]), pkh(r[2], r[3]), pkh(i[0], i[1]), pkh(i[2], i[3])}; }
DEV u32x4 wmul(f32x4 r, f32x4 i, u32x4 w) {
    float yr[4], yi[4];
#pragma unroll
    for (int e = 0; e < 4; ++e) {
        const unsigned wu = w[e];
        const f16x2 h = __builtin_bit_cast(f16x2, wu);
        yr[e] = fmaf(r[e], (float)h[0], -i[e] * (float)h[1]); yi[e] = fmaf(r[e], (float)h[1], i[e] * (float)h[0]);
    }
    return (u32x4){pkh(yr[0], yr[1]), pkh(yr[2], yr[3]), pkh(yi[0], yi[1]), pkh(yi[2], yi[3])};
}
DEV u32x4 wcvt(u32x4 w, float sr, float si) {
#pragma unroll
    for (int e = 0; e < 4; ++e) w[e] = pkh(lo2f(w[e]) * sr, hi2f(w[e]) * si);
    PIN(w);
    return w;
}
DEV u32x4 mkdesc(const void* p, unsigned n) { const u64 a = (u64)p; return (u32x4){(unsigned)a, (unsigned)(a >> 32) & 0xffffu, n, 0x00020000u}; }
DEV void dma16(u32x4 d, unsigned l, unsigned vo, unsigned so) {
    asm volatile("s_nop 0\n\tbuffer_load_dwordx4 %0, %1, %2 offen lds" :: "v"(vo), "s"(d), "s"(so), "{m0}"(l) : "memory");
}
DEV void sig(unsigned a, unsigned v) { asm volatile("s_mov_b64 exec, 1\n\tds_add_u32 %0, %1\n\ts_mov_b64 exec, -1" :: "v"(a), "v"(v) : "memory"); }
DEV unsigned sig_rtn(unsigned a) {
    unsigned v;
    asm volatile("s_mov_b64 exec, 1\n\tds_add_rtn_u32 %0, %1, %2\n\ts_mov_b64 exec, -1\n\ts_waitcnt lgkmcnt(0)" : "=&v"(v) : "v"(a), "v"(1u) : "memory");
    return __builtin_amdgcn_readfirstlane(v);
}
template <int SLP> DEV unsigned await(unsigned a, unsigned mask) {
    unsigned v, n, r, t;
    asm volatile("s_mov_b32 %1, 0x400000\n"
                 "1:\tds_read_b32 %0, %4\n\ts_waitcnt lgkmcnt(0)\n\tv_readfirstlane_b32 %2, %0\n\ts_and_b32 %3, %2, %5\n\ts_cbranch_scc1 2f\n\t"
                 "s_sleep %6\n\ts_sub_u32 %1, %1, 1\n\ts_cbranch_scc0 1b\n"
                 "2:" : "=&v"(v), "=&s"(n), "=&s"(r), "=&s"(t) : "v"(a), "s"(mask), "i"(SLP) : "memory", "scc");
    return r;
}
DEV void flag_put(rsrc_t rs, unsigned off, int gb, u32x2 v, int lane) {
    if (lane < NREP) __builtin_amdgcn_raw_buffer_store_b64(v, rs, lane * 2048, off + gb * 8, SC1);
}
DEV void flag_get(rsrc_t rs, unsigned off, int gb, int lane, u32x4 (&fl)[2]) {
    unsigned o = lane * 16; PIN(o);
#pragma unroll
    for (int i = 0; i < 2; ++i) fl[i] = __builtin_amdgcn_raw_buffer_load_b128(rs, o + i * 1024, off + (gb & (NREP - 1)) * 2048, SC1);
}
DEV bool flag_ok(const u32x4 (&fl)[2], u32x2 tag) {
    bool ok = true;
#pragma unroll
    for (int i = 0; i < 2; ++i) ok = ok & ((fl[i].x ^ tag.x) < 0x10000u) & (fl[i].y == tag.y) & ((fl[i].z ^ tag.x) < 0x10000u) & (fl[i].w == tag.y);
    return __all(ok);
}
template <int N> DEV unsigned h_poll(rsrc_t rs, int gb, int lane, u32x2 tag) {
    unsigned o = lane * FST, m = 0; PIN(o);
    u32x2 fl[N];
#pragma unroll
    for (int i = 0; i < N; ++i) fl[i] = __builtin_amdgcn_raw_buffer_load_b64(rs, o, OFF_FH + (gb & (HREP - 1)) * 256 * FST + (i ^ (CX(gb) >> 1)) * 64 * FST, SC1);
#pragma unroll
    for (int i = 0; i < N; ++i) {
        const u64 b = __ballot(((fl[i].x ^ tag.x) < 0x10000u) & (fl[i].y == tag.y));
        m |= ((unsigned)b == ~0u ? 8u << (8 * i) : 0u) | ((unsigned)(b >> 32) == ~0u ? 128u << (8 * i) : 0u);
    }
    return m;
}
DEV void sig_or(unsigned a, unsigned v) { asm volatile("s_mov_b64 exec, 1\n\tds_or_b32 %0, %1\n\ts_mov_b64 exec, -1" :: "v"(a), "v"(v) : "memory"); }

constexpr int A_STAGE = 32768, A_XT = 4 * A_STAGE, A_PRM = A_XT + 16384, A_AGG = A_PRM + 2048, A_FLG = A_AGG + 512, A_GW = A_FLG + 128,
              A_MAT = A_GW + 2048, SMEM = A_MAT + 11264;
constexpr int E1P = 1040, A_EX = A_GW, A_W = 2 * A_STAGE, A_E1 = A_W + 19456, A_HS = 3 * A_STAGE + 16384, A_R0 = 0;
constexpr int F_MID = 8, F_EM = 12, F_MAX = 16, F_TAG = 32, F_FULL = 40, F_FREE = 44, F_HC = 48, F_HR = 52, F_TC = 56, F_TR = 60, F_TT = 64, F_R0 = 68, F_WM = 80;

constexpr int XG = 0xFF;
DEV void slot_of(int gb, int& mp, int& j1) { gb ^= XG; mp = (gb & 7) * 8 + (gb >> 5); j1 = (gb >> 3) & 3; }
DEV u32x4 mat_sf(int k1, int c, int q) { const float dt = -(float)(k1 + 128 * c) * (1.0f / 2048.0f); return cmat1(dt * (float)(4 * q), dt); }
DEV u32x4 mat_si(int k1, int c, int q) { return cmat1((float)((k1 + 512 * q) * c) * (1.0f / 2048.0f), (float)c * (1.0f / 16.0f)); }

DEV void mk_tables(lptr smem, int gb, int wid, int lane) {
    int mp, j1; slot_of(gb, mp, j1);
    const int c = lane & 15, q = lane >> 4;
    const bool spc = mp == 0 && !(j1 & 1);
    const int k1 = mp ? mp : (j1 == 3 ? 64 : 0);
    lptr mt = smem + A_MAT + lane * 16;
    if (wid == 0) {
        const float dt = -(float)(j1 + 4 * (spc ? c & 7 : c)) * (1.0f / 64.0f), t0 = __builtin_amdgcn_fractf(dt * (float)(4 * q));
        float cc[4], ss[4];
#pragma unroll
        for (int e = 0; e < 4; ++e) { const float f = fmaf((float)e, dt, t0); cc[e] = __builtin_amdgcn_cosf(f); ss[e] = __builtin_amdgcn_sinf(f); }
        const unsigned c0 = pkb(cc[0], cc[1]), c1 = pkb(cc[2], cc[3]), s0 = pkb(ss[0], ss[1]), s1 = pkb(ss[2], ss[3]);
        const unsigned c0l = pkb(cc[0] - lo2f(c0), cc[1] - hi2f(c0)), c1l = pkb(cc[2] - lo2f(c1), cc[3] - hi2f(c1));
        const unsigned s0l = pkb(ss[0] - lo2f(s0), ss[1] - hi2f(s0)), s1l = pkb(ss[2] - lo2f(s1), ss[3] - hi2f(s1));
        u32x4 m[4] = {(u32x4){c0, c1, s0 ^ SG, s1 ^ SG}, (u32x4){s0, s1, c0, c1}, (u32x4){c0l, c1l, s0l ^ SG, s1l ^ SG}, (u32x4){s0l, s1l, c0l, c1l}};
        if (spc) {
            m[0] = c < 8 ? (u32x4){c0, c1, 0, 0} : (u32x4){0, 0, c0, c1}; m[1] = c < 8 ? (u32x4){s0, s1, 0, 0} : (u32x4){0, 0, s0, s1};
            m[2] = c < 8 ? (u32x4){c0l, c1l, 0, 0} : (u32x4){0, 0, c0l, c1l}; m[3] = c < 8 ? (u32x4){s0l, s1l, 0, 0} : (u32x4){0, 0, s0l, s1l};
        }
#pragma unroll
        for (int i = 0; i < 4; ++i) lst<u32x4>(mt + i * 1024, m[i]);
    } else if (wid == 1) {
        const int jq = spc ? (4 * q) & 7 : 4 * q;
        float cc[4], ss[4];
        const float t0 = __builtin_amdgcn_fractf((float)(c * (j1 + 4 * jq)) * (1.0f / 64.0f));
#pragma unroll
        for (int e = 0; e < 4; ++e) {
            const float f = fmaf((float)c, (float)e * (1.0f / 16.0f), t0);
            const float wg = !spc || (gb == XG && jq + e == 0) ? 0.125f : 0.25f;
            cc[e] = __builtin_amdgcn_cosf(f) * wg; ss[e] = -__builtin_amdgcn_sinf(f) * wg;
        }
        const unsigned c0 = pkh(cc[0], cc[1]), c1 = pkh(cc[2], cc[3]), n0 = pkh(ss[0], ss[1]), n1 = pkh(ss[2], ss[3]);
        u32x4 mia = (u32x4){c0, n0, c1, n1}, mib = (u32x4){n0, c0 ^ SG, n1, c1 ^ SG};
        if (spc) { mib = q < 2 ? (u32x4){0, 0, 0, 0} : mia ^ SG; if (q >= 2) mia = (u32x4){0, 0, 0, 0}; }
        lst<u32x4>(mt + 6 * 1024, mia); lst<u32x4>(mt + 7 * 1024, mib);
        lst<u32x4>(mt + 8 * 1024, cmat1((float)(c * q) * -0.25f, (float)c * (-1.0f / 16.0f)));
    } else {
        lst<u32x4>(mt + (2 + wid) * 1024, wid == 2 ? mat_sf(k1, c, q) : mat_si(k1, c, q));
        const int ja = (spc ? 0 : 8 * (wid - 2)) + (lane >> 3);
        const float dt = -(float)(j1 + 4 * ja) * (1.0f / 1024.0f), t0 = dt * (float)(2 * (lane & 7)), t1 = t0 + dt;
        lst<f32x4>(mt + (7 + wid) * 1024, (f32x4){__builtin_amdgcn_cosf(t0), __builtin_amdgcn_sinf(t0), __builtin_amdgcn_cosf(t1), __builtin_amdgcn_sinf(t1)});
    }
}

DEV void c_chunk(const u16* __restrict__ Cre, const u16* __restrict__ Cim, unsigned lb, int gb, int pt, int lw, int lane, int j) {
    const u32x4 rc = mkdesc(lw < 2 ? Cre : Cim, D_ * D_ * 2);
    const unsigned vo = ((lane >> 5) * 256 + ((lane >> 3) & 3) * 4) * 2048 + (((lane & 7) ^ (lane >> 3)) << 4);
#pragma unroll
    for (int i = 0; i < 8; ++i)
        dma16(rc, lb + (j & 3) * A_STAGE + lw * 8192 + i * 1024, vo, (512 * (i & 1) + 16 * pt + (i >> 1)) * 2048 + (j ^ CX(gb)) * 256 + (lw & 1) * 128);
}

DEV void role_mma(char* ws, float* __restrict__ out, lptr smem, int gb, int wid, int lane, u64 pc, int len) {
    int lt, pt; tile_of(gb, lt, pt);
    const int wr = wid >> 1, wc = wid & 1, c = lane & 15, q = lane >> 4;
    const rsrc_t rs = mkrsrc(ws, WS_END);
    const unsigned fb = ldsa(smem + A_FLG);
    TSD(); TS(0);
    u64 tag = __hip_atomic_load((GAS u64*)(ws + OFF_E), __ATOMIC_RELAXED, __HIP_MEMORY_SCOPE_WORKGROUP) + 0x9E3779B97F4A7C15ull;
    unsigned cpf = 0;
    if (CPF == 1) { if (wid == 3) cpf = __builtin_amdgcn_raw_buffer_load_b32(mkrsrc((const void*)pc, max(len, 0)), lane * 128, ((gb >> 3) % 6) * 8192, 0); }
    if (CPF == 2) cpf = __builtin_amdgcn_raw_buffer_load_b32(mkrsrc((const void*)pc, max(len, 0)), (lane & 15) * 128 + wid * 2048, ((gb >> 3) % 6) * 8192, 0);
    if (CPF == 3) { if (lane < 3) cpf = __builtin_amdgcn_raw_buffer_load_b32(mkrsrc((const void*)pc, max(len, 0)), lane * 128 + wid * 384, (gb >> 3) * 1536, 0); }
    SCHED();
    if (wid == 0 && lane < 32) lst<unsigned>(smem + A_FLG + lane * 4, 0u);
    mk_tables(smem, gb, wid, lane);
    f32x4 acc[4][4];
#pragma unroll
    for (int m = 0; m < 4; ++m)
#pragma unroll
        for (int n = 0; n < 4; ++n) acc[m][n] = Z4;
    u32x4 af[2][4], bf_[2][4];
#pragma unroll
    for (int m = 0; m < 4; ++m) { af[1][m] = (u32x4){0, 0, 0, 0}; bf_[1][m] = af[1][m]; }
    PIN(tag); PIN(cpf);
    if (wid == 0 && lane == 0) lst<u64>(smem + A_FLG + F_TAG, tag);
    tag = (u64)(unsigned)__builtin_amdgcn_readfirstlane((unsigned)tag) | (u64)(unsigned)__builtin_amdgcn_readfirstlane((unsigned)(tag >> 32)) << 32;
    __builtin_amdgcn_s_waitcnt(0xC07F);
    TS(13);
#pragma unroll 4
    for (int kt = 0; kt < 16; ++kt) {
        BAR();
        lptr tA = smem + (kt & 3) * A_STAGE;
        lptr tB = tA + 16384;
#pragma unroll
        for (int hf = 0; hf < 2; ++hf)
#pragma unroll
            for (int i = 0; i < 16; ++i) {
                const int nn = SNK && (i & 4) ? 3 - (i & 3) : i & 3;
                acc[i >> 2][nn] = mfb(af[1 - hf][i >> 2], bf_[1 - hf][nn], acc[i >> 2][nn]);
                constexpr int RD[16] = RDPAT;
                const int r = RD[i];
                if (r == 0) af[hf][0] = ldfrag(tA, wr * 64 + c, 4 * hf + q);
                else if (r > 0 && r < 5) bf_[hf][r - 1] = ldfrag(tB, wc * 64 + (r - 1) * 16 + c, 4 * hf + q);
                else if (r >= 5) af[hf][r - 4] = ldfrag(tA, wr * 64 + (r - 4) * 16 + c, 4 * hf + q);
                SCHED();
                if (PN >= 0) { asm volatile("s_nop %0" :: "i"(r >= 0 ? (PN > 0 ? PN - 1 : 0) : (PN >= 0 ? PN : 0))); SCHED(); }
            }
        LDS_WAIT();
    }
    __builtin_amdgcn_s_setprio(PRIOT);
    sig(fb + F_FREE, 1u << 20);
    float2 lam[2], coef[2], l16[2], l128[2];
#pragma unroll
    for (int n = 0; n < 2; ++n) {
        const f32x4 a = lld<f32x4>(smem + A_PRM + (wc * 32 + 2 * c + n) * 32), b = lld<f32x4>(smem + A_PRM + (wc * 32 + 2 * c + n) * 32 + 16);
        lam[n] = make_float2(a[0], a[1]); coef[n] = make_float2(a[2], a[3]); l16[n] = make_float2(b[0], b[1]); l128[n] = make_float2(b[2], b[3]);
    }
#pragma unroll
    for (int m = 0; m < 4; ++m)
#pragma unroll
        for (int n = 0; n < 4; ++n) acc[m][n] = mfb(af[1][m], bf_[1][n], acc[m][n]);
    float2 l32[2], l64[2];
#pragma unroll
    for (int n = 0; n < 2; ++n) { l32[n] = cmul(l16[n], l16[n]); l64[n] = cmul(l32[n], l32[n]); }
    TS(1);
    float2 h[2][16];
#pragma unroll
    for (int n = 0; n < 2; ++n) h[n][0] = make_float2(acc[0][n][0], acc[0][2 + n][0]);
#pragma unroll
    for (int t = 1; t < 16; ++t)
#pragma unroll
        for (int n = 0; n < 2; ++n) h[n][t] = cfma(lam[n], h[n][t - 1], make_float2(acc[t >> 2][n][t & 3], acc[t >> 2][2 + n][t & 3]));
    float2 loc[2], wag[2], wq[2];
#pragma unroll
    for (int n = 0; n < 2; ++n) {
        float2 a0, a1, a2, a3;
        rows4(h[n][15].x, a0.x, a1.x, a2.x, a3.x);
        rows4(h[n][15].y, a0.y, a1.y, a2.y, a3.y);
        const float2 s2 = cfma(l16[n], a0, a1), s3 = cfma(l16[n], s2, a2);
        wag[n] = cfma(l16[n], s3, a3);
        loc[n] = sel4(q, make_float2(0.f, 0.f), a0, s2, s3);
    }
    lptr aggp = smem + A_AGG + (wc * 16 + c) * 16;
    const unsigned flg = ldsa(smem + A_FLG) + wc * 4;
    float2 W0[2] = {make_float2(0.f, 0.f), make_float2(0.f, 0.f)};
    if (wr == 0) {
        if (q == 0) lst<f32x4>(aggp, (f32x4){wag[0].x, wag[0].y, wag[1].x, wag[1].y});
        asm volatile("" ::: "memory");
        if (lane == 0) lds_add(flg);
    } else {
#pragma unroll 1
        for (int spin = 0; spin < (1 << 20); ++spin) { if (lds_peek(flg)) break; }
        const f32x4 w0 = lld<f32x4>(aggp);
        W0[0] = make_float2(w0[0], w0[1]); W0[1] = make_float2(w0[2], w0[3]);
        const float2 g0 = cfma(l64[0], W0[0], wag[0]), g1 = cfma(l64[1], W0[1], wag[1]);
        if (lt < 15 && q < 2) {
            const float2 gg = q == 0 ? g0 : g1;
            st128<SC1>((u32x4){__float_as_uint(gg.x), (unsigned)tag, __float_as_uint(gg.y), (unsigned)(tag >> 32)},
                       rs, (2 * c + q) * 16 + OFF_GRAN + ((15 - lt) * 1024 + pt * 64 + wc * 32) * 16);
        }
    }
    TS(2);
#pragma unroll
    for (int n = 0; n < 2; ++n) { PIN(lam[n].x); PIN(lam[n].y); PIN(coef[n].x); PIN(coef[n].y); PIN(l16[n].x); PIN(l16[n].y); PIN(l128[n].x); PIN(l128[n].y); }
    float2 pw[2][8], l8[2], lq[2], l512[2];
#pragma unroll
    for (int n = 0; n < 2; ++n) {
        wq[n] = sel4(q, make_float2(1.f, 0.f), l16[n], l32[n], cmul(l32[n], l16[n]));
        loc[n] = cfma(wq[n], W0[n], loc[n]);
        if (wr) wq[n] = cmul(wq[n], l64[n]);
        pw[n][0] = cmul(coef[n], lam[n]);
        l8[n] = cmul(lam[n], lam[n]); l8[n] = cmul(l8[n], l8[n]); l8[n] = cmul(l8[n], l8[n]);
        const float2 l256 = cmul(l128[n], l128[n]);
        lq[n] = sel4(q, make_float2(1.f, 0.f), l128[n], l256, cmul(l256, l128[n]));
        l512[n] = cmul(l256, l256);
    }
#pragma unroll
    for (int n = 0; n < 2; ++n) {
        PIN(wq[n].x); PIN(wq[n].y); PIN(loc[n].x); PIN(loc[n].y); PIN(l8[n].x); PIN(l8[n].y); PIN(lq[n].x); PIN(lq[n].y); PIN(l512[n].x); PIN(l512[n].y);
    }
#pragma unroll
    for (int t = 0; t < 8; ++t) {
#pragma unroll
        for (int n = 0; n < 2; ++n) {
            if (t) pw[n][t] = cmul(pw[n][t - 1], lam[n]);
            h[n][2 * t] = cmul(coef[n], h[n][2 * t]); h[n][2 * t + 1] = cmul(coef[n], h[n][2 * t + 1]);
        }
#pragma unroll
        for (int n = 0; n < 2; ++n) { PIN(pw[n][t].x); PIN(pw[n][t].y); PIN(h[n][2 * t].x); PIN(h[n][2 * t].y); PIN(h[n][2 * t + 1].x); PIN(h[n][2 * t + 1].y); }
        SCHED();
    }
    await<0>(fb + F_FREE, 8u << 20);
    TS(3);
    if (lt > 0) {
        u32x4 gr[4][2];
        unsigned off = ((16 - lt + q) * 1024 + pt * 64 + wc * 32 + 2 * c) * 16;
#pragma unroll 1
        for (int spin = 0; spin < (1 << 20); ++spin) {
            bool ok = true;
            PIN(off);
#pragma unroll
            for (int j = 0; j < 4; ++j) {
                const rsrc_t rg = mkrsrc(ws + OFF_GRAN + j * 65536, (4 - j) * 65536);
                gr[j][0] = __builtin_amdgcn_raw_buffer_load_b128(rg, off, 0, SC1);
                gr[j][1] = __builtin_amdgcn_raw_buffer_load_b128(rg, off + 16, 0, SC1);
            }
#pragma unroll
            for (int j = 0; j < 4; ++j) {
                const bool v = lt - 1 - (4 * j + q) >= 0;
#pragma unroll
                for (int n = 0; n < 2; ++n) ok = ok & (!v | ((gr[j][n].y == (unsigned)tag) & (gr[j][n].w == (unsigned)(tag >> 32))));
            }
            if (__all(ok)) break;
            asm volatile("" ::: "memory");
        }
#pragma unroll
        for (int n = 0; n < 2; ++n) {
            float2 cs = make_float2(__uint_as_float(gr[3][n].x), __uint_as_float(gr[3][n].z));
#pragma unroll
            for (int j = 2; j >= 0; --j) cs = cfma(l512[n], cs, make_float2(__uint_as_float(gr[j][n].x), __uint_as_float(gr[j][n].z)));
            cs = cmul(cs, lq[n]);
            cs.x = rowsum4(cs.x); cs.y = rowsum4(cs.y);
            loc[n] = cfma(wq[n], cs, loc[n]);
        }
    }
    TS(4);
    lptr tb = smem + A_HS + wid * 4096;
    PIN(lane);
#pragma unroll
    for (int hf = 0; hf < 2; ++hf) {
        const float2 s0 = hf ? cmul(l8[0], loc[0]) : loc[0], s1 = hf ? cmul(l8[1], loc[1]) : loc[1];
#pragma unroll
        for (int t = 0; t < 8; ++t) {
            const float2 v0 = cfma(pw[0][t], s0, h[0][8 * hf + t]), v1 = cfma(pw[1][t], s1, h[1][8 * hf + t]);
            lst<unsigned>(tb + t * 256 + lane * 4, pkb(v0.x, v1.x));
            lst<unsigned>(tb + 2048 + t * 256 + lane * 4, pkb(-v0.y, -v1.y));
        }
        asm volatile("" ::: "memory");
        lptr rb = tb + (lane >> 4) * 256 + (lane & 3) * 64 + ((lane >> 2) & 3) * 16;
        const unsigned hoff = OFF_H + ((lane >> 4) * 64 + pt * 2 + wc) * 8192 + lt * 512 + wr * 256 + (lane & 15) * 16;
#pragma unroll
        for (int v = 0; v < 2; ++v)
#pragma unroll
            for (int pl = 0; pl < 2; ++pl)
                st128<SC1>(lld<u32x4>(rb + pl * 2048 + v * 1024), rs, hoff + ((2 * hf + v) * 256 + pl * 32) * 8192);
        asm volatile("" ::: "memory");
    }
    TS(5);
    wait_vm<0>();
    TS(6);
    const bool pub = sig_rtn(fb + F_HC) == 3u;
    if (pub) {
        const u32x2 tg = {(unsigned)tag, (unsigned)(tag >> 32)};
        if (lane < HREP) __builtin_amdgcn_raw_buffer_store_b64(tg, rs, lane * 256 * FST, OFF_FH + (pt * 16 + lt) * FST, SC1);
        TS(15);
        int spin; unsigned m = 0;
        if (PDL) __builtin_amdgcn_s_sleep(PDL);
        if (PD0 && lt == 0) __builtin_amdgcn_s_sleep(PD0);
        TS(19);
#pragma unroll 1
        for (spin = 0; spin < (1 << 20); ++spin) {
            m = h_poll<NP1>(rs, gb, lane, tg);
            if (spin == 0) TS(16);
            if (spin == 1) TS(17);
            if (spin == 2) TS(18);
            if ((m & HFIRST) == HFIRST) break;
        }
        sig_or(fb + F_FULL, m);
    } else await<1>(fb + F_FULL, 8u);
    __builtin_amdgcn_s_setprio(PRIOG);
    TS(7);
    PIN(lane);
    const unsigned vo = ((lane & 15) >> 2) * 256 + (lane >> 4) * 64 + (lane & 3) * 16, sH = OFF_H + lt * 524288 + wid * 2048;
    auto ldB = [&](int i, int n) { return __builtin_amdgcn_raw_buffer_load_b128(rs, vo + n * 1024, sH + ((i >> 3) ^ CX(gb)) * 32768 + ((i & 3) + (i & 4) * 8) * 8192, SC1); };
    lptr ab[2];
#pragma unroll
    for (int i = 0; i < 2; ++i) { ab[i] = smem + (lane & 15) * 128 + (((4 * i + (lane >> 4)) ^ (lane & 7)) << 4); PIN(ab[i]); }
    auto ldA = [&](int f) {
        const int a = (f >> 2) & 1;
        if ((f & 63) < 8 && f > 7 && !(f & 3)) { ab[a] += (f & 64) ? 2 * A_STAGE : -2 * A_STAGE; PIN(ab[a]); }
        return lld<u32x4>(ab[a] + ((f >> 3) & 7) * 8192 + (f & 3) * 2048);
    };
    u32x4 A[AD], B[BD][2];
#pragma unroll
    for (int i = 0; i < BD; ++i) { B[i][0] = ldB(i, 0); B[i][1] = ldB(i, 1); }
    SCHED();
    unsigned fv = fb; PIN(fv);
    await<0>(fv + F_FULL, 4u);
#pragma unroll
    for (int f = 0; f < AD; ++f) A[f] = ldA(f);
    f32x4 ac[4][2];
    unsigned fe = 0, fs = 0;
#pragma unroll
    for (int e = 0; e < 8; ++e) ac[e >> 1][e & 1] = Z4;
    SCHED();
    sfor<0, 64>([&](auto ic) INL {
        constexpr int i = decltype(ic)::v;
#pragma unroll
        for (int k = 0; k < 4; ++k) {
            const int f = 4 * i + k;
            ac[k][0] = mfb(B[i % BD][0], A[f % AD], ac[k][0]);
            SCHED();
            if (k == 2 && ((i + BD + 1) & 7) == 0 && i + BD + 1 < 64) fe = *(volatile LAS unsigned*)(smem + A_FLG + F_FULL);
            if (k == 3 && ((i + BD) & 7) == 0 && i + BD < 64) {
                fs = __builtin_amdgcn_readfirstlane(fe);
                if (!(fs & (8u << (4 * (((i + BD) >> 3) & 7))))) fs = await<0>(fv + F_FULL, 8u << (4 * (((i + BD) >> 3) & 7)));
                asm volatile("" ::: "memory");
                TS(20 + ((i + BD) >> 3));
            }
            if (k == 3 && i + BD < 64) B[i % BD][0] = ldB(i + BD, 0);
            if (k == 1 && i > 0 && i - 1 + BD < 64) B[(i + BD - 1) % BD][1] = ldB(i - 1 + BD, 1);
            SCHED();
            if (PG >= 0) { asm volatile("s_nop %0" :: "i"(PG >= 0 ? PG : 0)); SCHED(); }
            ac[k][1] = mfb(B[i % BD][1], A[f % AD], ac[k][1]);
            SCHED();
            if (PG >= 0) { asm volatile("s_nop %0" :: "i"(PG >= 0 ? PG : 0)); SCHED(); }
            if (f + AD < 256) {
                if (((f + AD) & 31) == 0) {
                    if (!(fs & (4u << (4 * ((f + AD) >> 5))))) await<0>(fv + F_FULL, 4u << (4 * ((f + AD) >> 5)));
                    asm volatile("" ::: "memory");
                }
                A[f % AD] = ldA(f + AD);
            }
            SCHED();
        }
        if ((i & 7) == 7 && i < 40) sig(fv + F_FREE, 1u << (4 * (i >> 3)));
        if (i == 0) { TS(8); TC(60); }
        if (i == 31) TS(9);
        if (i == 60) { TS(14); TC(61); }
    });
    TS(10);
    await<0>(fv + F_R0, 4u);
    TS(11);
    const rsrc_t ro = mkrsrc(out, S_ * D_ * 4);
    PIN(lane);
    const int c2 = lane & 15, q2 = lane >> 4;
    const unsigned oo = ((64 * q2 + 512 * wid + lt) * D_ + 256 * (c2 >> 2) + 16 * pt + 4 * (c2 & 3)) * 4;
#pragma unroll
    for (int n = 0; n < 2; ++n) {
        const f32x4 o2 = lld<f32x4>(smem + A_AGG + (32 * wid + 16 * n + 4 * q2) * 4);
#pragma unroll
        for (int e = 0; e < 4; ++e) {
            const f32x4 r0 = lld<f32x4>(smem + A_R0 + (wid * 2 + n) * 4096 + c2 * 256 + (((4 * q2 + e) ^ c2) << 4));
            const f32x4 o = {fmaf(o2[e], ac[0][n][e], r0[0]), fmaf(o2[e], ac[1][n][e], r0[1]), fmaf(o2[e], ac[2][n][e], r0[2]), fmaf(o2[e], ac[3][n][e], r0[3])};
            st128<WTO>(__builtin_bit_cast(u32x4, o), ro, oo + (256 * n + 16 * e) * D_ * 4);
        }
    }
    asm volatile("gm_code_end:");
    TS(12);
    TSE(0, wid == 0);
    TSE(2, pub);
}

#define TSARG
#define TSPASS
struct Gate {
    unsigned xa, wa, xv, gw; float sum; int rot;
    DEV void rd(int kt) { asm volatile("ds_read_b32 %0, %2\n\tds_read_b32 %1, %3" : "=&v"(xv), "=&v"(gw) : "v"(xa + (kt & 3) * A_STAGE), "v"(wa + (kt ^ rot) * 128) : "memory"); }
    DEV void acc() {
        asm volatile("s_waitcnt lgkmcnt(0)" : "+v"(xv), "+v"(gw) :: "memory");
        const unsigned pr = pkb(lo2f(xv) * lo2f(gw), hi2f(xv) * hi2f(gw));
        sum += lo2f(pr); sum += hi2f(pr); PIN(sum);
    }
};
DEV unsigned u_load(char* ws, lptr smem, int gb, int w, int lane, u32x4 (&ut)[2]) {
    int mp, j1; slot_of(gb, mp, j1);
    __builtin_amdgcn_s_setprio(PRIOM);
    const unsigned em = await<1>(ldsa(smem + A_FLG + F_EM), 0x8000u);
    asm volatile("buffer_load_dwordx4 %0, %2, %3, %4 offen sc1\n\tbuffer_load_dwordx4 %1, %2, %3, %4 offen offset:2048 sc1"
                 : "=&v"(ut[0]), "=&v"(ut[1]) : "v"((lane >> 2) * 128 + (lane & 3) * 16), "s"(mkdesc(ws, WS_END)),
                   "s"(OFF_U + ((mp >> 1) * 4 + j1) * 32768 + (mp & 1) * 64 + w * 4096) : "memory");
    return em;
}

DEV float role_load(const u16* __restrict__ x, const u16* __restrict__ Bre, const u16* __restrict__ Bim,
                    const u16* __restrict__ gate_w, char* ws, lptr smem, int gb, int lw, int lane TSARG) {
    __builtin_amdgcn_s_waitcnt(0x0F70);
    Gate g;
    int lt, pt; tile_of(gb, lt, pt);
    const int srow = lane >> 3, schunk = (lane & 7) ^ srow;
    const int rot = ROT;
    const u32x4 rx = mkdesc(x, S_ * D_ * 2), rb = mkdesc((lw & 1) ? Bim : Bre, D_ * D_ * 2);
    const unsigned lb = ldsa(smem);
    const unsigned vA = (16 * (srow >> 2) + (srow & 3)) * 2048 + schunk * 16, sA = (lt * 128 + 64 * (lw >> 1) + 8 * (lw & 1)) * 2048;
    const unsigned vB = 2 * srow * 2048 + schunk * 16, sB = (pt * 64 + 32 * (lw >> 1)) * 2048;
    auto stA = [&](int kk) {
#pragma unroll
        for (int i = 0; i < 4; ++i)
            dma16(rx, lb + (kk & 3) * A_STAGE + lw * 4096 + i * 1024, vA, sA + (32 * (i & 1) + 4 * (i >> 1)) * 2048 + (kk ^ rot) * 128);
    };
    auto stB = [&](int kk) {
#pragma unroll
        for (int i = 0; i < 4; ++i)
            dma16(rb, lb + (kk & 3) * A_STAGE + 16384 + lw * 4096 + i * 1024, vB, sB + (16 * (i & 1) + (i >> 1)) * 2048 + (kk ^ rot) * 128);
    };
    TS(0);
    __builtin_amdgcn_s_setprio(PRIOL);
    stA(0); TL(53); stB(0); TL(54);
    dma16(mkdesc(gate_w, D_ * 2), lb + A_GW + (lw & 1) * 1024, lane * 16, (lw & 1) * 1024);
#pragma unroll
    for (int i = 0; i < 4; ++i)
        dma16(rx, lb + A_XT + lw * 4096 + i * 1024, (lane >> 1) * 32768 + (lane & 1) * 16, (i * 512 + lt) * 2048 + lw * 512 + pt * 32);
    const int gi = 8 * pt + 2 * lw + (lane >> 5);
    g.xa = lb + gi * 128 + ((((lane & 31) >> 2) ^ (gi & 7)) << 4) + (lane & 3) * 4;
    g.wa = lb + A_GW + (lane & 31) * 4; g.sum = 0.f; g.rot = rot;
    TL(55); stA(1); stB(1); TL(56); stA(2); stB(2);
    TS(6);
    constexpr int K0 = 0;
#pragma unroll 1
    for (int kt = K0; kt < 13; ++kt) { wait_vm<16>(); TL(8 + kt); BAR(); TL(28 + kt); if (kt == 0) { TS(28); TC(60); } g.rd(kt); stA(kt + 3); stB(kt + 3); g.acc(); }
    if (lw == 3) {
        const rsrc_t rs = mkrsrc(ws, WS_END);
        u32x4 fl[2];
        u32x2 tag = lld<u32x2>(smem + A_FLG + F_TAG); PIN(tag);
        auto publish = [&]() { sig(lb + A_FLG + F_EM, (wave_pkmax(pkmax(pkmax(fl[0].x, fl[0].z), pkmax(fl[1].x, fl[1].z))) & 0xffu) | 0x8000u); };
        wait_vm<16>(); BAR();
        asm volatile("buffer_load_dwordx4 %0, %2, %3, %4 offen sc1\n\tbuffer_load_dwordx4 %1, %2, %3, %4 offen offset:1024 sc1"
                     : "=&v"(fl[0]), "=&v"(fl[1]) : "v"(lane * 16), "s"(mkdesc(ws, WS_END)), "s"(OFF_FU + (gb & (NREP - 1)) * 2048) : "memory");
        g.rd(13); g.acc();
        wait_vm<10>(); BAR();
        asm volatile("s_waitcnt vmcnt(0)" : "+v"(fl[0]), "+v"(fl[1]) :: "memory");
        const bool ok = flag_ok(fl, tag);
        if (ok) publish();
        g.rd(14); g.acc();
        BAR(); TS(43);
        if (!ok) {
#pragma unroll 1
            for (int spin = 0; spin < (1 << 20); ++spin) { flag_get(rs, OFF_FU, gb, lane, fl); if (flag_ok(fl, tag)) break; }
            publish();
        }
    } else {
        wait_vm<16>(); TL(21); BAR(); TL(41); g.rd(13); g.acc();
        wait_vm<8>(); TL(22); BAR(); TL(42); g.rd(14); g.acc();
        wait_vm<0>(); TL(23); BAR(); TS(43); TC(61);
    }
    g.rd(15); g.acc();
    sig(lb + A_FLG + F_FREE, 1u << 20);
    return g.sum;
}

DEV unsigned ldw(rsrc_t r, unsigned vo, unsigned so) { return __builtin_amdgcn_raw_buffer_load_b32(r, vo, so, 0); }
DEV u32x4 w_pack(u32x4 re, u32x4 im, unsigned sel) {
    PIN(re); PIN(im);
#pragma unroll
    for (int r = 0; r < 4; ++r) re[r] = __builtin_amdgcn_perm(im[r], re[r], sel);
    return re;
}
DEV unsigned amax4(u32x4 p) { return pkmax(pkmax(p.x & 0x7fff7fffu, p.y & 0x7fff7fffu), pkmax(p.z & 0x7fff7fffu, p.w & 0x7fff7fffu)); }

DEV unsigned role_aux(const u16* __restrict__ Cre, const u16* __restrict__ Cim, const u16* __restrict__ w_r, const u16* __restrict__ w_i, const u16* __restrict__ lam_re, const u16* __restrict__ lam_im,
                  const u16* __restrict__ log_dt, const u16* __restrict__ Dskip, char* ws, lptr smem, int gb, int aw, int lane, u32x2 (&xc)[8],
                  u32x4 (&ut)[2] TSARG) {
    int lt, pt; tile_of(gb, lt, pt);
    int mp, j1; slot_of(gb, mp, j1);
    const int c = lane & 15, q = lane >> 4;
    const bool spc = mp == 0 && !(j1 & 1);
    const rsrc_t rs = mkrsrc(ws, WS_END), rr = mkrsrc(w_r, S_ * KF * 2), ri = mkrsrc(w_i, S_ * KF * 2);
    TS(0);
    __builtin_amdgcn_s_setprio(PRIOA);
    u32x4 fch, fsh;
    const bool m0 = 16 * aw + c == 0;
    {
        const float th = (float)(16 * aw + c) * (1.0f / 128.0f);
#pragma unroll
        for (int i = 0; i < 4; ++i) {
            const float t0 = __builtin_amdgcn_fractf(th * (float)(16 * (i >> 1) + 2 * (i & 1) + 4 * q)), t1 = t0 + th;
            const float c0 = __builtin_amdgcn_cosf(t0), c1 = __builtin_amdgcn_cosf(t1);
            const float s0 = m0 ? 1.f : -__builtin_amdgcn_sinf(t0), s1 = m0 ? -1.f : -__builtin_amdgcn_sinf(t1);
            fch[i] = pkb(c0, c1); fsh[i] = pkb(s0, s1);
        }
        PIN(fch); PIN(fsh);
    }
    unsigned sg64 = (c & 1) ? SG : 0u, sgA = (c & 2) ? SG : 0u, sgB = ((c + 1) & 2) ? SG : 0u;
    constexpr unsigned WR = 128 * KF * 2;
    const int k1 = mp ? mp : spc ? (c >> 3) * 64 : (j1 == 1 ? 0 : 64);
    const int kd = j1 + 4 * (spc ? c & 7 : c) + 64 * (4 * aw + q);
    unsigned wo = aw < 2 ? (k1 * KF + kd) * 2 : ((128 - k1) * KF + 1023 - kd) * 2;
    unsigned wo0 = aw < 2 ? wo : k1 ? wo + 15 * WR : (1023 - kd) * 2;
    unsigned uoff = (lane >> 3) * 131072 + (lane & 7) * 16 + OFF_U + aw * 64 * 16384 + lt * 2048 + pt * 128;
    PIN(sg64); PIN(sgA); PIN(sgB); PIN(wo); PIN(wo0); PIN(uoff);
    unsigned lru, liu, ldu, um = 0, em;
    u32x2 tag, dk[4];
    u32x4 wre[5], wim[5];
    wre[4] = (u32x4){0, 0, 0, 0}; wim[4] = wre[4];
    if (XM & 1) for (int i = 0; i < 4; ++i) { wre[i] = fch; wim[i] = fsh; }
    f32x4 vr[4], vi[4];
    float dt = 0.f, er = 0.f; float2 lam = make_float2(0.f, 0.f);
#pragma unroll
    for (int d1 = 0; d1 < 4; ++d1) { vr[d1] = Z4; vi[d1] = Z4; }
    auto wx = [&]() INL {
        if (j1 == 0 && (aw == 3 || (gb == XG && aw < 2))) {
            u32x4 o;
            if (aw == 3) {
                const int ks = (spc ? (lane >> 5) * 64 : mp) + 128 * c, ng = (S_ - ks) & (S_ - 1);
                o = (u32x4){(q & 1) ? (ng * KF + 511) * 2u : ks * KF * 2u, (q & 1) ? (ks * KF + 511) * 2u : ng * KF * 2u, S_ * KF * 2u, S_ * KF * 2u};
            } else {
                const int sg = c < 8 ? 1 : -1;
#pragma unroll
                for (int r = 0; r < 4; ++r) o[r] = ((sg * (64 * aw + 128 * (4 * q + r)) & (S_ - 1)) * KF + (sg * (32 + 64 * c) & 1023)) * 2;
            }
            PIN(o);
#pragma unroll
            for (int r = 0; r < 4; ++r) { wre[4][r] = ldw(rr, o[r], 0); wim[4][r] = ldw(ri, o[r], 0); }
        }
    };
    auto prm = [&]() INL {
        lru = ldw(mkrsrc(lam_re, 2048), (lane >> 1) * 4, pt * 128); liu = ldw(mkrsrc(lam_im, 2048), (lane >> 1) * 4, pt * 128);
        ldu = ldw(mkrsrc(log_dt, 2048), (lane >> 1) * 4, pt * 128);
#pragma unroll
        for (int i = 0; i < 4; ++i) dk[i] = __builtin_amdgcn_raw_buffer_load_b64(mkrsrc(Dskip, D_ * 2), q * 8 + i * 512, pt * 32, 0);
    };
    if (PRS < 0) prm();
    u32x4 uo[4];
    float2 coef = make_float2(0.f, 0.f);
    sfor<0, 16>([&](auto ic) INL {
        constexpr int kt = decltype(ic)::v;
        constexpr int GS[4] = GSTEP, KSO[4] = {0, 2, 3, 1}, JO[4] = {0, 2, 1, 3}, WRW[7] = WROW;
        auto ust = [&](int k) INL {
#pragma unroll
            for (int u = 0; u < 4; ++u) if (US + u / UPS == k && !(XM & 2)) st128<SC1>(uo[u], rs, uoff + JO[u] * 32768);
        };
        auto mem = [&]() INL {
            if (kt >= 10 && !(XM & 1)) {
#pragma unroll
                for (int r = 0; r < 16; ++r) {
                    if (r < WRW[kt < 10 ? 0 : kt - 10] || r >= WRW[kt < 10 ? 0 : kt - 9]) continue;
                    const unsigned so = r ? (aw < 2 ? r : 15 - r) * WR : 0u;
                    wre[r >> 2][r & 3] = ldw(rr, r ? wo : wo0, so); wim[r >> 2][r & 3] = ldw(ri, r ? wo : wo0, so);
                }
            }
            if (kt == PRS) prm();
            if (kt == WXS) wx();
        };
        if (kt == UFS) wait_vm<0>();
        TL(8 + kt);
        BAR();
        if (VL == 2) ust(kt - 1);
        if (kt == 15) { em = u_load(ws, smem, gb, aw, lane, ut); TS(2); }
        u32x4 xa[4][4];
#pragma unroll
        for (int g = 0; g < 4; ++g) {
            if (GS[g] != kt) continue;
#pragma unroll
            for (int i = 0; i < 4; ++i) xa[g][i] = trfrag(smem + A_XT + i * 4096 + KSO[g] * 1024 + lane * 8, 512);
        }
        if (kt == XS) {
#pragma unroll
            for (int i = 0; i < 8; ++i) xc[i] = lld<u32x2>(smem + A_XT + (i & 3) * 4096 + (32 * aw + 16 * (i >> 2) + c) * 32 + q * 8);
        }
        if (VL != 1) mem();
        SCHED();
#pragma unroll
        for (int g = 0; g < 4; ++g) {
            if (GS[g] != kt) continue;
#pragma unroll
            for (int i = 0; i < 4; ++i) { vr[i] = mfb(xa[g][i], fch, vr[i]); vi[i] = mfb(xa[g][i], fsh, vi[i]); PIN(vr[i]); PIN(vi[i]); }
            if (g == 0 || g == 2) { fch ^= sg64; fsh ^= sg64; PIN(fch); PIN(fsh); }
            if (g == 1) {
                const bool od = c & 1;
                const u32x4 a = (od ? fsh : fch) ^ sgA, b = (od ? fch : fsh) ^ sgB;
                fch = a; fsh = b; PIN(fch); PIN(fsh);
            }
        }
        if (kt == B1S) {
            const f32x4 t0r = vr[0] + vr[2], t1r = vr[0] - vr[2], t2r = vr[1] + vr[3], t3r = vr[1] - vr[3];
            const f32x4 t0i = vi[0] + vi[2], t1i = vi[0] - vi[2], t2i = vi[1] + vi[3], t3i = vi[1] - vi[3];
            vr[0] = t0r; vr[1] = t1r; vr[2] = t2r; vr[3] = t3r; vi[0] = t0i; vi[1] = t1i; vi[2] = t2i; vi[3] = t3i;
#pragma unroll
            for (int i = 0; i < 4; ++i) { PIN(vr[i]); PIN(vi[i]); }
        }
        if (kt == XS) {
#pragma unroll
            for (int i = 0; i < 8; ++i) PIN(xc[i]);
        }
#pragma unroll
        for (int u = 0; u < 4; ++u) {
            if (US + u / UPS != kt) continue;
            const int j = JO[u];
            f32x4 a, b;
            if (j == 0) { a = vr[0] + vr[2]; b = vi[0] + vi[2]; }
            if (j == 2) { a = vr[0] - vr[2]; b = vi[0] - vi[2]; }
            if (j == 1) { a = vr[1] + vi[3]; b = vi[1] - vr[3]; if (aw == 0) { if (m0) { a = vr[1]; b = -vr[3]; } } }
            if (j == 3) { a = vr[1] - vi[3]; b = vi[1] + vr[3]; if (aw == 0) { if (m0) { a = vi[1]; b = vi[3]; } } }
            uo[u] = quads(smem + A_XT + aw * 4096 + j * 1024, lane, (u32x2){pkb(a[0], a[1]), pkb(a[2], a[3])}, (u32x2){pkb(b[0], b[1]), pkb(b[2], b[3])});
            um = pkmax(um, amax4(uo[u]));
            PIN(um); PIN(uo[u]);
#pragma unroll
            for (int i = 0; i < 4; ++i) if (u < 3 && ((i & 1) || u == 0)) { PIN(vr[i]); PIN(vi[i]); }
        }
        if (kt == UFS - 1) {
            um = wave_pkmax(pkmax(um, um >> 16));
            if (lane == 0) lst<unsigned>(smem + A_FLG + F_MAX + aw * 4, um);
            tag = lld<u32x2>(smem + A_FLG + F_TAG); PIN(tag);
        }
        if (kt == DS || kt == DS + 1) {
#pragma unroll
            for (int i = 4 * (kt - DS); i < 4 * (kt - DS) + 4; ++i) {
                const u32x2 d = dk[i & 3];
                xc[i & 7] = (u32x2){pkb(lo2f(xc[i & 7].x) * lo2f(d.x), hi2f(xc[i & 7].x) * hi2f(d.x)), pkb(lo2f(xc[i & 7].y) * lo2f(d.y), hi2f(xc[i & 7].y) * hi2f(d.y))};
                PIN(xc[i & 7]);
            }
        }
        if (kt == P0S) {
            PIN(lru); PIN(liu); PIN(ldu);
            if (lane & 1) { lru >>= 16; liu >>= 16; ldu >>= 16; }
            PIN(lru); PIN(liu); PIN(ldu);
        }
        if (kt >= P0S && kt < P0S + 4 && aw == 1) {
            const float lr = lo2f(lru), li = lo2f(liu);
            if (kt == P0S) { dt = bfr(expf(lo2f(ldu))); er = expf(dt * lr); PIN(dt); PIN(er); }
            if (kt == P0S + 1) { float sn, cs; sincosf(dt * li, &sn, &cs); lam = make_float2(er * cs, er * sn); PIN(lam.x); PIN(lam.y); }
            if (kt == P0S + 2) {
                const float nr = lam.x - 1.0f, ni = lam.y, den = lr * lr + li * li;
                coef = make_float2((nr * lr + ni * li) / den, (ni * lr - nr * li) / den);
                lst<f32x4>(smem + A_PRM + lane * 32, (f32x4){lam.x, lam.y, coef.x, coef.y});
            }
            if (kt == P0S + 3) {
                float2 l16 = lam;
#pragma unroll
                for (int i = 0; i < 4; ++i) l16 = cmul(l16, l16);
                float2 l128 = l16;
#pragma unroll
                for (int i = 0; i < 3; ++i) l128 = cmul(l128, l128);
                lst<f32x4>(smem + A_PRM + lane * 32 + 16, (f32x4){l16.x, l16.y, l128.x, l128.y});
            }
        }
        SCHED();
        if (kt == UFS && aw == 0) {
            const u32x4 m4 = lld<u32x4>(smem + A_FLG + F_MAX);
            const unsigned m = pkmax(pkmax(m4.x, m4.y), pkmax(m4.z, m4.w));
            flag_put(rs, OFF_FU, gb, (u32x2){(tag.x & 0xffff0000u) | ((m & 0xffffu) >> 7), tag.y}, lane);
        }
        if (VL != 2) ust(kt);
        if (VL == 1) mem();
        LDS_WAIT();
        SCHED();
    });
    unsigned mx = 0;
    const bool skip = j1 == 0 && !(aw & 1) && (lane & (spc ? 55 : 63)) == 0;
#pragma unroll
    for (int i = 0; i < 4; ++i) {
        const u32x4 p = w_pack(wre[i], wim[i], aw < 2 ? 0x05040100u : 0x07060302u);
        if (!skip) lst<u32x4>(smem + A_W + c * 1024 + i * 256 + (((4 * aw + q) ^ c) << 4), p);
        mx = pkmax(mx, amax4(p));
    }
    if (j1 == 0) {
        const u32x4 p = w_pack(wre[4], wim[4], aw == 3 && (q & 1) ? 0x07060302u : 0x05040100u);
        mx = pkmax(mx, amax4(p));
        const int it = spc ? (lane >> 5) * 8 : 0;
        if (aw == 3) lst<unsigned>(smem + A_W + it * 1024 + (c >> 2) * 256 + ((((q & 1) * 8) ^ it) << 4) + (c & 3) * 4,
                                  pkb(0.5f * (lo2f(p.x) + lo2f(p.y)), 0.5f * (hi2f(p.x) - hi2f(p.y))));
        else lst<u32x4>(smem + A_W + 16384 + aw * 1024 + lane * 16, p);
    }
    mx = wave_pkmax(pkmax(mx, mx >> 16));
    if (lane == 0) lst<unsigned>(smem + A_FLG + F_WM + aw * 4, mx);
    wait_vm<0>();
    SCHED();
    return em;
}

DEV int role_mid(const u16* __restrict__ Cre, const u16* __restrict__ Cim, char* ws, lptr smem,
                 int gb, int w, int lane, float gsum, u32x2& tag, u32x4 (&ut)[2], unsigned em TSARG) {
    int lt, pt; tile_of(gb, lt, pt);
    int mp, j1; slot_of(gb, mp, j1);
    const int c = lane & 15, q = lane >> 4;
    const bool spc = mp == 0 && !(j1 & 1), xtr = gb == XG, ld = w >= 4;
    const rsrc_t rs = mkrsrc(ws, WS_END);
    const unsigned lb = ldsa(smem), fb = lb + A_FLG;
    u32x4 wv[2], mt[6];
    f32x4 tf[2]; f32x2 ti[2];
    tag = lld<u32x2>(smem + A_FLG + F_TAG);
#pragma unroll
    for (int i = 0; i < 2; ++i) {
        tf[i] = lld<f32x4>(smem + A_MAT + 9216 + c * 128 + q * 32 + i * 16);
        ti[i] = lld<f32x2>(smem + A_MAT + 9216 + (2 * w + i) * 128 + c * 8);
    }
    lptr mb = smem + A_MAT + lane * 16;
#pragma unroll
    for (int i = 0; i < 6; ++i) mt[i] = lld<u32x4>(mb + (i < 4 ? i : i + 2) * 1024);
    u32x4 sf0 = lld<u32x4>(mb + 4096), si0 = lld<u32x4>(mb + 5120);
    const u32x4 f0 = lld<u32x4>(mb + 8192);
    SCHED();
    if (spc && ld) { sf0 = mat_sf(64, c, q); si0 = mat_si(64, c, q); }
    const CM sf = cexp(sf0), si = cexp(si0), ff = cexp(f0), fi = cexp((u32x4){f0.x ^ SG, f0.y ^ SG, f0.z, f0.w});
#pragma unroll
    for (int i = 0; i < 6; ++i) PIN(mt[i]);
    PIN(ti[0]); PIN(ti[1]); PIN(tag);
    const int eu = min(max((int)(em & 0xffu), 4), 254);
    const float su = __uint_as_float((unsigned)(258 - eu) << 23);
    tf[0] *= su; tf[1] *= su; PIN(tf[0]); PIN(tf[1]);
    u32x2 gv = {0, 0};
    if (ld) {
        const float gg = bfr(1.0f / (1.0f + expf(-bfr(half_sum(gsum)))));
        gv = (u32x2){__float_as_uint(gg), __float_as_uint(bfr(1.0f - gg))};
        PIN(gv);
    }
    await<0>(fb + F_FREE, 8u << 20);
    asm volatile("s_waitcnt vmcnt(%2)" : "+v"(ut[0]), "+v"(ut[1]) : "i"(16) : "memory");
    lptr ua = smem + A_XT + w * 2048;
#pragma unroll
    for (int i = 0; i < 2; ++i) lst<u32x4>(ua + i * 1024 + lane * 16, ut[i]);
    TS(3);
    asm volatile("" ::: "memory");
    {
        lptr ea = smem + A_E1 + c * E1P + q * 256 + 2 * w * 16;
#pragma unroll
        for (int i = 0; i < 2; ++i) ut[i] = trfrag(ua + i * 1024 + (lane >> 2) * 64 + (lane & 3) * 8, 32);
#pragma unroll
        for (int i = 0; i < 2; ++i) {
            f32x4 r = mfb(ut[i], mt[2], Z4), m = mfb(ut[i], mt[3], Z4);
            r = mfb(ut[i], mt[0], r); m = mfb(ut[i], mt[1], m);
            f32x4 yr, yi;
#pragma unroll
            for (int e = 0; e < 4; ++e) {
                const float tc = tf[e >> 1][2 * (e & 1)], ts = tf[e >> 1][2 * (e & 1) + 1];
                yr[e] = fmaf(r[e], tc, -m[e] * ts); yi[e] = fmaf(r[e], ts, m[e] * tc);
            }
            lst<u32x4>(ea + i * 16, pk8(yr, yi));
            if (xtr) {
                const unsigned sg = 0xbf803f80u;
                const f32x4 e = mfb(ut[i], c == 0 ? (u32x4){sg, sg, 0, 0} : (u32x4){0, 0, sg, sg}, Z4);
                if (c < 2) lst<u32x4>(ea + 16 * E1P + i * 16, pk8(e * su, Z4));
            }
        }
    }
    sig(fb + F_MID, 1u);
    if (ld) {
        const int gi = 8 * pt + 2 * (w - 4) + (lane >> 5), tau = (gi & 64) | ((gi & 12) << 2) | ((gi & 48) >> 2) | (gi & 3);
        if ((lane & 31) == 0) __builtin_amdgcn_raw_buffer_store_b64(gv, rs, (tau & 15) * 1024 + (lt * 8 + (tau >> 4)) * 8, OFF_G, SC1);
    }
    if (xtr && w == 0 && lane == 0) __builtin_amdgcn_raw_buffer_store_b64(tag, rs, 0, OFF_E, SC1);
    TS(4);
    await<0>(fb + F_MID, 24u);
    u32x4 dv[2];
#pragma unroll
    for (int i = 0; i < 2; ++i) dv[i] = lld<u32x4>(smem + A_E1 + (2 * w + i) * E1P + lane * 16);
    const u32x4 m4 = lld<u32x4>(smem + A_FLG + F_WM);
#pragma unroll
    for (int i = 0; i < 2; ++i) wv[i] = lld<u32x4>(smem + A_W + (2 * w + i) * 1024 + ((lane ^ (2 * w + i)) << 4));
#pragma unroll
    for (int i = 0; i < 2; ++i) dv[i] = pk8(mfh(dv[i], ff.re, Z4), mfh(dv[i], ff.im, Z4));
    const unsigned ew = min((pkmax(pkmax(m4.x, m4.y), pkmax(m4.z, m4.w)) & 0xffffu) >> 7, 245u);
    const float sw = __uint_as_float((246u - ew) << 23), swi = c < 8 ? sw : -sw;
    wv[0] = wcvt(wv[0], sw, swi); wv[1] = wcvt(wv[1], sw, swi);
#pragma unroll
    for (int i = 0; i < 2; ++i) dv[i] = wmul(mfh(sf.re, dv[i], Z4), mfh(sf.im, dv[i], Z4), wv[i]);
#pragma unroll
    for (int i = 0; i < 2; ++i) dv[i] = pk8(mfh(dv[i], si.re, Z4), mfh(dv[i], si.im, Z4));
    {
        f32x4 r[2], m[2];
#pragma unroll
        for (int i = 0; i < 2; ++i) {
            const f32x4 a = mfh(dv[i], fi.re, Z4), b = mfh(dv[i], fi.im, Z4);
#pragma unroll
            for (int e = 0; e < 4; ++e) { r[i][e] = fmaf(a[e], ti[i][0], b[e] * ti[i][1]); m[i][e] = fmaf(b[e], ti[i][0], -a[e] * ti[i][1]); }
        }
#pragma unroll
        for (int e = 0; e < 4; ++e)
            lst<u32x2>(smem + A_XT + (4 * q + e) * 1024 + (w >> 1) * 256 + c * 16 + (w & 1) * 8, (u32x2){pkh(r[0][e], r[1][e]), pkh(m[0][e], m[1][e])});
    }
    if (xtr && !(w & 3)) {
        const float dt = -(float)(32 + 64 * c) * (1.0f / 1024.0f);
        const CM f = cexp(cmat1(dt * (float)(4 * q), dt)), b = cexp(cmat1((float)(c * (32 + 256 * q)) * (1.0f / 1024.0f), (float)c * (1.0f / 16.0f)));
        u32x4 d = lld<u32x4>(smem + A_E1 + (16 + (w >> 2)) * E1P + lane * 16);
        d = pk8(mfh(d, f.re, Z4), mfh(d, f.im, Z4));
        d = wmul(mfh(sf.re, d, Z4), mfh(sf.im, d, Z4), wcvt(lld<u32x4>(smem + A_W + 16384 + w * 256 + lane * 16), sw, swi));
        d = pk8(mfh(d, si.re, Z4), mfh(d, si.im, Z4));
        const f32x4 r = mfh(d, b.re, Z4);
#pragma unroll
        for (int e = 0; e < 4; ++e) lst<float>(smem + A_EX + ((w >> 2) * 256 + (4 * q + e) * 16 + c) * 4, r[e]);
    }
    TS(5);
    if (ld) { wait_vm<1>(); sig(fb + F_FULL, 0x11u); }
    sig(fb + F_MID, 1u);
    await<0>(fb + F_MID, 16u);
    {
        f32x4 xa[2], xb[2];
#pragma unroll
        for (int i = 0; i < 2; ++i) { xa[i] = Z4; xb[i] = Z4; }
        if (xtr) {
            const float sg = (c & 1) ? -0.125f : 0.125f;
#pragma unroll
            for (int i = 0; i < 2; ++i) {
                xa[i] = lld<f32x4>(smem + A_EX + (2 * w + i) * 64 + q * 16) * sg;
                xb[i] = lld<f32x4>(smem + A_EX + 1024 + (2 * w + i) * 64 + q * 16) * -sg;
            }
        }
#pragma unroll
        for (int i = 0; i < 2; ++i) {
            const int s2 = 2 * w + i;
            const u32x4 ea = lld<u32x4>(smem + A_XT + s2 * 1024 + lane * 16);
            const f32x4 ta = mfh(ea, mt[4], xa[i]), tb = mfh(ea, mt[5], xb[i]);
            u32x4 tq = quads(smem + A_XT + s2 * 1024, lane, (u32x2){pkh(ta[0], ta[1]), pkh(ta[2], ta[3])}, (u32x2){pkh(tb[0], tb[1]), pkh(tb[2], tb[3])});
            PIN(tq);
            st128<SC1>(tq, rs, (lane >> 3) * 32768 + (lane & 7) * 16 + OFF_T + s2 * 262144 + j1 * 8192 + mp * 128);
        }
    }
    TS(7);
    tag.x = (tag.x & 0xffff0000u) | ew;
    return eu - 267;
}

DEV void t_done(rsrc_t rs, unsigned fb, int gb, u32x2 tag, int lane) {
    int mp, j1; slot_of(gb, mp, j1);
    if (sig_rtn(fb + F_TC) == 7u) flag_put(rs, OFF_FT, (mp >> 3) * 32 + j1 * 8 + (mp & 7), tag, lane);
}

DEV void role_cload(const u16* __restrict__ Cre, const u16* __restrict__ Cim, char* ws, lptr smem, int gb, int lw, int lane, u32x2 tag TSARG) {
    int lt, pt; tile_of(gb, lt, pt);
    const unsigned lb = ldsa(smem), fb = lb + A_FLG;
    __builtin_amdgcn_s_setprio(3);
    const bool early = lds_peek(fb + F_HC) & 4u;
    if (!early) {
        wait_vm<0>();
        t_done(mkrsrc(ws, WS_END), fb, gb, tag, lane);
        await<2>(fb + F_HC, 4u);
    }
    c_chunk(Cre, Cim, lb, gb, pt, lw, lane, 2); c_chunk(Cre, Cim, lb, gb, pt, lw, lane, 3);
    if (early) {
        wait_vm<16>();
        t_done(mkrsrc(ws, WS_END), fb, gb, tag, lane);
    }
    TS(44);
    wait_vm<0>();
    sig(fb + F_FULL, 0x1100u);
    TS(47);
#pragma unroll 1
    for (int j = 4; j < 8; ++j) {
        await<2>(fb + F_FREE, 4u << (4 * (j - 4)));
        c_chunk(Cre, Cim, lb, gb, pt, lw, lane, j);
        wait_vm<8>();
        if (j > 4) sig(fb + F_FULL, 1u << (4 * (j - 1)));
        TS(44 + j);
    }
    wait_vm<0>();
    sig(fb + F_FULL, 1u << 28);
    TSE(1, lw == 0);
}

DEV void role_last(char* ws, lptr smem, int gb, int w, int lane, u32x2 tag, int fe, u32x2 (&xc)[8] TSARG) {
    int s2t, dat; tile_of(gb, s2t, dat);
    const int c = lane & 15, q = lane >> 4;
    const rsrc_t rs = mkrsrc(ws, WS_END);
    const unsigned fb = ldsa(smem + A_FLG);
    SCHED();
    u32x4 fc[2][2], fs[2][2];
#pragma unroll
    for (int n = 0; n < 2; ++n) {
        float dt = (float)(32 * w + 16 * n + c) * (1.0f / 128.0f); PIN(dt);
        float cc[8], ss[8];
        const float t0 = __builtin_amdgcn_fractf(dt * (float)(4 * q)), t1 = __builtin_amdgcn_fractf(dt * (float)(16 + 4 * q));
#pragma unroll
        for (int e = 0; e < 8; ++e) { const float f = fmaf((float)(e & 3), dt, e < 4 ? t0 : t1); cc[e] = __builtin_amdgcn_cosf(f); ss[e] = __builtin_amdgcn_sinf(f); }
        fc[n][0] = (u32x4){pkh(cc[0], cc[1]), pkh(cc[2], cc[3]), pkh(cc[4], cc[5]), pkh(cc[6], cc[7])};
        fs[n][0] = (u32x4){pkh(ss[0], ss[1]), pkh(ss[2], ss[3]), pkh(ss[4], ss[5]), pkh(ss[6], ss[7])};
        fc[n][1] = ((c & 1) ? fs[n][0] : fc[n][0]) ^ (((c + 1) & 2) ? SG : 0u);
        fs[n][1] = ((c & 1) ? fc[n][0] : fs[n][0]) ^ ((c & 2) ? SG : 0u);
        PIN(fc[n][0]); PIN(fs[n][0]); PIN(fc[n][1]); PIN(fs[n][1]);
        if (n == 0) { wait_vm<0>(); t_done(rs, fb, gb, tag, lane); TS(44); }
    }
    TS(45);
    __builtin_amdgcn_s_setprio(3);
    if (w == 0) {
        u32x4 fl[2];
        unsigned m = await<2>(fb + F_FULL, 8u) & 0x88888888u;
#pragma unroll 1
        for (int spin = 0; spin < (1 << 20) && m != 0x88888888u; ++spin) {
            m = h_poll<4>(rs, gb, lane, tag);
            sig_or(fb + F_FULL, m);
        }
        TS(50);
#pragma unroll 1
        for (int spin = 0; spin < (1 << 20); ++spin) { flag_get(rs, OFF_FT, gb, lane, fl); if (flag_ok(fl, tag)) break; }
        const unsigned em = wave_pkmax(pkmax(pkmax(fl[0].x, fl[0].z), pkmax(fl[1].x, fl[1].z))) & 0xffu;
#pragma unroll
        for (int i = 0; i < 2; ++i) {
            const float a = ldexpf(1.f, (int)(fl[i].x & 0xffu) - (int)em), b = ldexpf(1.f, (int)(fl[i].z & 0xffu) - (int)em);
            lst<u32x2>(smem + A_MAT + i * 512 + lane * 8, (u32x2){pkh(a, a), pkh(b, b)});
        }
        sig(fb + F_TR, em | 0x8000u);
    }
    fe += await<1>(fb + F_TR, 0x8000u) & 0xffu;
    TS(46);
    u32x4 tv[4]; u32x2 gg[2];
#pragma unroll
    for (int i = 0; i < 4; ++i)
        tv[i] = __builtin_amdgcn_raw_buffer_load_b128(rs, (lane >> 2) * 128 + (lane & 3) * 16 + (i & 1) * 2048, OFF_T + (s2t * 8 + (dat >> 1)) * 32768 + w * 8192 + (dat & 1) * 64 + (i >> 1) * 4096, SC1);
#pragma unroll
    for (int n = 0; n < 2; ++n) gg[n] = __builtin_amdgcn_raw_buffer_load_b64(rs, c * 8 + n * 128, OFF_G + s2t * 1024 + w * 256, SC1);
    __builtin_amdgcn_s_setprio(PRIOR);
    unsigned ts[4];
#pragma unroll
    for (int i = 0; i < 4; ++i) ts[i] = lld<unsigned>(smem + A_MAT + i * 256 + (lane >> 5) * 128 + w * 32 + ((lane >> 2) & 7) * 4);
#pragma unroll
    for (int i = 0; i < 4; ++i) {
        tv[i] = __builtin_bit_cast(u32x4, __builtin_bit_cast(f16x8, tv[i]) * __builtin_bit_cast(f16x8, (u32x4){ts[i], ts[i], ts[i], ts[i]}));
        lst<u32x4>(smem + A_XT + w * 4096 + ((lane >> 1) & 1) * 2048 + i * 512 + (lane >> 2) * 32 + (lane & 1) * 16, tv[i]);
    }
    TS(47);
    sig(fb + F_TT, 1u);
    await<0>(fb + F_TT, 4u);
    f32x4 P0[2], P1[2], P2[2], P3a[2], P3b[2];
#pragma unroll
    for (int n = 0; n < 2; ++n) { P0[n] = Z4; P1[n] = Z4; P2[n] = Z4; P3a[n] = Z4; P3b[n] = Z4; }
    lptr tt = smem + A_XT + lane * 8;
#pragma unroll
    for (int h = 0; h < 2; ++h) {
        const u32x4 a1 = trfrag(tt + 1 * 4096 + h * 1024, 512), b1 = trfrag(tt + 1 * 4096 + 2048 + h * 1024, 512);
        const u32x4 a3 = trfrag(tt + 3 * 4096 + h * 1024, 512), b3 = trfrag(tt + 3 * 4096 + 2048 + h * 1024, 512);
        const u32x4 a0 = trfrag(tt + h * 1024, 512), b0 = trfrag(tt + 2048 + h * 1024, 512);
        const u32x4 a2 = trfrag(tt + 2 * 4096 + h * 1024, 512), b2 = trfrag(tt + 2 * 4096 + 2048 + h * 1024, 512);
#pragma unroll
        for (int n = 0; n < 2; ++n) {
            u32x4 cw = fc[n][h], sw_ = fs[n][h];
            P1[n] = mfh(a1, cw, P1[n]); P3a[n] = mfh(a1, sw_, P3a[n]); P3b[n] = mfh(b1, cw, P3b[n]); P1[n] = mfh(b1, sw_, P1[n]);
            if (h == 0) cw.x = q == 0 ? (cw.x & 0xffff0000u) | ((c & 1) ? 0xbc00u : 0x3c00u) : cw.x;
            P3a[n] = mfh(b3, cw, P3a[n]); P3b[n] = mfh(a3, sw_, P3b[n]); P1[n] = mfh(a3, cw, P1[n]); P1[n] = mfh(b3, sw_, P1[n]);
            if (h == 0) { cw.x = q == 0 ? (cw.x & 0xffff0000u) | 0x3800u : cw.x; sw_.x = q == 0 ? (sw_.x & 0xffff0000u) | ((c & 1) ? 0x3800u : 0xb800u) : sw_.x; }
            P0[n] = mfh(a0, cw, P0[n]); P2[n] = mfh(a2, cw, P2[n]); P0[n] = mfh(b0, sw_, P0[n]); P2[n] = mfh(b2, sw_, P2[n]);
        }
    }
    f32x4 r0[2][4];
#pragma unroll
    for (int n = 0; n < 2; ++n) {
        const float g = __uint_as_float(gg[n].x), og = __uint_as_float(gg[n].y);
        const f32x4 sp = P0[n] + P2[n], sm = P0[n] - P2[n], p3 = P3a[n] - P3b[n];
        const f32x4 av[4] = {sp + P1[n], sm - p3, sp - P1[n], sm + p3};
#pragma unroll
        for (int m = 0; m < 4; ++m) {
            const unsigned b0 = pkb(ldexpf(av[m][0], fe), ldexpf(av[m][1], fe)), b1 = pkb(ldexpf(av[m][2], fe), ldexpf(av[m][3], fe));
            const unsigned o0 = pkb(g * lo2f(b0), g * hi2f(b0)), o1 = pkb(g * lo2f(b1), g * hi2f(b1));
            const u32x2 p = xc[4 * n + m];
            r0[n][m] = (f32x4){fmaf(og, lo2f(p.x), lo2f(o0)), fmaf(og, hi2f(p.x), hi2f(o0)), fmaf(og, lo2f(p.y), lo2f(o1)), fmaf(og, hi2f(p.y), hi2f(o1))};
            PIN(r0[n][m]);
        }
    }
    TS(48);
    await<2>(fb + F_FREE, 4u << 16);
#pragma unroll
    for (int n = 0; n < 2; ++n) {
#pragma unroll
        for (int m = 0; m < 4; ++m) lst<f32x4>(smem + A_R0 + ((w * 2 + n) * 4 + m) * 1024 + q * 256 + ((c ^ (4 * m + q)) << 4), r0[n][m]);
        if (q == 0) lst<float>(smem + A_AGG + (32 * w + 16 * n + c) * 4, 2.0f * __uint_as_float(gg[n].y));
    }
    sig(fb + F_R0, 1u);
    TS(49);
    TSE(w ? 4 : 5, w == 0 || w == 2);
}

NOPK __global__ __launch_bounds__(768, 3) void gm_kernel(const u16* __restrict__ x, const u16* __restrict__ w_r, const u16* __restrict__ w_i,
                                                         const u16* __restrict__ Bre, const u16* __restrict__ Bim, const u16* __restrict__ Cre,
                                                         const u16* __restrict__ Cim, const u16* __restrict__ lam_re,
                                                         const u16* __restrict__ lam_im, const u16* __restrict__ log_dt,
                                                         const u16* __restrict__ Dskip, const u16* __restrict__ gate_w, char* ws, float* __restrict__ out) {
    extern __shared__ __attribute__((aligned(16))) char smem_[];
    lptr smem = (lptr)smem_;
    u64 pc; int len;
    asm volatile("s_getpc_b64 %0\ngm_code_beg:\n\ts_mov_b32 %1, gm_code_end - gm_code_beg" : "=s"(pc), "=s"(len));
    const int wid = __builtin_amdgcn_readfirstlane(threadIdx.x >> 6), lane = threadIdx.x & 63;
    int w = wid & 7, gb = blockIdx.x;
    asm volatile("" : "+s"(w), "+s"(gb));
    if (wid < 4) role_mma(ws, out, smem, blockIdx.x, wid, lane, pc, len);
    else {
        TSD();
        float gsum = 0.f;
        u32x2 xc[8], tag;
        u32x4 ut[2];
        unsigned em;
        if (wid < 8) {
            gsum = role_load(x, Bre, Bim, gate_w, ws, smem, blockIdx.x, wid - 4, lane TSPASS);
            em = u_load(ws, smem, blockIdx.x, wid, lane, ut);
            TS(2);
            int lt, pt; tile_of(blockIdx.x, lt, pt);
            c_chunk(Cre, Cim, ldsa(smem), blockIdx.x, pt, wid - 4, lane, 0);
            c_chunk(Cre, Cim, ldsa(smem), blockIdx.x, pt, wid - 4, lane, 1);
#pragma unroll
            for (int i = 0; i < 8; ++i) asm volatile("" : "=v"(xc[i]));
        } else em = role_aux(Cre, Cim, w_r, w_i, lam_re, lam_im, log_dt, Dskip, ws, smem, blockIdx.x, wid - 8, lane, xc, ut TSPASS);
        const int fe = role_mid(Cre, Cim, ws, smem, gb, w, lane, gsum, tag, ut, em TSPASS);
        if (w >= 4) role_cload(Cre, Cim, ws, smem, gb, w - 4, lane, tag TSPASS);
        else role_last(ws, smem, gb, w, lane, tag, fe, xc TSPASS);
    }
}

extern "C" void kernel_launch(void* const* d_in, const int* in_sizes, int n_in, void* d_out, int out_size, void* d_ws, size_t ws_size,
                              hipStream_t stream) {
    (void)hipFuncSetAttribute((const void*)gm_kernel, hipFuncAttributeMaxDynamicSharedMemorySize, SMEM);
    gm_kernel<<<256, 768, SMEM, stream>>>((const u16*)d_in[0], (const u16*)d_in[1], (const u16*)d_in[2], (const u16*)d_in[6], (const u16*)d_in[7],
                                          (const u16*)d_in[8], (const u16*)d_in[9], (const u16*)d_in[4], (const u16*)d_in[5], (const u16*)d_in[10],
                                          (const u16*)d_in[11], (const u16*)d_in[12], (char*)d_ws, (float*)d_out);
}
```

```cpp
#include <hip/hip_runtime.h>
#include <stdint.h>

typedef __attribute__((ext_vector_type(8))) __bf16 bf16x8;
typedef __attribute__((ext_vector_type(2))) __bf16 bf16x2;
typedef __attribute__((ext_vector_type(8))) _Float16 f16x8;
typedef __attribute__((ext_vector_type(2))) _Float16 f16x2;
typedef __attribute__((ext_vector_type(4))) float f32x4;
typedef __attribute__((ext_vector_type(2))) float f32x2;
typedef __attribute__((ext_vector_type(4))) unsigned u32x4;
typedef __attribute__((ext_vector_type(3))) unsigned u32x3;
typedef __attribute__((ext_vector_type(2))) unsigned u32x2;
typedef __attribute__((ext_vector_type(4))) short s16x4;
typedef __attribute__((ext_vector_type(2))) unsigned short u16x2;
typedef unsigned short u16;
typedef unsigned long long u64;
#define GAS __attribute__((address_space(1)))
#define LAS __attribute__((address_space(3)))
#define NOPK __attribute__((target("no-packed-fp32-ops")))
#define DEV __device__ __forceinline__

constexpr int S_ = 2048, D_ = 1024, KF = 513;

constexpr unsigned OFF_H = 0;
constexpr unsigned OFF_U = OFF_H + (8u << 20);
constexpr unsigned OFF_T = OFF_U + (4u << 20);
constexpr unsigned OFF_G = OFF_T + (4u << 20);
constexpr unsigned OFF_FU = OFF_G + 16384u;
constexpr unsigned OFF_FT = OFF_FU + 32768u;
constexpr unsigned OFF_FH = OFF_FT + 32768u;
constexpr unsigned OFF_GRAN = OFF_FH + 1048576u;
constexpr unsigned OFF_P = OFF_GRAN + 16u * 1024u * 16u;
constexpr unsigned OFF_E = OFF_P + 256u * 24576u;
constexpr unsigned WS_END = OFF_E + 128u;

#define TL(i)
#define TSD()
#define TS(i)
#define TC(i)
#define TSE(slot, on)

DEV unsigned pkb(float a, float b) { f32x2 v = {a, b}; return __builtin_bit_cast(unsigned, __builtin_convertvector(v, bf16x2)); }
DEV unsigned pkh(float a, float b) { f32x2 v = {a, b}; return __builtin_bit_cast(unsigned, __builtin_convertvector(v, f16x2)); }
DEV float lo2f(unsigned p) { return __uint_as_float(p << 16); }
DEV float hi2f(unsigned p) { return __uint_as_float(p & 0xffff0000u); }
DEV float bfr(float f) { return lo2f(pkb(f, f)); }
DEV float2 cmul(float2 a, float2 b) { return make_float2(a.x * b.x - a.y * b.y, a.x * b.y + a.y * b.x); }
DEV float2 cfma(float2 a, float2 b, float2 c) { return make_float2(fmaf(a.x, b.x, fmaf(-a.y, b.y, c.x)), fmaf(a.x, b.y, fmaf(a.y, b.x, c.y))); }
DEV f32x4 mfb(u32x4 a, u32x4 b, f32x4 c) { return __builtin_amdgcn_mfma_f32_16x16x32_bf16(__builtin_bit_cast(bf16x8, a), __builtin_bit_cast(bf16x8, b), c, 0, 0, 0); }
DEV f32x4 mfh(u32x4 a, u32x4 b, f32x4 c) { return __builtin_amdgcn_mfma_f32_16x16x32_f16(__builtin_bit_cast(f16x8, a), __builtin_bit_cast(f16x8, b), c, 0, 0, 0); }
DEV unsigned pkmax(unsigned a, unsigned b) { return __builtin_bit_cast(unsigned, __builtin_elementwise_max(__builtin_bit_cast(u16x2, a), __builtin_bit_cast(u16x2, b))); }
typedef LAS char* lptr;
DEV unsigned ldsa(lptr p) { return (unsigned)(uintptr_t)p; }
template <class T> DEV T lld(lptr p) { return *(LAS const T*)p; }
template <class T> DEV void lst(lptr p, T v) { *(LAS T*)p = v; }
DEV u32x2 trread(lptr p) { return __builtin_bit_cast(u32x2, __builtin_amdgcn_ds_read_tr16_b64_v4i16((LAS s16x4*)p)); }
DEV u32x4 trfrag(lptr p, int second) { const u32x2 a = trread(p), b = trread(p + second); return (u32x4){a.x, a.y, b.x, b.y}; }
typedef __amdgpu_buffer_rsrc_t rsrc_t;
DEV u32x4 ldfrag(lptr tile, int row, int chunk) { return lld<u32x4>(tile + row * 128 + ((chunk ^ (row & 7)) << 4)); }
template <int N> DEV void wait_vm() { asm volatile("s_waitcnt vmcnt(%0)" :: "i"(N) : "memory"); }
#define LDS_WAIT() asm volatile("s_waitcnt lgkmcnt(0)" ::: "memory")
#define SCHED() __builtin_amdgcn_sched_barrier(0)
#define BAR() do { __builtin_amdgcn_s_barrier(); asm volatile("" ::: "memory"); } while (0)
#define PIN(x) asm volatile("" : "+v"(x))
template <int I> struct IC { static constexpr int v = I; };
template <int I, int N, class F> DEV void sfor(F&& f) { if constexpr (I < N) { f(IC<I>{}); sfor<I + 1, N>(f); } }
#define INL __attribute__((always_inline))
DEV rsrc_t mkrsrc(const void* p, unsigned n) { return __builtin_amdgcn_make_buffer_rsrc((void*)p, 0, n, 0x00020000); }
constexpr int SC1 = 16;
#define WTO 16
template <int AUX> DEV void st128(u32x4 v, rsrc_t rs, unsigned off) { __builtin_amdgcn_raw_buffer_store_b128(v, rs, off, 0, AUX); }
DEV void lds_add(unsigned a) { asm volatile("ds_add_u32 %0, %1" :: "v"(a), "v"(1u) : "memory"); }
DEV unsigned lds_peek(unsigned a) {
    unsigned v;
    asm volatile("ds_read_b32 %0, %1\n\ts_waitcnt lgkmcnt(0)" : "=v"(v) : "v"(a) : "memory");
    return __builtin_amdgcn_readfirstlane(v);
}
DEV void tile_of(int gb, int& ta, int& tb) {
    const int xcd = gb & 7, loc = gb >> 3;
    ta = (xcd >> 1) * 4 + (loc >> 3);
    tb = (xcd & 1) * 8 + (loc & 7);
}
#define DMAP 1
DEV int da_of(int pt) { return DMAP ? ((pt & 7) << 1) | (pt >> 3) : pt; }
DEV void rows4(float a, float& a0, float& a1, float& a2, float& a3) {
    const unsigned u = __float_as_uint(a);
    const auto r = __builtin_amdgcn_permlane32_swap(u, u, false, false);
    const auto p = __builtin_amdgcn_permlane16_swap(r[0], r[0], false, false);
    const auto s = __builtin_amdgcn_permlane16_swap(r[1], r[1], false, false);
    a0 = __uint_as_float(p[0]); a1 = __uint_as_float(p[1]); a2 = __uint_as_float(s[0]); a3 = __uint_as_float(s[1]);
}
DEV float rowsum4(float a) {
    const unsigned u = __float_as_uint(a);
    const auto r = __builtin_amdgcn_permlane32_swap(u, u, false, false);
    const unsigned t = __float_as_uint(__uint_as_float(r[0]) + __uint_as_float(r[1]));
    const auto p = __builtin_amdgcn_permlane16_swap(t, t, false, false);
    return __uint_as_float(p[0]) + __uint_as_float(p[1]);
}
DEV float2 sel4(int q, float2 a, float2 b, float2 c, float2 d) {
    PIN(d.x); PIN(d.y);
    const float2 e = (q & 1) ? b : a, f = (q & 1) ? d : c;
    return (q & 2) ? f : e;
}
template <int R> DEV unsigned ror16(unsigned v) { return __builtin_amdgcn_update_dpp(0u, v, 0x120 + R, 0xf, 0xf, false); }
DEV unsigned wave_pkmax(unsigned m) {
    m = pkmax(m, ror16<8>(m)); m = pkmax(m, ror16<4>(m)); m = pkmax(m, ror16<2>(m)); m = pkmax(m, ror16<1>(m));
    const auto r = __builtin_amdgcn_permlane32_swap(m, m, false, false);
    m = pkmax(r[0], r[1]);
    const auto p = __builtin_amdgcn_permlane16_swap(m, m, false, false);
    return pkmax(p[0], p[1]);
}
DEV float half_sum(float v) {
    v += __uint_as_float(ror16<8>(__float_as_uint(v))); v += __uint_as_float(ror16<4>(__float_as_uint(v)));
    v += __uint_as_float(ror16<2>(__float_as_uint(v))); v += __uint_as_float(ror16<1>(__float_as_uint(v)));
    const auto p = __builtin_amdgcn_permlane16_swap(__float_as_uint(v), __float_as_uint(v), false, false);
    return __uint_as_float(p[0]) + __uint_as_float(p[1]);
}
DEV u32x4 quads(lptr buf, int lane, u32x2 p, u32x2 n) {
    lptr wp = buf + (lane & 15) * 64 + ((lane >> 4) & 1) * 8;
    const int v = (lane >> 5) ^ ((lane >> 2) & 3);
    lst<u32x2>(wp + (v << 4), p); lst<u32x2>(wp + ((v ^ 2) << 4), n);
    asm volatile("" ::: "memory");
    return lld<u32x4>(buf + ((lane ^ (lane >> 4)) << 4));
}

#define PRIOA 2
#define NREP 8
#define PRIOM 1
#define PRIOT 2
#define PRIOG 2
#define AD 8
#define NB 12
#define NP 4
#define AM 1
#define CP 0
#define HP 0
#define XE 0
#define TSL 16
#define CSL 1
#define WG2 2
#define WG1 1
#define SPC 1
#define PRV 0
#define PRX 0
#define TLS 0
#define RDL 0
#define WROW {0, 0, 0, 0, 8}
#define ROT (lt & 12)
#define PRIOL 3
#define XM 0
#define UFS 7
#define PRIOR 0
#define PRS 2
#define WXS 13
#define CPF 1
#define FST 8
#define HREP 8
#define NP1 1
#define PDL 24
#define PDE 0
#define PDM 0x80
#define CP2 1
#define CPM 7
#define PRIOE 2
#define MSL 1
#define URS 6
#define UPRE 1
#define UCK 13
#define PRIOD 3
#define PRIOW 0
#define CDL 0
#define ATS 14
#define UPL 10
#define C1E 1
#define TP0 32
#define PD0 32
#define CXM 6
#define CX(g) ((g) & CXM)
#define GSTEP {0, 0, 1, 1}
#define XS 1
#define B1S 1
#define US 2
#define UPS 1
#define DS 8
#define P0S 9
#define VL 1
#define PN -1
#define PG -1
#define SNK 1
#define HFIRST 8u
#define RDPAT {0, 1, 2, -1, 3, 4, -1, 5, 6, -1, 7, -1, -1, -1, -1, -1}
#define Z4 ((f32x4){0.f, 0.f, 0.f, 0.f})
constexpr unsigned SG = 0x80008000u;
struct CM { u32x4 re, im; };
DEV u32x4 cmat1(float t0, float dt) {
    float cc[4], ss[4];
    t0 = __builtin_amdgcn_fractf(t0);
#pragma unroll
    for (int e = 0; e < 4; ++e) { const float f = fmaf((float)e, dt, t0); cc[e] = __builtin_amdgcn_cosf(f); ss[e] = __builtin_amdgcn_sinf(f); }
    return (u32x4){pkh(ss[0], ss[1]), pkh(ss[2], ss[3]), pkh(cc[0], cc[1]), pkh(cc[2], cc[3])};
}
DEV CM cexp(u32x4 v) { CM m; m.re = (u32x4){v.z, v.w, v.x ^ SG, v.y ^ SG}; m.im = v; PIN(m.re); PIN(m.im); return m; }
DEV u32x4 pk8(f32x4 r, f32x4 i) { return (u32x4){pkh(r[0], r[1]), pkh(r[2], r[3]), pkh(i[0], i[1]), pkh(i[2], i[3])}; }
DEV u32x4 wmul(f32x4 r, f32x4 i, u32x4 w) {
    float yr[4], yi[4];
#pragma unroll
    for (int e = 0; e < 4; ++e) {
        const unsigned wu = w[e];
        const f16x2 h = __builtin_bit_cast(f16x2, wu);
        yr[e] = fmaf(r[e], (float)h[0], -i[e] * (float)h[1]); yi[e] = fmaf(r[e], (float)h[1], i[e] * (float)h[0]);
    }
    return (u32x4){pkh(yr[0], yr[1]), pkh(yr[2], yr[3]), pkh(yi[0], yi[1]), pkh(yi[2], yi[3])};
}
DEV u32x4 wcvt(u32x4 w, float sr, float si) {
#pragma unroll
    for (int e = 0; e < 4; ++e) w[e] = pkh(lo2f(w[e]) * sr, hi2f(w[e]) * si);
    PIN(w);
    return w;
}
DEV u32x4 mkdesc(const void* p, unsigned n) { const u64 a = (u64)p; return (u32x4){(unsigned)a, (unsigned)(a >> 32) & 0xffffu, n, 0x00020000u}; }
DEV void dma16(u32x4 d, unsigned l, unsigned vo, unsigned so) {
    asm volatile("s_nop 0\n\tbuffer_load_dwordx4 %0, %1, %2 offen lds" :: "v"(vo), "s"(d), "s"(so), "{m0}"(l) : "memory");
}
DEV void dma16s(u32x4 d, unsigned l, unsigned vo, unsigned so) {
    asm volatile("s_nop 0\n\tbuffer_load_dwordx4 %0, %1, %2 offen sc1 lds" :: "v"(vo), "s"(d), "s"(so), "{m0}"(l) : "memory");
}
DEV void sig(unsigned a, unsigned v) { asm volatile("s_mov_b64 exec, 1\n\tds_add_u32 %0, %1\n\ts_mov_b64 exec, -1" :: "v"(a), "v"(v) : "memory"); }
template <int O> DEV void sig_o(unsigned a, unsigned v) { asm volatile("s_mov_b64 exec, 1\n\tds_add_u32 %0, %1 offset:%2\n\ts_mov_b64 exec, -1" :: "v"(a), "v"(v), "i"(O) : "memory"); }
DEV unsigned sig_rtn(unsigned a) {
    unsigned v;
    asm volatile("s_mov_b64 exec, 1\n\tds_add_rtn_u32 %0, %1, %2\n\ts_mov_b64 exec, -1\n\ts_waitcnt lgkmcnt(0)" : "=&v"(v) : "v"(a), "v"(1u) : "memory");
    return __builtin_amdgcn_readfirstlane(v);
}
template <int SLP> DEV unsigned await(unsigned a, unsigned mask) {
    unsigned v, n, r, t;
    asm volatile("s_mov_b32 %1, 0x400000\n"
                 "1:\tds_read_b32 %0, %4\n\ts_waitcnt lgkmcnt(0)\n\tv_readfirstlane_b32 %2, %0\n\ts_and_b32 %3, %2, %5\n\ts_cbranch_scc1 2f\n\t"
                 "s_sleep %6\n\ts_sub_u32 %1, %1, 1\n\ts_cbranch_scc0 1b\n"
                 "2:" : "=&v"(v), "=&s"(n), "=&s"(r), "=&s"(t) : "v"(a), "s"(mask), "i"(SLP) : "memory", "scc");
    return r;
}
DEV void need_bit(unsigned a, unsigned& r, unsigned mask) {
    unsigned v, n, t;
    asm volatile("s_and_b32 %3, %2, %5\n\ts_cbranch_scc1 2f\n\ts_mov_b32 %1, 0x400000\n"
                 "1:\tds_read_b32 %0, %4\n\ts_waitcnt lgkmcnt(0)\n\tv_readfirstlane_b32 %2, %0\n\ts_and_b32 %3, %2, %5\n\ts_cbranch_scc1 2f\n\t"
                 "s_sub_u32 %1, %1, 1\n\ts_cbranch_scc0 1b\n"
                 "2:" : "=&v"(v), "=&s"(n), "+s"(r), "=&s"(t) : "v"(a), "i"(mask) : "memory", "scc");
}
#define MF2(a, b, c) asm("v_mfma_f32_16x16x32_bf16 %0, %1, %2, %0" : "+v"(c) : "v"(a), "v"(b))
DEV void flag_put(rsrc_t rs, unsigned off, int gb, u32x2 v, int lane) {
    if (lane < NREP) __builtin_amdgcn_raw_buffer_store_b64(v, rs, lane * 2048, off + gb * 8, SC1);
}
DEV void flag_get(rsrc_t rs, unsigned off, int gb, int lane, u32x4 (&fl)[2]) {
    unsigned o = lane * 16; PIN(o);
#pragma unroll
    for (int i = 0; i < 2; ++i) fl[i] = __builtin_amdgcn_raw_buffer_load_b128(rs, o + i * 1024, off + (gb & (NREP - 1)) * 2048, SC1);
}
DEV bool flag_ok(const u32x4 (&fl)[2], u32x2 tag) {
    bool ok = true;
#pragma unroll
    for (int i = 0; i < 2; ++i) ok = ok & ((fl[i].x ^ tag.x) < 0x10000u) & (fl[i].y == tag.y) & ((fl[i].z ^ tag.x) < 0x10000u) & (fl[i].w == tag.y);
    return __all(ok);
}
template <int N> DEV unsigned h_poll(rsrc_t rs, int gb, int kh, int lane, u32x2 tag) {
    unsigned o = (lane ^ ((CX(gb) & 3) << 4)) * FST, m = 0; PIN(o);
    u32x2 fl[N];
#pragma unroll
    for (int i = 0; i < N; ++i) fl[i] = __builtin_amdgcn_raw_buffer_load_b64(rs, o, OFF_FH + ((gb & (HREP - 1)) * 256 + kh * 128 + (i ^ (CX(gb) >> 2)) * 64) * FST, SC1);
#pragma unroll
    for (int i = 0; i < N; ++i) {
        const u64 b = __ballot(((fl[i].x ^ tag.x) < 0x10000u) & (fl[i].y == tag.y));
#pragma unroll
        for (int j = 0; j < 4; ++j) m |= ((unsigned)(b >> (16 * j)) & 0xffffu) == 0xffffu ? 8u << (16 * i + 4 * j) : 0u;
    }
    return m;
}
DEV void sig_or(unsigned a, unsigned v) { asm volatile("s_mov_b64 exec, 1\n\tds_or_b32 %0, %1\n\ts_mov_b64 exec, -1" :: "v"(a), "v"(v) : "memory"); }

constexpr int A_STAGE = 32768, A_XT = 4 * A_STAGE, A_PRM = A_XT + 16384, A_AGG = A_PRM + 2048, A_FLG = A_AGG + 512, A_GW = A_FLG + 128,
              A_MAT = A_GW + 2048, SMEM = A_MAT + 11264;
constexpr int E1P = 1040, A_EX = A_GW, A_E1 = 0, A_XC = 18 * E1P + 224, A_W = A_XC + 16384, A_HS = 2 * A_STAGE - 8192, A_R0 = 2 * A_STAGE, A_OG = A_MAT + 9216;
static_assert(A_XC % 256 == 0 && A_W + 18432 + 512 <= A_HS, "");
constexpr int F_MID = 8, F_EM = 12, F_MAX = 16, F_TAG = 32, F_FULL = 40, F_FREE = 44, F_HC = 48, F_HR = 52, F_TC = 56, F_TR = 60, F_TT = 64, F_R0 = 68, F_XC = 72, F_ST = 76, F_WM = 80;

constexpr int XG = 0xFF;
DEV void slot_of(int gb, int& mp, int& j1) { gb ^= XG; mp = (gb & 7) * 8 + (gb >> 5); j1 = (gb >> 3) & 3; }
DEV u32x4 mat_sf(int k1, int c, int q) { const float dt = -(float)(k1 + 128 * c) * (1.0f / 2048.0f); return cmat1(dt * (float)(4 * q), dt); }
DEV u32x4 mat_si(int k1, int c, int q) { return cmat1((float)((k1 + 512 * q) * c) * (1.0f / 2048.0f), (float)c * (1.0f / 16.0f)); }

DEV void mk_tables(lptr smem, int gb, int wid, int lane) {
    int mp, j1; slot_of(gb, mp, j1);
    const int c = lane & 15, q = lane >> 4;
    const bool spc = mp == 0 && !(j1 & 1);
    const int k1 = mp ? mp : (j1 == 3 ? 64 : 0);
    lptr mt = smem + A_MAT + lane * 16;
    if (wid == 0) {
        const float dt = -(float)(j1 + 4 * (spc ? c & 7 : c)) * (1.0f / 64.0f), t0 = __builtin_amdgcn_fractf(dt * (float)(4 * q));
        float cc[4], ss[4];
#pragma unroll
        for (int e = 0; e < 4; ++e) { const float f = fmaf((float)e, dt, t0); cc[e] = __builtin_amdgcn_cosf(f); ss[e] = __builtin_amdgcn_sinf(f); }
        const unsigned c0 = pkb(cc[0], cc[1]), c1 = pkb(cc[2], cc[3]), s0 = pkb(ss[0], ss[1]), s1 = pkb(ss[2], ss[3]);
        const unsigned c0l = pkb(cc[0] - lo2f(c0), cc[1] - hi2f(c0)), c1l = pkb(cc[2] - lo2f(c1), cc[3] - hi2f(c1));
        const unsigned s0l = pkb(ss[0] - lo2f(s0), ss[1] - hi2f(s0)), s1l = pkb(ss[2] - lo2f(s1), ss[3] - hi2f(s1));
        u32x4 m[4] = {(u32x4){c0, c1, s0 ^ SG, s1 ^ SG}, (u32x4){s0, s1, c0, c1}, (u32x4){c0l, c1l, s0l ^ SG, s1l ^ SG}, (u32x4){s0l, s1l, c0l, c1l}};
        if (spc) {
            m[0] = c < 8 ? (u32x4){c0, c1, 0, 0} : (u32x4){0, 0, c0, c1}; m[1] = c < 8 ? (u32x4){s0, s1, 0, 0} : (u32x4){0, 0, s0, s1};
            m[2] = c < 8 ? (u32x4){c0l, c1l, 0, 0} : (u32x4){0, 0, c0l, c1l}; m[3] = c < 8 ? (u32x4){s0l, s1l, 0, 0} : (u32x4){0, 0, s0l, s1l};
        }
#pragma unroll
        for (int i = 0; i < 4; ++i) lst<u32x4>(mt + i * 1024, m[i]);
    } else if (wid == 1) {
        const int jq = spc ? (4 * q) & 7 : 4 * q;
        float cc[4], ss[4];
        const float t0 = __builtin_amdgcn_fractf((float)(c * (j1 + 4 * jq)) * (1.0f / 64.0f));
#pragma unroll
        for (int e = 0; e < 4; ++e) {
            const float f = fmaf((float)c, (float)e * (1.0f / 16.0f), t0);
            const float wg = !spc || (gb == XG && jq + e == 0) ? 0.125f : 0.25f;
            cc[e] = __builtin_amdgcn_cosf(f) * wg; ss[e] = -__builtin_amdgcn_sinf(f) * wg;
        }
        const unsigned c0 = pkh(cc[0], cc[1]), c1 = pkh(cc[2], cc[3]), n0 = pkh(ss[0], ss[1]), n1 = pkh(ss[2], ss[3]);
        u32x4 mia = (u32x4){c0, n0, c1, n1}, mib = (u32x4){n0, c0 ^ SG, n1, c1 ^ SG};
        if (spc) { mib = q < 2 ? (u32x4){0, 0, 0, 0} : mia ^ SG; if (q >= 2) mia = (u32x4){0, 0, 0, 0}; }
        lst<u32x4>(mt + 6 * 1024, mia); lst<u32x4>(mt + 7 * 1024, mib);
        lst<u32x4>(mt + 8 * 1024, cmat1((float)(c * q) * -0.25f, (float)c * (-1.0f / 16.0f)));
    } else {
        lst<u32x4>(mt + (2 + wid) * 1024, wid == 2 ? mat_sf(k1, c, q) : mat_si(k1, c, q));
        const int ja = (spc ? 0 : 8 * (wid - 2)) + (lane >> 3);
        const float dt = -(float)(j1 + 4 * ja) * (1.0f / 1024.0f), t0 = dt * (float)(2 * (lane & 7)), t1 = t0 + dt;
        lst<f32x4>(mt + (7 + wid) * 1024, (f32x4){__builtin_amdgcn_cosf(t0), __builtin_amdgcn_sinf(t0), __builtin_amdgcn_cosf(t1), __builtin_amdgcn_sinf(t1)});
    }
}

template <int SLP = 0> DEV void c_chunk(const u16* __restrict__ Cre, const u16* __restrict__ Cim, unsigned lb, int gb, int da, int lw, int lane, int j) {
    const u32x4 rc = mkdesc(lw < 2 ? Cre : Cim, D_ * D_ * 2);
    const unsigned vo = (lane >> 3) * 8192 + (((lane & 7) ^ (lane >> 3)) << 4);
#pragma unroll
    for (int i = 0; i < 8; ++i) {
        if (SLP && i) __builtin_amdgcn_s_sleep(SLP);
        dma16(rc, lb + ((j + 2) & 3) * A_STAGE + lw * 8192 + i * 1024, vo, (512 * (lw & 1) + 256 * (i & 1) + 32 * (da >> 1) + (i >> 1)) * 2048 + (8 * (da & 1) + (j ^ CX(gb))) * 128);
    }
}
DEV void touch(u32x4 d, unsigned l, unsigned vo, unsigned so) {
    asm volatile("s_nop 0\n\tbuffer_load_dword %0, %1, %2 offen sc1 lds" :: "v"(vo), "s"(d), "s"(so), "{m0}"(l) : "memory");
}
DEV void c_pref(const u16* __restrict__ Cre, const u16* __restrict__ Cim, unsigned lb, int gb, int lt, int da, int lane, int j) {
    touch(mkdesc((lt & 2) ? Cim : Cre, D_ * D_ * 2), lb + A_PRM, ((lane >> 5) * 256 + (lane & 31)) * 2048,
          (512 * (lt & 1) + 32 * (da >> 1)) * 2048 + (8 * (da & 1) + (j ^ CX(gb))) * 128);
}
constexpr int KS1 = 32 - 4 * NP, KS2 = 4 * NP, NST = KS1 + 2 * KS2;
constexpr int st_k(int st) { return st < 32 ? st : st - KS2; }
constexpr int b_st(int b) { return b < 2 * KS1 ? b >> 1 : b - KS1; }
constexpr int b_n(int b) { return b < 2 * KS1 ? b & 1 : b >= KS1 + 32; }
constexpr bool b_new(int b) { return b < KS1 + 32 && !b_n(b) && !(b_st(b) & 3); }

DEV void role_mma(char* ws, float* __restrict__ out, lptr smem, int gb, int wid, int lane, u64 pc, int len) {
    int lt, pt; tile_of(gb, lt, pt);
    const int wr = wid >> 1, wc = wid & 1, c = lane & 15, q = lane >> 4;
    const rsrc_t rs = mkrsrc(ws, WS_END);
    const unsigned fb = ldsa(smem + A_FLG);
    TSD(); TS(0);
    u64 tag = __hip_atomic_load((GAS u64*)(ws + OFF_E), __ATOMIC_RELAXED, __HIP_MEMORY_SCOPE_WORKGROUP) + 0x9E3779B97F4A7C15ull;
    unsigned cpf = 0;
    if (CPF == 1) { if (wid == 3) cpf = __builtin_amdgcn_raw_buffer_load_b32(mkrsrc((const void*)pc, max(len, 0)), lane * 128, ((gb >> 3) % CPM) * 8192, 0); }
    SCHED();
    if (wid == 0 && lane < 32) lst<unsigned>(smem + A_FLG + lane * 4, 0u);
    mk_tables(smem, gb, wid, lane);
    f32x4 acc[4][4];
#pragma unroll
    for (int m = 0; m < 4; ++m)
#pragma unroll
        for (int n = 0; n < 4; ++n) acc[m][n] = Z4;
    u32x4 af[2][4], bf_[2][4];
#pragma unroll
    for (int m = 0; m < 4; ++m) { af[1][m] = (u32x4){0, 0, 0, 0}; bf_[1][m] = af[1][m]; }
    lptr fp[8];
#pragma unroll
    for (int i = 0; i < 8; ++i) { fp[i] = smem + (i & 2) * A_STAGE + ((i & 4 ? 128 + wc * 64 : wr * 64) + c) * 128 + (((4 * (i & 1) + q) ^ (c & 7)) << 4); PIN(fp[i]); }
    PIN(tag); PIN(cpf);
    if (wid == 0 && lane == 0) lst<u64>(smem + A_FLG + F_TAG, tag);
    tag = (u64)(unsigned)__builtin_amdgcn_readfirstlane((unsigned)tag) | (u64)(unsigned)__builtin_amdgcn_readfirstlane((unsigned)(tag >> 32)) << 32;
    __builtin_amdgcn_s_waitcnt(0xC07F);
    TS(13);
    unsigned fst = fb + F_ST, sv = 0;
    auto step = [&](int sl, bool pf) INL {
#pragma unroll
        for (int hf = 0; hf < 2; ++hf)
#pragma unroll
            for (int i = 0; i < 16; ++i) {
                const int nn = SNK && (i & 4) ? 3 - (i & 3) : i & 3;
                acc[i >> 2][nn] = mfb(af[1 - hf][i >> 2], bf_[1 - hf][nn], acc[i >> 2][nn]);
                constexpr int RD[16] = RDPAT;
                const int r = RD[i];
                if (r == 0) af[hf][0] = lld<u32x4>(fp[(sl & 2) + hf] + (sl & 1) * A_STAGE);
                else if (r > 0 && r < 5) bf_[hf][r - 1] = lld<u32x4>(fp[4 + (sl & 2) + hf] + (sl & 1) * A_STAGE + (r - 1) * 2048);
                else if (r >= 5) af[hf][r - 4] = lld<u32x4>(fp[(sl & 2) + hf] + (sl & 1) * A_STAGE + (r - 4) * 2048);
                if (pf && hf && i == 11) sv = *(volatile LAS unsigned*)(lptr)(uintptr_t)fst;
                SCHED();
            }
        LDS_WAIT();
    };
#pragma unroll 1
    for (int kt = 0; kt < 3; ++kt) {
#pragma unroll
        for (int i = 0; i < 4; ++i) { BAR(); step(i, false); }
    }
    PIN(fst);
    BAR(); step(0, false);
    BAR(); __builtin_amdgcn_s_setprio(PRIOE); step(1, true);
#pragma unroll
    for (int i = 0; i < 2; ++i) {
        unsigned ss = __builtin_amdgcn_readfirstlane(sv);
        need_bit(fst, ss, 4u << (4 * i));
        TL(30 + i);
        step(2 + i, i == 0);
    }
    __builtin_amdgcn_s_setprio(PRIOT);
    sig(fb + F_FREE, 1u << 20);
    float2 lam[2], coef[2], l16[2], l128[2];
#pragma unroll
    for (int n = 0; n < 2; ++n) {
        const f32x4 a = lld<f32x4>(smem + A_PRM + (wc * 32 + 2 * c + n) * 32), b = lld<f32x4>(smem + A_PRM + (wc * 32 + 2 * c + n) * 32 + 16);
        lam[n] = make_float2(a[0], a[1]); coef[n] = make_float2(a[2], a[3]); l16[n] = make_float2(b[0], b[1]); l128[n] = make_float2(b[2], b[3]);
    }
#pragma unroll
    for (int m = 0; m < 4; ++m)
#pragma unroll
        for (int n = 0; n < 4; ++n) acc[m][n] = mfb(af[1][m], bf_[1][n], acc[m][n]);
    float2 l32[2], l64[2];
#pragma unroll
    for (int n = 0; n < 2; ++n) { l32[n] = cmul(l16[n], l16[n]); l64[n] = cmul(l32[n], l32[n]); }
    TS(1);
    float2 h[2][16];
#pragma unroll
    for (int n = 0; n < 2; ++n) h[n][0] = make_float2(acc[0][n][0], acc[0][2 + n][0]);
#pragma unroll
    for (int t = 1; t < 16; ++t)
#pragma unroll
        for (int n = 0; n < 2; ++n) h[n][t] = cfma(lam[n], h[n][t - 1], make_float2(acc[t >> 2][n][t & 3], acc[t >> 2][2 + n][t & 3]));
    float2 loc[2], wag[2], wq[2];
#pragma unroll
    for (int n = 0; n < 2; ++n) {
        float2 a0, a1, a2, a3;
        rows4(h[n][15].x, a0.x, a1.x, a2.x, a3.x);
        rows4(h[n][15].y, a0.y, a1.y, a2.y, a3.y);
        const float2 s2 = cfma(l16[n], a0, a1), s3 = cfma(l16[n], s2, a2);
        wag[n] = cfma(l16[n], s3, a3);
        loc[n] = sel4(q, make_float2(0.f, 0.f), a0, s2, s3);
    }
    lptr aggp = smem + A_AGG + (wc * 16 + c) * 16;
    const unsigned flg = ldsa(smem + A_FLG) + wc * 4;
    float2 W0[2] = {make_float2(0.f, 0.f), make_float2(0.f, 0.f)};
    if (wr == 0) {
        if (q == 0) lst<f32x4>(aggp, (f32x4){wag[0].x, wag[0].y, wag[1].x, wag[1].y});
        asm volatile("" ::: "memory");
        if (lane == 0) lds_add(flg);
    } else {
#pragma unroll 1
        for (int spin = 0; spin < (1 << 20); ++spin) { if (lds_peek(flg)) break; }
        const f32x4 w0 = lld<f32x4>(aggp);
        W0[0] = make_float2(w0[0], w0[1]); W0[1] = make_float2(w0[2], w0[3]);
        const float2 g0 = cfma(l64[0], W0[0], wag[0]), g1 = cfma(l64[1], W0[1], wag[1]);
        if (lt < 15 && q < 2) {
            const float2 gg = q == 0 ? g0 : g1;
            st128<SC1>((u32x4){__float_as_uint(gg.x), (unsigned)tag, __float_as_uint(gg.y), (unsigned)(tag >> 32)},
                       rs, (2 * c + q) * 16 + OFF_GRAN + ((15 - lt) * 1024 + pt * 64 + wc * 32) * 16);
        }
    }
    TS(2);
    __builtin_amdgcn_s_setprio(PRIOW);
#pragma unroll
    for (int n = 0; n < 2; ++n) { PIN(lam[n].x); PIN(lam[n].y); PIN(coef[n].x); PIN(coef[n].y); PIN(l16[n].x); PIN(l16[n].y); PIN(l128[n].x); PIN(l128[n].y); }
    float2 pw[2][8], l8[2], lq[2], l512[2];
#pragma unroll
    for (int n = 0; n < 2; ++n) {
        wq[n] = sel4(q, make_float2(1.f, 0.f), l16[n], l32[n], cmul(l32[n], l16[n]));
        loc[n] = cfma(wq[n], W0[n], loc[n]);
        if (wr) wq[n] = cmul(wq[n], l64[n]);
        pw[n][0] = cmul(coef[n], lam[n]);
        l8[n] = cmul(lam[n], lam[n]); l8[n] = cmul(l8[n], l8[n]); l8[n] = cmul(l8[n], l8[n]);
        const float2 l256 = cmul(l128[n], l128[n]);
        lq[n] = sel4(q, make_float2(1.f, 0.f), l128[n], l256, cmul(l256, l128[n]));
        l512[n] = cmul(l256, l256);
    }
#pragma unroll
    for (int n = 0; n < 2; ++n) {
        PIN(wq[n].x); PIN(wq[n].y); PIN(loc[n].x); PIN(loc[n].y); PIN(l8[n].x); PIN(l8[n].y); PIN(lq[n].x); PIN(lq[n].y); PIN(l512[n].x); PIN(l512[n].y);
    }
#pragma unroll
    for (int t = 0; t < 8; ++t) {
#pragma unroll
        for (int n = 0; n < 2; ++n) {
            if (t) pw[n][t] = cmul(pw[n][t - 1], lam[n]);
            h[n][2 * t] = cmul(coef[n], h[n][2 * t]); h[n][2 * t + 1] = cmul(coef[n], h[n][2 * t + 1]);
        }
#pragma unroll
        for (int n = 0; n < 2; ++n) { PIN(pw[n][t].x); PIN(pw[n][t].y); PIN(h[n][2 * t].x); PIN(h[n][2 * t].y); PIN(h[n][2 * t + 1].x); PIN(h[n][2 * t + 1].y); }
        SCHED();
    }
    __builtin_amdgcn_s_setprio(PRIOT);
    await<0>(fb + F_FREE, 8u << 20);
    TS(3);
    if (lt > 0) {
        u32x4 gr[4][2];
        unsigned off = ((16 - lt + q) * 1024 + pt * 64 + wc * 32 + 2 * c) * 16;
#pragma unroll 1
        for (int spin = 0; spin < (1 << 20); ++spin) {
            bool ok = true;
            PIN(off);
#pragma unroll
            for (int j = 0; j < 4; ++j) {
                const rsrc_t rg = mkrsrc(ws + OFF_GRAN + j * 65536, (4 - j) * 65536);
                gr[j][0] = __builtin_amdgcn_raw_buffer_load_b128(rg, off, 0, SC1);
                gr[j][1] = __builtin_amdgcn_raw_buffer_load_b128(rg, off + 16, 0, SC1);
            }
#pragma unroll
            for (int j = 0; j < 4; ++j) {
                const bool v = lt - 1 - (4 * j + q) >= 0;
#pragma unroll
                for (int n = 0; n < 2; ++n) ok = ok & (!v | ((gr[j][n].y == (unsigned)tag) & (gr[j][n].w == (unsigned)(tag >> 32))));
            }
            if (__all(ok)) break;
            asm volatile("" ::: "memory");
        }
#pragma unroll
        for (int n = 0; n < 2; ++n) {
            float2 cs = make_float2(__uint_as_float(gr[3][n].x), __uint_as_float(gr[3][n].z));
#pragma unroll
            for (int j = 2; j >= 0; --j) cs = cfma(l512[n], cs, make_float2(__uint_as_float(gr[j][n].x), __uint_as_float(gr[j][n].z)));
            cs = cmul(cs, lq[n]);
            cs.x = rowsum4(cs.x); cs.y = rowsum4(cs.y);
            loc[n] = cfma(wq[n], cs, loc[n]);
        }
    }
    TS(4);
    lptr tb = smem + A_HS + wid * 2048;
    PIN(lane);
#pragma unroll
    for (int r = 0; r < 4; ++r) {
        const float2 s0 = r > 1 ? cmul(l8[0], loc[0]) : loc[0], s1 = r > 1 ? cmul(l8[1], loc[1]) : loc[1];
#pragma unroll
        for (int t = 0; t < 4; ++t) {
            const float2 v0 = cfma(pw[0][(4 * r + t) & 7], s0, h[0][4 * r + t]), v1 = cfma(pw[1][(4 * r + t) & 7], s1, h[1][4 * r + t]);
            lst<unsigned>(tb + t * 256 + lane * 4, pkb(v0.x, v1.x));
            lst<unsigned>(tb + 1024 + t * 256 + lane * 4, pkb(-v0.y, -v1.y));
        }
        asm volatile("" ::: "memory");
        lptr rb = tb + (lane >> 4) * 256 + (lane & 3) * 64 + ((lane >> 2) & 3) * 16;
        const unsigned hoff = OFF_H + ((lane >> 4) * 64 + pt * 2 + wc) * 8192 + lt * 512 + wr * 256 + (lane & 15) * 16;
#pragma unroll
        for (int pl = 0; pl < 2; ++pl) st128<SC1>(lld<u32x4>(rb + pl * 1024), rs, hoff + (r * 256 + pl * 32) * 8192);
        asm volatile("" ::: "memory");
    }
    TS(5);
    if (CP2 == 1) { if (wid == 3) cpf = __builtin_amdgcn_raw_buffer_load_b32(mkrsrc((const void*)pc, max(len, 0)), lane * 128, ((gb >> 3) % CPM) * 8192, 0); }
    if (CP2 == 2) cpf = __builtin_amdgcn_raw_buffer_load_b32(mkrsrc((const void*)pc, max(len, 0)), (lane & 15) * 128 + wid * 2048, ((gb >> 3) % CPM) * 8192, 0);
    const int da = da_of(pt), kh = da & 1;
    u32x2 xi[2][2][4];
    await<0>(fb + F_XC, 4u);
    {
        const int c2 = lane & 15, q2 = lane >> 4;
#pragma unroll
        for (int n = 0; n < 2; ++n)
#pragma unroll
            for (int i = 0; i < 8; ++i)
                xi[n][i >> 2][i & 3] = lld<u32x2>(smem + A_XC + wid * 4096 + (n ^ kh ^ 1) * 2048 + (4 * q2 + (i & 3)) * 128 +
                                                  (((2 * (i >> 2) + (c2 >> 3)) ^ ((2 * q2 + ((i >> 1) & 1)) & 3)) << 5) + (c2 & 3) * 8);
#pragma unroll
        for (int i = 0; i < 16; ++i) PIN(xi[i >> 3][(i >> 2) & 1][i & 3]);
    }
    if (CP2) PIN(cpf);
    wait_vm<0>();
    TS(6);
    const bool pub = sig_rtn(fb + F_HC) == 3u;
    const u32x2 tg = {(unsigned)tag, (unsigned)(tag >> 32)};
    if (pub) {
        if (lane < HREP) __builtin_amdgcn_raw_buffer_store_b64(tg, rs, lane * 256 * FST, OFF_FH + (pt * 16 + lt) * FST, SC1);
        TS(15);
    }
    f32x4 ac[8][2];
    {
        const unsigned mk = ((lane >> 2) & 1) == kh ? ~0u : 0u;
#pragma unroll
        for (int n = 0; n < 2; ++n)
#pragma unroll
            for (int i = 0; i < 8; ++i) {
                const u32x2 v = xi[n][i >> 2][i & 3] & mk;
                ac[i & 4][n][i & 3] = lo2f(v.x); ac[(i & 4) + 1][n][i & 3] = hi2f(v.x); ac[(i & 4) + 2][n][i & 3] = lo2f(v.y); ac[(i & 4) + 3][n][i & 3] = hi2f(v.y);
            }
#pragma unroll
        for (int i = 0; i < 16; ++i) PIN(ac[i >> 1][i & 1]);
    }
    PIN(lane);
    unsigned vo[2], so[32];
    vo[0] = ((lane & 15) >> 2) * 256 + (lane >> 4) * 64 + (lane & 3) * 16 + (kh ^ 1) * 1024; vo[1] = vo[0] ^ 1024;
    PIN(vo[0]); PIN(vo[1]);
#pragma unroll
    for (int k = 0; k < 32; ++k) {
        so[k] = OFF_H + lt * 524288 + kh * 131072 + wid * 2048 + (((XE & 2) ? 0 : k >> 2) ^ CX(gb)) * 16384 + ((k & 1) + (k & 2) * 16) * 8192;
        if (SPC || k < 8) asm volatile("" : "+s"(so[k]));
    }
    auto ldB = [&](int b) { return __builtin_amdgcn_raw_buffer_load_b128(rs, vo[b_n(b)], so[st_k(b_st(b))], SC1); };
    lptr ab[4];
#pragma unroll
    for (int i = 0; i < 4; ++i) { ab[i] = smem + (1 - (i >> 1)) * 2 * A_STAGE + (lane & 15) * 128 + (((4 * (i & 1) + (lane >> 4)) ^ (lane & 7)) << 4); PIN(ab[i]); }
    auto ldA = [&](int f) {
        const int k = st_k(f >> 3);
        return lld<u32x4>(ab[(k & 1) + ((k >> 2) & 2)] + ((k >> 1) & 3) * 16384 + (f & 7) * 2048);
    };
    unsigned fv = fb + F_FULL; PIN(fv);
    if (pub) {
        int spin; unsigned m = 0;
        if (PDL) __builtin_amdgcn_s_sleep(PDL);
        if (PDE && !(PDM >> (gb & 7) & 1)) __builtin_amdgcn_s_sleep(PDE);
        if (PD0 && lt == 0) __builtin_amdgcn_s_sleep(PD0);
        TS(19);
#pragma unroll 1
        for (spin = 0; spin < (1 << 20); ++spin) {
            m = h_poll<NP1>(rs, gb, kh, lane, tg);
            if (spin == 0) TS(16);
            if (spin == 1) TS(17);
            if (spin == 2) TS(18);
            if ((m & HFIRST) == HFIRST) break;
        }
        sig_or(fb + F_FULL, m);
    }
    unsigned fe = 0, fc = 0, fs = await<1>(fb + F_FULL, 8u);
    __builtin_amdgcn_s_setprio(PRIOG);
    TS(7);
    auto need = [&](unsigned bit) INL { need_bit(fv, fs, bit); };
    u32x4 A[AD], B[NB];
#pragma unroll
    for (int b = 0; b < NB; ++b) { if (b && b_new(b)) need(8u << (b_st(b) & 28)); B[b] = ldB(b); SCHED(); }
    SCHED();
    need(4u);
#pragma unroll
    for (int f = 0; f < AD; ++f) A[f] = ldA(f);
    SCHED();
    const unsigned po = OFF_P + ((lt * 16 + da) * 4 + wid) * 6144;
    unsigned pk[16], oo[4], fr;
    lptr og, rp;
    sfor<0, NST>([&](auto ic) INL {
        constexpr int st = decltype(ic)::v, b0 = st < KS1 ? 2 * st : st + KS1, n0 = st >= 32;
        auto nextB = [&](int b) INL {
            if (b + NB >= 64) return;
            if (b_new(b + NB)) { fs = __builtin_amdgcn_readfirstlane(fe); need(8u << (b_st(b + NB) & 28)); TL(20 + (b_st(b + NB) >> 2)); }
            B[b % NB] = ldB(b + NB);
        };
#pragma unroll
        for (int k = 0; k < 8; ++k) {
            const int f = 8 * st + k;
            constexpr int WG = st < KS1 ? WG1 : WG2;
            if (WG > 1 && k % WG == 0 && f + AD < 8 * NST) { __builtin_amdgcn_s_waitcnt(0xC07F | (AD - WG) << 8); SCHED(); }
            MF2(B[b0 % NB], A[f % AD], ac[k][n0]);
            SCHED();
            if (k == (st < KS1 ? 3 : 0) && b0 + NB < 64 && (b_new(b0 + NB) || (st < KS1 && b_new(b0 + 1 + NB)))) fe = *(volatile LAS unsigned*)(lptr)(uintptr_t)fv;
            if (k == 7) nextB(b0);
            if (k == 1 && st > 0 && st <= KS1) nextB(b0 - 1);
            if (st == 32) {
#pragma unroll
                for (int i = 0; i < 2; ++i) { pk[2 * k + i] = pkb(ac[(k & 4) + 2 * i][0][k & 3], ac[(k & 4) + 2 * i + 1][0][k & 3]); PIN(pk[2 * k + i]); }
#pragma unroll
                for (int v = 0; v < 5; ++v)
                    if ((3 * v + 2) >> 1 == k) st128<SC1>((u32x4){pk[3 * v], pk[3 * v + 1], pk[3 * v + 2], tg.x}, rs, po + v * 1024 + lane * 16);
                if (k == 7) { __builtin_amdgcn_raw_buffer_store_b64((u32x2){pk[15], tg.x}, rs, po + 5120 + lane * 8, 0, SC1); TS(29); }
            }
            SCHED();
            if (st < KS1) { MF2(B[(b0 + 1) % NB], A[f % AD], ac[k][1]); SCHED(); }
            if (f + AD < 8 * NST) {
                const int g = f + AD;
                if (!(g & 31) && g < 256) { if (g > 64) fs = __builtin_amdgcn_readfirstlane(fc); need(4u << (g >> 5) * 4); }
                if (!((g + AD) & 31) && g + AD < 256 && g + AD > 64) fc = *(volatile LAS unsigned*)(lptr)(uintptr_t)fv;
                A[f % AD] = ldA(g);
                if ((g & 31) == 31 && st_k(g >> 3) < 20 && ((g >> 3) < KS1 || g >= 256)) sig_o<F_FREE - F_FULL>(fv, 1u << (st_k(g >> 3) & 28));
            }
            SCHED();
        }
        if (st == 34) {
            PIN(lane);
            const int c2 = lane & 15, q2 = lane >> 4;
#pragma unroll
            for (int e = 0; e < 4; ++e) { oo[e] = ((64 * q2 + 512 * wid + 256 * kh + 16 * e + lt) * D_ + 256 * (c2 >> 3) + 32 * (da >> 1) + 4 * (c2 & 7)) * 4; PIN(oo[e]); }
            og = smem + A_OG + (16 * wid + 4 * q2) * 4; rp = smem + A_R0 + wid * 8192 + lane * 16; fr = fb + F_R0;
            PIN(og); PIN(rp); PIN(fr);
            SCHED();
        }
        if (st == 0) { TS(8); TC(60); }
        if (st == KS1 - 1) { TS(9); TC(55); }
        if (st == 31) { TS(28); TC(56); }
        if (st == NST - 3) { TS(14); TC(61); }
    });
    asm volatile("s_nop 7");
    TS(10);
    await<0>(fr, 1u << wid);
    TS(11);
    const rsrc_t ro = mkrsrc(out, S_ * D_ * 4);
    const f32x4 o2 = lld<f32x4>(og);
    f32x4 r0[8];
#pragma unroll
    for (int i = 0; i < 8; ++i) r0[i] = lld<f32x4>(rp + i * 1024);
    SCHED();
#pragma unroll
    for (int i = 0; i < 8; ++i) {
        const int e = i & 3, m = i & 4;
#pragma unroll
        for (int j = 0; j < 4; ++j) r0[i][j] = fmaf(o2[e], ac[m + j][1][e], r0[i][j]);
        st128<WTO>(__builtin_bit_cast(u32x4, r0[i]), ro, oo[e] + 512 * m);
        SCHED();
    }
    asm volatile("gm_end0:");
    TS(12);
    TSE(0, wid == 0);
    TSE(2, pub);
}

#define TSARG
#define TSPASS
struct Gate {
    unsigned xa, wa, xv, gw; float sum; int rot;
    DEV void rd(int kt) { asm volatile("ds_read_b32 %0, %2\n\tds_read_b32 %1, %3" : "=&v"(xv), "=&v"(gw) : "v"(xa + (kt & 3) * A_STAGE), "v"(wa + (kt ^ rot) * 128) : "memory"); }
    DEV void acc() {
        asm volatile("s_waitcnt lgkmcnt(0)" : "+v"(xv), "+v"(gw) :: "memory");
        const unsigned pr = pkb(lo2f(xv) * lo2f(gw), hi2f(xv) * hi2f(gw));
        sum += lo2f(pr); sum += hi2f(pr); PIN(sum);
    }
};
struct Tab { CM sf, si, ff, fi; u32x4 mt[2], xf, xb; f32x2 ti[2]; };
DEV void mid_tables(lptr smem, int gb, int w, int lane, Tab& t) {
    int mp, j1; slot_of(gb, mp, j1);
    const int c = lane & 15, q = lane >> 4;
    lptr mb = smem + A_MAT + lane * 16;
#pragma unroll
    for (int i = 0; i < 2; ++i) { t.ti[i] = lld<f32x2>(smem + A_MAT + 9216 + (2 * w + i) * 128 + c * 8); t.mt[i] = lld<u32x4>(mb + (6 + i) * 1024); }
    u32x4 sf0 = lld<u32x4>(mb + 4096), si0 = lld<u32x4>(mb + 5120);
    const u32x4 f0 = lld<u32x4>(mb + 8192);
    SCHED();
    asm volatile("" : "=v"(t.xf), "=v"(t.xb));
    if (w >= 4 && mp == 0 && !(j1 & 1)) {
        sf0 = mat_sf(64, c, q); si0 = mat_si(64, c, q);
        const float dt = -(float)(32 + 64 * c) * (1.0f / 1024.0f);
        t.xf = cmat1(dt * (float)(4 * q), dt); t.xb = cmat1((float)(c * (32 + 256 * q)) * (1.0f / 1024.0f), (float)c * (1.0f / 16.0f));
    }
    PIN(t.xf); PIN(t.xb);
    t.sf = cexp(sf0); t.si = cexp(si0); t.ff = cexp(f0); t.fi = cexp((u32x4){f0.x ^ SG, f0.y ^ SG, f0.z, f0.w});
    PIN(t.mt[0]); PIN(t.mt[1]); PIN(t.ti[0]); PIN(t.ti[1]);
}
DEV int u_exp(unsigned em) { return min(max((int)(em & 0xffu), 4), 254); }

DEV void role_load(const u16* __restrict__ x, const u16* __restrict__ Bre, const u16* __restrict__ Bim, const u16* __restrict__ Cre, const u16* __restrict__ Cim,
                   const u16* __restrict__ gate_w, char* ws, lptr smem, int gb, int lw, int lane, Tab& tab TSARG) {
    __builtin_amdgcn_s_waitcnt(0x0F70);
    Gate g;
    int lt, pt; tile_of(gb, lt, pt);
    const int srow = lane >> 3, schunk = (lane & 7) ^ srow;
    const int rot = ROT;
    const u32x4 rx = mkdesc(x, S_ * D_ * 2), rb = mkdesc((lw & 1) ? Bim : Bre, D_ * D_ * 2);
    const unsigned lb = ldsa(smem);
    const unsigned vA = (16 * (srow >> 2) + (srow & 3)) * 2048 + schunk * 16, sA = (lt * 128 + 64 * (lw >> 1) + 8 * (lw & 1)) * 2048;
    const unsigned vB = 2 * srow * 2048 + schunk * 16, sB = (pt * 64 + 32 * (lw >> 1)) * 2048;
    auto stA = [&](int kk) {
#pragma unroll
        for (int i = 0; i < 4; ++i)
            dma16(rx, lb + (kk & 3) * A_STAGE + lw * 4096 + i * 1024, vA, sA + (32 * (i & 1) + 4 * (i >> 1)) * 2048 + (kk ^ rot) * 128);
    };
    auto stB = [&](int kk) {
#pragma unroll
        for (int i = 0; i < 4; ++i)
            dma16(rb, lb + (kk & 3) * A_STAGE + 16384 + lw * 4096 + i * 1024, vB, sB + (16 * (i & 1) + (i >> 1)) * 2048 + (kk ^ rot) * 128);
    };
    TS(0);
    __builtin_amdgcn_s_setprio(PRIOL);
    stA(0); stB(0);
    dma16(mkdesc(gate_w, D_ * 2), lb + A_GW + (lw & 1) * 1024, lane * 16, (lw & 1) * 1024);
#pragma unroll
    for (int i = 0; i < 4; ++i)
        dma16(rx, lb + A_XT + lw * 4096 + i * 1024, (lane >> 1) * 32768 + (lane & 1) * 16, (i * 512 + lt) * 2048 + lw * 512 + da_of(pt) * 32);
    const int gi = 8 * pt + 2 * lw + (lane >> 5);
    g.xa = lb + gi * 128 + ((((lane & 31) >> 2) ^ (gi & 7)) << 4) + (lane & 3) * 4;
    g.wa = lb + A_GW + (lane & 31) * 4; g.sum = 0.f; g.rot = rot;
    stA(1); stB(1); stA(2); stB(2);
    TS(6);
    const int c = lane & 15, q = lane >> 4;
    const unsigned fb = lb + A_FLG;
    lptr ub = smem + A_XT + lw * 4096 + (lane >> 2) * 64 + (lane & 3) * 8, ea = smem + A_E1 + c * E1P + q * 256 + lw * 64;
    lptr mb = smem + A_MAT + lane * 16, tb = smem + A_MAT + 9216 + c * 128 + q * 32;
    PIN(ub); PIN(ea); PIN(mb); PIN(tb);
#pragma unroll 1
    for (int kt = 0; kt < 13; ++kt) { wait_vm<16>(); TL(8 + kt); BAR(); TL(28 + kt); if (kt == 0) { TS(28); TC(60); } g.rd(kt); stA(kt + 3); stB(kt + 3); g.acc(); }
    u32x4 mt[4], ut[4];
    f32x4 tf[2];
    wait_vm<16>(); TL(21); BAR(); TL(41); g.rd(13);
#pragma unroll
    for (int i = 0; i < 4; ++i) mt[i] = lld<u32x4>(mb + i * 1024);
#pragma unroll
    for (int i = 0; i < 2; ++i) tf[i] = lld<f32x4>(tb + i * 16);
    g.acc();
#pragma unroll
    for (int i = 0; i < 4; ++i) PIN(mt[i]);
    PIN(tf[0]); PIN(tf[1]);
    wait_vm<8>(); sig(fb + F_ST, 1u); TL(42);
    wait_vm<0>(); sig(fb + F_ST, 0x10u);
    __builtin_amdgcn_s_setprio(PRIOD);
    await<0>(fb + F_ST, 0x40u);
    TS(43); TC(61);
    g.rd(14); g.acc(); g.rd(15); g.acc();
    sig(fb + F_FREE, 1u << 20);
    TS(2);
    const unsigned em = await<1>(fb + F_EM, 0x10000u << lw);
    TS(3);
#pragma unroll
    for (int i = 0; i < 4; ++i) ut[i] = trfrag(ub + i * 1024, 32);
    const float su = __uint_as_float((unsigned)(258 - u_exp(em)) << 23);
    tf[0] *= su; tf[1] *= su; PIN(tf[0]); PIN(tf[1]);
#pragma unroll
    for (int i = 0; i < 4; ++i) {
        f32x4 r = mfb(ut[i], mt[2], Z4), m = mfb(ut[i], mt[3], Z4);
        r = mfb(ut[i], mt[0], r); m = mfb(ut[i], mt[1], m);
        f32x4 yr, yi;
#pragma unroll
        for (int e = 0; e < 4; ++e) {
            const float tc = tf[e >> 1][2 * (e & 1)], ts = tf[e >> 1][2 * (e & 1) + 1];
            yr[e] = fmaf(r[e], tc, -m[e] * ts); yi[e] = fmaf(r[e], ts, m[e] * tc);
        }
        lst<u32x4>(ea + i * 16, pk8(yr, yi));
    }
    if (gb == XG) {
        const unsigned sg = 0xbf803f80u;
#pragma unroll
        for (int i = 0; i < 4; ++i) {
            const f32x4 e = mfb(ut[i], c == 0 ? (u32x4){sg, sg, 0, 0} : (u32x4){0, 0, sg, sg}, Z4);
            if (c < 2) lst<u32x4>(ea + 16 * E1P + i * 16, pk8(e * su, Z4));
        }
    }
    sig(fb + F_MID, 1u);
    TS(54);
    __builtin_amdgcn_s_setprio(PRIOM);
    mid_tables(smem, gb, lw + 4, lane, tab);
    if (CDL) __builtin_amdgcn_s_sleep(CDL);
    TS(53);
    await<1>(fb + F_FREE, 8u << 20);
    c_chunk(Cre, Cim, lb, gb, da_of(pt), lw, lane, 0);
    if (C1E) c_chunk(Cre, Cim, lb, gb, da_of(pt), lw, lane, 1);
    TS(52);
    const float gg = bfr(1.0f / (1.0f + expf(-bfr(half_sum(g.sum)))));
    const int tau = (gi & 64) | ((gi & 12) << 2) | ((gi & 48) >> 2) | (gi & 3);
    if ((lane & 31) == 0) __builtin_amdgcn_raw_buffer_store_b64((u32x2){__float_as_uint(gg), __float_as_uint(bfr(1.0f - gg))}, mkrsrc(ws, WS_END),
                                                                (tau & 15) * 1024 + (lt * 8 + (tau >> 4)) * 8, OFF_G, SC1);
}

DEV unsigned ldw(rsrc_t r, unsigned vo, unsigned so) { return __builtin_amdgcn_raw_buffer_load_b32(r, vo, so, 0); }
DEV unsigned amax4(u32x4 p) { return pkmax(pkmax(p.x & 0x7fff7fffu, p.y & 0x7fff7fffu), pkmax(p.z & 0x7fff7fffu, p.w & 0x7fff7fffu)); }

DEV void role_aux(const u16* __restrict__ w_r, const u16* __restrict__ w_i, const u16* __restrict__ lam_re, const u16* __restrict__ lam_im,
                  const u16* __restrict__ log_dt, const u16* __restrict__ Dskip, char* ws, lptr smem, int gb, int aw, int lane, Tab& tab TSARG) {
    int lt, pt; tile_of(gb, lt, pt);
    int mp, j1; slot_of(gb, mp, j1);
    const int c = lane & 15, q = lane >> 4;
    const bool spc = mp == 0 && !(j1 & 1);
    const rsrc_t rs = mkrsrc(ws, WS_END), rr = mkrsrc(w_r, S_ * KF * 2), ri = mkrsrc(w_i, S_ * KF * 2);
    TS(0);
    __builtin_amdgcn_s_setprio(PRIOA);
    u32x4 fch, fsh;
    const bool m0 = 16 * aw + c == 0;
    {
        const float th = (float)(16 * aw + c) * (1.0f / 128.0f);
#pragma unroll
        for (int i = 0; i < 4; ++i) {
            const float t0 = __builtin_amdgcn_fractf(th * (float)(16 * (i >> 1) + 2 * (i & 1) + 4 * q)), t1 = t0 + th;
            const float c0 = __builtin_amdgcn_cosf(t0), c1 = __builtin_amdgcn_cosf(t1);
            const float s0 = m0 ? 1.f : -__builtin_amdgcn_sinf(t0), s1 = m0 ? -1.f : -__builtin_amdgcn_sinf(t1);
            fch[i] = pkb(c0, c1); fsh[i] = pkb(s0, s1);
        }
        PIN(fch); PIN(fsh);
    }
    unsigned sg64 = (c & 1) ? SG : 0u, sgA = (c & 2) ? SG : 0u, sgB = ((c + 1) & 2) ? SG : 0u;
    constexpr unsigned WR = 128 * KF * 2;
    const bool xh = gb == XG && aw >= 2;
    const int c0 = 2 * (lane & 7) + xh, jb = (lane >> 3) + 4 * (aw & 2);
    const int k1 = mp ? mp : spc ? (lane & 4) * 16 : (j1 == 1 ? 0 : 64);
    const int kd = j1 + 4 * (spc ? c0 & 7 : c0) + 64 * jb;
    unsigned wo = (aw < 2 ? (k1 * KF + kd) * 2 + aw * 8 * WR : ((128 - k1) * KF + 1020 - kd) * 2 + (3 - aw) * 8 * WR) & ~3u;
    unsigned wo0 = aw < 2 ? wo : k1 || aw == 3 ? wo + 7 * WR : wo - 9 * WR;
    unsigned uoff = (lane >> 3) * 131072 + (lane & 7) * 16 + OFF_U + aw * 64 * 16384 + lt * 2048 + da_of(pt) * 128;
    PIN(sg64); PIN(sgA); PIN(sgB); PIN(wo); PIN(wo0); PIN(uoff);
    unsigned lru, liu, ldu, um = 0;
    u32x2 tag, dk[4], xc[8];
    u32x4 fl[2];
    asm volatile("" : "=v"(fl[0]), "=v"(fl[1]));
    u32x3 wre[8], wim[8];
    u32x4 wsc = {0, 0, 0, 0};
    f32x4 vr[4], vi[4];
    float dt = 0.f, er = 0.f; float2 lam = make_float2(0.f, 0.f);
#pragma unroll
    for (int d1 = 0; d1 < 4; ++d1) { vr[d1] = Z4; vi[d1] = Z4; }
    auto wx = [&]() INL {
        if (j1 == 0 && aw == 3) {
            const int ks = (spc ? (lane >> 5) * 64 : mp) + 128 * c, ng = (S_ - ks) & (S_ - 1);
            unsigned o0 = (q & 1) ? (ng * KF + 511) * 2u : ks * KF * 2u, o1 = (q & 1) ? (ks * KF + 511) * 2u : ng * KF * 2u;
            PIN(o0); PIN(o1);
            wsc = (u32x4){ldw(rr, o0, 0), ldw(rr, o1, 0), ldw(ri, o0, 0), ldw(ri, o1, 0)};
        }
    };
    auto prm = [&]() INL {
        lru = ldw(mkrsrc(lam_re, 2048), (lane >> 1) * 4, pt * 128); liu = ldw(mkrsrc(lam_im, 2048), (lane >> 1) * 4, pt * 128);
        ldu = ldw(mkrsrc(log_dt, 2048), (lane >> 1) * 4, pt * 128);
#pragma unroll
        for (int i = 0; i < 4; ++i) dk[i] = __builtin_amdgcn_raw_buffer_load_b64(mkrsrc(Dskip, D_ * 2), q * 8 + i * 512, da_of(pt) * 32, 0);
    };
    if (PRS < 0) prm();
    const unsigned fb = ldsa(smem + A_FLG);
    bool uok = false;
    auto upub = [&]() INL { sig(fb + F_EM, (wave_pkmax(pkmax(pkmax(fl[0].x, fl[0].z), pkmax(fl[1].x, fl[1].z))) & 0xffu) | 0x8000u); };
    u32x4 uo[4];
    float2 coef = make_float2(0.f, 0.f);
    sfor<0, 14>([&](auto ic) INL {
        constexpr int kt = decltype(ic)::v;
        constexpr int GS[4] = GSTEP, KSO[4] = {0, 2, 3, 1}, JO[4] = {0, 2, 1, 3}, WRW[5] = WROW;
        auto ust = [&](int k) INL {
#pragma unroll
            for (int u = 0; u < 4; ++u) if (US + u / UPS == k && !(XM & 2)) st128<SC1>(uo[u], rs, uoff + JO[u] * 32768);
        };
        auto mem = [&]() INL {
            if (kt >= 10) {
#pragma unroll
                for (int r = 0; r < 8; ++r) {
                    if (r < WRW[kt < 10 ? 0 : kt - 10] || r >= WRW[kt < 10 ? 0 : kt - 9]) continue;
                    const unsigned so = r ? (aw < 2 ? r : 7 - r) * WR : 0u;
                    wre[r] = __builtin_amdgcn_raw_buffer_load_b96(rr, r ? wo : wo0, so, 0); wim[r] = __builtin_amdgcn_raw_buffer_load_b96(ri, r ? wo : wo0, so, 0);
                    if (r + 1 == URS) { SCHED(); wait_vm<2 * (URS - WRW[UCK - 10])>(); sig(fb + F_EM, 0x10000u << aw); TS(6); SCHED(); }
                }
            }
            if (kt == PRS) prm();
            if (kt == WXS) wx();
            if (kt == UPL && aw == 2) flag_get(rs, OFF_FU, gb, lane, fl);
        };
        if (kt == UFS) wait_vm<0>();
        if (kt == UFS + 1) { uoff = (lane >> 2) * 128 + (lane & 3) * 16; PIN(uoff); }
        if (kt == UCK && UPRE && aw == 2) { uok = flag_ok(fl, tag); if (uok) { upub(); LDS_WAIT(); } }
        TL(8 + kt);
        BAR();
        if (VL == 2) ust(kt - 1);
        if (kt == UCK) {
            if (aw == 2 && !uok) {
                int spin;
#pragma unroll 1
                for (spin = 0; spin < (1 << 20); ++spin) { if (flag_ok(fl, tag)) break; flag_get(rs, OFF_FU, gb, lane, fl); }
                upub();
            }
            await<0>(fb + F_EM, 0x8000u);
            const u32x4 rw = mkdesc(ws, WS_END);
#pragma unroll
            for (int i = 0; i < 4; ++i)
                dma16s(rw, ldsa(smem + A_XT) + aw * 4096 + i * 1024, uoff, OFF_U + ((mp >> 1) * 4 + j1) * 32768 + (mp & 1) * 64 + aw * 8192 + i * 2048);
            TS(2);
        }
        if (kt == ATS) mid_tables(smem, gb, aw, lane, tab);
        u32x4 xa[4][4];
#pragma unroll
        for (int g = 0; g < 4; ++g) {
            if (GS[g] != kt) continue;
#pragma unroll
            for (int i = 0; i < 4; ++i) xa[g][i] = trfrag(smem + A_XT + i * 4096 + KSO[g] * 1024 + lane * 8, 512);
        }
        if (kt == XS) {
#pragma unroll
            for (int i = 0; i < 8; ++i) xc[i] = lld<u32x2>(smem + A_XT + (i & 3) * 4096 + (32 * aw + 16 * (i >> 2) + c) * 32 + q * 8);
        }
        if (VL != 1) mem();
        SCHED();
#pragma unroll
        for (int g = 0; g < 4; ++g) {
            if (GS[g] != kt) continue;
#pragma unroll
            for (int i = 0; i < 4; ++i) { vr[i] = mfb(xa[g][i], fch, vr[i]); vi[i] = mfb(xa[g][i], fsh, vi[i]); PIN(vr[i]); PIN(vi[i]); }
            if (g == 0 || g == 2) { fch ^= sg64; fsh ^= sg64; PIN(fch); PIN(fsh); }
            if (g == 1) {
                const bool od = c & 1;
                const u32x4 a = (od ? fsh : fch) ^ sgA, b = (od ? fch : fsh) ^ sgB;
                fch = a; fsh = b; PIN(fch); PIN(fsh);
            }
        }
        if (kt == B1S) {
            const f32x4 t0r = vr[0] + vr[2], t1r = vr[0] - vr[2], t2r = vr[1] + vr[3], t3r = vr[1] - vr[3];
            const f32x4 t0i = vi[0] + vi[2], t1i = vi[0] - vi[2], t2i = vi[1] + vi[3], t3i = vi[1] - vi[3];
            vr[0] = t0r; vr[1] = t1r; vr[2] = t2r; vr[3] = t3r; vi[0] = t0i; vi[1] = t1i; vi[2] = t2i; vi[3] = t3i;
#pragma unroll
            for (int i = 0; i < 4; ++i) { PIN(vr[i]); PIN(vi[i]); }
        }
        if (kt == XS) {
#pragma unroll
            for (int i = 0; i < 8; ++i) PIN(xc[i]);
        }
#pragma unroll
        for (int u = 0; u < 4; ++u) {
            if (US + u / UPS != kt) continue;
            const int j = JO[u];
            f32x4 a, b;
            if (j == 0) { a = vr[0] + vr[2]; b = vi[0] + vi[2]; }
            if (j == 2) { a = vr[0] - vr[2]; b = vi[0] - vi[2]; }
            if (j == 1) { a = vr[1] + vi[3]; b = vi[1] - vr[3]; if (aw == 0) { if (m0) { a = vr[1]; b = -vr[3]; } } }
            if (j == 3) { a = vr[1] - vi[3]; b = vi[1] + vr[3]; if (aw == 0) { if (m0) { a = vi[1]; b = vi[3]; } } }
            uo[u] = quads(smem + A_XT + aw * 4096 + j * 1024, lane, (u32x2){pkb(a[0], a[1]), pkb(a[2], a[3])}, (u32x2){pkb(b[0], b[1]), pkb(b[2], b[3])});
            um = pkmax(um, amax4(uo[u]));
            PIN(um); PIN(uo[u]);
#pragma unroll
            for (int i = 0; i < 4; ++i) if (u < 3 && ((i & 1) || u == 0)) { PIN(vr[i]); PIN(vi[i]); }
        }
        if (kt == UFS - 1) {
            um = wave_pkmax(pkmax(um, um >> 16));
            if (lane == 0) lst<unsigned>(smem + A_FLG + F_MAX + aw * 4, um);
            tag = lld<u32x2>(smem + A_FLG + F_TAG); PIN(tag);
        }
        if (kt == DS || kt == DS + 1) {
#pragma unroll
            for (int i = 4 * (kt - DS); i < 4 * (kt - DS) + 4; ++i) {
                if (kt == DS) { dk[i & 3] = (u32x2){pkb(0.5f * lo2f(dk[i & 3].x), 0.5f * hi2f(dk[i & 3].x)), pkb(0.5f * lo2f(dk[i & 3].y), 0.5f * hi2f(dk[i & 3].y))}; PIN(dk[i & 3]); }
                const u32x2 d = dk[i & 3];
                xc[i & 7] = (u32x2){pkb(lo2f(xc[i & 7].x) * lo2f(d.x), hi2f(xc[i & 7].x) * hi2f(d.x)), pkb(lo2f(xc[i & 7].y) * lo2f(d.y), hi2f(xc[i & 7].y) * hi2f(d.y))};
                PIN(xc[i & 7]);
            }
        }
        if (kt == P0S) {
            PIN(lru); PIN(liu); PIN(ldu);
            if (lane & 1) { lru >>= 16; liu >>= 16; ldu >>= 16; }
            PIN(lru); PIN(liu); PIN(ldu);
        }
        if (kt >= P0S && kt < P0S + 4 && aw == 1) {
            const float lr = lo2f(lru), li = lo2f(liu);
            if (kt == P0S) { dt = bfr(expf(lo2f(ldu))); er = expf(dt * lr); PIN(dt); PIN(er); }
            if (kt == P0S + 1) { float sn, cs; sincosf(dt * li, &sn, &cs); lam = make_float2(er * cs, er * sn); PIN(lam.x); PIN(lam.y); }
            if (kt == P0S + 2) {
                const float nr = lam.x - 1.0f, ni = lam.y, den = lr * lr + li * li;
                coef = make_float2((nr * lr + ni * li) / den, (ni * lr - nr * li) / den);
                lst<f32x4>(smem + A_PRM + lane * 32, (f32x4){lam.x, lam.y, coef.x, coef.y});
            }
            if (kt == P0S + 3) {
                float2 l16 = lam;
#pragma unroll
                for (int i = 0; i < 4; ++i) l16 = cmul(l16, l16);
                float2 l128 = l16;
#pragma unroll
                for (int i = 0; i < 3; ++i) l128 = cmul(l128, l128);
                lst<f32x4>(smem + A_PRM + lane * 32 + 16, (f32x4){l16.x, l16.y, l128.x, l128.y});
            }
        }
        SCHED();
        if (kt == UFS && aw == 0) {
            const u32x4 m4 = lld<u32x4>(smem + A_FLG + F_MAX);
            const unsigned m = pkmax(pkmax(m4.x, m4.y), pkmax(m4.z, m4.w));
            flag_put(rs, OFF_FU, gb, (u32x2){(tag.x & 0xffff0000u) | ((m & 0xffffu) >> 7), tag.y}, lane);
        }
        if (VL != 2) ust(kt);
        if (VL == 1) mem();
        LDS_WAIT();
        SCHED();
    });
    TS(54);
    if (gb == XG && aw == 0 && lane == 0) __builtin_amdgcn_raw_buffer_store_b64(tag, rs, 0, OFF_E, SC1);
    __builtin_amdgcn_s_setprio(PRIOM);
    if (ATS == 14) mid_tables(smem, gb, aw, lane, tab);
    await<1>(fb + F_FREE, 8u << 20);
#pragma unroll
    for (int i = 0; i < 8; ++i) lst<u32x2>(smem + A_XC + aw * 4096 + (i >> 2) * 2048 + c * 128 + (((i & 3) ^ ((c >> 1) & 3)) << 5) + q * 8, xc[i]);
    sig(ldsa(smem + A_FLG + F_XC), 1u);
    TS(3);
    unsigned mx = 0;
    const bool skip = j1 == 0 && (lane & (spc ? 59 : 63)) == 0 && !xh, x8 = xh && (lane & 3) == 3;
    lptr wd[2];
    wd[0] = smem + A_W + (aw & 1) * 512 + c0 * 1024 + ((jb ^ c0) << 4);
    wd[1] = smem + A_W + (aw & 1) * 512 + (x8 ? 16384 + (lane & 4) * 256 + (jb << 4) : (c0 + 1) * 1024 + ((jb ^ (c0 + 1)) << 4));
    if (skip) wd[0] = smem + A_W + 18432;
    if (aw >= 2) { lptr t = wd[0]; wd[0] = wd[1]; wd[1] = t; }
    const unsigned wsel = (mp + j1) & 1 ? 0x07060302u : 0x05040100u;
    PIN(wd[0]); PIN(wd[1]);
    u32x4 wp[2][2];
#pragma unroll
    for (int i = 0; i < 2; ++i) {
#pragma unroll
        for (int e = 0; e < 4; ++e) {
            wp[0][i][e] = __builtin_amdgcn_perm(wim[4 * i + e].x, wre[4 * i + e].x, wsel);
            wp[1][i][e] = __builtin_amdgcn_perm(wim[4 * i + e].z, wre[4 * i + e].z, wsel);
        }
        lst<u32x4>(wd[0] + i * 256, wp[0][i]); lst<u32x4>(wd[1] + i * 256, wp[1][i]);
        mx = pkmax(mx, pkmax(amax4(wp[0][i]), amax4(wp[1][i])));
    }
    if (j1 == 0) {
        if (aw == 3) {
            PIN(wsc);
            const unsigned sel = (q & 1) ? 0x07060302u : 0x05040100u, p0 = __builtin_amdgcn_perm(wsc.z, wsc.x, sel), p1 = __builtin_amdgcn_perm(wsc.w, wsc.y, sel);
            mx = pkmax(mx, pkmax(p0 & 0x7fff7fffu, p1 & 0x7fff7fffu));
            const int it = spc ? (lane >> 5) * 8 : 0;
            lst<unsigned>(smem + A_W + it * 1024 + (c >> 2) * 256 + ((((q & 1) * 8) ^ it) << 4) + (c & 3) * 4,
                          pkb(0.5f * (lo2f(p0) + lo2f(p1)), 0.5f * (hi2f(p0) - hi2f(p1))));
        }
        if (gb == XG && (xh ? x8 : !(lane & 3) && !skip)) {
            lptr wm = smem + A_W + (xh ? 16384 + (lane & 4) * 256 + ((15 - jb) << 4) : c0 * 1024 + (((16 - jb) ^ c0) << 4));
#pragma unroll
            for (int r = 0; r < 8; ++r) {
                const int km = (16 - ((lane >> 2) & 1) - 8 * (aw & 1) - r) & 15;
                lst<unsigned>(wm + (km >> 2) * 256 + (km & 3) * 4, wp[0][r >> 2][r & 3]);
            }
        }
    }
    mx = wave_pkmax(pkmax(mx, mx >> 16));
    if (lane == 0) lst<unsigned>(smem + A_FLG + F_WM + aw * 4, mx);
    sig(ldsa(smem + A_FLG + F_MID), 1u);
    wait_vm<0>();
    SCHED();
}

DEV int role_mid(char* ws, lptr smem, int gb, int w, int lane, u32x2& tag, const Tab& tab TSARG) {
    int mp, j1; slot_of(gb, mp, j1);
    const int c = lane & 15, q = lane >> 4;
    const bool xtr = gb == XG, ld = w >= 4;
    const rsrc_t rs = mkrsrc(ws, WS_END);
    const unsigned lb = ldsa(smem), fb = lb + A_FLG;
    const CM &sf = tab.sf, &si = tab.si, &ff = tab.ff, &fi = tab.fi;
    const f32x2 (&ti)[2] = tab.ti;
    lptr mb = smem + A_MAT + lane * 16;
    u32x4 wv[2];
    TS(4);
    await<MSL>(fb + F_MID, 24u);
    u32x4 dv[2];
#pragma unroll
    for (int i = 0; i < 2; ++i) dv[i] = lld<u32x4>(smem + A_E1 + (2 * w + i) * E1P + lane * 16);
    const u32x4 m4 = lld<u32x4>(smem + A_FLG + F_WM);
    tag = lld<u32x2>(smem + A_FLG + F_TAG);
    const unsigned em = lld<unsigned>(smem + A_FLG + F_EM);
#pragma unroll
    for (int i = 0; i < 2; ++i) wv[i] = lld<u32x4>(smem + A_W + (2 * w + i) * 1024 + ((lane ^ (2 * w + i)) << 4));
#pragma unroll
    for (int i = 0; i < 2; ++i) dv[i] = pk8(mfh(dv[i], ff.re, Z4), mfh(dv[i], ff.im, Z4));
    const unsigned ew = min((pkmax(pkmax(m4.x, m4.y), pkmax(m4.z, m4.w)) & 0xffffu) >> 7, 245u);
    const float sw = __uint_as_float((246u - ew) << 23), swi = c < 8 ? sw : -sw;
    wv[0] = wcvt(wv[0], sw, swi); wv[1] = wcvt(wv[1], sw, swi);
#pragma unroll
    for (int i = 0; i < 2; ++i) dv[i] = wmul(mfh(sf.re, dv[i], Z4), mfh(sf.im, dv[i], Z4), wv[i]);
#pragma unroll
    for (int i = 0; i < 2; ++i) dv[i] = pk8(mfh(dv[i], si.re, Z4), mfh(dv[i], si.im, Z4));
    {
        f32x4 r[2], m[2];
#pragma unroll
        for (int i = 0; i < 2; ++i) {
            const f32x4 a = mfh(dv[i], fi.re, Z4), b = mfh(dv[i], fi.im, Z4);
#pragma unroll
            for (int e = 0; e < 4; ++e) { r[i][e] = fmaf(a[e], ti[i][0], b[e] * ti[i][1]); m[i][e] = fmaf(b[e], ti[i][0], -a[e] * ti[i][1]); }
        }
#pragma unroll
        for (int e = 0; e < 4; ++e)
            lst<u32x2>(smem + A_XT + (4 * q + e) * 1024 + (w >> 1) * 256 + c * 16 + (w & 1) * 8, (u32x2){pkh(r[0][e], r[1][e]), pkh(m[0][e], m[1][e])});
    }
    if (xtr && (w & 6) == 4) {
        const int it = 5 - w;
        const CM f = cexp(tab.xf), sf2 = cexp(it ? sf.im : lld<u32x4>(mb + 4096)), si2 = cexp(it ? si.im : lld<u32x4>(mb + 5120));
        u32x4 d = lld<u32x4>(smem + A_E1 + (16 + it) * E1P + lane * 16);
        d = pk8(mfh(d, f.re, Z4), mfh(d, f.im, Z4));
        d = wmul(mfh(sf2.re, d, Z4), mfh(sf2.im, d, Z4), wcvt(lld<u32x4>(smem + A_W + 16384 + it * 1024 + lane * 16), sw, swi));
        d = pk8(mfh(d, si2.re, Z4), mfh(d, si2.im, Z4));
        const f32x4 r = mfh(d, cexp(tab.xb).re, Z4);
#pragma unroll
        for (int e = 0; e < 4; ++e) lst<float>(smem + A_EX + (it * 256 + (4 * q + e) * 16 + c) * 4, r[e]);
    }
    TS(5);
    sig(fb + F_MID, 1u);
    await<0>(fb + F_MID, 16u);
    {
        f32x4 xa[2], xe[2];
#pragma unroll
        for (int i = 0; i < 2; ++i) { xa[i] = Z4; xe[i] = Z4; }
        if (xtr) {
            const float sg = (c & 1) ? -0.125f : 0.125f;
#pragma unroll
            for (int i = 0; i < 2; ++i) {
                xa[i] = lld<f32x4>(smem + A_EX + (2 * w + i) * 64 + q * 16) * sg;
                xe[i] = lld<f32x4>(smem + A_EX + 1024 + (2 * w + i) * 64 + q * 16) * -sg;
            }
        }
#pragma unroll
        for (int i = 0; i < 2; ++i) {
            const int s2 = 2 * w + i;
            const u32x4 ea = lld<u32x4>(smem + A_XT + s2 * 1024 + lane * 16);
            const f32x4 ta = mfh(ea, tab.mt[0], xa[i]), tb = mfh(ea, tab.mt[1], xe[i]);
            u32x4 tq = quads(smem + A_XT + s2 * 1024, lane, (u32x2){pkh(ta[0], ta[1]), pkh(ta[2], ta[3])}, (u32x2){pkh(tb[0], tb[1]), pkh(tb[2], tb[3])});
            PIN(tq);
            st128<SC1>(tq, rs, (lane >> 3) * 32768 + (lane & 7) * 16 + OFF_T + s2 * 262144 + j1 * 8192 + mp * 128);
        }
    }
    TS(7);
    tag.x = (tag.x & 0xffff0000u) | ew;
    return u_exp(em) - 267;
}

DEV void t_done(rsrc_t rs, unsigned fb, int gb, u32x2 tag, int lane) {
    int mp, j1; slot_of(gb, mp, j1);
    if (sig_rtn(fb + F_TC) == 7u) flag_put(rs, OFF_FT, (mp >> 3) * 32 + j1 * 8 + (mp & 7), tag, lane);
}

DEV void role_cload(const u16* __restrict__ Cre, const u16* __restrict__ Cim, char* ws, lptr smem, int gb, int lw, int lane, u32x2 tag TSARG) {
    int lt, pt; tile_of(gb, lt, pt);
    pt = da_of(pt);
    const unsigned lb = ldsa(smem), fb = lb + A_FLG;
    __builtin_amdgcn_s_setprio(3);
    if (!C1E) c_chunk(Cre, Cim, lb, gb, pt, lw, lane, 1);
    wait_vm<C1E ? 0 : 8>();
    t_done(mkrsrc(ws, WS_END), fb, gb, tag, lane);
    TS(44);
    await<2>(fb + F_HC, 4u);
    c_chunk(Cre, Cim, lb, gb, pt, lw, lane, 2);
    wait_vm<8>();
    sig(fb + F_FULL, 0x11u);
    c_chunk(Cre, Cim, lb, gb, pt, lw, lane, 3);
    wait_vm<0>();
    sig(fb + F_FULL, 0x1100u);
    TS(47);
    if (lw == 1) {
        unsigned m = await<2>(fb + F_FULL, 8u) & 0x88888888u;
#pragma unroll 1
        for (int spin = 0; spin < (1 << 20) && m != 0x88888888u; ++spin) {
            m = h_poll<2>(mkrsrc(ws, WS_END), gb, pt & 1, lane, tag);
            sig_or(fb + F_FULL, m);
        }
    }
#pragma unroll 1
    for (int j = 4; j < 8; ++j) {
        await<2>(fb + F_FREE, 4u << (4 * (j - 4)));
        if (!(XE & 1)) c_chunk<CSL>(Cre, Cim, lb, gb, pt, lw, lane, (XE & 8) ? j & 3 : j);
        wait_vm<8>();
        if (j > 4) sig(fb + F_FULL, 1u << (4 * (j - 1)));
        TS(44 + j);
    }
    wait_vm<0>();
    sig(fb + F_FULL, 1u << 28);
    asm volatile("gm_end1:");
    TSE(1, lw == 0);
}

DEV void role_last(char* ws, lptr smem, int gb, int w, int lane, u32x2 tag, int fe TSARG) {
    int s2t, dat; tile_of(gb, s2t, dat);
    dat = da_of(dat);
    const int c = lane & 15, q = lane >> 4, kh = dat & 1;
    const rsrc_t rs = mkrsrc(ws, WS_END);
    const unsigned fb = ldsa(smem + A_FLG);
    SCHED();
    u32x4 fc[2], fs[2];
    {
        float dt = (float)(32 * w + 16 * kh + c) * (1.0f / 128.0f); PIN(dt);
        float cc[8], ss[8];
        const float t0 = __builtin_amdgcn_fractf(dt * (float)(4 * q)), t1 = __builtin_amdgcn_fractf(dt * (float)(16 + 4 * q));
#pragma unroll
        for (int e = 0; e < 8; ++e) { const float f = fmaf((float)(e & 3), dt, e < 4 ? t0 : t1); cc[e] = __builtin_amdgcn_cosf(f); ss[e] = __builtin_amdgcn_sinf(f); }
        fc[0] = (u32x4){pkh(cc[0], cc[1]), pkh(cc[2], cc[3]), pkh(cc[4], cc[5]), pkh(cc[6], cc[7])};
        fs[0] = (u32x4){pkh(ss[0], ss[1]), pkh(ss[2], ss[3]), pkh(ss[4], ss[5]), pkh(ss[6], ss[7])};
        fc[1] = ((c & 1) ? fs[0] : fc[0]) ^ (((c + 1) & 2) ? SG : 0u);
        fs[1] = ((c & 1) ? fc[0] : fs[0]) ^ ((c & 2) ? SG : 0u);
        PIN(fc[0]); PIN(fs[0]); PIN(fc[1]); PIN(fs[1]);
        wait_vm<0>(); t_done(rs, fb, gb, tag, lane); TS(44);
    }
    u32x4 cA = fc[0], cB = fc[0], sB = fs[0];
    if (q == 0) { cA.x = (cA.x & 0xffff0000u) | ((c & 1) ? 0xbc00u : 0x3c00u); cB.x = (cB.x & 0xffff0000u) | 0x3800u; sB.x = (sB.x & 0xffff0000u) | ((c & 1) ? 0x3800u : 0xb800u); }
    lptr tw = smem + A_XT + ((lane >> 2) & 1) * 8192 + w * 2048 + ((lane >> 1) & 1) * 1024 + (((lane >> 3) ^ (lane & 6)) << 5) + (lane & 1) * 16;
    lptr tsc = smem + A_MAT + w * 32 + (lane >> 3) * 4, tr[4], rw[4], rr[2];
#pragma unroll
    for (int i = 0; i < 4; ++i) {
        tr[i] = smem + A_XT + (i >> 1) * 8192 + (i & 1) * 1024 + ((lane * 8) ^ (i * 64));
        rw[i] = smem + A_MAT + 1024 + w * 2048 + c * 128 + ((i ^ ((c >> 1) & 3)) << 5) + q * 8;
        PIN(tr[i]); PIN(rw[i]);
    }
#pragma unroll
    for (int i = 0; i < 2; ++i) { rr[i] = smem + A_MAT + 1024 + w * 2048 + (4 * q + 2 * i) * 128 + (((c >> 2) ^ ((2 * q + i) & 3)) << 5) + (c & 3) * 8; PIN(rr[i]); }
    lptr aw_ = smem + A_OG + (16 * w + c) * 4, ar_ = smem + A_OG + (16 * w + 4 * q) * 4, r0w = smem + A_R0 + w * 8192 + lane * 16;
    unsigned o = lane * 16, ft = fb + F_TT;
    PIN(cA); PIN(cB); PIN(sB); PIN(tw); PIN(tsc); PIN(aw_); PIN(ar_); PIN(r0w); PIN(o); PIN(ft);
    TS(45);
    __builtin_amdgcn_s_setprio(3);
    if (w == 0) {
        u32x4 fl[2];
        if (TP0) __builtin_amdgcn_s_sleep(TP0);
        TS(50);
#pragma unroll 1
        for (int spin = 0; spin < (1 << 20); ++spin) { if (TSL) __builtin_amdgcn_s_sleep(TSL); flag_get(rs, OFF_FT, gb, lane, fl); if (flag_ok(fl, tag)) break; }
        const unsigned em = wave_pkmax(pkmax(pkmax(fl[0].x, fl[0].z), pkmax(fl[1].x, fl[1].z))) & 0xffu;
#pragma unroll
        for (int i = 0; i < 2; ++i) {
            const float a = ldexpf(1.f, (int)(fl[i].x & 0xffu) - (int)em), b = ldexpf(1.f, (int)(fl[i].z & 0xffu) - (int)em);
            lst<u32x2>(smem + A_MAT + i * 512 + lane * 8, (u32x2){pkh(a, a), pkh(b, b)});
        }
        sig(fb + F_TR, em | 0x8000u);
    }
    fe += await<1>(fb + F_TR, 0x8000u) & 0xffu;
    TS(46);
    u32x4 tv[8];
#pragma unroll
    for (int i = 0; i < 8; ++i) {
        tv[i] = __builtin_amdgcn_raw_buffer_load_b128(rs, o + (i & 3) * 1024, OFF_T + (s2t * 8 + (dat >> 1)) * 32768 + w * 8192 + (i >> 2) * 4096, SC1);
        if (TLS) __builtin_amdgcn_s_sleep(TLS);
        SCHED();
    }
    const u32x2 gg = __builtin_amdgcn_raw_buffer_load_b64(rs, c * 8, OFF_G + s2t * 1024 + w * 256 + kh * 128, SC1);
    const unsigned tg0 = lld<unsigned>(smem + A_FLG + F_TAG);
    unsigned ts[8];
#pragma unroll
    for (int i = 0; i < 8; ++i) ts[i] = lld<unsigned>(tsc + i * 128);
    __builtin_amdgcn_s_setprio(PRIOR);
    auto scale = [&](int h) INL {
#pragma unroll
        for (int i = 4 * h; i < 4 * h + 4; ++i) {
            tv[i] = __builtin_bit_cast(u32x4, __builtin_bit_cast(f16x8, tv[i]) * __builtin_bit_cast(f16x8, (u32x4){ts[i], ts[i], ts[i], ts[i]}));
            PIN(tv[i]);
        }
    };
    auto put = [&](int h) INL {
#pragma unroll
        for (int i = 0; i < 4; ++i) lst<u32x4>(tw + i * 256, tv[4 * h + i]);
        sig(ft, h ? 0x100u : 1u);
    };
    u32x4 fa[2][4], fb_[2][4];
    auto get = [&]() INL {
        constexpr int JO[4] = {1, 3, 0, 2};
#pragma unroll
        for (int i = 0; i < 8; ++i) { fa[i >> 2][JO[i & 3]] = trfrag(tr[(i >> 2) * 2] + JO[i & 3] * 2048, 512); fb_[i >> 2][JO[i & 3]] = trfrag(tr[(i >> 2) * 2 + 1] + JO[i & 3] * 2048, 512); SCHED(); }
    };
    f32x4 P0[2], P1[2], P2[2], P3a[2], P3b[2];
#pragma unroll
    for (int n = 0; n < 2; ++n) { P0[n] = Z4; P1[n] = Z4; P2[n] = Z4; P3a[n] = Z4; P3b[n] = Z4; }
    auto mm = [&](u32x4 c1, u32x4 s1, u32x4 c3, u32x4 c0, u32x4 s0) INL {
#pragma unroll
        for (int n = 0; n < 2; ++n) {
            P1[n] = mfh(fa[n][1], c1, P1[n]); P3a[n] = mfh(fa[n][1], s1, P3a[n]); P3b[n] = mfh(fb_[n][1], c1, P3b[n]); P1[n] = mfh(fb_[n][1], s1, P1[n]);
            SCHED();
            P3b[n] = mfh(fa[n][3], s1, P3b[n]); P1[n] = mfh(fa[n][3], c3, P1[n]); P3a[n] = mfh(fb_[n][3], c3, P3a[n]); P1[n] = mfh(fb_[n][3], s1, P1[n]);
            SCHED();
            P0[n] = mfh(fa[n][0], c0, P0[n]); P0[n] = mfh(fb_[n][0], s0, P0[n]); P2[n] = mfh(fa[n][2], c0, P2[n]); P2[n] = mfh(fb_[n][2], s0, P2[n]);
            SCHED();
        }
    };
    scale(0); put(0);
    TS(47);
    scale(1);
    __builtin_amdgcn_s_setprio(PRX);
    await<0>(ft, 4u);
    get();
    sig(ft, 0x10u);
    await<0>(ft, 0x40u);
    put(1);
    SCHED();
    mm(fc[0], fs[0], cA, cB, sB);
    await<0>(ft, 0x400u);
    get();
    mm(fc[1], fs[1], fc[1], fc[1], fs[1]);
    TS(53);
    __builtin_amdgcn_s_setprio(PRV);
    u32x2 ga[2][4];
    const float g = ldexpf(__uint_as_float(gg.x), fe);
    lst<float>(aw_, 2.0f * __uint_as_float(gg.y));
#pragma unroll
    for (int n = 0; n < 2; ++n) {
        const f32x4 sp = P0[n] + P2[n], sm = P0[n] - P2[n], p3 = P3a[n] - P3b[n];
        const f32x4 av[4] = {sp + P1[n], sm - p3, sp - P1[n], sm + p3};
#pragma unroll
        for (int m = 0; m < 4; ++m) {
            const unsigned b0 = pkb(av[m][0], av[m][1]), b1 = pkb(av[m][2], av[m][3]);
            ga[n][m] = (u32x2){pkb(g * lo2f(b0), g * hi2f(b0)), pkb(g * lo2f(b1), g * hi2f(b1))};
        }
    }
    f32x4 r0[8];
    u32x2 rv[8];
#pragma unroll
    for (int mh = 0; mh < 2; ++mh) {
#pragma unroll
        for (int i = 0; i < 4; ++i) lst<u32x2>(rw[i], ga[i & 1][2 * mh + (i >> 1)]);
        asm volatile("" ::: "memory");
#pragma unroll
        for (int e = 0; e < 4; ++e) rv[4 * mh + e] = lld<u32x2>(rr[e >> 1] + (e & 1) * 128);
        asm volatile("" ::: "memory");
    }
    const f32x4 o2 = lld<f32x4>(ar_);
#pragma unroll
    for (int i = 0; i < 8; ++i) { r0[i] = (f32x4){lo2f(rv[i].x), hi2f(rv[i].x), lo2f(rv[i].y), hi2f(rv[i].y)}; PIN(r0[i]); }
    TS(48);
    __builtin_amdgcn_s_setprio(3);
    await<2>(fb + F_FREE, 4u << 16);
    if (RDL) __builtin_amdgcn_s_sleep(RDL);
    TS(51);
    u32x4 pv[5]; u32x2 pl;
    const unsigned po = OFF_P + ((s2t * 16 + (dat ^ 1)) * 4 + w) * 6144;
    int spin;
#pragma unroll 1
    for (spin = 0; spin < (1 << 20); ++spin) {
        PIN(o);
#pragma unroll
        for (int v = 0; v < 4; ++v) pv[v] = __builtin_amdgcn_raw_buffer_load_b128(rs, o + v * 1024, po, SC1);
        pv[4] = __builtin_amdgcn_raw_buffer_load_b128(rs, o, po + 4096, SC1);
        pl = __builtin_amdgcn_raw_buffer_load_b64(rs, o >> 1, po + 5120, SC1);
        if (__all((pv[0].w == tg0) & (pv[1].w == tg0) & (pv[2].w == tg0) & (pv[3].w == tg0) & (pv[4].w == tg0) & (pl.y == tg0))) break;
        asm volatile("" ::: "memory");
    }
    TS(52);
#pragma unroll
    for (int i = 0; i < 8; ++i) {
        const unsigned a = pv[(2 * i) / 3][(2 * i) % 3], b = i == 7 ? pl.x : pv[((2 * i + 1) / 3) % 5][(2 * i + 1) % 3];
        const float s = o2[i & 3];
        lst<f32x4>(r0w + i * 1024, (f32x4){fmaf(s, lo2f(a), r0[i][0]), fmaf(s, hi2f(a), r0[i][1]), fmaf(s, lo2f(b), r0[i][2]), fmaf(s, hi2f(b), r0[i][3])});
    }
    sig(fb + F_R0, 1u << w);
    asm volatile("gm_end2:");
    TS(49);
    TSE(w ? 4 : 5, w == 0 || w == 2);
}

NOPK __global__ __launch_bounds__(768, 3) void gm_kernel(const u16* __restrict__ x, const u16* __restrict__ w_r, const u16* __restrict__ w_i,
                                                         const u16* __restrict__ Bre, const u16* __restrict__ Bim, const u16* __restrict__ Cre,
                                                         const u16* __restrict__ Cim, const u16* __restrict__ lam_re,
                                                         const u16* __restrict__ lam_im, const u16* __restrict__ log_dt,
                                                         const u16* __restrict__ Dskip, const u16* __restrict__ gate_w, char* ws, float* __restrict__ out) {
    extern __shared__ __attribute__((aligned(16))) char smem_[];
    lptr smem = (lptr)smem_;
    u64 pc; int len;
    asm volatile("s_getpc_b64 %0\ngm_beg:\n\ts_mov_b32 %1, gm_end0 - gm_beg + 128\n\ts_max_i32 %1, %1, gm_end1 - gm_beg + 128\n\ts_max_i32 %1, %1, gm_end2 - gm_beg + 128"
                 : "=s"(pc), "=&s"(len) :: "scc");
    const int wid = __builtin_amdgcn_readfirstlane(threadIdx.x >> 6), lane = threadIdx.x & 63;
    int w = wid & 7, gb = blockIdx.x;
    asm volatile("" : "+s"(w), "+s"(gb));
    if (wid < 4) role_mma(ws, out, smem, blockIdx.x, wid, lane, pc, len);
    else {
        TSD();
        u32x2 tag;
        Tab tab;
        if (wid < 8) role_load(x, Bre, Bim, Cre, Cim, gate_w, ws, smem, blockIdx.x, wid - 4, lane, tab TSPASS);
        else role_aux(w_r, w_i, lam_re, lam_im, log_dt, Dskip, ws, smem, blockIdx.x, wid - 8, lane, tab TSPASS);
        const int fe = role_mid(ws, smem, gb, w, lane, tag, tab TSPASS);
        if (w >= 4) role_cload(Cre, Cim, ws, smem, gb, w - 4, lane, tag TSPASS);
        else role_last(ws, smem, gb, w, lane, tag, fe TSPASS);
    }
}

extern "C" void kernel_launch(void* const* d_in, const int* in_sizes, int n_in, void* d_out, int out_size, void* d_ws, size_t ws_size,
                              hipStream_t stream) {
    (void)hipFuncSetAttribute((const void*)gm_kernel, hipFuncAttributeMaxDynamicSharedMemorySize, SMEM);
    gm_kernel<<<256, 768, SMEM, stream>>>((const u16*)d_in[0], (const u16*)d_in[1], (const u16*)d_in[2], (const u16*)d_in[6], (const u16*)d_in[7],
                                          (const u16*)d_in[8], (const u16*)d_in[9], (const u16*)d_in[4], (const u16*)d_in[5], (const u16*)d_in[10],
                                          (const u16*)d_in[11], (const u16*)d_in[12], (char*)d_ws, (float*)d_out);
}
```

```cpp
#include <hip/hip_runtime.h>
#include <stdint.h>

typedef __attribute__((ext_vector_type(8))) __bf16 bf16x8;
typedef __attribute__((ext_vector_type(2))) __bf16 bf16x2;
typedef __attribute__((ext_vector_type(8))) _Float16 f16x8;
typedef __attribute__((ext_vector_type(2))) _Float16 f16x2;
typedef __attribute__((ext_vector_type(4))) float f32x4;
typedef __attribute__((ext_vector_type(2))) float f32x2;
typedef __attribute__((ext_vector_type(4))) unsigned u32x4;
typedef __attribute__((ext_vector_type(2))) unsigned u32x2;
typedef __attribute__((ext_vector_type(4))) short s16x4;
typedef __attribute__((ext_vector_type(2))) unsigned short u16x2;
typedef unsigned short u16;
typedef unsigned long long u64;
#define GAS __attribute__((address_space(1)))
#define LAS __attribute__((address_space(3)))
#define NOPK __attribute__((target("no-packed-fp32-ops")))
#define DEV __device__ __forceinline__

constexpr int S_ = 2048, D_ = 1024, KF = 513;

constexpr unsigned OFF_H = 0;
constexpr unsigned OFF_U = OFF_H + (8u << 20);
constexpr unsigned OFF_T = OFF_U + (4u << 20);
constexpr unsigned OFF_G = OFF_T + (4u << 20);
constexpr unsigned OFF_FU = OFF_G + 16384u;
constexpr unsigned OFF_FT = OFF_FU + 32768u;
constexpr unsigned OFF_FH = OFF_FT + 32768u;
constexpr unsigned OFF_GRAN = OFF_FH + 1048576u;
constexpr unsigned OFF_E = OFF_GRAN + 16u * 1024u * 16u;
constexpr unsigned WS_END = OFF_E + 128u;

#define TL(i)
#define TSD()
#define TS(i)
#define TC(i)
#define TSE(slot, on)

DEV unsigned pkb(float a, float b) { f32x2 v = {a, b}; return __builtin_bit_cast(unsigned, __builtin_convertvector(v, bf16x2)); }
DEV unsigned pkh(float a, float b) { f32x2 v = {a, b}; return __builtin_bit_cast(unsigned, __builtin_convertvector(v, f16x2)); }
DEV float lo2f(unsigned p) { return __uint_as_float(p << 16); }
DEV float hi2f(unsigned p) { return __uint_as_float(p & 0xffff0000u); }
DEV float bfr(float f) { return lo2f(pkb(f, f)); }
DEV float2 cmul(float2 a, float2 b) { return make_float2(a.x * b.x - a.y * b.y, a.x * b.y + a.y * b.x); }
DEV float2 cfma(float2 a, float2 b, float2 c) { return make_float2(fmaf(a.x, b.x, fmaf(-a.y, b.y, c.x)), fmaf(a.x, b.y, fmaf(a.y, b.x, c.y))); }
DEV f32x4 mfb(u32x4 a, u32x4 b, f32x4 c) { return __builtin_amdgcn_mfma_f32_16x16x32_bf16(__builtin_bit_cast(bf16x8, a), __builtin_bit_cast(bf16x8, b), c, 0, 0, 0); }
DEV f32x4 mfh(u32x4 a, u32x4 b, f32x4 c) { return __builtin_amdgcn_mfma_f32_16x16x32_f16(__builtin_bit_cast(f16x8, a), __builtin_bit_cast(f16x8, b), c, 0, 0, 0); }
DEV unsigned pkmax(unsigned a, unsigned b) { return __builtin_bit_cast(unsigned, __builtin_elementwise_max(__builtin_bit_cast(u16x2, a), __builtin_bit_cast(u16x2, b))); }
typedef LAS char* lptr;
DEV unsigned ldsa(lptr p) { return (unsigned)(uintptr_t)p; }
template <class T> DEV T lld(lptr p) { return *(LAS const T*)p; }
template <class T> DEV void lst(lptr p, T v) { *(LAS T*)p = v; }
DEV u32x2 trread(lptr p) { return __builtin_bit_cast(u32x2, __builtin_amdgcn_ds_read_tr16_b64_v4i16((LAS s16x4*)p)); }
DEV u32x4 trfrag(lptr p, int second) { const u32x2 a = trread(p), b = trread(p + second); return (u32x4){a.x, a.y, b.x, b.y}; }
typedef __amdgpu_buffer_rsrc_t rsrc_t;
DEV u32x4 ldfrag(lptr tile, int row, int chunk) { return lld<u32x4>(tile + row * 128 + ((chunk ^ (row & 7)) << 4)); }
template <int N> DEV void wait_vm() { asm volatile("s_waitcnt vmcnt(%0)" :: "i"(N) : "memory"); }
#define LDS_WAIT() asm volatile("s_waitcnt lgkmcnt(0)" ::: "memory")
#define SCHED() __builtin_amdgcn_sched_barrier(0)
#define BAR() do { __builtin_amdgcn_s_barrier(); asm volatile("" ::: "memory"); } while (0)
#define PIN(x) asm volatile("" : "+v"(x))
template <int I> struct IC { static constexpr int v = I; };
template <int I, int N, class F> DEV void sfor(F&& f) { if constexpr (I < N) { f(IC<I>{}); sfor<I + 1, N>(f); } }
#define INL __attribute__((always_inline))
DEV rsrc_t mkrsrc(const void* p, unsigned n) { return __builtin_amdgcn_make_buffer_rsrc((void*)p, 0, n, 0x00020000); }
constexpr int SC1 = 16;
#define WTO 16
template <int AUX> DEV void st128(u32x4 v, rsrc_t rs, unsigned off) { __builtin_amdgcn_raw_buffer_store_b128(v, rs, off, 0, AUX); }
DEV void lds_add(unsigned a) { asm volatile("ds_add_u32 %0, %1" :: "v"(a), "v"(1u) : "memory"); }
DEV unsigned lds_peek(unsigned a) {
    unsigned v;
    asm volatile("ds_read_b32 %0, %1\n\ts_waitcnt lgkmcnt(0)" : "=v"(v) : "v"(a) : "memory");
    return __builtin_amdgcn_readfirstlane(v);
}
DEV void tile_of(int gb, int& ta, int& tb) {
    const int xcd = gb & 7, loc = gb >> 3;
    ta = (xcd >> 1) * 4 + (loc >> 3);
    tb = (xcd & 1) * 8 + (loc & 7);
}
DEV void rows4(float a, float& a0, float& a1, float& a2, float& a3) {
    const unsigned u = __float_as_uint(a);
    const auto r = __builtin_amdgcn_permlane32_swap(u, u, false, false);
    const auto p = __builtin_amdgcn_permlane16_swap(r[0], r[0], false, false);
    const auto s = __builtin_amdgcn_permlane16_swap(r[1], r[1], false, false);
    a0 = __uint_as_float(p[0]); a1 = __uint_as_float(p[1]); a2 = __uint_as_float(s[0]); a3 = __uint_as_float(s[1]);
}
DEV float rowsum4(float a) {
    const unsigned u = __float_as_uint(a);
    const auto r = __builtin_amdgcn_permlane32_swap(u, u, false, false);
    const unsigned t = __float_as_uint(__uint_as_float(r[0]) + __uint_as_float(r[1]));
    const auto p = __builtin_amdgcn_permlane16_swap(t, t, false, false);
    return __uint_as_float(p[0]) + __uint_as_float(p[1]);
}
DEV float2 sel4(int q, float2 a, float2 b, float2 c, float2 d) {
    PIN(d.x); PIN(d.y);
    const float2 e = (q & 1) ? b : a, f = (q & 1) ? d : c;
    return (q & 2) ? f : e;
}
template <int R> DEV unsigned ror16(unsigned v) { return __builtin_amdgcn_update_dpp(0u, v, 0x120 + R, 0xf, 0xf, false); }
DEV unsigned wave_pkmax(unsigned m) {
    m = pkmax(m, ror16<8>(m)); m = pkmax(m, ror16<4>(m)); m = pkmax(m, ror16<2>(m)); m = pkmax(m, ror16<1>(m));
    const auto r = __builtin_amdgcn_permlane32_swap(m, m, false, false);
    m = pkmax(r[0], r[1]);
    const auto p = __builtin_amdgcn_permlane16_swap(m, m, false, false);
    return pkmax(p[0], p[1]);
}
DEV float half_sum(float v) {
    v += __uint_as_float(ror16<8>(__float_as_uint(v))); v += __uint_as_float(ror16<4>(__float_as_uint(v)));
    v += __uint_as_float(ror16<2>(__float_as_uint(v))); v += __uint_as_float(ror16<1>(__float_as_uint(v)));
    const auto p = __builtin_amdgcn_permlane16_swap(__float_as_uint(v), __float_as_uint(v), false, false);
    return __uint_as_float(p[0]) + __uint_as_float(p[1]);
}
DEV u32x4 quads(lptr buf, int lane, u32x2 p, u32x2 n) {
    lptr wp = buf + (lane & 15) * 64 + ((lane >> 4) & 1) * 8;
    const int v = (lane >> 5) ^ ((lane >> 2) & 3);
    lst<u32x2>(wp + (v << 4), p); lst<u32x2>(wp + ((v ^ 2) << 4), n);
    asm volatile("" ::: "memory");
    return lld<u32x4>(buf + ((lane ^ (lane >> 4)) << 4));
}

#define PRIOA 2
#define NREP 8
#define PRIOM 1
#define PRIOT 2
#define PRIOG 2
#define AD 6
#define BD 7
#define WROW {0, 0, 0, 0, 6, 11, 16}
#define ROT (lt & 12)
#define PRIOL 3
#define XM 0
#define UFS 8
#define PRIOR 0
#define PRS 2
#define WXS 11
#define CPF 1
#define FST 8
#define HREP 8
#define NP1 1
#define PDL 48
#define PDE 0
#define PDM 0x80
#define PD0 32
#define CXM 6
#define CX(g) ((g) & CXM)
#define GSTEP {0, 0, 1, 1}
#define XS 2
#define B1S 2
#define US 3
#define UPS 1
#define DS 8
#define P0S 9
#define VL 1
#define PN -1
#define PG -1
#define SNK 1
#define HFIRST 8u
#define RDPAT {0, 1, 2, -1, 3, 4, -1, 5, 6, -1, 7, -1, -1, -1, -1, -1}
#define Z4 ((f32x4){0.f, 0.f, 0.f, 0.f})
constexpr unsigned SG = 0x80008000u;
struct CM { u32x4 re, im; };
DEV u32x4 cmat1(float t0, float dt) {
    float cc[4], ss[4];
    t0 = __builtin_amdgcn_fractf(t0);
#pragma unroll
    for (int e = 0; e < 4; ++e) { const float f = fmaf((float)e, dt, t0); cc[e] = __builtin_amdgcn_cosf(f); ss[e] = __builtin_amdgcn_sinf(f); }
    return (u32x4){pkh(ss[0], ss[1]), pkh(ss[2], ss[3]), pkh(cc[0], cc[1]), pkh(cc[2], cc[3])};
}
DEV CM cexp(u32x4 v) { CM m; m.re = (u32x4){v.z, v.w, v.x ^ SG, v.y ^ SG}; m.im = v; PIN(m.re); PIN(m.im); return m; }
DEV u32x4 pk8(f32x4 r, f32x4 i) { return (u32x4){pkh(r[0], r[1]), pkh(r[2], r[3]), pkh(i[0], i[1]), pkh(i[2], i[3])}; }
DEV u32x4 wmul(f32x4 r, f32x4 i, u32x4 w) {
    float yr[4], yi[4];
#pragma unroll
    for (int e = 0; e < 4; ++e) {
        const unsigned wu = w[e];
        const f16x2 h = __builtin_bit_cast(f16x2, wu);
        yr[e] = fmaf(r[e], (float)h[0], -i[e] * (float)h[1]); yi[e] = fmaf(r[e], (float)h[1], i[e] * (float)h[0]);
    }
    return (u32x4){pkh(yr[0], yr[1]), pkh(yr[2], yr[3]), pkh(yi[0], yi[1]), pkh(yi[2], yi[3])};
}
DEV u32x4 wcvt(u32x4 w, float sr, float si) {
#pragma unroll
    for (int e = 0; e < 4; ++e) w[e] = pkh(lo2f(w[e]) * sr, hi2f(w[e]) * si);
    PIN(w);
    return w;
}
DEV u32x4 mkdesc(const void* p, unsigned n) { const u64 a = (u64)p; return (u32x4){(unsigned)a, (unsigned)(a >> 32) & 0xffffu, n, 0x00020000u}; }
DEV void dma16(u32x4 d, unsigned l, unsigned vo, unsigned so) {
    asm volatile("s_nop 0\n\tbuffer_load_dwordx4 %0, %1, %2 offen lds" :: "v"(vo), "s"(d), "s"(so), "{m0}"(l) : "memory");
}
DEV void sig(unsigned a, unsigned v) { asm volatile("s_mov_b64 exec, 1\n\tds_add_u32 %0, %1\n\ts_mov_b64 exec, -1" :: "v"(a), "v"(v) : "memory"); }
DEV unsigned sig_rtn(unsigned a) {
    unsigned v;
    asm volatile("s_mov_b64 exec, 1\n\tds_add_rtn_u32 %0, %1, %2\n\ts_mov_b64 exec, -1\n\ts_waitcnt lgkmcnt(0)" : "=&v"(v) : "v"(a), "v"(1u) : "memory");
    return __builtin_amdgcn_readfirstlane(v);
}
template <int SLP> DEV unsigned await(unsigned a, unsigned mask) {
    unsigned v, n, r, t;
    asm volatile("s_mov_b32 %1, 0x400000\n"
                 "1:\tds_read_b32 %0, %4\n\ts_waitcnt lgkmcnt(0)\n\tv_readfirstlane_b32 %2, %0\n\ts_and_b32 %3, %2, %5\n\ts_cbranch_scc1 2f\n\t"
                 "s_sleep %6\n\ts_sub_u32 %1, %1, 1\n\ts_cbranch_scc0 1b\n"
                 "2:" : "=&v"(v), "=&s"(n), "=&s"(r), "=&s"(t) : "v"(a), "s"(mask), "i"(SLP) : "memory", "scc");
    return r;
}
DEV void flag_put(rsrc_t rs, unsigned off, int gb, u32x2 v, int lane) {
    if (lane < NREP) __builtin_amdgcn_raw_buffer_store_b64(v, rs, lane * 2048, off + gb * 8, SC1);
}
DEV void flag_get(rsrc_t rs, unsigned off, int gb, int lane, u32x4 (&fl)[2]) {
    unsigned o = lane * 16; PIN(o);
#pragma unroll
    for (int i = 0; i < 2; ++i) fl[i] = __builtin_amdgcn_raw_buffer_load_b128(rs, o + i * 1024, off + (gb & (NREP - 1)) * 2048, SC1);
}
DEV bool flag_ok(const u32x4 (&fl)[2], u32x2 tag) {
    bool ok = true;
#pragma unroll
    for (int i = 0; i < 2; ++i) ok = ok & ((fl[i].x ^ tag.x) < 0x10000u) & (fl[i].y == tag.y) & ((fl[i].z ^ tag.x) < 0x10000u) & (fl[i].w == tag.y);
    return __all(ok);
}
template <int N> DEV unsigned h_poll(rsrc_t rs, int gb, int lane, u32x2 tag) {
    unsigned o = lane * FST, m = 0; PIN(o);
    u32x2 fl[N];
#pragma unroll
    for (int i = 0; i < N; ++i) fl[i] = __builtin_amdgcn_raw_buffer_load_b64(rs, o, OFF_FH + (gb & (HREP - 1)) * 256 * FST + (i ^ (CX(gb) >> 1)) * 64 * FST, SC1);
#pragma unroll
    for (int i = 0; i < N; ++i) {
        const u64 b = __ballot(((fl[i].x ^ tag.x) < 0x10000u) & (fl[i].y == tag.y));
        m |= ((unsigned)b == ~0u ? 8u << (8 * i) : 0u) | ((unsigned)(b >> 32) == ~0u ? 128u << (8 * i) : 0u);
    }
    return m;
}
DEV void sig_or(unsigned a, unsigned v) { asm volatile("s_mov_b64 exec, 1\n\tds_or_b32 %0, %1\n\ts_mov_b64 exec, -1" :: "v"(a), "v"(v) : "memory"); }

constexpr int A_STAGE = 32768, A_XT = 4 * A_STAGE, A_PRM = A_XT + 16384, A_AGG = A_PRM + 2048, A_FLG = A_AGG + 512, A_GW = A_FLG + 128,
              A_MAT = A_GW + 2048, SMEM = A_MAT + 11264;
constexpr int E1P = 1040, A_EX = A_GW, A_W = 2 * A_STAGE, A_E1 = A_W + 19456, A_HS = 3 * A_STAGE + 16384, A_R0 = 0;
constexpr int F_MID = 8, F_EM = 12, F_MAX = 16, F_TAG = 32, F_FULL = 40, F_FREE = 44, F_HC = 48, F_HR = 52, F_TC = 56, F_TR = 60, F_TT = 64, F_R0 = 68, F_WM = 80;

constexpr int XG = 0xFF;
DEV void slot_of(int gb, int& mp, int& j1) { gb ^= XG; mp = (gb & 7) * 8 + (gb >> 5); j1 = (gb >> 3) & 3; }
DEV u32x4 mat_sf(int k1, int c, int q) { const float dt = -(float)(k1 + 128 * c) * (1.0f / 2048.0f); return cmat1(dt * (float)(4 * q), dt); }
DEV u32x4 mat_si(int k1, int c, int q) { return cmat1((float)((k1 + 512 * q) * c) * (1.0f / 2048.0f), (float)c * (1.0f / 16.0f)); }

DEV void mk_tables(lptr smem, int gb, int wid, int lane) {
    int mp, j1; slot_of(gb, mp, j1);
    const int c = lane & 15, q = lane >> 4;
    const bool spc = mp == 0 && !(j1 & 1);
    const int k1 = mp ? mp : (j1 == 3 ? 64 : 0);
    lptr mt = smem + A_MAT + lane * 16;
    if (wid == 0) {
        const float dt = -(float)(j1 + 4 * (spc ? c & 7 : c)) * (1.0f / 64.0f), t0 = __builtin_amdgcn_fractf(dt * (float)(4 * q));
        float cc[4], ss[4];
#pragma unroll
        for (int e = 0; e < 4; ++e) { const float f = fmaf((float)e, dt, t0); cc[e] = __builtin_amdgcn_cosf(f); ss[e] = __builtin_amdgcn_sinf(f); }
        const unsigned c0 = pkb(cc[0], cc[1]), c1 = pkb(cc[2], cc[3]), s0 = pkb(ss[0], ss[1]), s1 = pkb(ss[2], ss[3]);
        const unsigned c0l = pkb(cc[0] - lo2f(c0), cc[1] - hi2f(c0)), c1l = pkb(cc[2] - lo2f(c1), cc[3] - hi2f(c1));
        const unsigned s0l = pkb(ss[0] - lo2f(s0), ss[1] - hi2f(s0)), s1l = pkb(ss[2] - lo2f(s1), ss[3] - hi2f(s1));
        u32x4 m[4] = {(u32x4){c0, c1, s0 ^ SG, s1 ^ SG}, (u32x4){s0, s1, c0, c1}, (u32x4){c0l, c1l, s0l ^ SG, s1l ^ SG}, (u32x4){s0l, s1l, c0l, c1l}};
        if (spc) {
            m[0] = c < 8 ? (u32x4){c0, c1, 0, 0} : (u32x4){0, 0, c0, c1}; m[1] = c < 8 ? (u32x4){s0, s1, 0, 0} : (u32x4){0, 0, s0, s1};
            m[2] = c < 8 ? (u32x4){c0l, c1l, 0, 0} : (u32x4){0, 0, c0l, c1l}; m[3] = c < 8 ? (u32x4){s0l, s1l, 0, 0} : (u32x4){0, 0, s0l, s1l};
        }
#pragma unroll
        for (int i = 0; i < 4; ++i) lst<u32x4>(mt + i * 1024, m[i]);
    } else if (wid == 1) {
        const int jq = spc ? (4 * q) & 7 : 4 * q;
        float cc[4], ss[4];
        const float t0 = __builtin_amdgcn_fractf((float)(c * (j1 + 4 * jq)) * (1.0f / 64.0f));
#pragma unroll
        for (int e = 0; e < 4; ++e) {
            const float f = fmaf((float)c, (float)e * (1.0f / 16.0f), t0);
            const float wg = !spc || (gb == XG && jq + e == 0) ? 0.125f : 0.25f;
            cc[e] = __builtin_amdgcn_cosf(f) * wg; ss[e] = -__builtin_amdgcn_sinf(f) * wg;
        }
        const unsigned c0 = pkh(cc[0], cc[1]), c1 = pkh(cc[2], cc[3]), n0 = pkh(ss[0], ss[1]), n1 = pkh(ss[2], ss[3]);
        u32x4 mia = (u32x4){c0, n0, c1, n1}, mib = (u32x4){n0, c0 ^ SG, n1, c1 ^ SG};
        if (spc) { mib = q < 2 ? (u32x4){0, 0, 0, 0} : mia ^ SG; if (q >= 2) mia = (u32x4){0, 0, 0, 0}; }
        lst<u32x4>(mt + 6 * 1024, mia); lst<u32x4>(mt + 7 * 1024, mib);
        lst<u32x4>(mt + 8 * 1024, cmat1((float)(c * q) * -0.25f, (float)c * (-1.0f / 16.0f)));
    } else {
        lst<u32x4>(mt + (2 + wid) * 1024, wid == 2 ? mat_sf(k1, c, q) : mat_si(k1, c, q));
        const int ja = (spc ? 0 : 8 * (wid - 2)) + (lane >> 3);
        const float dt = -(float)(j1 + 4 * ja) * (1.0f / 1024.0f), t0 = dt * (float)(2 * (lane & 7)), t1 = t0 + dt;
        lst<f32x4>(mt + (7 + wid) * 1024, (f32x4){__builtin_amdgcn_cosf(t0), __builtin_amdgcn_sinf(t0), __builtin_amdgcn_cosf(t1), __builtin_amdgcn_sinf(t1)});
    }
}

DEV void c_chunk(const u16* __restrict__ Cre, const u16* __restrict__ Cim, unsigned lb, int gb, int pt, int lw, int lane, int j) {
    const u32x4 rc = mkdesc(lw < 2 ? Cre : Cim, D_ * D_ * 2);
    const unsigned vo = ((lane >> 5) * 256 + ((lane >> 3) & 3) * 4) * 2048 + (((lane & 7) ^ (lane >> 3)) << 4);
#pragma unroll
    for (int i = 0; i < 8; ++i)
        dma16(rc, lb + (j & 3) * A_STAGE + lw * 8192 + i * 1024, vo, (512 * (i & 1) + 16 * pt + (i >> 1)) * 2048 + (j ^ CX(gb)) * 256 + (lw & 1) * 128);
}

DEV void role_mma(char* ws, float* __restrict__ out, lptr smem, int gb, int wid, int lane, u64 pc, int len) {
    int lt, pt; tile_of(gb, lt, pt);
    const int wr = wid >> 1, wc = wid & 1, c = lane & 15, q = lane >> 4;
    const rsrc_t rs = mkrsrc(ws, WS_END);
    const unsigned fb = ldsa(smem + A_FLG);
    TSD(); TS(0);
    u64 tag = __hip_atomic_load((GAS u64*)(ws + OFF_E), __ATOMIC_RELAXED, __HIP_MEMORY_SCOPE_WORKGROUP) + 0x9E3779B97F4A7C15ull;
    unsigned cpf = 0;
    if (CPF == 1) { if (wid == 3) cpf = __builtin_amdgcn_raw_buffer_load_b32(mkrsrc((const void*)pc, max(len, 0)), lane * 128, ((gb >> 3) % 6) * 8192, 0); }
    if (CPF == 2) cpf = __builtin_amdgcn_raw_buffer_load_b32(mkrsrc((const void*)pc, max(len, 0)), (lane & 15) * 128 + wid * 2048, ((gb >> 3) % 6) * 8192, 0);
    if (CPF == 3) { if (lane < 3) cpf = __builtin_amdgcn_raw_buffer_load_b32(mkrsrc((const void*)pc, max(len, 0)), lane * 128 + wid * 384, (gb >> 3) * 1536, 0); }
    SCHED();
    if (wid == 0 && lane < 32) lst<unsigned>(smem + A_FLG + lane * 4, 0u);
    mk_tables(smem, gb, wid, lane);
    f32x4 acc[4][4];
#pragma unroll
    for (int m = 0; m < 4; ++m)
#pragma unroll
        for (int n = 0; n < 4; ++n) acc[m][n] = Z4;
    u32x4 af[2][4], bf_[2][4];
#pragma unroll
    for (int m = 0; m < 4; ++m) { af[1][m] = (u32x4){0, 0, 0, 0}; bf_[1][m] = af[1][m]; }
    PIN(tag); PIN(cpf);
    if (wid == 0 && lane == 0) lst<u64>(smem + A_FLG + F_TAG, tag);
    tag = (u64)(unsigned)__builtin_amdgcn_readfirstlane((unsigned)tag) | (u64)(unsigned)__builtin_amdgcn_readfirstlane((unsigned)(tag >> 32)) << 32;
    __builtin_amdgcn_s_waitcnt(0xC07F);
    TS(13);
#pragma unroll 4
    for (int kt = 0; kt < 16; ++kt) {
        BAR();
        lptr tA = smem + (kt & 3) * A_STAGE;
        lptr tB = tA + 16384;
#pragma unroll
        for (int hf = 0; hf < 2; ++hf)
#pragma unroll
            for (int i = 0; i < 16; ++i) {
                const int nn = SNK && (i & 4) ? 3 - (i & 3) : i & 3;
                acc[i >> 2][nn] = mfb(af[1 - hf][i >> 2], bf_[1 - hf][nn], acc[i >> 2][nn]);
                constexpr int RD[16] = RDPAT;
                const int r = RD[i];
                if (r == 0) af[hf][0] = ldfrag(tA, wr * 64 + c, 4 * hf + q);
                else if (r > 0 && r < 5) bf_[hf][r - 1] = ldfrag(tB, wc * 64 + (r - 1) * 16 + c, 4 * hf + q);
                else if (r >= 5) af[hf][r - 4] = ldfrag(tA, wr * 64 + (r - 4) * 16 + c, 4 * hf + q);
                SCHED();
                if (PN >= 0) { asm volatile("s_nop %0" :: "i"(r >= 0 ? (PN > 0 ? PN - 1 : 0) : (PN >= 0 ? PN : 0))); SCHED(); }
            }
        LDS_WAIT();
    }
    __builtin_amdgcn_s_setprio(PRIOT);
    sig(fb + F_FREE, 1u << 20);
    float2 lam[2], coef[2], l16[2], l128[2];
#pragma unroll
    for (int n = 0; n < 2; ++n) {
        const f32x4 a = lld<f32x4>(smem + A_PRM + (wc * 32 + 2 * c + n) * 32), b = lld<f32x4>(smem + A_PRM + (wc * 32 + 2 * c + n) * 32 + 16);
        lam[n] = make_float2(a[0], a[1]); coef[n] = make_float2(a[2], a[3]); l16[n] = make_float2(b[0], b[1]); l128[n] = make_float2(b[2], b[3]);
    }
#pragma unroll
    for (int m = 0; m < 4; ++m)
#pragma unroll
        for (int n = 0; n < 4; ++n) acc[m][n] = mfb(af[1][m], bf_[1][n], acc[m][n]);
    float2 l32[2], l64[2];
#pragma unroll
    for (int n = 0; n < 2; ++n) { l32[n] = cmul(l16[n], l16[n]); l64[n] = cmul(l32[n], l32[n]); }
    TS(1);
    float2 h[2][16];
#pragma unroll
    for (int n = 0; n < 2; ++n) h[n][0] = make_float2(acc[0][n][0], acc[0][2 + n][0]);
#pragma unroll
    for (int t = 1; t < 16; ++t)
#pragma unroll
        for (int n = 0; n < 2; ++n) h[n][t] = cfma(lam[n], h[n][t - 1], make_float2(acc[t >> 2][n][t & 3], acc[t >> 2][2 + n][t & 3]));
    float2 loc[2], wag[2], wq[2];
#pragma unroll
    for (int n = 0; n < 2; ++n) {
        float2 a0, a1, a2, a3;
        rows4(h[n][15].x, a0.x, a1.x, a2.x, a3.x);
        rows4(h[n][15].y, a0.y, a1.y, a2.y, a3.y);
        const float2 s2 = cfma(l16[n], a0, a1), s3 = cfma(l16[n], s2, a2);
        wag[n] = cfma(l16[n], s3, a3);
        loc[n] = sel4(q, make_float2(0.f, 0.f), a0, s2, s3);
    }
    lptr aggp = smem + A_AGG + (wc * 16 + c) * 16;
    const unsigned flg = ldsa(smem + A_FLG) + wc * 4;
    float2 W0[2] = {make_float2(0.f, 0.f), make_float2(0.f, 0.f)};
    if (wr == 0) {
        if (q == 0) lst<f32x4>(aggp, (f32x4){wag[0].x, wag[0].y, wag[1].x, wag[1].y});
        asm volatile("" ::: "memory");
        if (lane == 0) lds_add(flg);
    } else {
#pragma unroll 1
        for (int spin = 0; spin < (1 << 20); ++spin) { if (lds_peek(flg)) break; }
        const f32x4 w0 = lld<f32x4>(aggp);
        W0[0] = make_float2(w0[0], w0[1]); W0[1] = make_float2(w0[2], w0[3]);
        const float2 g0 = cfma(l64[0], W0[0], wag[0]), g1 = cfma(l64[1], W0[1], wag[1]);
        if (lt < 15 && q < 2) {
            const float2 gg = q == 0 ? g0 : g1;
            st128<SC1>((u32x4){__float_as_uint(gg.x), (unsigned)tag, __float_as_uint(gg.y), (unsigned)(tag >> 32)},
                       rs, (2 * c + q) * 16 + OFF_GRAN + ((15 - lt) * 1024 + pt * 64 + wc * 32) * 16);
        }
    }
    TS(2);
#pragma unroll
    for (int n = 0; n < 2; ++n) { PIN(lam[n].x); PIN(lam[n].y); PIN(coef[n].x); PIN(coef[n].y); PIN(l16[n].x); PIN(l16[n].y); PIN(l128[n].x); PIN(l128[n].y); }
    float2 pw[2][8], l8[2], lq[2], l512[2];
#pragma unroll
    for (int n = 0; n < 2; ++n) {
        wq[n] = sel4(q, make_float2(1.f, 0.f), l16[n], l32[n], cmul(l32[n], l16[n]));
        loc[n] = cfma(wq[n], W0[n], loc[n]);
        if (wr) wq[n] = cmul(wq[n], l64[n]);
        pw[n][0] = cmul(coef[n], lam[n]);
        l8[n] = cmul(lam[n], lam[n]); l8[n] = cmul(l8[n], l8[n]); l8[n] = cmul(l8[n], l8[n]);
        const float2 l256 = cmul(l128[n], l128[n]);
        lq[n] = sel4(q, make_float2(1.f, 0.f), l128[n], l256, cmul(l256, l128[n]));
        l512[n] = cmul(l256, l256);
    }
#pragma unroll
    for (int n = 0; n < 2; ++n) {
        PIN(wq[n].x); PIN(wq[n].y); PIN(loc[n].x); PIN(loc[n].y); PIN(l8[n].x); PIN(l8[n].y); PIN(lq[n].x); PIN(lq[n].y); PIN(l512[n].x); PIN(l512[n].y);
    }
#pragma unroll
    for (int t = 0; t < 8; ++t) {
#pragma unroll
        for (int n = 0; n < 2; ++n) {
            if (t) pw[n][t] = cmul(pw[n][t - 1], lam[n]);
            h[n][2 * t] = cmul(coef[n], h[n][2 * t]); h[n][2 * t + 1] = cmul(coef[n], h[n][2 * t + 1]);
        }
#pragma unroll
        for (int n = 0; n < 2; ++n) { PIN(pw[n][t].x); PIN(pw[n][t].y); PIN(h[n][2 * t].x); PIN(h[n][2 * t].y); PIN(h[n][2 * t + 1].x); PIN(h[n][2 * t + 1].y); }
        SCHED();
    }
    await<0>(fb + F_FREE, 8u << 20);
    TS(3);
    if (lt > 0) {
        u32x4 gr[4][2];
        unsigned off = ((16 - lt + q) * 1024 + pt * 64 + wc * 32 + 2 * c) * 16;
#pragma unroll 1
        for (int spin = 0; spin < (1 << 20); ++spin) {
            bool ok = true;
            PIN(off);
#pragma unroll
            for (int j = 0; j < 4; ++j) {
                const rsrc_t rg = mkrsrc(ws + OFF_GRAN + j * 65536, (4 - j) * 65536);
                gr[j][0] = __builtin_amdgcn_raw_buffer_load_b128(rg, off, 0, SC1);
                gr[j][1] = __builtin_amdgcn_raw_buffer_load_b128(rg, off + 16, 0, SC1);
            }
#pragma unroll
            for (int j = 0; j < 4; ++j) {
                const bool v = lt - 1 - (4 * j + q) >= 0;
#pragma unroll
                for (int n = 0; n < 2; ++n) ok = ok & (!v | ((gr[j][n].y == (unsigned)tag) & (gr[j][n].w == (unsigned)(tag >> 32))));
            }
            if (__all(ok)) break;
            asm volatile("" ::: "memory");
        }
#pragma unroll
        for (int n = 0; n < 2; ++n) {
            float2 cs = make_float2(__uint_as_float(gr[3][n].x), __uint_as_float(gr[3][n].z));
#pragma unroll
            for (int j = 2; j >= 0; --j) cs = cfma(l512[n], cs, make_float2(__uint_as_float(gr[j][n].x), __uint_as_float(gr[j][n].z)));
            cs = cmul(cs, lq[n]);
            cs.x = rowsum4(cs.x); cs.y = rowsum4(cs.y);
            loc[n] = cfma(wq[n], cs, loc[n]);
        }
    }
    TS(4);
    lptr tb = smem + A_HS + wid * 4096;
    PIN(lane);
#pragma unroll
    for (int hf = 0; hf < 2; ++hf) {
        const float2 s0 = hf ? cmul(l8[0], loc[0]) : loc[0], s1 = hf ? cmul(l8[1], loc[1]) : loc[1];
#pragma unroll
        for (int t = 0; t < 8; ++t) {
            const float2 v0 = cfma(pw[0][t], s0, h[0][8 * hf + t]), v1 = cfma(pw[1][t], s1, h[1][8 * hf + t]);
            lst<unsigned>(tb + t * 256 + lane * 4, pkb(v0.x, v1.x));
            lst<unsigned>(tb + 2048 + t * 256 + lane * 4, pkb(-v0.y, -v1.y));
        }
        asm volatile("" ::: "memory");
        lptr rb = tb + (lane >> 4) * 256 + (lane & 3) * 64 + ((lane >> 2) & 3) * 16;
        const unsigned hoff = OFF_H + ((lane >> 4) * 64 + pt * 2 + wc) * 8192 + lt * 512 + wr * 256 + (lane & 15) * 16;
#pragma unroll
        for (int v = 0; v < 2; ++v)
#pragma unroll
            for (int pl = 0; pl < 2; ++pl)
                st128<SC1>(lld<u32x4>(rb + pl * 2048 + v * 1024), rs, hoff + ((2 * hf + v) * 256 + pl * 32) * 8192);
        asm volatile("" ::: "memory");
    }
    TS(5);
    wait_vm<0>();
    TS(6);
    const bool pub = sig_rtn(fb + F_HC) == 3u;
    if (pub) {
        const u32x2 tg = {(unsigned)tag, (unsigned)(tag >> 32)};
        if (lane < HREP) __builtin_amdgcn_raw_buffer_store_b64(tg, rs, lane * 256 * FST, OFF_FH + (pt * 16 + lt) * FST, SC1);
        TS(15);
        int spin; unsigned m = 0;
        if (PDL) __builtin_amdgcn_s_sleep(PDL);
        if (PDE && !(PDM >> (gb & 7) & 1)) __builtin_amdgcn_s_sleep(PDE);
        if (PD0 && lt == 0) __builtin_amdgcn_s_sleep(PD0);
        TS(19);
#pragma unroll 1
        for (spin = 0; spin < (1 << 20); ++spin) {
            m = h_poll<NP1>(rs, gb, lane, tg);
            if (spin == 0) TS(16);
            if (spin == 1) TS(17);
            if (spin == 2) TS(18);
            if ((m & HFIRST) == HFIRST) break;
        }
        sig_or(fb + F_FULL, m);
    } else await<1>(fb + F_FULL, 8u);
    __builtin_amdgcn_s_setprio(PRIOG);
    TS(7);
    PIN(lane);
    const unsigned vo = ((lane & 15) >> 2) * 256 + (lane >> 4) * 64 + (lane & 3) * 16, sH = OFF_H + lt * 524288 + wid * 2048;
    auto ldB = [&](int i, int n) { return __builtin_amdgcn_raw_buffer_load_b128(rs, vo + n * 1024, sH + ((i >> 3) ^ CX(gb)) * 32768 + ((i & 3) + (i & 4) * 8) * 8192, SC1); };
    lptr ab[2];
#pragma unroll
    for (int i = 0; i < 2; ++i) { ab[i] = smem + (lane & 15) * 128 + (((4 * i + (lane >> 4)) ^ (lane & 7)) << 4); PIN(ab[i]); }
    auto ldA = [&](int f) {
        const int a = (f >> 2) & 1;
        if ((f & 63) < 8 && f > 7 && !(f & 3)) { ab[a] += (f & 64) ? 2 * A_STAGE : -2 * A_STAGE; PIN(ab[a]); }
        return lld<u32x4>(ab[a] + ((f >> 3) & 7) * 8192 + (f & 3) * 2048);
    };
    u32x4 A[AD], B[BD][2];
#pragma unroll
    for (int i = 0; i < BD; ++i) { B[i][0] = ldB(i, 0); B[i][1] = ldB(i, 1); }
    SCHED();
    unsigned fv = fb; PIN(fv);
    await<0>(fv + F_FULL, 4u);
#pragma unroll
    for (int f = 0; f < AD; ++f) A[f] = ldA(f);
    f32x4 ac[4][2];
    unsigned fe = 0, fs = 0;
#pragma unroll
    for (int e = 0; e < 8; ++e) ac[e >> 1][e & 1] = Z4;
    SCHED();
    sfor<0, 64>([&](auto ic) INL {
        constexpr int i = decltype(ic)::v;
#pragma unroll
        for (int k = 0; k < 4; ++k) {
            const int f = 4 * i + k;
            ac[k][0] = mfb(B[i % BD][0], A[f % AD], ac[k][0]);
            SCHED();
            if (k == 2 && ((i + BD + 1) & 7) == 0 && i + BD + 1 < 64) fe = *(volatile LAS unsigned*)(smem + A_FLG + F_FULL);
            if (k == 3 && ((i + BD) & 7) == 0 && i + BD < 64) {
                fs = __builtin_amdgcn_readfirstlane(fe);
                if (!(fs & (8u << (4 * (((i + BD) >> 3) & 7))))) fs = await<0>(fv + F_FULL, 8u << (4 * (((i + BD) >> 3) & 7)));
                asm volatile("" ::: "memory");
                TS(20 + ((i + BD) >> 3));
            }
            if (k == 3 && i + BD < 64) B[i % BD][0] = ldB(i + BD, 0);
            if (k == 1 && i > 0 && i - 1 + BD < 64) B[(i + BD - 1) % BD][1] = ldB(i - 1 + BD, 1);
            SCHED();
            if (PG >= 0) { asm volatile("s_nop %0" :: "i"(PG >= 0 ? PG : 0)); SCHED(); }
            ac[k][1] = mfb(B[i % BD][1], A[f % AD], ac[k][1]);
            SCHED();
            if (PG >= 0) { asm volatile("s_nop %0" :: "i"(PG >= 0 ? PG : 0)); SCHED(); }
            if (f + AD < 256) {
                if (((f + AD) & 31) == 0) {
                    if (!(fs & (4u << (4 * ((f + AD) >> 5))))) await<0>(fv + F_FULL, 4u << (4 * ((f + AD) >> 5)));
                    asm volatile("" ::: "memory");
                }
                A[f % AD] = ldA(f + AD);
            }
            SCHED();
        }
        if ((i & 7) == 7 && i < 40) sig(fv + F_FREE, 1u << (4 * (i >> 3)));
        if (i == 0) { TS(8); TC(60); }
        if (i == 31) TS(9);
        if (i == 60) { TS(14); TC(61); }
    });
    TS(10);
    await<0>(fv + F_R0, 4u);
    TS(11);
    const rsrc_t ro = mkrsrc(out, S_ * D_ * 4);
    PIN(lane);
    const int c2 = lane & 15, q2 = lane >> 4;
    const unsigned oo = ((64 * q2 + 512 * wid + lt) * D_ + 256 * (c2 >> 2) + 16 * pt + 4 * (c2 & 3)) * 4;
#pragma unroll
    for (int n = 0; n < 2; ++n) {
        const f32x4 o2 = lld<f32x4>(smem + A_AGG + (32 * wid + 16 * n + 4 * q2) * 4);
#pragma unroll
        for (int e = 0; e < 4; ++e) {
            const f32x4 r0 = lld<f32x4>(smem + A_R0 + (wid * 2 + n) * 4096 + c2 * 256 + (((4 * q2 + e) ^ c2) << 4));
            const f32x4 o = {fmaf(o2[e], ac[0][n][e], r0[0]), fmaf(o2[e], ac[1][n][e], r0[1]), fmaf(o2[e], ac[2][n][e], r0[2]), fmaf(o2[e], ac[3][n][e], r0[3])};
            st128<WTO>(__builtin_bit_cast(u32x4, o), ro, oo + (256 * n + 16 * e) * D_ * 4);
        }
    }
    asm volatile("gm_code_end:");
    TS(12);
    TSE(0, wid == 0);
    TSE(2, pub);
}

#define TSARG
#define TSPASS
struct Gate {
    unsigned xa, wa, xv, gw; float sum; int rot;
    DEV void rd(int kt) { asm volatile("ds_read_b32 %0, %2\n\tds_read_b32 %1, %3" : "=&v"(xv), "=&v"(gw) : "v"(xa + (kt & 3) * A_STAGE), "v"(wa + (kt ^ rot) * 128) : "memory"); }
    DEV void acc() {
        asm volatile("s_waitcnt lgkmcnt(0)" : "+v"(xv), "+v"(gw) :: "memory");
        const unsigned pr = pkb(lo2f(xv) * lo2f(gw), hi2f(xv) * hi2f(gw));
        sum += lo2f(pr); sum += hi2f(pr); PIN(sum);
    }
};
DEV unsigned u_load(char* ws, lptr smem, int gb, int w, int lane, u32x4 (&ut)[2]) {
    int mp, j1; slot_of(gb, mp, j1);
    __builtin_amdgcn_s_setprio(PRIOM);
    const unsigned em = await<1>(ldsa(smem + A_FLG + F_EM), 0x8000u);
    asm volatile("buffer_load_dwordx4 %0, %2, %3, %4 offen sc1\n\tbuffer_load_dwordx4 %1, %2, %3, %4 offen offset:2048 sc1"
                 : "=&v"(ut[0]), "=&v"(ut[1]) : "v"((lane >> 2) * 128 + (lane & 3) * 16), "s"(mkdesc(ws, WS_END)),
                   "s"(OFF_U + ((mp >> 1) * 4 + j1) * 32768 + (mp & 1) * 64 + w * 4096) : "memory");
    return em;
}

DEV float role_load(const u16* __restrict__ x, const u16* __restrict__ Bre, const u16* __restrict__ Bim,
                    const u16* __restrict__ gate_w, char* ws, lptr smem, int gb, int lw, int lane TSARG) {
    __builtin_amdgcn_s_waitcnt(0x0F70);
    Gate g;
    int lt, pt; tile_of(gb, lt, pt);
    const int srow = lane >> 3, schunk = (lane & 7) ^ srow;
    const int rot = ROT;
    const u32x4 rx = mkdesc(x, S_ * D_ * 2), rb = mkdesc((lw & 1) ? Bim : Bre, D_ * D_ * 2);
    const unsigned lb = ldsa(smem);
    const unsigned vA = (16 * (srow >> 2) + (srow & 3)) * 2048 + schunk * 16, sA = (lt * 128 + 64 * (lw >> 1) + 8 * (lw & 1)) * 2048;
    const unsigned vB = 2 * srow * 2048 + schunk * 16, sB = (pt * 64 + 32 * (lw >> 1)) * 2048;
    auto stA = [&](int kk) {
#pragma unroll
        for (int i = 0; i < 4; ++i)
            dma16(rx, lb + (kk & 3) * A_STAGE + lw * 4096 + i * 1024, vA, sA + (32 * (i & 1) + 4 * (i >> 1)) * 2048 + (kk ^ rot) * 128);
    };
    auto stB = [&](int kk) {
#pragma unroll
        for (int i = 0; i < 4; ++i)
            dma16(rb, lb + (kk & 3) * A_STAGE + 16384 + lw * 4096 + i * 1024, vB, sB + (16 * (i & 1) + (i >> 1)) * 2048 + (kk ^ rot) * 128);
    };
    TS(0);
    __builtin_amdgcn_s_setprio(PRIOL);
    stA(0); TL(53); stB(0); TL(54);
    dma16(mkdesc(gate_w, D_ * 2), lb + A_GW + (lw & 1) * 1024, lane * 16, (lw & 1) * 1024);
#pragma unroll
    for (int i = 0; i < 4; ++i)
        dma16(rx, lb + A_XT + lw * 4096 + i * 1024, (lane >> 1) * 32768 + (lane & 1) * 16, (i * 512 + lt) * 2048 + lw * 512 + pt * 32);
    const int gi = 8 * pt + 2 * lw + (lane >> 5);
    g.xa = lb + gi * 128 + ((((lane & 31) >> 2) ^ (gi & 7)) << 4) + (lane & 3) * 4;
    g.wa = lb + A_GW + (lane & 31) * 4; g.sum = 0.f; g.rot = rot;
    TL(55); stA(1); stB(1); TL(56); stA(2); stB(2);
    TS(6);
    constexpr int K0 = 0;
#pragma unroll 1
    for (int kt = K0; kt < 13; ++kt) { wait_vm<16>(); TL(8 + kt); BAR(); TL(28 + kt); if (kt == 0) { TS(28); TC(60); } g.rd(kt); stA(kt + 3); stB(kt + 3); g.acc(); }
    if (lw == 3) {
        const rsrc_t rs = mkrsrc(ws, WS_END);
        u32x4 fl[2];
        u32x2 tag = lld<u32x2>(smem + A_FLG + F_TAG); PIN(tag);
        auto publish = [&]() { sig(lb + A_FLG + F_EM, (wave_pkmax(pkmax(pkmax(fl[0].x, fl[0].z), pkmax(fl[1].x, fl[1].z))) & 0xffu) | 0x8000u); };
        wait_vm<16>(); BAR();
        asm volatile("buffer_load_dwordx4 %0, %2, %3, %4 offen sc1\n\tbuffer_load_dwordx4 %1, %2, %3, %4 offen offset:1024 sc1"
                     : "=&v"(fl[0]), "=&v"(fl[1]) : "v"(lane * 16), "s"(mkdesc(ws, WS_END)), "s"(OFF_FU + (gb & (NREP - 1)) * 2048) : "memory");
        g.rd(13); g.acc();
        wait_vm<10>(); BAR();
        asm volatile("s_waitcnt vmcnt(0)" : "+v"(fl[0]), "+v"(fl[1]) :: "memory");
        const bool ok = flag_ok(fl, tag);
        if (ok) publish();
        g.rd(14); g.acc();
        BAR(); TS(43);
        if (!ok) {
#pragma unroll 1
            for (int spin = 0; spin < (1 << 20); ++spin) { flag_get(rs, OFF_FU, gb, lane, fl); if (flag_ok(fl, tag)) break; }
            publish();
        }
    } else {
        wait_vm<16>(); TL(21); BAR(); TL(41); g.rd(13); g.acc();
        wait_vm<8>(); TL(22); BAR(); TL(42); g.rd(14); g.acc();
        wait_vm<0>(); TL(23); BAR(); TS(43); TC(61);
    }
    g.rd(15); g.acc();
    sig(lb + A_FLG + F_FREE, 1u << 20);
    return g.sum;
}

DEV unsigned ldw(rsrc_t r, unsigned vo, unsigned so) { return __builtin_amdgcn_raw_buffer_load_b32(r, vo, so, 0); }
DEV u32x4 w_pack(u32x4 re, u32x4 im, unsigned sel) {
    PIN(re); PIN(im);
#pragma unroll
    for (int r = 0; r < 4; ++r) re[r] = __builtin_amdgcn_perm(im[r], re[r], sel);
    return re;
}
DEV unsigned amax4(u32x4 p) { return pkmax(pkmax(p.x & 0x7fff7fffu, p.y & 0x7fff7fffu), pkmax(p.z & 0x7fff7fffu, p.w & 0x7fff7fffu)); }

DEV unsigned role_aux(const u16* __restrict__ Cre, const u16* __restrict__ Cim, const u16* __restrict__ w_r, const u16* __restrict__ w_i, const u16* __restrict__ lam_re, const u16* __restrict__ lam_im,
                  const u16* __restrict__ log_dt, const u16* __restrict__ Dskip, char* ws, lptr smem, int gb, int aw, int lane, u32x2 (&xc)[8],
                  u32x4 (&ut)[2] TSARG) {
    int lt, pt; tile_of(gb, lt, pt);
    int mp, j1; slot_of(gb, mp, j1);
    const int c = lane & 15, q = lane >> 4;
    const bool spc = mp == 0 && !(j1 & 1);
    const rsrc_t rs = mkrsrc(ws, WS_END), rr = mkrsrc(w_r, S_ * KF * 2), ri = mkrsrc(w_i, S_ * KF * 2);
    TS(0);
    __builtin_amdgcn_s_setprio(PRIOA);
    u32x4 fch, fsh;
    const bool m0 = 16 * aw + c == 0;
    {
        const float th = (float)(16 * aw + c) * (1.0f / 128.0f);
#pragma unroll
        for (int i = 0; i < 4; ++i) {
            const float t0 = __builtin_amdgcn_fractf(th * (float)(16 * (i >> 1) + 2 * (i & 1) + 4 * q)), t1 = t0 + th;
            const float c0 = __builtin_amdgcn_cosf(t0), c1 = __builtin_amdgcn_cosf(t1);
            const float s0 = m0 ? 1.f : -__builtin_amdgcn_sinf(t0), s1 = m0 ? -1.f : -__builtin_amdgcn_sinf(t1);
            fch[i] = pkb(c0, c1); fsh[i] = pkb(s0, s1);
        }
        PIN(fch); PIN(fsh);
    }
    unsigned sg64 = (c & 1) ? SG : 0u, sgA = (c & 2) ? SG : 0u, sgB = ((c + 1) & 2) ? SG : 0u;
    constexpr unsigned WR = 128 * KF * 2;
    const int k1 = mp ? mp : spc ? (c >> 3) * 64 : (j1 == 1 ? 0 : 64);
    const bool xl = gb == XG && !(c & 7);
    const int kd = j1 + 4 * (xl && aw >= 2 ? 8 : spc ? c & 7 : c) + 64 * (4 * aw + q);
    unsigned wo = aw < 2 ? (k1 * KF + kd) * 2 : ((128 - k1) * KF + 1023 - kd) * 2;
    unsigned wo0 = aw < 2 ? wo : k1 ? wo + 15 * WR : (1023 - kd) * 2;
    unsigned uoff = (lane >> 3) * 131072 + (lane & 7) * 16 + OFF_U + aw * 64 * 16384 + lt * 2048 + pt * 128;
    PIN(sg64); PIN(sgA); PIN(sgB); PIN(wo); PIN(wo0); PIN(uoff);
    unsigned lru, liu, ldu, um = 0, em;
    u32x2 tag, dk[4];
    u32x4 wre[4], wim[4];
    u32x4 wsc = {0, 0, 0, 0};
    if (XM & 1) for (int i = 0; i < 4; ++i) { wre[i] = fch; wim[i] = fsh; }
    f32x4 vr[4], vi[4];
    float dt = 0.f, er = 0.f; float2 lam = make_float2(0.f, 0.f);
#pragma unroll
    for (int d1 = 0; d1 < 4; ++d1) { vr[d1] = Z4; vi[d1] = Z4; }
    auto wx = [&]() INL {
        if (j1 == 0 && aw == 3) {
            const int ks = (spc ? (lane >> 5) * 64 : mp) + 128 * c, ng = (S_ - ks) & (S_ - 1);
            unsigned o0 = (q & 1) ? (ng * KF + 511) * 2u : ks * KF * 2u, o1 = (q & 1) ? (ks * KF + 511) * 2u : ng * KF * 2u;
            PIN(o0); PIN(o1);
            wsc = (u32x4){ldw(rr, o0, 0), ldw(rr, o1, 0), ldw(ri, o0, 0), ldw(ri, o1, 0)};
        }
    };
    auto prm = [&]() INL {
        lru = ldw(mkrsrc(lam_re, 2048), (lane >> 1) * 4, pt * 128); liu = ldw(mkrsrc(lam_im, 2048), (lane >> 1) * 4, pt * 128);
        ldu = ldw(mkrsrc(log_dt, 2048), (lane >> 1) * 4, pt * 128);
#pragma unroll
        for (int i = 0; i < 4; ++i) dk[i] = __builtin_amdgcn_raw_buffer_load_b64(mkrsrc(Dskip, D_ * 2), q * 8 + i * 512, pt * 32, 0);
    };
    if (PRS < 0) prm();
    u32x4 uo[4];
    float2 coef = make_float2(0.f, 0.f);
    sfor<0, 16>([&](auto ic) INL {
        constexpr int kt = decltype(ic)::v;
        constexpr int GS[4] = GSTEP, KSO[4] = {0, 2, 3, 1}, JO[4] = {0, 2, 1, 3}, WRW[7] = WROW;
        auto ust = [&](int k) INL {
#pragma unroll
            for (int u = 0; u < 4; ++u) if (US + u / UPS == k && !(XM & 2)) st128<SC1>(uo[u], rs, uoff + JO[u] * 32768);
        };
        auto mem = [&]() INL {
            if (kt >= 10 && !(XM & 1)) {
#pragma unroll
                for (int r = 0; r < 16; ++r) {
                    if (r < WRW[kt < 10 ? 0 : kt - 10] || r >= WRW[kt < 10 ? 0 : kt - 9]) continue;
                    const unsigned so = r ? (aw < 2 ? r : 15 - r) * WR : 0u;
                    wre[r >> 2][r & 3] = ldw(rr, r ? wo : wo0, so); wim[r >> 2][r & 3] = ldw(ri, r ? wo : wo0, so);
                }
            }
            if (kt == PRS) prm();
            if (kt == WXS) wx();
        };
        if (kt == UFS) wait_vm<0>();
        TL(8 + kt);
        BAR();
        if (VL == 2) ust(kt - 1);
        if (kt == 15) { em = u_load(ws, smem, gb, aw, lane, ut); TS(2); }
        u32x4 xa[4][4];
#pragma unroll
        for (int g = 0; g < 4; ++g) {
            if (GS[g] != kt) continue;
#pragma unroll
            for (int i = 0; i < 4; ++i) xa[g][i] = trfrag(smem + A_XT + i * 4096 + KSO[g] * 1024 + lane * 8, 512);
        }
        if (kt == XS) {
#pragma unroll
            for (int i = 0; i < 8; ++i) xc[i] = lld<u32x2>(smem + A_XT + (i & 3) * 4096 + (32 * aw + 16 * (i >> 2) + c) * 32 + q * 8);
        }
        if (VL != 1) mem();
        SCHED();
#pragma unroll
        for (int g = 0; g < 4; ++g) {
            if (GS[g] != kt) continue;
#pragma unroll
            for (int i = 0; i < 4; ++i) { vr[i] = mfb(xa[g][i], fch, vr[i]); vi[i] = mfb(xa[g][i], fsh, vi[i]); PIN(vr[i]); PIN(vi[i]); }
            if (g == 0 || g == 2) { fch ^= sg64; fsh ^= sg64; PIN(fch); PIN(fsh); }
            if (g == 1) {
                const bool od = c & 1;
                const u32x4 a = (od ? fsh : fch) ^ sgA, b = (od ? fch : fsh) ^ sgB;
                fch = a; fsh = b; PIN(fch); PIN(fsh);
            }
        }
        if (kt == B1S) {
            const f32x4 t0r = vr[0] + vr[2], t1r = vr[0] - vr[2], t2r = vr[1] + vr[3], t3r = vr[1] - vr[3];
            const f32x4 t0i = vi[0] + vi[2], t1i = vi[0] - vi[2], t2i = vi[1] + vi[3], t3i = vi[1] - vi[3];
            vr[0] = t0r; vr[1] = t1r; vr[2] = t2r; vr[3] = t3r; vi[0] = t0i; vi[1] = t1i; vi[2] = t2i; vi[3] = t3i;
#pragma unroll
            for (int i = 0; i < 4; ++i) { PIN(vr[i]); PIN(vi[i]); }
        }
        if (kt == XS) {
#pragma unroll
            for (int i = 0; i < 8; ++i) PIN(xc[i]);
        }
#pragma unroll
        for (int u = 0; u < 4; ++u) {
            if (US + u / UPS != kt) continue;
            const int j = JO[u];
            f32x4 a, b;
            if (j == 0) { a = vr[0] + vr[2]; b = vi[0] + vi[2]; }
            if (j == 2) { a = vr[0] - vr[2]; b = vi[0] - vi[2]; }
            if (j == 1) { a = vr[1] + vi[3]; b = vi[1] - vr[3]; if (aw == 0) { if (m0) { a = vr[1]; b = -vr[3]; } } }
            if (j == 3) { a = vr[1] - vi[3]; b = vi[1] + vr[3]; if (aw == 0) { if (m0) { a = vi[1]; b = vi[3]; } } }
            uo[u] = quads(smem + A_XT + aw * 4096 + j * 1024, lane, (u32x2){pkb(a[0], a[1]), pkb(a[2], a[3])}, (u32x2){pkb(b[0], b[1]), pkb(b[2], b[3])});
            um = pkmax(um, amax4(uo[u]));
            PIN(um); PIN(uo[u]);
#pragma unroll
            for (int i = 0; i < 4; ++i) if (u < 3 && ((i & 1) || u == 0)) { PIN(vr[i]); PIN(vi[i]); }
        }
        if (kt == UFS - 1) {
            um = wave_pkmax(pkmax(um, um >> 16));
            if (lane == 0) lst<unsigned>(smem + A_FLG + F_MAX + aw * 4, um);
            tag = lld<u32x2>(smem + A_FLG + F_TAG); PIN(tag);
        }
        if (kt == DS || kt == DS + 1) {
#pragma unroll
            for (int i = 4 * (kt - DS); i < 4 * (kt - DS) + 4; ++i) {
                const u32x2 d = dk[i & 3];
                xc[i & 7] = (u32x2){pkb(lo2f(xc[i & 7].x) * lo2f(d.x), hi2f(xc[i & 7].x) * hi2f(d.x)), pkb(lo2f(xc[i & 7].y) * lo2f(d.y), hi2f(xc[i & 7].y) * hi2f(d.y))};
                PIN(xc[i & 7]);
            }
        }
        if (kt == P0S) {
            PIN(lru); PIN(liu); PIN(ldu);
            if (lane & 1) { lru >>= 16; liu >>= 16; ldu >>= 16; }
            PIN(lru); PIN(liu); PIN(ldu);
        }
        if (kt >= P0S && kt < P0S + 4 && aw == 1) {
            const float lr = lo2f(lru), li = lo2f(liu);
            if (kt == P0S) { dt = bfr(expf(lo2f(ldu))); er = expf(dt * lr); PIN(dt); PIN(er); }
            if (kt == P0S + 1) { float sn, cs; sincosf(dt * li, &sn, &cs); lam = make_float2(er * cs, er * sn); PIN(lam.x); PIN(lam.y); }
            if (kt == P0S + 2) {
                const float nr = lam.x - 1.0f, ni = lam.y, den = lr * lr + li * li;
                coef = make_float2((nr * lr + ni * li) / den, (ni * lr - nr * li) / den);
                lst<f32x4>(smem + A_PRM + lane * 32, (f32x4){lam.x, lam.y, coef.x, coef.y});
            }
            if (kt == P0S + 3) {
                float2 l16 = lam;
#pragma unroll
                for (int i = 0; i < 4; ++i) l16 = cmul(l16, l16);
                float2 l128 = l16;
#pragma unroll
                for (int i = 0; i < 3; ++i) l128 = cmul(l128, l128);
                lst<f32x4>(smem + A_PRM + lane * 32 + 16, (f32x4){l16.x, l16.y, l128.x, l128.y});
            }
        }
        SCHED();
        if (kt == UFS && aw == 0) {
            const u32x4 m4 = lld<u32x4>(smem + A_FLG + F_MAX);
            const unsigned m = pkmax(pkmax(m4.x, m4.y), pkmax(m4.z, m4.w));
            flag_put(rs, OFF_FU, gb, (u32x2){(tag.x & 0xffff0000u) | ((m & 0xffffu) >> 7), tag.y}, lane);
        }
        if (VL != 2) ust(kt);
        if (VL == 1) mem();
        LDS_WAIT();
        SCHED();
    });
    unsigned mx = 0;
    const bool xh = xl && aw >= 2, skip = j1 == 0 && !(aw & 1) && (lane & (spc ? 55 : 63)) == 0 && !xh;
    lptr wd = smem + A_W + (xh ? 16384 + c * 128 + ((4 * aw + q) << 4) : c * 1024 + (((4 * aw + q) ^ c) << 4));
#pragma unroll
    for (int i = 0; i < 4; ++i) {
        wre[i] = w_pack(wre[i], wim[i], aw < 2 ? 0x05040100u : 0x07060302u);
        if (!skip) lst<u32x4>(wd + i * 256, wre[i]);
        mx = pkmax(mx, amax4(wre[i]));
    }
    if (j1 == 0) {
        if (aw == 3) {
            PIN(wsc);
            const unsigned sel = (q & 1) ? 0x07060302u : 0x05040100u, p0 = __builtin_amdgcn_perm(wsc.z, wsc.x, sel), p1 = __builtin_amdgcn_perm(wsc.w, wsc.y, sel);
            mx = pkmax(mx, pkmax(p0 & 0x7fff7fffu, p1 & 0x7fff7fffu));
            const int it = spc ? (lane >> 5) * 8 : 0;
            lst<unsigned>(smem + A_W + it * 1024 + (c >> 2) * 256 + ((((q & 1) * 8) ^ it) << 4) + (c & 3) * 4,
                          pkb(0.5f * (lo2f(p0) + lo2f(p1)), 0.5f * (hi2f(p0) - hi2f(p1))));
        }
        if (xl && !skip) {
            lptr wm = smem + A_W + (xh ? 16384 + c * 128 + ((15 - 4 * aw - q) << 4) : c * 1024 + (((16 - 4 * aw - q) ^ c) << 4));
#pragma unroll
            for (int r = 0; r < 16; ++r) {
                const int k0 = (16 - r) & 15, k1m = 15 - r;
                lst<unsigned>(wm + (c ? (k1m >> 2) * 256 + (k1m & 3) * 4 : (k0 >> 2) * 256 + (k0 & 3) * 4), wre[r >> 2][r & 3]);
            }
        }
    }
    mx = wave_pkmax(pkmax(mx, mx >> 16));
    if (lane == 0) lst<unsigned>(smem + A_FLG + F_WM + aw * 4, mx);
    wait_vm<0>();
    SCHED();
    TS(53);
    return em;
}

DEV int role_mid(const u16* __restrict__ Cre, const u16* __restrict__ Cim, char* ws, lptr smem,
                 int gb, int w, int lane, float gsum, u32x2& tag, u32x4 (&ut)[2], unsigned em TSARG) {
    int lt, pt; tile_of(gb, lt, pt);
    int mp, j1; slot_of(gb, mp, j1);
    const int c = lane & 15, q = lane >> 4;
    const bool spc = mp == 0 && !(j1 & 1), xtr = gb == XG, ld = w >= 4;
    const rsrc_t rs = mkrsrc(ws, WS_END);
    const unsigned lb = ldsa(smem), fb = lb + A_FLG;
    u32x4 wv[2], mt[6];
    f32x4 tf[2]; f32x2 ti[2];
    tag = lld<u32x2>(smem + A_FLG + F_TAG);
#pragma unroll
    for (int i = 0; i < 2; ++i) {
        tf[i] = lld<f32x4>(smem + A_MAT + 9216 + c * 128 + q * 32 + i * 16);
        ti[i] = lld<f32x2>(smem + A_MAT + 9216 + (2 * w + i) * 128 + c * 8);
    }
    lptr mb = smem + A_MAT + lane * 16;
#pragma unroll
    for (int i = 0; i < 6; ++i) mt[i] = lld<u32x4>(mb + (i < 4 ? i : i + 2) * 1024);
    u32x4 sf0 = lld<u32x4>(mb + 4096), si0 = lld<u32x4>(mb + 5120);
    const u32x4 f0 = lld<u32x4>(mb + 8192);
    SCHED();
    if (spc && ld) { sf0 = mat_sf(64, c, q); si0 = mat_si(64, c, q); }
    u32x4 xf, xb;
    if (xtr && (w & 6) == 4) {
        const float dt = -(float)(32 + 64 * c) * (1.0f / 1024.0f);
        xf = cmat1(dt * (float)(4 * q), dt); xb = cmat1((float)(c * (32 + 256 * q)) * (1.0f / 1024.0f), (float)c * (1.0f / 16.0f));
    } else { asm volatile("" : "=v"(xf), "=v"(xb)); }
    PIN(xf); PIN(xb);
    const CM sf = cexp(sf0), si = cexp(si0), ff = cexp(f0), fi = cexp((u32x4){f0.x ^ SG, f0.y ^ SG, f0.z, f0.w});
#pragma unroll
    for (int i = 0; i < 6; ++i) PIN(mt[i]);
    PIN(ti[0]); PIN(ti[1]); PIN(tag);
    const int eu = min(max((int)(em & 0xffu), 4), 254);
    const float su = __uint_as_float((unsigned)(258 - eu) << 23);
    tf[0] *= su; tf[1] *= su; PIN(tf[0]); PIN(tf[1]);
    u32x2 gv = {0, 0};
    if (ld) {
        const float gg = bfr(1.0f / (1.0f + expf(-bfr(half_sum(gsum)))));
        gv = (u32x2){__float_as_uint(gg), __float_as_uint(bfr(1.0f - gg))};
        PIN(gv);
    }
    TS(54);
    await<0>(fb + F_FREE, 8u << 20);
    asm volatile("s_waitcnt vmcnt(%2)" : "+v"(ut[0]), "+v"(ut[1]) : "i"(16) : "memory");
    lptr ua = smem + A_XT + w * 2048;
#pragma unroll
    for (int i = 0; i < 2; ++i) lst<u32x4>(ua + i * 1024 + lane * 16, ut[i]);
    TS(3);
    asm volatile("" ::: "memory");
    {
        lptr ea = smem + A_E1 + c * E1P + q * 256 + 2 * w * 16;
#pragma unroll
        for (int i = 0; i < 2; ++i) ut[i] = trfrag(ua + i * 1024 + (lane >> 2) * 64 + (lane & 3) * 8, 32);
#pragma unroll
        for (int i = 0; i < 2; ++i) {
            f32x4 r = mfb(ut[i], mt[2], Z4), m = mfb(ut[i], mt[3], Z4);
            r = mfb(ut[i], mt[0], r); m = mfb(ut[i], mt[1], m);
            f32x4 yr, yi;
#pragma unroll
            for (int e = 0; e < 4; ++e) {
                const float tc = tf[e >> 1][2 * (e & 1)], ts = tf[e >> 1][2 * (e & 1) + 1];
                yr[e] = fmaf(r[e], tc, -m[e] * ts); yi[e] = fmaf(r[e], ts, m[e] * tc);
            }
            lst<u32x4>(ea + i * 16, pk8(yr, yi));
            if (xtr) {
                const unsigned sg = 0xbf803f80u;
                const f32x4 e = mfb(ut[i], c == 0 ? (u32x4){sg, sg, 0, 0} : (u32x4){0, 0, sg, sg}, Z4);
                if (c < 2) lst<u32x4>(ea + 16 * E1P + i * 16, pk8(e * su, Z4));
            }
        }
    }
    sig(fb + F_MID, 1u);
    if (ld) {
        const int gi = 8 * pt + 2 * (w - 4) + (lane >> 5), tau = (gi & 64) | ((gi & 12) << 2) | ((gi & 48) >> 2) | (gi & 3);
        if ((lane & 31) == 0) __builtin_amdgcn_raw_buffer_store_b64(gv, rs, (tau & 15) * 1024 + (lt * 8 + (tau >> 4)) * 8, OFF_G, SC1);
    }
    if (xtr && w == 0 && lane == 0) __builtin_amdgcn_raw_buffer_store_b64(tag, rs, 0, OFF_E, SC1);
    TS(4);
    await<0>(fb + F_MID, 24u);
    u32x4 dv[2];
#pragma unroll
    for (int i = 0; i < 2; ++i) dv[i] = lld<u32x4>(smem + A_E1 + (2 * w + i) * E1P + lane * 16);
    const u32x4 m4 = lld<u32x4>(smem + A_FLG + F_WM);
#pragma unroll
    for (int i = 0; i < 2; ++i) wv[i] = lld<u32x4>(smem + A_W + (2 * w + i) * 1024 + ((lane ^ (2 * w + i)) << 4));
#pragma unroll
    for (int i = 0; i < 2; ++i) dv[i] = pk8(mfh(dv[i], ff.re, Z4), mfh(dv[i], ff.im, Z4));
    const unsigned ew = min((pkmax(pkmax(m4.x, m4.y), pkmax(m4.z, m4.w)) & 0xffffu) >> 7, 245u);
    const float sw = __uint_as_float((246u - ew) << 23), swi = c < 8 ? sw : -sw;
    wv[0] = wcvt(wv[0], sw, swi); wv[1] = wcvt(wv[1], sw, swi);
#pragma unroll
    for (int i = 0; i < 2; ++i) dv[i] = wmul(mfh(sf.re, dv[i], Z4), mfh(sf.im, dv[i], Z4), wv[i]);
#pragma unroll
    for (int i = 0; i < 2; ++i) dv[i] = pk8(mfh(dv[i], si.re, Z4), mfh(dv[i], si.im, Z4));
    {
        f32x4 r[2], m[2];
#pragma unroll
        for (int i = 0; i < 2; ++i) {
            const f32x4 a = mfh(dv[i], fi.re, Z4), b = mfh(dv[i], fi.im, Z4);
#pragma unroll
            for (int e = 0; e < 4; ++e) { r[i][e] = fmaf(a[e], ti[i][0], b[e] * ti[i][1]); m[i][e] = fmaf(b[e], ti[i][0], -a[e] * ti[i][1]); }
        }
#pragma unroll
        for (int e = 0; e < 4; ++e)
            lst<u32x2>(smem + A_XT + (4 * q + e) * 1024 + (w >> 1) * 256 + c * 16 + (w & 1) * 8, (u32x2){pkh(r[0][e], r[1][e]), pkh(m[0][e], m[1][e])});
    }
    if (xtr && (w & 6) == 4) {
        const int it = 5 - w;
        const CM f = cexp(xf), sf2 = cexp(it ? sf.im : lld<u32x4>(mb + 4096)), si2 = cexp(it ? si.im : lld<u32x4>(mb + 5120));
        u32x4 d = lld<u32x4>(smem + A_E1 + (16 + it) * E1P + lane * 16);
        d = pk8(mfh(d, f.re, Z4), mfh(d, f.im, Z4));
        d = wmul(mfh(sf2.re, d, Z4), mfh(sf2.im, d, Z4), wcvt(lld<u32x4>(smem + A_W + 16384 + it * 1024 + lane * 16), sw, swi));
        d = pk8(mfh(d, si2.re, Z4), mfh(d, si2.im, Z4));
        const f32x4 r = mfh(d, cexp(xb).re, Z4);
#pragma unroll
        for (int e = 0; e < 4; ++e) lst<float>(smem + A_EX + (it * 256 + (4 * q + e) * 16 + c) * 4, r[e]);
    }
    TS(5);
    if (ld) { wait_vm<1>(); sig(fb + F_FULL, 0x11u); }
    sig(fb + F_MID, 1u);
    await<0>(fb + F_MID, 16u);
    {
        f32x4 xa[2], xc[2];
#pragma unroll
        for (int i = 0; i < 2; ++i) { xa[i] = Z4; xc[i] = Z4; }
        if (xtr) {
            const float sg = (c & 1) ? -0.125f : 0.125f;
#pragma unroll
            for (int i = 0; i < 2; ++i) {
                xa[i] = lld<f32x4>(smem + A_EX + (2 * w + i) * 64 + q * 16) * sg;
                xc[i] = lld<f32x4>(smem + A_EX + 1024 + (2 * w + i) * 64 + q * 16) * -sg;
            }
        }
#pragma unroll
        for (int i = 0; i < 2; ++i) {
            const int s2 = 2 * w + i;
            const u32x4 ea = lld<u32x4>(smem + A_XT + s2 * 1024 + lane * 16);
            const f32x4 ta = mfh(ea, mt[4], xa[i]), tb = mfh(ea, mt[5], xc[i]);
            u32x4 tq = quads(smem + A_XT + s2 * 1024, lane, (u32x2){pkh(ta[0], ta[1]), pkh(ta[2], ta[3])}, (u32x2){pkh(tb[0], tb[1]), pkh(tb[2], tb[3])});
            PIN(tq);
            st128<SC1>(tq, rs, (lane >> 3) * 32768 + (lane & 7) * 16 + OFF_T + s2 * 262144 + j1 * 8192 + mp * 128);
        }
    }
    TS(7);
    tag.x = (tag.x & 0xffff0000u) | ew;
    return eu - 267;
}

DEV void t_done(rsrc_t rs, unsigned fb, int gb, u32x2 tag, int lane) {
    int mp, j1; slot_of(gb, mp, j1);
    if (sig_rtn(fb + F_TC) == 7u) flag_put(rs, OFF_FT, (mp >> 3) * 32 + j1 * 8 + (mp & 7), tag, lane);
}

DEV void role_cload(const u16* __restrict__ Cre, const u16* __restrict__ Cim, char* ws, lptr smem, int gb, int lw, int lane, u32x2 tag TSARG) {
    int lt, pt; tile_of(gb, lt, pt);
    const unsigned lb = ldsa(smem), fb = lb + A_FLG;
    __builtin_amdgcn_s_setprio(3);
    const bool early = lds_peek(fb + F_HC) & 4u;
    if (!early) {
        wait_vm<0>();
        t_done(mkrsrc(ws, WS_END), fb, gb, tag, lane);
        await<2>(fb + F_HC, 4u);
    }
    c_chunk(Cre, Cim, lb, gb, pt, lw, lane, 2); c_chunk(Cre, Cim, lb, gb, pt, lw, lane, 3);
    if (early) {
        wait_vm<16>();
        t_done(mkrsrc(ws, WS_END), fb, gb, tag, lane);
    }
    TS(44);
    wait_vm<0>();
    sig(fb + F_FULL, 0x1100u);
    TS(47);
#pragma unroll 1
    for (int j = 4; j < 8; ++j) {
        await<2>(fb + F_FREE, 4u << (4 * (j - 4)));
        c_chunk(Cre, Cim, lb, gb, pt, lw, lane, j);
        wait_vm<8>();
        if (j > 4) sig(fb + F_FULL, 1u << (4 * (j - 1)));
        TS(44 + j);
    }
    wait_vm<0>();
    sig(fb + F_FULL, 1u << 28);
    TSE(1, lw == 0);
}

DEV void role_last(char* ws, lptr smem, int gb, int w, int lane, u32x2 tag, int fe, u32x2 (&xc)[8] TSARG) {
    int s2t, dat; tile_of(gb, s2t, dat);
    const int c = lane & 15, q = lane >> 4;
    const rsrc_t rs = mkrsrc(ws, WS_END);
    const unsigned fb = ldsa(smem + A_FLG);
    SCHED();
    u32x4 fc[2][2], fs[2][2];
#pragma unroll
    for (int n = 0; n < 2; ++n) {
        float dt = (float)(32 * w + 16 * n + c) * (1.0f / 128.0f); PIN(dt);
        float cc[8], ss[8];
        const float t0 = __builtin_amdgcn_fractf(dt * (float)(4 * q)), t1 = __builtin_amdgcn_fractf(dt * (float)(16 + 4 * q));
#pragma unroll
        for (int e = 0; e < 8; ++e) { const float f = fmaf((float)(e & 3), dt, e < 4 ? t0 : t1); cc[e] = __builtin_amdgcn_cosf(f); ss[e] = __builtin_amdgcn_sinf(f); }
        fc[n][0] = (u32x4){pkh(cc[0], cc[1]), pkh(cc[2], cc[3]), pkh(cc[4], cc[5]), pkh(cc[6], cc[7])};
        fs[n][0] = (u32x4){pkh(ss[0], ss[1]), pkh(ss[2], ss[3]), pkh(ss[4], ss[5]), pkh(ss[6], ss[7])};
        fc[n][1] = ((c & 1) ? fs[n][0] : fc[n][0]) ^ (((c + 1) & 2) ? SG : 0u);
        fs[n][1] = ((c & 1) ? fc[n][0] : fs[n][0]) ^ ((c & 2) ? SG : 0u);
        PIN(fc[n][0]); PIN(fs[n][0]); PIN(fc[n][1]); PIN(fs[n][1]);
        if (n == 0) { wait_vm<0>(); t_done(rs, fb, gb, tag, lane); TS(44); }
    }
    TS(45);
    __builtin_amdgcn_s_setprio(3);
    if (w == 0) {
        u32x4 fl[2];
        unsigned m = await<2>(fb + F_FULL, 8u) & 0x88888888u;
#pragma unroll 1
        for (int spin = 0; spin < (1 << 20) && m != 0x88888888u; ++spin) {
            m = h_poll<4>(rs, gb, lane, tag);
            sig_or(fb + F_FULL, m);
        }
        TS(50);
#pragma unroll 1
        for (int spin = 0; spin < (1 << 20); ++spin) { flag_get(rs, OFF_FT, gb, lane, fl); if (flag_ok(fl, tag)) break; }
        const unsigned em = wave_pkmax(pkmax(pkmax(fl[0].x, fl[0].z), pkmax(fl[1].x, fl[1].z))) & 0xffu;
#pragma unroll
        for (int i = 0; i < 2; ++i) {
            const float a = ldexpf(1.f, (int)(fl[i].x & 0xffu) - (int)em), b = ldexpf(1.f, (int)(fl[i].z & 0xffu) - (int)em);
            lst<u32x2>(smem + A_MAT + i * 512 + lane * 8, (u32x2){pkh(a, a), pkh(b, b)});
        }
        sig(fb + F_TR, em | 0x8000u);
    }
    fe += await<1>(fb + F_TR, 0x8000u) & 0xffu;
    TS(46);
    u32x4 tv[4]; u32x2 gg[2];
#pragma unroll
    for (int i = 0; i < 4; ++i)
        tv[i] = __builtin_amdgcn_raw_buffer_load_b128(rs, (lane >> 2) * 128 + (lane & 3) * 16 + (i & 1) * 2048, OFF_T + (s2t * 8 + (dat >> 1)) * 32768 + w * 8192 + (dat & 1) * 64 + (i >> 1) * 4096, SC1);
#pragma unroll
    for (int n = 0; n < 2; ++n) gg[n] = __builtin_amdgcn_raw_buffer_load_b64(rs, c * 8 + n * 128, OFF_G + s2t * 1024 + w * 256, SC1);
    __builtin_amdgcn_s_setprio(PRIOR);
    unsigned ts[4];
#pragma unroll
    for (int i = 0; i < 4; ++i) ts[i] = lld<unsigned>(smem + A_MAT + i * 256 + (lane >> 5) * 128 + w * 32 + ((lane >> 2) & 7) * 4);
#pragma unroll
    for (int i = 0; i < 4; ++i) {
        tv[i] = __builtin_bit_cast(u32x4, __builtin_bit_cast(f16x8, tv[i]) * __builtin_bit_cast(f16x8, (u32x4){ts[i], ts[i], ts[i], ts[i]}));
        lst<u32x4>(smem + A_XT + w * 4096 + ((lane >> 1) & 1) * 2048 + i * 512 + (lane >> 2) * 32 + (lane & 1) * 16, tv[i]);
    }
    TS(47);
    sig(fb + F_TT, 1u);
    await<0>(fb + F_TT, 4u);
    f32x4 P0[2], P1[2], P2[2], P3a[2], P3b[2];
#pragma unroll
    for (int n = 0; n < 2; ++n) { P0[n] = Z4; P1[n] = Z4; P2[n] = Z4; P3a[n] = Z4; P3b[n] = Z4; }
    lptr tt = smem + A_XT + lane * 8;
#pragma unroll
    for (int h = 0; h < 2; ++h) {
        const u32x4 a1 = trfrag(tt + 1 * 4096 + h * 1024, 512), b1 = trfrag(tt + 1 * 4096 + 2048 + h * 1024, 512);
        const u32x4 a3 = trfrag(tt + 3 * 4096 + h * 1024, 512), b3 = trfrag(tt + 3 * 4096 + 2048 + h * 1024, 512);
        const u32x4 a0 = trfrag(tt + h * 1024, 512), b0 = trfrag(tt + 2048 + h * 1024, 512);
        const u32x4 a2 = trfrag(tt + 2 * 4096 + h * 1024, 512), b2 = trfrag(tt + 2 * 4096 + 2048 + h * 1024, 512);
#pragma unroll
        for (int n = 0; n < 2; ++n) {
            u32x4 cw = fc[n][h], sw_ = fs[n][h];
            P1[n] = mfh(a1, cw, P1[n]); P3a[n] = mfh(a1, sw_, P3a[n]); P3b[n] = mfh(b1, cw, P3b[n]); P1[n] = mfh(b1, sw_, P1[n]);
            if (h == 0) cw.x = q == 0 ? (cw.x & 0xffff0000u) | ((c & 1) ? 0xbc00u : 0x3c00u) : cw.x;
            P3a[n] = mfh(b3, cw, P3a[n]); P3b[n] = mfh(a3, sw_, P3b[n]); P1[n] = mfh(a3, cw, P1[n]); P1[n] = mfh(b3, sw_, P1[n]);
            if (h == 0) { cw.x = q == 0 ? (cw.x & 0xffff0000u) | 0x3800u : cw.x; sw_.x = q == 0 ? (sw_.x & 0xffff0000u) | ((c & 1) ? 0x3800u : 0xb800u) : sw_.x; }
            P0[n] = mfh(a0, cw, P0[n]); P2[n] = mfh(a2, cw, P2[n]); P0[n] = mfh(b0, sw_, P0[n]); P2[n] = mfh(b2, sw_, P2[n]);
        }
    }
    f32x4 r0[2][4];
#pragma unroll
    for (int n = 0; n < 2; ++n) {
        const float g = __uint_as_float(gg[n].x), og = __uint_as_float(gg[n].y);
        const f32x4 sp = P0[n] + P2[n], sm = P0[n] - P2[n], p3 = P3a[n] - P3b[n];
        const f32x4 av[4] = {sp + P1[n], sm - p3, sp - P1[n], sm + p3};
#pragma unroll
        for (int m = 0; m < 4; ++m) {
            const unsigned b0 = pkb(ldexpf(av[m][0], fe), ldexpf(av[m][1], fe)), b1 = pkb(ldexpf(av[m][2], fe), ldexpf(av[m][3], fe));
            const unsigned o0 = pkb(g * lo2f(b0), g * hi2f(b0)), o1 = pkb(g * lo2f(b1), g * hi2f(b1));
            const u32x2 p = xc[4 * n + m];
            r0[n][m] = (f32x4){fmaf(og, lo2f(p.x), lo2f(o0)), fmaf(og, hi2f(p.x), hi2f(o0)), fmaf(og, lo2f(p.y), lo2f(o1)), fmaf(og, hi2f(p.y), hi2f(o1))};
            PIN(r0[n][m]);
        }
    }
    TS(48);
    await<2>(fb + F_FREE, 4u << 16);
#pragma unroll
    for (int n = 0; n < 2; ++n) {
#pragma unroll
        for (int m = 0; m < 4; ++m) lst<f32x4>(smem + A_R0 + ((w * 2 + n) * 4 + m) * 1024 + q * 256 + ((c ^ (4 * m + q)) << 4), r0[n][m]);
        if (q == 0) lst<float>(smem + A_AGG + (32 * w + 16 * n + c) * 4, 2.0f * __uint_as_float(gg[n].y));
    }
    sig(fb + F_R0, 1u);
    TS(49);
    TSE(w ? 4 : 5, w == 0 || w == 2);
}

NOPK __global__ __launch_bounds__(768, 3) void gm_kernel(const u16* __restrict__ x, const u16* __restrict__ w_r, const u16* __restrict__ w_i,
                                                         const u16* __restrict__ Bre, const u16* __restrict__ Bim, const u16* __restrict__ Cre,
                                                         const u16* __restrict__ Cim, const u16* __restrict__ lam_re,
                                                         const u16* __restrict__ lam_im, const u16* __restrict__ log_dt,
                                                         const u16* __restrict__ Dskip, const u16* __restrict__ gate_w, char* ws, float* __restrict__ out) {
    extern __shared__ __attribute__((aligned(16))) char smem_[];
    lptr smem = (lptr)smem_;
    u64 pc; int len;
    asm volatile("s_getpc_b64 %0\ngm_code_beg:\n\ts_mov_b32 %1, gm_code_end - gm_code_beg" : "=s"(pc), "=s"(len));
    const int wid = __builtin_amdgcn_readfirstlane(threadIdx.x >> 6), lane = threadIdx.x & 63;
    int w = wid & 7, gb = blockIdx.x;
    asm volatile("" : "+s"(w), "+s"(gb));
    if (wid < 4) role_mma(ws, out, smem, blockIdx.x, wid, lane, pc, len);
    else {
        TSD();
        float gsum = 0.f;
        u32x2 xc[8], tag;
        u32x4 ut[2];
        unsigned em;
        if (wid < 8) {
            gsum = role_load(x, Bre, Bim, gate_w, ws, smem, blockIdx.x, wid - 4, lane TSPASS);
            em = u_load(ws, smem, blockIdx.x, wid, lane, ut);
            TS(2);
            int lt, pt; tile_of(blockIdx.x, lt, pt);
            c_chunk(Cre, Cim, ldsa(smem), blockIdx.x, pt, wid - 4, lane, 0);
            c_chunk(Cre, Cim, ldsa(smem), blockIdx.x, pt, wid - 4, lane, 1);
#pragma unroll
            for (int i = 0; i < 8; ++i) asm volatile("" : "=v"(xc[i]));
        } else em = role_aux(Cre, Cim, w_r, w_i, lam_re, lam_im, log_dt, Dskip, ws, smem, blockIdx.x, wid - 8, lane, xc, ut TSPASS);
        const int fe = role_mid(Cre, Cim, ws, smem, gb, w, lane, gsum, tag, ut, em TSPASS);
        if (w >= 4) role_cload(Cre, Cim, ws, smem, gb, w - 4, lane, tag TSPASS);
        else role_last(ws, smem, gb, w, lane, tag, fe, xc TSPASS);
    }
}

extern "C" void kernel_launch(void* const* d_in, const int* in_sizes, int n_in, void* d_out, int out_size, void* d_ws, size_t ws_size,
                              hipStream_t stream) {
    (void)hipFuncSetAttribute((const void*)gm_kernel, hipFuncAttributeMaxDynamicSharedMemorySize, SMEM);
    gm_kernel<<<256, 768, SMEM, stream>>>((const u16*)d_in[0], (const u16*)d_in[1], (const u16*)d_in[2], (const u16*)d_in[6], (const u16*)d_in[7],
                                          (const u16*)d_in[8], (const u16*)d_in[9], (const u16*)d_in[4], (const u16*)d_in[5], (const u16*)d_in[10],
                                          (const u16*)d_in[11], (const u16*)d_in[12], (char*)d_ws, (float*)d_out);
}
```

```cpp
#include <hip/hip_runtime.h>
#include <stdint.h>

typedef __attribute__((ext_vector_type(8))) __bf16 bf16x8;
typedef __attribute__((ext_vector_type(2))) __bf16 bf16x2;
typedef __attribute__((ext_vector_type(8))) _Float16 f16x8;
typedef __attribute__((ext_vector_type(2))) _Float16 f16x2;
typedef __attribute__((ext_vector_type(4))) float f32x4;
typedef __attribute__((ext_vector_type(2))) float f32x2;
typedef __attribute__((ext_vector_type(4))) unsigned u32x4;
typedef __attribute__((ext_vector_type(3))) unsigned u32x3;
typedef __attribute__((ext_vector_type(2))) unsigned u32x2;
typedef __attribute__((ext_vector_type(4))) short s16x4;
typedef __attribute__((ext_vector_type(2))) unsigned short u16x2;
typedef unsigned short u16;
typedef unsigned long long u64;
#define GAS __attribute__((address_space(1)))
#define LAS __attribute__((address_space(3)))
#define NOPK __attribute__((target("no-packed-fp32-ops")))
#define DEV __device__ __forceinline__

constexpr int S_ = 2048, D_ = 1024, KF = 513;

constexpr unsigned OFF_H = 0;
constexpr unsigned OFF_U = OFF_H + (8u << 20);
constexpr unsigned OFF_T = OFF_U + (4u << 20);
constexpr unsigned OFF_G = OFF_T + (4u << 20);
constexpr unsigned OFF_FU = OFF_G + 16384u;
constexpr unsigned OFF_FT = OFF_FU + 16384u;
constexpr unsigned OFF_FH = OFF_FT + 16384u;
constexpr unsigned OFF_GRAN = OFF_FH + 16384u;
constexpr unsigned OFF_E = OFF_GRAN + 16u * 1024u * 16u;
constexpr unsigned WS_END = OFF_E + 128u;

#define TL(i)
#define TSD()
#define TS(i)
#define TC(i)
#define TSE(slot, on)

DEV unsigned pkb(float a, float b) { f32x2 v = {a, b}; return __builtin_bit_cast(unsigned, __builtin_convertvector(v, bf16x2)); }
DEV unsigned pkh(float a, float b) { f32x2 v = {a, b}; return __builtin_bit_cast(unsigned, __builtin_convertvector(v, f16x2)); }
DEV float lo2f(unsigned p) { return __uint_as_float(p << 16); }
DEV float hi2f(unsigned p) { return __uint_as_float(p & 0xffff0000u); }
DEV float bfr(float f) { return lo2f(pkb(f, f)); }
DEV float2 cmul(float2 a, float2 b) { return make_float2(a.x * b.x - a.y * b.y, a.x * b.y + a.y * b.x); }
DEV float2 cfma(float2 a, float2 b, float2 c) { return make_float2(fmaf(a.x, b.x, fmaf(-a.y, b.y, c.x)), fmaf(a.x, b.y, fmaf(a.y, b.x, c.y))); }
DEV f32x4 mfb(u32x4 a, u32x4 b, f32x4 c) { return __builtin_amdgcn_mfma_f32_16x16x32_bf16(__builtin_bit_cast(bf16x8, a), __builtin_bit_cast(bf16x8, b), c, 0, 0, 0); }
DEV f32x4 mfh(u32x4 a, u32x4 b, f32x4 c) { return __builtin_amdgcn_mfma_f32_16x16x32_f16(__builtin_bit_cast(f16x8, a), __builtin_bit_cast(f16x8, b), c, 0, 0, 0); }
DEV unsigned pkmax(unsigned a, unsigned b) { return __builtin_bit_cast(unsigned, __builtin_elementwise_max(__builtin_bit_cast(u16x2, a), __builtin_bit_cast(u16x2, b))); }
typedef LAS char* lptr;
DEV unsigned ldsa(lptr p) { return (unsigned)(uintptr_t)p; }
template <class T> DEV T lld(lptr p) { return *(LAS const T*)p; }
template <class T> DEV void lst(lptr p, T v) { *(LAS T*)p = v; }
DEV u32x2 trread(lptr p) { return __builtin_bit_cast(u32x2, __builtin_amdgcn_ds_read_tr16_b64_v4i16((LAS s16x4*)p)); }
DEV u32x4 trfrag(lptr p, int second) { const u32x2 a = trread(p), b = trread(p + second); return (u32x4){a.x, a.y, b.x, b.y}; }
typedef __amdgpu_buffer_rsrc_t rsrc_t;
DEV u32x4 ldfrag(lptr tile, int row, int chunk) { return lld<u32x4>(tile + row * 128 + ((chunk ^ (row & 7)) << 4)); }
template <int N> DEV void wait_vm() { asm volatile("s_waitcnt vmcnt(%0)" :: "i"(N) : "memory"); }
#define LDS_WAIT() asm volatile("s_waitcnt lgkmcnt(0)" ::: "memory")
#define SCHED() __builtin_amdgcn_sched_barrier(0)
#define BAR() do { __builtin_amdgcn_s_barrier(); asm volatile("" ::: "memory"); } while (0)
#define PIN(x) asm volatile("" : "+v"(x))
template <int I> struct IC { static constexpr int v = I; };
template <int I, int N, class F> DEV void sfor(F&& f) { if constexpr (I < N) { f(IC<I>{}); sfor<I + 1, N>(f); } }
#define INL __attribute__((always_inline))
DEV rsrc_t mkrsrc(const void* p, unsigned n) { return __builtin_amdgcn_make_buffer_rsrc((void*)p, 0, n, 0x00020000); }
constexpr int SC1 = 16;
#define WTO 16
template <int AUX> DEV void st128(u32x4 v, rsrc_t rs, unsigned off) { __builtin_amdgcn_raw_buffer_store_b128(v, rs, off, 0, AUX); }
DEV void lds_add(unsigned a) { asm volatile("ds_add_u32 %0, %1" :: "v"(a), "v"(1u) : "memory"); }
DEV unsigned lds_peek(unsigned a) {
    unsigned v;
    asm volatile("ds_read_b32 %0, %1\n\ts_waitcnt lgkmcnt(0)" : "=v"(v) : "v"(a) : "memory");
    return __builtin_amdgcn_readfirstlane(v);
}
DEV void tile_of(int gb, int& ta, int& tb) {
    const int xcd = gb & 7, loc = gb >> 3;
    ta = (xcd >> 1) * 4 + (loc >> 3);
    tb = (xcd & 1) * 8 + (loc & 7);
}
#define DMAP 1
DEV int da_of(int pt) { return DMAP ? ((pt & 7) << 1) | (pt >> 3) : pt; }
DEV void rows4(float a, float& a0, float& a1, float& a2, float& a3) {
    const unsigned u = __float_as_uint(a);
    const auto r = __builtin_amdgcn_permlane32_swap(u, u, false, false);
    const auto p = __builtin_amdgcn_permlane16_swap(r[0], r[0], false, false);
    const auto s = __builtin_amdgcn_permlane16_swap(r[1], r[1], false, false);
    a0 = __uint_as_float(p[0]); a1 = __uint_as_float(p[1]); a2 = __uint_as_float(s[0]); a3 = __uint_as_float(s[1]);
}
DEV float rowsum4(float a) {
    const unsigned u = __float_as_uint(a);
    const auto r = __builtin_amdgcn_permlane32_swap(u, u, false, false);
    const unsigned t = __float_as_uint(__uint_as_float(r[0]) + __uint_as_float(r[1]));
    const auto p = __builtin_amdgcn_permlane16_swap(t, t, false, false);
    return __uint_as_float(p[0]) + __uint_as_float(p[1]);
}
DEV float2 sel4(int q, float2 a, float2 b, float2 c, float2 d) {
    PIN(d.x); PIN(d.y);
    const float2 e = (q & 1) ? b : a, f = (q & 1) ? d : c;
    return (q & 2) ? f : e;
}
template <int R> DEV unsigned ror16(unsigned v) { return __builtin_amdgcn_update_dpp(0u, v, 0x120 + R, 0xf, 0xf, false); }
DEV unsigned wave_pkmax(unsigned m) {
    m = pkmax(m, ror16<8>(m)); m = pkmax(m, ror16<4>(m)); m = pkmax(m, ror16<2>(m)); m = pkmax(m, ror16<1>(m));
    const auto r = __builtin_amdgcn_permlane32_swap(m, m, false, false);
    m = pkmax(r[0], r[1]);
    const auto p = __builtin_amdgcn_permlane16_swap(m, m, false, false);
    return pkmax(p[0], p[1]);
}
DEV float half_sum(float v) {
    v += __uint_as_float(ror16<8>(__float_as_uint(v))); v += __uint_as_float(ror16<4>(__float_as_uint(v)));
    v += __uint_as_float(ror16<2>(__float_as_uint(v))); v += __uint_as_float(ror16<1>(__float_as_uint(v)));
    const auto p = __builtin_amdgcn_permlane16_swap(__float_as_uint(v), __float_as_uint(v), false, false);
    return __uint_as_float(p[0]) + __uint_as_float(p[1]);
}
DEV u32x4 quads(lptr buf, int lane, u32x2 p, u32x2 n) {
    lptr wp = buf + (lane & 15) * 64 + ((lane >> 4) & 1) * 8;
    const int v = (lane >> 5) ^ ((lane >> 2) & 3);
    lst<u32x2>(wp + (v << 4), p); lst<u32x2>(wp + ((v ^ 2) << 4), n);
    asm volatile("" ::: "memory");
    return lld<u32x4>(buf + ((lane ^ (lane >> 4)) << 4));
}

#define PRIOA 2
#define NREP 8
#define PRIOM 1
#define PRIOT 2
#define PRIOG 2
#define AD 8
#define NB 12
#define NP 4
#define AM 1
#define CP 0
#define HP 0
#define XE 0
#define TSL 16
#define CSL 1
#define WG2 2
#define WG1 1
#define SPC 1
#define PRV 0
#define PRX 0
#define TLS 0
#define RDL 0
#define WROW {0, 0, 0, 0, 8}
#define ROT (lt & 12)
#define PRIOL 3
#define XM 0
#define UFS 6
#define PRIOR 0
#define PRS 2
#define WXS 13
#define CPF 1
#define FST 8
#define HREP 8
#define NP1 1
#define PDL 8
#define PDE 0
#define PDM 0x80
#define NB2 NB
#define XP 4
#define PU 0
#define GDL 20
#define BAL -1
#define BA2 0
#define FTC 1
#define LBS 0
#define PT 0
#define PTS 4
#define BSL 0
#define BS2 0
#define PZ1 0
#define PZ2 0
#define PZ3 0
#define PZA 0
#define PZB 48
#define XLP 0
#define PRIOI 0
#define RST 44
#define SIL 1
#define CPM 7
#define PRIOE 2
#define MSL 1
#define URS 6
#define UPRE 1
#define UCK 13
#define PRIOD 3
#define PRIOW 0
#define CDL 0
#define ATS 14
#define UPL 10
#define C1E 1
#define TP0 56
#define PD0 32
#define CXM 6
#define CX(g) ((g) & CXM)
#define GSTEP {0, 0, 0, 0}
#define XS 1
#define B1S 0
#define US 1
#define UPS 1
#define DS 8
#define P0S 8
#define VL 1
#define PN -1
#define PG -1
#define SNK 1
#define HFIRST 8u
#define RDPAT {0, 1, 2, -1, 3, 4, -1, 5, 6, -1, 7, -1, -1, -1, -1, -1}
#define Z4 ((f32x4){0.f, 0.f, 0.f, 0.f})
constexpr unsigned SG = 0x80008000u;
struct CM { u32x4 re, im; };
DEV u32x4 cmat1(float t0, float dt) {
    float cc[4], ss[4];
    t0 = __builtin_amdgcn_fractf(t0);
#pragma unroll
    for (int e = 0; e < 4; ++e) { const float f = fmaf((float)e, dt, t0); cc[e] = __builtin_amdgcn_cosf(f); ss[e] = __builtin_amdgcn_sinf(f); }
    return (u32x4){pkh(ss[0], ss[1]), pkh(ss[2], ss[3]), pkh(cc[0], cc[1]), pkh(cc[2], cc[3])};
}
DEV CM cexp(u32x4 v) { CM m; m.re = (u32x4){v.z, v.w, v.x ^ SG, v.y ^ SG}; m.im = v; PIN(m.re); PIN(m.im); return m; }
DEV u32x4 pk8(f32x4 r, f32x4 i) { return (u32x4){pkh(r[0], r[1]), pkh(r[2], r[3]), pkh(i[0], i[1]), pkh(i[2], i[3])}; }
DEV u32x4 wmul(f32x4 r, f32x4 i, u32x4 w) {
    float yr[4], yi[4];
#pragma unroll
    for (int e = 0; e < 4; ++e) {
        const unsigned wu = w[e];
        const f16x2 h = __builtin_bit_cast(f16x2, wu);
        yr[e] = fmaf(r[e], (float)h[0], -i[e] * (float)h[1]); yi[e] = fmaf(r[e], (float)h[1], i[e] * (float)h[0]);
    }
    return (u32x4){pkh(yr[0], yr[1]), pkh(yr[2], yr[3]), pkh(yi[0], yi[1]), pkh(yi[2], yi[3])};
}
DEV u32x4 wcvt(u32x4 w, float sr, float si) {
#pragma unroll
    for (int e = 0; e < 4; ++e) w[e] = pkh(lo2f(w[e]) * sr, hi2f(w[e]) * si);
    PIN(w);
    return w;
}
DEV u32x4 mkdesc(const void* p, unsigned n) { const u64 a = (u64)p; return (u32x4){(unsigned)a, (unsigned)(a >> 32) & 0xffffu, n, 0x00020000u}; }
DEV void dma16(u32x4 d, unsigned l, unsigned vo, unsigned so) {
    asm volatile("s_nop 0\n\tbuffer_load_dwordx4 %0, %1, %2 offen lds" :: "v"(vo), "s"(d), "s"(so), "{m0}"(l) : "memory");
}
DEV void dma16s(u32x4 d, unsigned l, unsigned vo, unsigned so) {
    asm volatile("s_nop 0\n\tbuffer_load_dwordx4 %0, %1, %2 offen sc1 lds" :: "v"(vo), "s"(d), "s"(so), "{m0}"(l) : "memory");
}
DEV void sig(unsigned a, unsigned v) { asm volatile("s_mov_b64 exec, 1\n\tds_add_u32 %0, %1\n\ts_mov_b64 exec, -1" :: "v"(a), "v"(v) : "memory"); }
template <int O> DEV void sig_o(unsigned a, unsigned v) { asm volatile("s_mov_b64 exec, 1\n\tds_add_u32 %0, %1 offset:%2\n\ts_mov_b64 exec, -1" :: "v"(a), "v"(v), "i"(O) : "memory"); }
DEV unsigned sig_rtn(unsigned a) {
    unsigned v;
    asm volatile("s_mov_b64 exec, 1\n\tds_add_rtn_u32 %0, %1, %2\n\ts_mov_b64 exec, -1\n\ts_waitcnt lgkmcnt(0)" : "=&v"(v) : "v"(a), "v"(1u) : "memory");
    return __builtin_amdgcn_readfirstlane(v);
}
template <int SLP> DEV unsigned await(unsigned a, unsigned mask) {
    unsigned v, n, r, t;
    asm volatile("s_mov_b32 %1, 0x400000\n"
                 "1:\tds_read_b32 %0, %4\n\ts_waitcnt lgkmcnt(0)\n\tv_readfirstlane_b32 %2, %0\n\ts_and_b32 %3, %2, %5\n\ts_cbranch_scc1 2f\n\t"
                 "s_sleep %6\n\ts_sub_u32 %1, %1, 1\n\ts_cbranch_scc0 1b\n"
                 "2:" : "=&v"(v), "=&s"(n), "=&s"(r), "=&s"(t) : "v"(a), "s"(mask), "i"(SLP) : "memory", "scc");
    return r;
}
#define BMF "v_mfma_f32_16x16x32_bf16 %0, %2, %3, %0\n\tv_mfma_f32_16x16x32_bf16 %1, %3, %2, %1\n\t"
template <int SLP> DEV void burn(int n, u32x4& a, u32x4& b) {
    f32x4 d0, d1;
    asm volatile("1:\t" BMF BMF "s_nop %5\n\ts_sub_u32 %4, %4, 1\n\ts_cbranch_scc0 1b\n\ts_nop 7\n\ts_nop 3"
                 : "=&v"(d0), "=&v"(d1), "+v"(a), "+v"(b), "+s"(n) : "i"(SLP) : "scc");
}
template <int SLP> DEV unsigned await_burn(unsigned ad, unsigned mask, u32x4 a, u32x4 b) {
    unsigned v, n, r, t; f32x4 d0, d1;
    asm volatile("s_mov_b32 %5, 0x400000\n"
                 "1:\tds_read_b32 %4, %8\n\t" BMF BMF "s_waitcnt lgkmcnt(0)\n\tv_readfirstlane_b32 %6, %4\n\ts_and_b32 %7, %6, %9\n\ts_cbranch_scc1 2f\n\t"
                 ".rept %10\n\t" BMF ".endr\n\ts_sub_u32 %5, %5, 1\n\ts_cbranch_scc0 1b\n"
                 "2:\ts_nop 7\n\ts_nop 3" : "=&v"(d0), "=&v"(d1), "+v"(a), "+v"(b), "=&v"(v), "=&s"(n), "=&s"(r), "=&s"(t) : "v"(ad), "s"(mask), "i"(SLP) : "memory", "scc");
    return r;
}
DEV void need_bit(unsigned a, unsigned& r, unsigned mask) {
    unsigned v, n, t;
    asm volatile("s_and_b32 %3, %2, %5\n\ts_cbranch_scc1 2f\n\ts_mov_b32 %1, 0x400000\n"
                 "1:\tds_read_b32 %0, %4\n\ts_waitcnt lgkmcnt(0)\n\tv_readfirstlane_b32 %2, %0\n\ts_and_b32 %3, %2, %5\n\ts_cbranch_scc1 2f\n\t"
                 "s_sub_u32 %1, %1, 1\n\ts_cbranch_scc0 1b\n"
                 "2:" : "=&v"(v), "=&s"(n), "+s"(r), "=&s"(t) : "v"(a), "i"(mask) : "memory", "scc");
}
#define MF2(a, b, c) asm("v_mfma_f32_16x16x32_bf16 %0, %1, %2, %0" : "+v"(c) : "v"(a), "v"(b))
DEV void flag_put(rsrc_t rs, unsigned off, int gb, u32x2 v, int lane) {
    if (lane < NREP) __builtin_amdgcn_raw_buffer_store_b64(v, rs, lane * 2048, off + gb * 8, SC1);
}
DEV void flag_get(rsrc_t rs, unsigned off, int gb, int lane, u32x4 (&fl)[2]) {
    unsigned o = lane * 16; PIN(o);
#pragma unroll
    for (int i = 0; i < 2; ++i) fl[i] = __builtin_amdgcn_raw_buffer_load_b128(rs, o + i * 1024, off + (gb & (NREP - 1)) * 2048, SC1);
}
DEV bool flag_ok(const u32x4 (&fl)[2], u32x2 tag) {
    bool ok = true;
#pragma unroll
    for (int i = 0; i < 2; ++i) ok = ok & ((fl[i].x ^ tag.x) < 0x10000u) & (fl[i].y == tag.y) & ((fl[i].z ^ tag.x) < 0x10000u) & (fl[i].w == tag.y);
    return __all(ok);
}
template <int N> DEV unsigned h_poll(rsrc_t rs, int gb, int kh, int lane, u32x2 tag) {
    unsigned o = (lane ^ ((CX(gb) & 3) << 4)) * FST, m = 0; PIN(o);
    u32x2 fl[N];
#pragma unroll
    for (int i = 0; i < N; ++i) fl[i] = __builtin_amdgcn_raw_buffer_load_b64(rs, o, OFF_FH + ((gb & (HREP - 1)) * 256 + kh * 128 + (i ^ (CX(gb) >> 2)) * 64) * FST, SC1);
#pragma unroll
    for (int i = 0; i < N; ++i) {
        const u64 b = __ballot(((fl[i].x ^ tag.x) < 0x10000u) & (fl[i].y == tag.y));
#pragma unroll
        for (int j = 0; j < 4; ++j) m |= ((unsigned)(b >> (16 * j)) & 0xffffu) == 0xffffu ? 8u << (16 * i + 4 * j) : 0u;
    }
    return m;
}
DEV void sig_or(unsigned a, unsigned v) { asm volatile("s_mov_b64 exec, 1\n\tds_or_b32 %0, %1\n\ts_mov_b64 exec, -1" :: "v"(a), "v"(v) : "memory"); }

constexpr int A_STAGE = 32768, A_XT = 4 * A_STAGE, A_PRM = A_XT + 16384, A_AGG = A_PRM + 2048, A_FLG = A_AGG + 512, A_GW = A_FLG + 128,
              A_MAT = A_GW + 2048, SMEM = A_MAT + 11264;
constexpr int E1P = 1040, A_EX = A_GW, A_E1 = 0, A_PW = 18 * E1P + 224, A_W = A_STAGE, A_HS = 2 * A_STAGE - 8192, A_OG = A_MAT + 9216;
static_assert(A_PW % 256 == 0 && A_W + 18432 + 512 <= A_HS, "");
constexpr int F_MID = 8, F_EM = 12, F_MAX = 16, F_TAG = 32, F_FULL = 40, F_FREE = 44, F_HC = 48, F_HR = 52, F_TC = 56, F_TR = 60, F_TT = 64, F_ST = 76, F_WM = 80, F_R0 = 96;

constexpr int XG = 0xFF;
DEV void slot_of(int gb, int& mp, int& j1) { gb ^= XG; mp = (gb & 7) * 8 + (gb >> 5); j1 = (gb >> 3) & 3; }
DEV u32x4 mat_sf(int k1, int c, int q) { const float dt = -(float)(k1 + 128 * c) * (1.0f / 2048.0f); return cmat1(dt * (float)(4 * q), dt); }
DEV u32x4 mat_si(int k1, int c, int q) { return cmat1((float)((k1 + 512 * q) * c) * (1.0f / 2048.0f), (float)c * (1.0f / 16.0f)); }

DEV void mk_tables(lptr smem, int gb, int wid, int lane) {
    int mp, j1; slot_of(gb, mp, j1);
    const int c = lane & 15, q = lane >> 4;
    const bool spc = mp == 0 && !(j1 & 1);
    const int k1 = mp ? mp : (j1 == 3 ? 64 : 0);
    lptr mt = smem + A_MAT + lane * 16;
    if (wid == 0) {
        const float dt = -(float)(j1 + 4 * (spc ? c & 7 : c)) * (1.0f / 64.0f), t0 = __builtin_amdgcn_fractf(dt * (float)(4 * q));
        float cc[4], ss[4];
#pragma unroll
        for (int e = 0; e < 4; ++e) { const float f = fmaf((float)e, dt, t0); cc[e] = __builtin_amdgcn_cosf(f); ss[e] = __builtin_amdgcn_sinf(f); }
        const unsigned c0 = pkb(cc[0], cc[1]), c1 = pkb(cc[2], cc[3]), s0 = pkb(ss[0], ss[1]), s1 = pkb(ss[2], ss[3]);
        const unsigned c0l = pkb(cc[0] - lo2f(c0), cc[1] - hi2f(c0)), c1l = pkb(cc[2] - lo2f(c1), cc[3] - hi2f(c1));
        const unsigned s0l = pkb(ss[0] - lo2f(s0), ss[1] - hi2f(s0)), s1l = pkb(ss[2] - lo2f(s1), ss[3] - hi2f(s1));
        u32x4 m[4] = {(u32x4){c0, c1, s0 ^ SG, s1 ^ SG}, (u32x4){s0, s1, c0, c1}, (u32x4){c0l, c1l, s0l ^ SG, s1l ^ SG}, (u32x4){s0l, s1l, c0l, c1l}};
        if (spc) {
            m[0] = c < 8 ? (u32x4){c0, c1, 0, 0} : (u32x4){0, 0, c0, c1}; m[1] = c < 8 ? (u32x4){s0, s1, 0, 0} : (u32x4){0, 0, s0, s1};
            m[2] = c < 8 ? (u32x4){c0l, c1l, 0, 0} : (u32x4){0, 0, c0l, c1l}; m[3] = c < 8 ? (u32x4){s0l, s1l, 0, 0} : (u32x4){0, 0, s0l, s1l};
        }
#pragma unroll
        for (int i = 0; i < 4; ++i) lst<u32x4>(mt + i * 1024, m[i]);
    } else if (wid == 1) {
        const int jq = spc ? (4 * q) & 7 : 4 * q;
        float cc[4], ss[4];
        const float t0 = __builtin_amdgcn_fractf((float)(c * (j1 + 4 * jq)) * (1.0f / 64.0f));
#pragma unroll
        for (int e = 0; e < 4; ++e) {
            const float f = fmaf((float)c, (float)e * (1.0f / 16.0f), t0);
            const float wg = !spc || (gb == XG && jq + e == 0) ? 0.125f : 0.25f;
            cc[e] = __builtin_amdgcn_cosf(f) * wg; ss[e] = -__builtin_amdgcn_sinf(f) * wg;
        }
        const unsigned c0 = pkh(cc[0], cc[1]), c1 = pkh(cc[2], cc[3]), n0 = pkh(ss[0], ss[1]), n1 = pkh(ss[2], ss[3]);
        u32x4 mia = (u32x4){c0, n0, c1, n1}, mib = (u32x4){n0, c0 ^ SG, n1, c1 ^ SG};
        if (spc) { mib = q < 2 ? (u32x4){0, 0, 0, 0} : mia ^ SG; if (q >= 2) mia = (u32x4){0, 0, 0, 0}; }
        lst<u32x4>(mt + 6 * 1024, mia); lst<u32x4>(mt + 7 * 1024, mib);
        lst<u32x4>(mt + 8 * 1024, cmat1((float)(c * q) * -0.25f, (float)c * (-1.0f / 16.0f)));
    } else {
        lst<u32x4>(mt + (2 + wid) * 1024, wid == 2 ? mat_sf(k1, c, q) : mat_si(k1, c, q));
        const int ja = (spc ? 0 : 8 * (wid - 2)) + (lane >> 3);
        const float dt = -(float)(j1 + 4 * ja) * (1.0f / 1024.0f), t0 = dt * (float)(2 * (lane & 7)), t1 = t0 + dt;
        lst<f32x4>(mt + (7 + wid) * 1024, (f32x4){__builtin_amdgcn_cosf(t0), __builtin_amdgcn_sinf(t0), __builtin_amdgcn_cosf(t1), __builtin_amdgcn_sinf(t1)});
    }
}

template <int SLP = 0> DEV void c_chunk(const u16* __restrict__ Cre, const u16* __restrict__ Cim, unsigned lb, int gb, int da, int lw, int lane, int j) {
    const u32x4 rc = mkdesc(lw < 2 ? Cre : Cim, D_ * D_ * 2);
    const unsigned vo = (lane >> 3) * 8192 + (((lane & 7) ^ (lane >> 3)) << 4);
#pragma unroll
    for (int i = 0; i < 8; ++i) {
        if (SLP && i) __builtin_amdgcn_s_sleep(SLP);
        dma16(rc, lb + ((j + 2) & 3) * A_STAGE + lw * 8192 + i * 1024, vo, (512 * (lw & 1) + 256 * (i & 1) + 32 * (da >> 1) + (i >> 1)) * 2048 + (8 * (da & 1) + (j ^ CX(gb))) * 128);
    }
}
DEV void touch(u32x4 d, unsigned l, unsigned vo, unsigned so) {
    asm volatile("s_nop 0\n\tbuffer_load_dword %0, %1, %2 offen sc1 lds" :: "v"(vo), "s"(d), "s"(so), "{m0}"(l) : "memory");
}
DEV void c_pref(const u16* __restrict__ Cre, const u16* __restrict__ Cim, unsigned lb, int gb, int lt, int da, int lane, int j) {
    touch(mkdesc((lt & 2) ? Cim : Cre, D_ * D_ * 2), lb + A_PRM, ((lane >> 5) * 256 + (lane & 31)) * 2048,
          (512 * (lt & 1) + 32 * (da >> 1)) * 2048 + (8 * (da & 1) + (j ^ CX(gb))) * 128);
}
constexpr int KS1 = 32 - 4 * NP, KS2 = 4 * NP, NST = KS1 + 2 * KS2;
constexpr int st_k(int st) { return st < 32 ? st : st - KS2; }
constexpr int b_st(int b) { return b < 2 * KS1 ? b >> 1 : b - KS1; }
constexpr int b_n(int b) { return b < 2 * KS1 ? b & 1 : b >= KS1 + 32; }
constexpr int b_nx(int b) { return b < 2 * KS1 ? b + NB : b + NB2 < 64 && b + NB2 >= 2 * KS1 + NB ? b + NB2 : -1; }
constexpr bool b_new(int b) { return b < KS1 + 32 && !b_n(b) && !(b_st(b) & 3); }

DEV void role_mma(const u16* __restrict__ x, const u16* __restrict__ Dskip, char* ws, float* __restrict__ out, lptr smem, int gb, int wid, int lane, u64 pc, int len) {
    int lt, pt; tile_of(gb, lt, pt);
    const int wr = wid >> 1, wc = wid & 1, c = lane & 15, q = lane >> 4;
    const rsrc_t rs = mkrsrc(ws, WS_END);
    const unsigned fb = ldsa(smem + A_FLG);
    TSD(); TS(0);
    u64 tag = __hip_atomic_load((GAS u64*)(ws + OFF_E), __ATOMIC_RELAXED, __HIP_MEMORY_SCOPE_WORKGROUP) + 0x9E3779B97F4A7C15ull;
    unsigned cpf = 0;
    if (CPF == 1) { if (wid == 3) cpf = __builtin_amdgcn_raw_buffer_load_b32(mkrsrc((const void*)pc, max(len, 0)), lane * 128, ((gb >> 3) % CPM) * 8192, 0); }
    if (FTC == 1) { if (wid == 2) cpf = __builtin_amdgcn_raw_buffer_load_b32(mkrsrc(ws + OFF_G + gb * 256, 256), lane * 64, 0, SC1); }
    SCHED();
    if (wid == 0 && lane < 32) lst<unsigned>(smem + A_FLG + lane * 4, 0u);
    mk_tables(smem, gb, wid, lane);
    f32x4 acc[4][4];
#pragma unroll
    for (int m = 0; m < 4; ++m)
#pragma unroll
        for (int n = 0; n < 4; ++n) acc[m][n] = Z4;
    u32x4 af[2][4], bf_[2][4];
#pragma unroll
    for (int m = 0; m < 4; ++m) { af[1][m] = (u32x4){0, 0, 0, 0}; bf_[1][m] = af[1][m]; }
    lptr fp[8];
#pragma unroll
    for (int i = 0; i < 8; ++i) { fp[i] = smem + (i & 2) * A_STAGE + ((i & 4 ? 128 + wc * 64 : wr * 64) + c) * 128 + (((4 * (i & 1) + q) ^ (c & 7)) << 4); PIN(fp[i]); }
    PIN(tag); PIN(cpf);
    if (wid == 0 && lane == 0) lst<u64>(smem + A_FLG + F_TAG, tag);
    tag = (u64)(unsigned)__builtin_amdgcn_readfirstlane((unsigned)tag) | (u64)(unsigned)__builtin_amdgcn_readfirstlane((unsigned)(tag >> 32)) << 32;
    __builtin_amdgcn_s_waitcnt(0xC07F);
    TS(13);
    unsigned fst = fb + F_ST, sv = 0;
    auto step = [&](int sl, bool pf) INL {
#pragma unroll
        for (int hf = 0; hf < 2; ++hf)
#pragma unroll
            for (int i = 0; i < 16; ++i) {
                const int nn = SNK && (i & 4) ? 3 - (i & 3) : i & 3;
                acc[i >> 2][nn] = mfb(af[1 - hf][i >> 2], bf_[1 - hf][nn], acc[i >> 2][nn]);
                constexpr int RD[16] = RDPAT;
                const int r = RD[i];
                if (r == 0) af[hf][0] = lld<u32x4>(fp[(sl & 2) + hf] + (sl & 1) * A_STAGE);
                else if (r > 0 && r < 5) bf_[hf][r - 1] = lld<u32x4>(fp[4 + (sl & 2) + hf] + (sl & 1) * A_STAGE + (r - 1) * 2048);
                else if (r >= 5) af[hf][r - 4] = lld<u32x4>(fp[(sl & 2) + hf] + (sl & 1) * A_STAGE + (r - 4) * 2048);
                if (pf && hf && i == 11) sv = *(volatile LAS unsigned*)(lptr)(uintptr_t)fst;
                SCHED();
            }
        LDS_WAIT();
    };
#pragma unroll 1
    for (int kt = 0; kt < 3; ++kt) {
#pragma unroll
        for (int i = 0; i < 4; ++i) { BAR(); step(i, false); }
    }
    PIN(fst);
    BAR(); step(0, false);
    BAR(); __builtin_amdgcn_s_setprio(PRIOE); step(1, true);
#pragma unroll
    for (int i = 0; i < 2; ++i) {
        unsigned ss = __builtin_amdgcn_readfirstlane(sv);
        need_bit(fst, ss, 4u << (4 * i));
        TL(30 + i);
        if (i) need_bit(fst, ss, 0x100u);
        step(2 + i, i == 0);
    }
    __builtin_amdgcn_s_setprio(PRIOT);
    sig(fb + F_FREE, 1u << 20);
    float2 lam[2], coef[2], l16[2], l64[2], l512[2], cw[2], wq[2], lq[2];
#pragma unroll
    for (int n = 0; n < 2; ++n) {
        const f32x4 a = lld<f32x4>(smem + A_PRM + (wc * 32 + 2 * c + n) * 32), b = lld<f32x4>(smem + A_PRM + (wc * 32 + 2 * c + n) * 32 + 16);
        lam[n] = make_float2(a[0], a[1]); l16[n] = make_float2(a[2], a[3]); l64[n] = make_float2(b[0], b[1]); l512[n] = make_float2(b[2], b[3]);
    }
    float2 h[2];
#pragma unroll
    for (int m = 0; m < 5; ++m) {
#pragma unroll
        for (int n = 0; n < 4; ++n) {
            if (m < 4) acc[m][n] = mfb(af[1][m], bf_[1][n], acc[m][n]);
            if (m == 1 && n == 0) { h[0] = make_float2(acc[0][0][0], acc[0][2][0]); h[1] = make_float2(acc[0][1][0], acc[0][3][0]); }
            else if (m) {
#pragma unroll
                for (int i = 0; i < 2; ++i) h[i] = cfma(lam[i], h[i], make_float2(acc[m - 1][i][n], acc[m - 1][2 + i][n]));
            }
            if (SIL) SCHED();
        }
        if (m == 3) {
#pragma unroll
            for (int n = 0; n < 2; ++n) {
                const f32x4 a = lld<f32x4>(smem + A_PW + 4096 + (wc * 32 + 2 * c + n) * 16), b = lld<f32x4>(smem + A_PW + q * 1024 + (wc * 32 + 2 * c + n) * 16);
                coef[n] = make_float2(a[0], a[1]); cw[n] = wr ? make_float2(a[2], a[3]) : coef[n]; wq[n] = make_float2(b[0], b[1]); lq[n] = make_float2(b[2], b[3]);
            }
        }
    }
    TS(1);
    float2 loc[2], wag[2];
#pragma unroll
    for (int n = 0; n < 2; ++n) {
        float2 a0, a1, a2, a3;
        rows4(h[n].x, a0.x, a1.x, a2.x, a3.x);
        rows4(h[n].y, a0.y, a1.y, a2.y, a3.y);
        const float2 s2 = cfma(l16[n], a0, a1), s3 = cfma(l16[n], s2, a2);
        wag[n] = cfma(l16[n], s3, a3);
        loc[n] = sel4(q, make_float2(0.f, 0.f), a0, s2, s3);
    }
    lptr aggp = smem + A_AGG + (wc * 16 + c) * 16;
    const unsigned flg = ldsa(smem + A_FLG) + wc * 4;
    float2 W0[2] = {make_float2(0.f, 0.f), make_float2(0.f, 0.f)};
    if (wr == 0) {
        if (q == 0) lst<f32x4>(aggp, (f32x4){wag[0].x, wag[0].y, wag[1].x, wag[1].y});
        asm volatile("" ::: "memory");
        if (lane == 0) lds_add(flg);
    } else {
#pragma unroll 1
        for (int spin = 0; spin < (1 << 20); ++spin) { if (lds_peek(flg)) break; }
        const f32x4 w0 = lld<f32x4>(aggp);
        W0[0] = make_float2(w0[0], w0[1]); W0[1] = make_float2(w0[2], w0[3]);
        const float2 g0 = cfma(l64[0], W0[0], wag[0]), g1 = cfma(l64[1], W0[1], wag[1]);
        if (lt < 15 && q < 2) {
            const float2 gg = q == 0 ? g0 : g1;
            st128<SC1>((u32x4){__float_as_uint(gg.x), (unsigned)tag, __float_as_uint(gg.y), (unsigned)(tag >> 32)},
                       rs, (2 * c + q) * 16 + OFF_GRAN + ((15 - lt) * 1024 + pt * 64 + wc * 32) * 16);
        }
    }
    TS(2);
    __builtin_amdgcn_s_setprio(PRIOW);
    const int da = da_of(pt), kh = da & 1;
    u32x2 xi[8], dk[2];
    auto xload = [&]() INL {
        const unsigned co = (256 * ((lane >> 3) & 1) + 4 * (lane & 7)) * 2;
        SCHED();
#pragma unroll
        for (int i = 0; i < 8; ++i)
            xi[i] = __builtin_amdgcn_raw_buffer_load_b64(mkrsrc(x, S_ * D_ * 2), (lane >> 4) * 64 * D_ * 2 + co + (i >> 2) * 1024, ((512 * wid + 256 * kh + 16 * (i & 3) + lt) * D_ + 32 * (da >> 1)) * 2, 0);
#pragma unroll
        for (int i = 0; i < 2; ++i) dk[i] = __builtin_amdgcn_raw_buffer_load_b64(mkrsrc(Dskip, D_ * 2), co + i * 1024, 64 * (da >> 1), 0);
        SCHED();
    };
    if (XP == 0) xload();
#pragma unroll
    for (int n = 0; n < 2; ++n) {
        PIN(coef[n].x); PIN(coef[n].y);
        loc[n] = cmul(coef[n], cfma(wq[n], W0[n], loc[n]));
        wq[n] = cmul(wq[n], cw[n]);
        PIN(wq[n].x); PIN(wq[n].y); PIN(loc[n].x); PIN(loc[n].y);
    }
    float2 u[2][16];
#pragma unroll
    for (int t = 0; t < 16; ++t) {
#pragma unroll
        for (int n = 0; n < 2; ++n) { u[n][t] = cmul(coef[n], make_float2(acc[t >> 2][n][t & 3], acc[t >> 2][2 + n][t & 3])); PIN(u[n][t].x); PIN(u[n][t].y); }
        SCHED();
    }
    lptr tb = smem + A_HS + wid * 2048 + lane * 4, rb = smem + A_HS + wid * 2048 + (lane >> 4) * 256 + (lane & 3) * 64 + ((lane >> 2) & 3) * 16;
    unsigned hoff = OFF_H + ((lane >> 4) * 64 + pt * 2 + wc) * 8192 + lt * 512 + wr * 256 + (lane & 15) * 16;
    PIN(tb); PIN(rb); PIN(hoff);
    __builtin_amdgcn_s_setprio(PRIOT);
    await<0>(fb + F_FREE, 8u << 20);
    if (XP == 1) xload();
    if (GDL) __builtin_amdgcn_s_sleep(GDL);
    TS(3);
    {
        u32x4 gr[4][2];
        unsigned off = ((16 - lt + q) * 1024 + pt * 64 + wc * 32 + 2 * c) * 16;
        auto gload = [&]() INL {
            PIN(off);
#pragma unroll
            for (int j = 0; j < 4; ++j) {
                const rsrc_t rg = mkrsrc(ws + OFF_GRAN + j * 65536, (4 - j) * 65536);
                gr[j][0] = __builtin_amdgcn_raw_buffer_load_b128(rg, off, 0, SC1);
                gr[j][1] = __builtin_amdgcn_raw_buffer_load_b128(rg, off + 16, 0, SC1);
            }
        };
        auto gok = [&]() INL {
            bool ok = true;
#pragma unroll
            for (int j = 0; j < 4; ++j) {
                const bool v = lt - 1 - (4 * j + q) >= 0;
#pragma unroll
                for (int n = 0; n < 2; ++n) ok = ok & (!v | ((gr[j][n].y == (unsigned)tag) & (gr[j][n].w == (unsigned)(tag >> 32))));
            }
            return (bool)__all(ok);
        };
        gload();
        if (XP == 4) xload();
        if (!gok()) {
            int spin;
#pragma unroll 1
            for (spin = 1; spin < (1 << 20); ++spin) {
                asm volatile("" ::: "memory");
                gload();
                if (gok()) break;
            }
        }
        TS(25);
#pragma unroll
        for (int n = 0; n < 2; ++n) {
            float2 cs = make_float2(__uint_as_float(gr[3][n].x), __uint_as_float(gr[3][n].z));
#pragma unroll
            for (int j = 2; j >= 0; --j) cs = cfma(l512[n], cs, make_float2(__uint_as_float(gr[j][n].x), __uint_as_float(gr[j][n].z)));
            cs = cmul(cs, lq[n]);
            cs.x = rowsum4(cs.x); cs.y = rowsum4(cs.y);
            loc[n] = cfma(wq[n], cs, loc[n]);
        }
    }
    TS(4);
#pragma unroll
    for (int r = 0; r < 4; ++r) {
#pragma unroll
        for (int t = 0; t < 4; ++t) {
            loc[0] = cfma(lam[0], loc[0], u[0][4 * r + t]); loc[1] = cfma(lam[1], loc[1], u[1][4 * r + t]);
            lst<unsigned>(tb + t * 256, pkb(loc[0].x, loc[1].x));
            lst<unsigned>(tb + 1024 + t * 256, pkb(-loc[0].y, -loc[1].y));
        }
        asm volatile("" ::: "memory");
#pragma unroll
        for (int pl = 0; pl < 2; ++pl) st128<SC1>(lld<u32x4>(rb + pl * 1024), rs, hoff + (r * 256 + pl * 32) * 8192);
        asm volatile("" ::: "memory");
    }
    TS(5);
    PIN(lane);
    auto cp2 = [&]() INL { if (wid == 3) cpf = __builtin_amdgcn_raw_buffer_load_b32(mkrsrc((const void*)pc, max(len, 0)), lane * 128, ((gb >> 3) % CPM) * 8192, 0); };
    if (XLP == 0) cp2();
    if (XP == 2) xload();
    wait_vm<XP == 2 ? 10 : 0>();
    TS(6);
    const bool pub = sig_rtn(fb + F_HC) == 3u;
    const u32x2 tg = {(unsigned)tag, (unsigned)(tag >> 32)};
    if (pub) {
        if (lane < HREP) __builtin_amdgcn_raw_buffer_store_b64(tg, rs, lane * 256 * FST, OFF_FH + (pt * 16 + lt) * FST, SC1);
        TS(15);
    }
    if (XLP == 1) cp2();
    if (XP == 3) xload();
    __builtin_amdgcn_s_setprio(PRIOI);
    f32x4 ac[8][2];
#pragma unroll
    for (int i = 0; i < 8; ++i) {
        PIN(xi[i]);
        const u32x2 d = dk[i >> 2], v = xi[i];
        const unsigned p0 = pkb(lo2f(v.x) * (0.5f * lo2f(d.x)), hi2f(v.x) * (0.5f * hi2f(d.x))), p1 = pkb(lo2f(v.y) * (0.5f * lo2f(d.y)), hi2f(v.y) * (0.5f * hi2f(d.y)));
        ac[i & 4][1][i & 3] = lo2f(p0); ac[(i & 4) + 1][1][i & 3] = hi2f(p0); ac[(i & 4) + 2][1][i & 3] = lo2f(p1); ac[(i & 4) + 3][1][i & 3] = hi2f(p1);
        ac[i][0] = Z4;
    }
#pragma unroll
    for (int i = 0; i < 16; ++i) PIN(ac[i >> 1][i & 1]);
    unsigned vo[2], so[32];
    vo[0] = ((lane & 15) >> 2) * 256 + (lane >> 4) * 64 + (lane & 3) * 16 + (kh ^ 1) * 1024; vo[1] = vo[0] ^ 1024;
    PIN(vo[0]); PIN(vo[1]);
#pragma unroll
    for (int k = 0; k < 32; ++k) {
        so[k] = OFF_H + lt * 524288 + kh * 131072 + wid * 2048 + (((XE & 2) ? 0 : k >> 2) ^ CX(gb)) * 16384 + ((k & 1) + (k & 2) * 16) * 8192;
        if (SPC || k < 8) asm volatile("" : "+s"(so[k]));
    }
    auto ldB = [&](int b) { return __builtin_amdgcn_raw_buffer_load_b128(rs, vo[b_n(b)], so[st_k(b_st(b))], SC1); };
    lptr ab[4];
#pragma unroll
    for (int i = 0; i < 4; ++i) { ab[i] = smem + (1 - (i >> 1)) * 2 * A_STAGE + (lane & 15) * 128 + (((4 * (i & 1) + (lane >> 4)) ^ (lane & 7)) << 4); PIN(ab[i]); }
    auto ldA = [&](int f) {
        const int k = st_k(f >> 3);
        return lld<u32x4>(ab[(k & 1) + ((k >> 2) & 2)] + ((k >> 1) & 3) * 16384 + (f & 7) * 2048);
    };
    unsigned fv = fb + F_FULL; PIN(fv);
    asm volatile("" :: "v"(cpf));
    __builtin_amdgcn_s_setprio(PRIOG);
    TS(27);
    u32x4 ba = {xi[0].x, xi[1].y, xi[2].x, xi[3].y}, bb = {xi[4].x, xi[5].y, xi[6].x, xi[7].y};
    if (pub) {
        int spin; unsigned m = 0;
        if (BAL >= 0) burn<0>(PDL, ba, bb); else
        if (PDL) __builtin_amdgcn_s_sleep(PDL);
        if (PDE && !(PDM >> (gb & 7) & 1)) __builtin_amdgcn_s_sleep(PDE);
        if (PD0 && lt == 0) __builtin_amdgcn_s_sleep(PD0);
        TS(19);
#pragma unroll 1
        for (spin = 0; spin < (1 << 20); ++spin) {
            m = h_poll<NP1>(rs, gb, kh, lane, tg);
            if (spin == 0) TS(16);
            if (spin == 1) TS(17);
            if (spin == 2) TS(18);
            if ((m & HFIRST) == HFIRST) break;
        }
        sig_or(fb + F_FULL, m);
    }
    unsigned fe = 0, fc = 0, fs = BAL >= 0 ? await_burn<BAL < 0 ? 0 : BAL>(fb + F_FULL, 8u, ba, bb) : await<1>(fb + F_FULL, 8u);
    TS(7);
    auto need = [&](unsigned bit) INL { need_bit(fv, fs, bit); };
    u32x4 A[AD], B[64];
#pragma unroll
    for (int b = 0; b < NB; ++b) { if (b && b_new(b)) need(8u << (b_st(b) & 28)); B[b] = ldB(b); SCHED(); }
    SCHED();
    need(4u);
#pragma unroll
    for (int f = 0; f < AD; ++f) A[f] = ldA(f);
    SCHED();
    if (BSL) { for (int i = 0; i < BSL; ++i) __builtin_amdgcn_s_sleep(16); }
    if (BA2) burn<BS2>(BA2, A[0], A[1]);
    const unsigned po = OFF_U + ((lt * 16 + da) * 4 + wid) * 4096;
    unsigned pk[16], oo[4], fr, pa;
    lptr og, rp[2];
    u32x4 pv[4]; u32x2 ga[8]; f32x4 o2;
    auto recv = [&](int v) INL { if (v < 4) pv[v] = __builtin_amdgcn_raw_buffer_load_b128(rs, pa + v * 1024, po ^ 16384, SC1); };
    sfor<0, NST>([&](auto ic) INL {
        constexpr int st = decltype(ic)::v, b0 = st < KS1 ? 2 * st : st + KS1, n0 = st >= 32;
        auto nextB = [&](int b) INL {
            if (b_nx(b) < 0 || (LBS && b_nx(b) >= 60)) return;
            if (b_new(b_nx(b))) { fs = __builtin_amdgcn_readfirstlane(fe); need(8u << (b_st(b_nx(b)) & 28)); TL(20 + (b_st(b_nx(b)) >> 2)); }
            B[b_nx(b)] = ldB(b_nx(b));
        };
#pragma unroll
        for (int k = 0; k < 8; ++k) {
            const int f = 8 * st + k;
            constexpr int WG = st < KS1 ? WG1 : WG2;
            if (WG > 1 && k % WG == 0 && f + AD < 8 * NST) { __builtin_amdgcn_s_waitcnt(0xC07F | (AD - WG) << 8); SCHED(); }
            MF2(B[b0], A[f % AD], ac[k][n0]);
            SCHED();
            if (k == (st < KS1 ? 3 : 0) && b_nx(b0) >= 0 && (b_new(b_nx(b0)) || (st < KS1 && b_new(b_nx(b0 + 1))))) fe = *(volatile LAS unsigned*)(lptr)(uintptr_t)fv;
            if (k == 7) nextB(b0);
            if (k == 1 && st > 0 && st <= KS1) nextB(b0 - 1);
            if (st == 31 && k == 2) fe = *(volatile LAS unsigned*)(lptr)(uintptr_t)(fv + F_TR - F_FULL);
            if (st == 31 && k == 7) { fs = __builtin_amdgcn_readfirstlane(fe); need_bit(fv + F_TR - F_FULL, fs, 0x8000u); }
            if (st == 32) {
#pragma unroll
                for (int i = 0; i < 2; ++i) {
                    pk[2 * k + i] = pkb(ac[(k & 4) + 2 * i][0][k & 3], ac[(k & 4) + 2 * i + 1][0][k & 3]);
                    if (!i && !(k & 1)) pk[2 * k] = __builtin_amdgcn_perm(pk[2 * k], __float_as_uint(ac[k & 4][0][k & 3]), 0x07060302u) | 1u;
                    PIN(pk[2 * k + i]);
                }
                if (LBS && k >= 4) B[60 + (k & 3)] = ldB(60 + (k & 3));
            }
            if (st == 32 + LBS) {
#pragma unroll
                for (int v = 0; v < 4; ++v)
                    if ((LBS ? v : 2 * v + 1) == k) st128<SC1>((u32x4){pk[4 * v], pk[4 * v + 1], pk[4 * v + 2], pk[4 * v + 3]}, rs, po + v * 1024 + lane * 16);
                if (k == 7) TS(29);
            }
            if (st == RST) recv(k);
            if (k == 4 && st >= PZA && st < PZB) { constexpr int z = st < KS1 ? PZ1 : st < 32 ? PZ2 : PZ3; if (z) asm volatile(".rept %0\n\ts_nop 15\n\t.endr\n\ts_nop %1" :: "i"((z - 1) / 16), "i"((z - 1) % 16)); }
            if (st == NST - 2 && k == 0) fe = *(volatile LAS unsigned*)(lptr)(uintptr_t)fr;
            if (f + AD == 8 * NST) {
                fs = __builtin_amdgcn_readfirstlane(fe); need_bit(fr, fs, 1u);
                o2 = lld<f32x4>(og);
#pragma unroll
                for (int i = 0; i < 8; ++i) ga[i] = lld<u32x2>(rp[(i >> 1) & 1] + (i >> 2) * 2048 + (i & 1) * 128);
            }
            SCHED();
            if (st < KS1) { MF2(B[b0 + 1], A[f % AD], ac[k][1]); SCHED(); }
            if (f + AD < 8 * NST) {
                const int g = f + AD;
                if (!(g & 31) && g < 256) { if (g > 64) fs = __builtin_amdgcn_readfirstlane(fc); need(4u << (g >> 5) * 4); }
                if (!((g + AD) & 31) && g + AD < 256 && g + AD > 64) fc = *(volatile LAS unsigned*)(lptr)(uintptr_t)fv;
                A[f % AD] = ldA(g);
                if ((g & 31) == 31 && st_k(g >> 3) < 16 && ((g >> 3) < KS1 || g >= 256)) sig_o<F_FREE - F_FULL>(fv, 1u << (st_k(g >> 3) & 28));
            }
            SCHED();
        }
        if (st == 34) {
            PIN(lane);
            const int c2 = lane & 15, q2 = lane >> 4;
#pragma unroll
            for (int e = 0; e < 4; ++e) { oo[e] = ((64 * q2 + 512 * wid + 256 * kh + 16 * e + lt) * D_ + 256 * (c2 >> 3) + 32 * (da >> 1) + 4 * (c2 & 7)) * 4; PIN(oo[e]); }
            og = smem + A_OG + (16 * wid + 4 * q2) * 4; fr = fb + F_R0 + wid * 4; pa = lane * 16;
#pragma unroll
            for (int i = 0; i < 2; ++i) { rp[i] = smem + A_XT + wid * 4096 + (4 * q2 + 2 * i) * 128 + (((c2 >> 2) ^ ((2 * q2 + i) & 3)) << 5) + (c2 & 3) * 8; PIN(rp[i]); }
            PIN(og); PIN(fr); PIN(pa);
            SCHED();
        }
        if (st == 0) { TS(8); TC(60); }
        if (st == KS1 - 1) { TS(9); TC(55); }
        if (st == 31) { TS(28); TC(56); }
        if (st == NST - 3) { TS(14); TC(61); }
    });
    asm volatile("s_nop 7\n\ts_nop 3");
    SCHED();
    TS(10);
    if (RST >= NST) {
#pragma unroll
        for (int v = 0; v < 4; ++v) recv(v);
        SCHED();
    }
    f32x4 r0[8];
#pragma unroll
    for (int i = 0; i < 8; ++i) {
        const int e = i & 3, m = i & 4;
        r0[i] = (f32x4){fmaf(o2[e], ac[m][1][e], lo2f(ga[i].x)), fmaf(o2[e], ac[m + 1][1][e], hi2f(ga[i].x)), fmaf(o2[e], ac[m + 2][1][e], lo2f(ga[i].y)), fmaf(o2[e], ac[m + 3][1][e], hi2f(ga[i].y))};
        PIN(r0[i]);
    }
    TS(26);
    int spin;
#pragma unroll 1
    for (spin = 0; spin < (1 << 20); ++spin) {
        if (__all(pv[0].x & pv[1].x & pv[2].x & pv[3].x & 1u)) break;
        PIN(pa);
#pragma unroll
        for (int v = 0; v < 4; ++v) recv(v);
        asm volatile("" ::: "memory");
    }
    TS(11);
    const rsrc_t ro = mkrsrc(out, S_ * D_ * 4);
#pragma unroll
    for (int i = 0; i < 8; ++i) {
        const unsigned a = pv[i >> 1][2 * (i & 1)], b = pv[i >> 1][2 * (i & 1) + 1];
        const float s = o2[i & 3];
        r0[i] = (f32x4){fmaf(s, lo2f(a), r0[i][0]), fmaf(s, hi2f(a), r0[i][1]), fmaf(s, lo2f(b), r0[i][2]), fmaf(s, hi2f(b), r0[i][3])};
        st128<WTO>(__builtin_bit_cast(u32x4, r0[i]), ro, oo[i & 3] + 512 * (i & 4));
        SCHED();
    }
    asm volatile("gm_end0:");
    TS(12);
    TSE(0, wid == 0);
    TSE(2, pub);
}

#define TSARG
#define TSPASS
struct Gate {
    unsigned xa, wa, xv, gw; float sum; int rot;
    DEV void rd(int kt) { asm volatile("ds_read_b32 %0, %2\n\tds_read_b32 %1, %3" : "=&v"(xv), "=&v"(gw) : "v"(xa + (kt & 3) * A_STAGE), "v"(wa + (kt ^ rot) * 128) : "memory"); }
    DEV void acc() {
        asm volatile("s_waitcnt lgkmcnt(0)" : "+v"(xv), "+v"(gw) :: "memory");
        const unsigned pr = pkb(lo2f(xv) * lo2f(gw), hi2f(xv) * hi2f(gw));
        sum += lo2f(pr); sum += hi2f(pr); PIN(sum);
    }
};
struct Tab { CM sf, si, ff, fi; u32x4 mt[2], xf, xb; f32x2 ti[2]; };
DEV void mid_tables(lptr smem, int gb, int w, int lane, Tab& t) {
    int mp, j1; slot_of(gb, mp, j1);
    const int c = lane & 15, q = lane >> 4;
    lptr mb = smem + A_MAT + lane * 16;
#pragma unroll
    for (int i = 0; i < 2; ++i) { t.ti[i] = lld<f32x2>(smem + A_MAT + 9216 + (2 * w + i) * 128 + c * 8); t.mt[i] = lld<u32x4>(mb + (6 + i) * 1024); }
    u32x4 sf0 = lld<u32x4>(mb + 4096), si0 = lld<u32x4>(mb + 5120);
    const u32x4 f0 = lld<u32x4>(mb + 8192);
    SCHED();
    asm volatile("" : "=v"(t.xf), "=v"(t.xb));
    if (w >= 4 && mp == 0 && !(j1 & 1)) {
        sf0 = mat_sf(64, c, q); si0 = mat_si(64, c, q);
        const float dt = -(float)(32 + 64 * c) * (1.0f / 1024.0f);
        t.xf = cmat1(dt * (float)(4 * q), dt); t.xb = cmat1((float)(c * (32 + 256 * q)) * (1.0f / 1024.0f), (float)c * (1.0f / 16.0f));
    }
    PIN(t.xf); PIN(t.xb);
    t.sf = cexp(sf0); t.si = cexp(si0); t.ff = cexp(f0); t.fi = cexp((u32x4){f0.x ^ SG, f0.y ^ SG, f0.z, f0.w});
    PIN(t.mt[0]); PIN(t.mt[1]); PIN(t.ti[0]); PIN(t.ti[1]);
}
DEV int u_exp(unsigned em) { return min(max((int)(em & 0xffu), 4), 254); }

DEV void role_load(const u16* __restrict__ x, const u16* __restrict__ Bre, const u16* __restrict__ Bim, const u16* __restrict__ Cre, const u16* __restrict__ Cim,
                   const u16* __restrict__ gate_w, char* ws, lptr smem, int gb, int lw, int lane, Tab& tab TSARG) {
    __builtin_amdgcn_s_waitcnt(0x0F70);
    Gate g;
    int lt, pt; tile_of(gb, lt, pt);
    const int srow = lane >> 3, schunk = (lane & 7) ^ srow;
    const int rot = ROT;
    const u32x4 rx = mkdesc(x, S_ * D_ * 2), rb = mkdesc((lw & 1) ? Bim : Bre, D_ * D_ * 2);
    const unsigned lb = ldsa(smem);
    const unsigned vA = (16 * (srow >> 2) + (srow & 3)) * 2048 + schunk * 16, sA = (lt * 128 + 64 * (lw >> 1) + 8 * (lw & 1)) * 2048;
    const unsigned vB = 2 * srow * 2048 + schunk * 16, sB = (pt * 64 + 32 * (lw >> 1)) * 2048;
    auto stA = [&](int kk) {
#pragma unroll
        for (int i = 0; i < 4; ++i)
            dma16(rx, lb + (kk & 3) * A_STAGE + lw * 4096 + i * 1024, vA, sA + (32 * (i & 1) + 4 * (i >> 1)) * 2048 + (kk ^ rot) * 128);
    };
    auto stB = [&](int kk) {
#pragma unroll
        for (int i = 0; i < 4; ++i)
            dma16(rb, lb + (kk & 3) * A_STAGE + 16384 + lw * 4096 + i * 1024, vB, sB + (16 * (i & 1) + (i >> 1)) * 2048 + (kk ^ rot) * 128);
    };
    TS(0);
    __builtin_amdgcn_s_setprio(PRIOL);
    stA(0); stB(0);
    dma16(mkdesc(gate_w, D_ * 2), lb + A_GW + (lw & 1) * 1024, lane * 16, (lw & 1) * 1024);
#pragma unroll
    for (int i = 0; i < 4; ++i)
        dma16(rx, lb + A_XT + lw * 4096 + i * 1024, (lane >> 1) * 32768 + (lane & 1) * 16, (i * 512 + lt) * 2048 + lw * 512 + pt * 32);
    const int gi = 8 * pt + 2 * lw + (lane >> 5);
    g.xa = lb + gi * 128 + ((((lane & 31) >> 2) ^ (gi & 7)) << 4) + (lane & 3) * 4;
    g.wa = lb + A_GW + (lane & 31) * 4; g.sum = 0.f; g.rot = rot;
    stA(1); stB(1); stA(2); stB(2);
    TS(6);
    const int c = lane & 15, q = lane >> 4;
    const unsigned fb = lb + A_FLG;
    lptr ub = smem + A_XT + lw * 4096 + (lane >> 2) * 64 + (lane & 3) * 8, ea = smem + A_E1 + c * E1P + q * 256 + lw * 64;
    lptr mb = smem + A_MAT + lane * 16, tb = smem + A_MAT + 9216 + c * 128 + q * 32;
    PIN(ub); PIN(ea); PIN(mb); PIN(tb);
#pragma unroll 1
    for (int kt = 0; kt < 13; ++kt) { wait_vm<16>(); TL(8 + kt); BAR(); TL(28 + kt); if (kt == 0) { TS(28); TC(60); } g.rd(kt); stA(kt + 3); stB(kt + 3); g.acc(); }
    u32x4 mt[4], ut[4];
    f32x4 tf[2];
    wait_vm<16>(); TL(21); BAR(); TL(41); g.rd(13);
#pragma unroll
    for (int i = 0; i < 4; ++i) mt[i] = lld<u32x4>(mb + i * 1024);
#pragma unroll
    for (int i = 0; i < 2; ++i) tf[i] = lld<f32x4>(tb + i * 16);
    g.acc();
#pragma unroll
    for (int i = 0; i < 4; ++i) PIN(mt[i]);
    PIN(tf[0]); PIN(tf[1]);
    wait_vm<8>(); sig(fb + F_ST, 1u); TL(42);
    wait_vm<0>(); sig(fb + F_ST, 0x10u);
    __builtin_amdgcn_s_setprio(PRIOD);
    await<0>(fb + F_ST, 0x40u);
    TS(43); TC(61);
    g.rd(14); g.acc(); g.rd(15); g.acc();
    sig(fb + F_FREE, 1u << 20);
    TS(2);
    __builtin_amdgcn_s_setprio(PRIOM);
    mid_tables(smem, gb, lw + 4, lane, tab);
    float gg = bfr(1.0f / (1.0f + expf(-bfr(half_sum(g.sum))))), og = bfr(1.0f - gg);
    const int tau = (gi & 64) | ((gi & 12) << 2) | ((gi & 48) >> 2) | (gi & 3);
    unsigned go = (lane & 31) ? ~0u : OFF_G + (tau & 15) * 1024 + (lt * 8 + (tau >> 4)) * 8;
    PIN(gg); PIN(og); PIN(go);
    __builtin_amdgcn_s_setprio(PRIOD);
    TS(53);
    const unsigned em = await<1>(fb + F_EM, 0x10000u << lw);
    TS(3);
#pragma unroll
    for (int i = 0; i < 4; ++i) ut[i] = trfrag(ub + i * 1024, 32);
    const float su = __uint_as_float((unsigned)(258 - u_exp(em)) << 23);
    tf[0] *= su; tf[1] *= su; PIN(tf[0]); PIN(tf[1]);
#pragma unroll
    for (int i = 0; i < 4; ++i) {
        f32x4 r = mfb(ut[i], mt[2], Z4), m = mfb(ut[i], mt[3], Z4);
        r = mfb(ut[i], mt[0], r); m = mfb(ut[i], mt[1], m);
        f32x4 yr, yi;
#pragma unroll
        for (int e = 0; e < 4; ++e) {
            const float tc = tf[e >> 1][2 * (e & 1)], ts = tf[e >> 1][2 * (e & 1) + 1];
            yr[e] = fmaf(r[e], tc, -m[e] * ts); yi[e] = fmaf(r[e], ts, m[e] * tc);
        }
        lst<u32x4>(ea + i * 16, pk8(yr, yi));
    }
    if (gb == XG) {
        const unsigned sg = 0xbf803f80u;
#pragma unroll
        for (int i = 0; i < 4; ++i) {
            const f32x4 e = mfb(ut[i], c == 0 ? (u32x4){sg, sg, 0, 0} : (u32x4){0, 0, sg, sg}, Z4);
            if (c < 2) lst<u32x4>(ea + 16 * E1P + i * 16, pk8(e * su, Z4));
        }
    }
    sig(fb + F_MID, 1u);
    TS(54);
    __builtin_amdgcn_s_setprio(PRIOM);
    await<1>(fb + F_FREE, 8u << 20);
    c_chunk(Cre, Cim, lb, gb, da_of(pt), lw, lane, 0);
    if (C1E) c_chunk(Cre, Cim, lb, gb, da_of(pt), lw, lane, 1);
    __builtin_amdgcn_raw_buffer_store_b64((u32x2){__float_as_uint(gg), __float_as_uint(og)}, mkrsrc(ws, WS_END), go, 0, SC1);
    TS(52);
}

DEV unsigned ldw(rsrc_t r, unsigned vo, unsigned so) { return __builtin_amdgcn_raw_buffer_load_b32(r, vo, so, 0); }
DEV unsigned amax4(u32x4 p) { return pkmax(pkmax(p.x & 0x7fff7fffu, p.y & 0x7fff7fffu), pkmax(p.z & 0x7fff7fffu, p.w & 0x7fff7fffu)); }

DEV void role_aux(const u16* __restrict__ w_r, const u16* __restrict__ w_i, const u16* __restrict__ lam_re, const u16* __restrict__ lam_im,
                  const u16* __restrict__ log_dt, char* ws, lptr smem, int gb, int aw, int lane, Tab& tab TSARG) {
    int lt, pt; tile_of(gb, lt, pt);
    int mp, j1; slot_of(gb, mp, j1);
    const int c = lane & 15, q = lane >> 4;
    const bool spc = mp == 0 && !(j1 & 1);
    const rsrc_t rs = mkrsrc(ws, WS_END), rr = mkrsrc(w_r, S_ * KF * 2), ri = mkrsrc(w_i, S_ * KF * 2);
    TS(0);
    __builtin_amdgcn_s_setprio(PRIOA);
    u32x4 fch, fsh;
    const bool m0 = 16 * aw + c == 0;
    {
        const float th = (float)(16 * aw + c) * (1.0f / 128.0f);
#pragma unroll
        for (int i = 0; i < 4; ++i) {
            const float t0 = __builtin_amdgcn_fractf(th * (float)(16 * (i >> 1) + 2 * (i & 1) + 4 * q)), t1 = t0 + th;
            const float c0 = __builtin_amdgcn_cosf(t0), c1 = __builtin_amdgcn_cosf(t1);
            const float s0 = m0 ? 1.f : -__builtin_amdgcn_sinf(t0), s1 = m0 ? -1.f : -__builtin_amdgcn_sinf(t1);
            fch[i] = pkb(c0, c1); fsh[i] = pkb(s0, s1);
        }
        PIN(fch); PIN(fsh);
    }
    unsigned sg64 = (c & 1) ? SG : 0u, sgA = (c & 2) ? SG : 0u, sgB = ((c + 1) & 2) ? SG : 0u;
    constexpr unsigned WR = 128 * KF * 2;
    const bool xh = gb == XG && aw >= 2;
    const int c0 = 2 * (lane & 7) + xh, jb = (lane >> 3) + 4 * (aw & 2);
    const int k1 = mp ? mp : spc ? (lane & 4) * 16 : (j1 == 1 ? 0 : 64);
    const int kd = j1 + 4 * (spc ? c0 & 7 : c0) + 64 * jb;
    unsigned wo = (aw < 2 ? (k1 * KF + kd) * 2 + aw * 8 * WR : ((128 - k1) * KF + 1020 - kd) * 2 + (3 - aw) * 8 * WR) & ~3u;
    unsigned wo0 = aw < 2 ? wo : k1 || aw == 3 ? wo + 7 * WR : wo - 9 * WR;
    unsigned uoff = (lane >> 3) * 131072 + (lane & 7) * 16 + OFF_U + aw * 64 * 16384 + lt * 2048 + pt * 128;
    PIN(sg64); PIN(sgA); PIN(sgB); PIN(wo); PIN(wo0); PIN(uoff);
    unsigned lru, liu, ldu, um = 0;
    u32x2 tag;
    u32x4 fl[2];
    asm volatile("" : "=v"(fl[0]), "=v"(fl[1]));
    u32x3 wre[8], wim[8];
    u32x4 wsc = {0, 0, 0, 0};
    f32x4 vr[4], vi[4];
    float dt = 0.f, er = 0.f; float2 lam = make_float2(0.f, 0.f);
#pragma unroll
    for (int d1 = 0; d1 < 4; ++d1) { vr[d1] = Z4; vi[d1] = Z4; }
    auto wx = [&]() INL {
        if (j1 == 0 && aw == 3) {
            const int ks = (spc ? (lane >> 5) * 64 : mp) + 128 * c, ng = (S_ - ks) & (S_ - 1);
            unsigned o0 = (q & 1) ? (ng * KF + 511) * 2u : ks * KF * 2u, o1 = (q & 1) ? (ks * KF + 511) * 2u : ng * KF * 2u;
            PIN(o0); PIN(o1);
            wsc = (u32x4){ldw(rr, o0, 0), ldw(rr, o1, 0), ldw(ri, o0, 0), ldw(ri, o1, 0)};
        }
    };
    auto prm = [&]() INL {
        lru = ldw(mkrsrc(lam_re, 2048), (lane >> 1) * 4, pt * 128); liu = ldw(mkrsrc(lam_im, 2048), (lane >> 1) * 4, pt * 128);
        ldu = ldw(mkrsrc(log_dt, 2048), (lane >> 1) * 4, pt * 128);
    };
    if (PRS < 0) prm();
    const unsigned fb = ldsa(smem + A_FLG);
    bool uok = false;
    auto upub = [&]() INL { sig(fb + F_EM, (wave_pkmax(pkmax(pkmax(fl[0].x, fl[0].z), pkmax(fl[1].x, fl[1].z))) & 0xffu) | 0x8000u); };
    u32x4 uo[4];
    f32x4 pq[4];
    asm volatile("" : "=v"(pq[0]), "=v"(pq[1]), "=v"(pq[2]), "=v"(pq[3]));
    sfor<0, 14>([&](auto ic) INL {
        constexpr int kt = decltype(ic)::v;
        constexpr int GS[4] = GSTEP, KSO[4] = {0, 2, 3, 1}, JO[4] = {0, 2, 1, 3}, WRW[5] = WROW;
        auto ust = [&](int k) INL {
#pragma unroll
            for (int u = 0; u < 4; ++u) if (US + u / UPS == k && !(XM & 2)) st128<SC1>(uo[u], rs, uoff + JO[u] * 32768);
        };
        auto mem = [&]() INL {
            if (kt >= 10) {
#pragma unroll
                for (int r = 0; r < 8; ++r) {
                    if (r < WRW[kt < 10 ? 0 : kt - 10] || r >= WRW[kt < 10 ? 0 : kt - 9]) continue;
                    const unsigned so = r ? (aw < 2 ? r : 7 - r) * WR : 0u;
                    wre[r] = __builtin_amdgcn_raw_buffer_load_b96(rr, r ? wo : wo0, so, 0); wim[r] = __builtin_amdgcn_raw_buffer_load_b96(ri, r ? wo : wo0, so, 0);
                    if (r + 1 == URS) { SCHED(); wait_vm<2 * (URS - WRW[UCK - 10])>(); sig(fb + F_EM, 0x10000u << aw); TS(6); SCHED(); }
                }
            }
            if (kt == PRS) prm();
            if (kt == WXS) wx();
            if (kt == UPL && aw == 2) flag_get(rs, OFF_FU, gb, lane, fl);
        };
        if (kt == UFS) wait_vm<0>();
        if (kt == UFS + 1) { uoff = (lane >> 2) * 128 + (lane & 3) * 16; PIN(uoff); }
        if (kt == UCK && UPRE && aw == 2) { uok = flag_ok(fl, tag); if (uok) { upub(); LDS_WAIT(); } }
        TL(8 + kt);
        BAR();
        if (VL == 2) ust(kt - 1);
        if (kt == 13 && aw == 1) {
            lst<f32x4>(smem + A_PW + lane * 16, (f32x4){1.f, 0.f, 1.f, 0.f});
#pragma unroll
            for (int i = 0; i < 4; ++i) lst<f32x4>(smem + A_PW + (i + 1) * 1024 + lane * 16, pq[i]);
            sig(fb + F_ST, 0x100u);
        }
        if (kt == UCK) {
            if (aw == 2 && !uok) {
                int spin;
#pragma unroll 1
                for (spin = 0; spin < (1 << 20); ++spin) { if (flag_ok(fl, tag)) break; flag_get(rs, OFF_FU, gb, lane, fl); }
                upub();
            }
            await<0>(fb + F_EM, 0x8000u);
            const u32x4 rw = mkdesc(ws, WS_END);
#pragma unroll
            for (int i = 0; i < 4; ++i)
                dma16s(rw, ldsa(smem + A_XT) + aw * 4096 + i * 1024, uoff, OFF_U + ((mp >> 1) * 4 + j1) * 32768 + (mp & 1) * 64 + aw * 8192 + i * 2048);
            TS(2);
        }
        if (kt == ATS) mid_tables(smem, gb, aw, lane, tab);
        u32x4 xa[4][4];
#pragma unroll
        for (int g = 0; g < 4; ++g) {
            if (GS[g] != kt) continue;
#pragma unroll
            for (int i = 0; i < 4; ++i) xa[g][i] = trfrag(smem + A_XT + i * 4096 + KSO[g] * 1024 + lane * 8, 512);
        }
        if (VL != 1) mem();
        SCHED();
#pragma unroll
        for (int g = 0; g < 4; ++g) {
            if (GS[g] != kt) continue;
#pragma unroll
            for (int i = 0; i < 4; ++i) { vr[i] = mfb(xa[g][i], fch, vr[i]); vi[i] = mfb(xa[g][i], fsh, vi[i]); PIN(vr[i]); PIN(vi[i]); }
            if (g == 0 || g == 2) { fch ^= sg64; fsh ^= sg64; PIN(fch); PIN(fsh); }
            if (g == 1) {
                const bool od = c & 1;
                const u32x4 a = (od ? fsh : fch) ^ sgA, b = (od ? fch : fsh) ^ sgB;
                fch = a; fsh = b; PIN(fch); PIN(fsh);
            }
        }
        if (kt == B1S) {
            const f32x4 t0r = vr[0] + vr[2], t1r = vr[0] - vr[2], t2r = vr[1] + vr[3], t3r = vr[1] - vr[3];
            const f32x4 t0i = vi[0] + vi[2], t1i = vi[0] - vi[2], t2i = vi[1] + vi[3], t3i = vi[1] - vi[3];
            vr[0] = t0r; vr[1] = t1r; vr[2] = t2r; vr[3] = t3r; vi[0] = t0i; vi[1] = t1i; vi[2] = t2i; vi[3] = t3i;
#pragma unroll
            for (int i = 0; i < 4; ++i) { PIN(vr[i]); PIN(vi[i]); }
        }
#pragma unroll
        for (int u = 0; u < 4; ++u) {
            if (US + u / UPS != kt) continue;
            const int j = JO[u];
            f32x4 a, b;
            if (j == 0) { a = vr[0] + vr[2]; b = vi[0] + vi[2]; }
            if (j == 2) { a = vr[0] - vr[2]; b = vi[0] - vi[2]; }
            if (j == 1) { a = vr[1] + vi[3]; b = vi[1] - vr[3]; if (aw == 0) { if (m0) { a = vr[1]; b = -vr[3]; } } }
            if (j == 3) { a = vr[1] - vi[3]; b = vi[1] + vr[3]; if (aw == 0) { if (m0) { a = vi[1]; b = vi[3]; } } }
            uo[u] = quads(smem + A_XT + aw * 4096 + j * 1024, lane, (u32x2){pkb(a[0], a[1]), pkb(a[2], a[3])}, (u32x2){pkb(b[0], b[1]), pkb(b[2], b[3])});
            uo[u].x = (uo[u].x + ((uo[u].x >> 1) & 1u)) & ~1u;
            um = pkmax(um, amax4(uo[u]));
            PIN(um); PIN(uo[u]);
#pragma unroll
            for (int i = 0; i < 4; ++i) if (u < 3 && ((i & 1) || u == 0)) { PIN(vr[i]); PIN(vi[i]); }
        }
        if (kt == UFS - 1) {
            um = wave_pkmax(pkmax(um, um >> 16));
            if (lane == 0) lst<unsigned>(smem + A_FLG + F_MAX + aw * 4, um);
            tag = lld<u32x2>(smem + A_FLG + F_TAG); PIN(tag);
        }
        if (kt == P0S) {
            PIN(lru); PIN(liu); PIN(ldu);
            if (lane & 1) { lru >>= 16; liu >>= 16; ldu >>= 16; }
            PIN(lru); PIN(liu); PIN(ldu);
        }
        if (kt >= P0S && kt < P0S + 5 && aw == 1) {
            const float lr = lo2f(lru), li = lo2f(liu);
            if (kt == P0S) { dt = bfr(expf(lo2f(ldu))); er = expf(dt * lr); PIN(dt); PIN(er); }
            if (kt == P0S + 1) { float sn, cs; sincosf(dt * li, &sn, &cs); lam = make_float2(er * cs, er * sn); PIN(lam.x); PIN(lam.y); }
            if (kt == P0S + 2) {
                const float nr = lam.x - 1.0f, ni = lam.y, den = lr * lr + li * li;
                pq[3][0] = (nr * lr + ni * li) / den; pq[3][1] = (ni * lr - nr * li) / den;
                PIN(pq[3]);
            }
            if (kt == P0S + 3) {
                float2 l16 = lam;
#pragma unroll
                for (int i = 0; i < 4; ++i) l16 = cmul(l16, l16);
                const float2 l32 = cmul(l16, l16), l48 = cmul(l32, l16);
                lst<f32x4>(smem + A_PRM + lane * 32, (f32x4){lam.x, lam.y, l16.x, l16.y});
                lam = cmul(l32, l32);
                pq[0][0] = l16.x; pq[0][1] = l16.y; pq[1][0] = l32.x; pq[1][1] = l32.y; pq[2][0] = l48.x; pq[2][1] = l48.y;
                PIN(pq[0]); PIN(pq[1]); PIN(pq[2]); PIN(lam.x); PIN(lam.y);
            }
            if (kt == P0S + 4) {
                const float2 l128 = cmul(lam, lam), l256 = cmul(l128, l128), l384 = cmul(l256, l128), l512 = cmul(l256, l256), cl = cmul(make_float2(pq[3][0], pq[3][1]), lam);
                lst<f32x4>(smem + A_PRM + lane * 32 + 16, (f32x4){lam.x, lam.y, l512.x, l512.y});
                pq[0][2] = l128.x; pq[0][3] = l128.y; pq[1][2] = l256.x; pq[1][3] = l256.y; pq[2][2] = l384.x; pq[2][3] = l384.y; pq[3][2] = cl.x; pq[3][3] = cl.y;
                PIN(pq[0]); PIN(pq[1]); PIN(pq[2]); PIN(pq[3]);
            }
        }
        SCHED();
        if (kt == UFS && aw == 0) {
            const u32x4 m4 = lld<u32x4>(smem + A_FLG + F_MAX);
            const unsigned m = pkmax(pkmax(m4.x, m4.y), pkmax(m4.z, m4.w));
            flag_put(rs, OFF_FU, gb, (u32x2){(tag.x & 0xffff0000u) | ((m & 0xffffu) >> 7), tag.y}, lane);
        }
        if (VL != 2) ust(kt);
        if (VL == 1) mem();
        LDS_WAIT();
        SCHED();
    });
    TS(54);
    if (gb == XG && aw == 0 && lane == 0) __builtin_amdgcn_raw_buffer_store_b64(tag, rs, 0, OFF_E, SC1);
    __builtin_amdgcn_s_setprio(PRIOM);
    if (ATS == 14) mid_tables(smem, gb, aw, lane, tab);
    await<1>(fb + F_FREE, 8u << 20);
    TS(3);
    unsigned mx = 0;
    const bool skip = j1 == 0 && (lane & (spc ? 59 : 63)) == 0 && !xh, x8 = xh && (lane & 3) == 3;
    lptr wd[2];
    wd[0] = smem + A_W + (aw & 1) * 512 + c0 * 1024 + ((jb ^ c0) << 4);
    wd[1] = smem + A_W + (aw & 1) * 512 + (x8 ? 16384 + (lane & 4) * 256 + (jb << 4) : (c0 + 1) * 1024 + ((jb ^ (c0 + 1)) << 4));
    if (skip) wd[0] = smem + A_W + 18432;
    if (aw >= 2) { lptr t = wd[0]; wd[0] = wd[1]; wd[1] = t; }
    const unsigned wsel = (mp + j1) & 1 ? 0x07060302u : 0x05040100u;
    PIN(wd[0]); PIN(wd[1]);
    u32x4 wp[2][2];
#pragma unroll
    for (int i = 0; i < 2; ++i) {
#pragma unroll
        for (int e = 0; e < 4; ++e) {
            wp[0][i][e] = __builtin_amdgcn_perm(wim[4 * i + e].x, wre[4 * i + e].x, wsel);
            wp[1][i][e] = __builtin_amdgcn_perm(wim[4 * i + e].z, wre[4 * i + e].z, wsel);
        }
        lst<u32x4>(wd[0] + i * 256, wp[0][i]); lst<u32x4>(wd[1] + i * 256, wp[1][i]);
        mx = pkmax(mx, pkmax(amax4(wp[0][i]), amax4(wp[1][i])));
    }
    if (j1 == 0) {
        if (aw == 3) {
            PIN(wsc);
            const unsigned sel = (q & 1) ? 0x07060302u : 0x05040100u, p0 = __builtin_amdgcn_perm(wsc.z, wsc.x, sel), p1 = __builtin_amdgcn_perm(wsc.w, wsc.y, sel);
            mx = pkmax(mx, pkmax(p0 & 0x7fff7fffu, p1 & 0x7fff7fffu));
            const int it = spc ? (lane >> 5) * 8 : 0;
            lst<unsigned>(smem + A_W + it * 1024 + (c >> 2) * 256 + ((((q & 1) * 8) ^ it) << 4) + (c & 3) * 4,
                          pkb(0.5f * (lo2f(p0) + lo2f(p1)), 0.5f * (hi2f(p0) - hi2f(p1))));
        }
        if (gb == XG && (xh ? x8 : !(lane & 3) && !skip)) {
            lptr wm = smem + A_W + (xh ? 16384 + (lane & 4) * 256 + ((15 - jb) << 4) : c0 * 1024 + (((16 - jb) ^ c0) << 4));
#pragma unroll
            for (int r = 0; r < 8; ++r) {
                const int km = (16 - ((lane >> 2) & 1) - 8 * (aw & 1) - r) & 15;
                lst<unsigned>(wm + (km >> 2) * 256 + (km & 3) * 4, wp[0][r >> 2][r & 3]);
            }
        }
    }
    mx = wave_pkmax(pkmax(mx, mx >> 16));
    if (lane == 0) lst<unsigned>(smem + A_FLG + F_WM + aw * 4, mx);
    sig(ldsa(smem + A_FLG + F_MID), 1u);
    wait_vm<0>();
    SCHED();
}

DEV int role_mid(char* ws, lptr smem, int gb, int w, int lane, u32x2& tag, const Tab& tab TSARG) {
    int mp, j1; slot_of(gb, mp, j1);
    const int c = lane & 15, q = lane >> 4;
    const bool xtr = gb == XG, ld = w >= 4;
    const rsrc_t rs = mkrsrc(ws, WS_END);
    const unsigned lb = ldsa(smem), fb = lb + A_FLG;
    const CM &sf = tab.sf, &si = tab.si, &ff = tab.ff, &fi = tab.fi;
    const f32x2 (&ti)[2] = tab.ti;
    lptr mb = smem + A_MAT + lane * 16;
    u32x4 wv[2];
    TS(4);
    await<MSL>(fb + F_MID, 24u);
    u32x4 dv[2];
#pragma unroll
    for (int i = 0; i < 2; ++i) dv[i] = lld<u32x4>(smem + A_E1 + (2 * w + i) * E1P + lane * 16);
    const u32x4 m4 = lld<u32x4>(smem + A_FLG + F_WM);
    tag = lld<u32x2>(smem + A_FLG + F_TAG);
    const unsigned em = lld<unsigned>(smem + A_FLG + F_EM);
#pragma unroll
    for (int i = 0; i < 2; ++i) wv[i] = lld<u32x4>(smem + A_W + (2 * w + i) * 1024 + ((lane ^ (2 * w + i)) << 4));
#pragma unroll
    for (int i = 0; i < 2; ++i) dv[i] = pk8(mfh(dv[i], ff.re, Z4), mfh(dv[i], ff.im, Z4));
    const unsigned ew = min((pkmax(pkmax(m4.x, m4.y), pkmax(m4.z, m4.w)) & 0xffffu) >> 7, 245u);
    const float sw = __uint_as_float((246u - ew) << 23), swi = c < 8 ? sw : -sw;
    wv[0] = wcvt(wv[0], sw, swi); wv[1] = wcvt(wv[1], sw, swi);
#pragma unroll
    for (int i = 0; i < 2; ++i) dv[i] = wmul(mfh(sf.re, dv[i], Z4), mfh(sf.im, dv[i], Z4), wv[i]);
#pragma unroll
    for (int i = 0; i < 2; ++i) dv[i] = pk8(mfh(dv[i], si.re, Z4), mfh(dv[i], si.im, Z4));
    {
        f32x4 r[2], m[2];
#pragma unroll
        for (int i = 0; i < 2; ++i) {
            const f32x4 a = mfh(dv[i], fi.re, Z4), b = mfh(dv[i], fi.im, Z4);
#pragma unroll
            for (int e = 0; e < 4; ++e) { r[i][e] = fmaf(a[e], ti[i][0], b[e] * ti[i][1]); m[i][e] = fmaf(b[e], ti[i][0], -a[e] * ti[i][1]); }
        }
#pragma unroll
        for (int e = 0; e < 4; ++e)
            lst<u32x2>(smem + A_XT + (4 * q + e) * 1024 + (w >> 1) * 256 + c * 16 + (w & 1) * 8, (u32x2){pkh(r[0][e], r[1][e]), pkh(m[0][e], m[1][e])});
    }
    if (xtr && (w & 6) == 4) {
        const int it = 5 - w;
        const CM f = cexp(tab.xf), sf2 = cexp(it ? sf.im : lld<u32x4>(mb + 4096)), si2 = cexp(it ? si.im : lld<u32x4>(mb + 5120));
        u32x4 d = lld<u32x4>(smem + A_E1 + (16 + it) * E1P + lane * 16);
        d = pk8(mfh(d, f.re, Z4), mfh(d, f.im, Z4));
        d = wmul(mfh(sf2.re, d, Z4), mfh(sf2.im, d, Z4), wcvt(lld<u32x4>(smem + A_W + 16384 + it * 1024 + lane * 16), sw, swi));
        d = pk8(mfh(d, si2.re, Z4), mfh(d, si2.im, Z4));
        const f32x4 r = mfh(d, cexp(tab.xb).re, Z4);
#pragma unroll
        for (int e = 0; e < 4; ++e) lst<float>(smem + A_EX + (it * 256 + (4 * q + e) * 16 + c) * 4, r[e]);
    }
    TS(5);
    sig(fb + F_MID, 1u);
    await<0>(fb + F_MID, 16u);
    {
        f32x4 xa[2], xe[2];
#pragma unroll
        for (int i = 0; i < 2; ++i) { xa[i] = Z4; xe[i] = Z4; }
        if (xtr) {
            const float sg = (c & 1) ? -0.125f : 0.125f;
#pragma unroll
            for (int i = 0; i < 2; ++i) {
                xa[i] = lld<f32x4>(smem + A_EX + (2 * w + i) * 64 + q * 16) * sg;
                xe[i] = lld<f32x4>(smem + A_EX + 1024 + (2 * w + i) * 64 + q * 16) * -sg;
            }
        }
#pragma unroll
        for (int i = 0; i < 2; ++i) {
            const int s2 = 2 * w + i;
            const u32x4 ea = lld<u32x4>(smem + A_XT + s2 * 1024 + lane * 16);
            const f32x4 ta = mfh(ea, tab.mt[0], xa[i]), tb = mfh(ea, tab.mt[1], xe[i]);
            u32x4 tq = quads(smem + A_XT + s2 * 1024, lane, (u32x2){pkh(ta[0], ta[1]), pkh(ta[2], ta[3])}, (u32x2){pkh(tb[0], tb[1]), pkh(tb[2], tb[3])});
            PIN(tq);
            st128<SC1>(tq, rs, (lane >> 3) * 32768 + (lane & 7) * 16 + OFF_T + s2 * 262144 + j1 * 8192 + mp * 128);
        }
    }
    TS(7);
    tag.x = (tag.x & 0xffff0000u) | ew;
    return u_exp(em) - 267;
}

DEV void t_done(rsrc_t rs, unsigned fb, int gb, u32x2 tag, int lane) {
    int mp, j1; slot_of(gb, mp, j1);
    if (sig_rtn(fb + F_TC) == 7u) flag_put(rs, OFF_FT, (mp >> 3) * 32 + j1 * 8 + (mp & 7), tag, lane);
}

DEV void role_cload(const u16* __restrict__ Cre, const u16* __restrict__ Cim, char* ws, lptr smem, int gb, int lw, int lane, u32x2 tag TSARG) {
    int lt, pt; tile_of(gb, lt, pt);
    pt = da_of(pt);
    const unsigned lb = ldsa(smem), fb = lb + A_FLG;
    __builtin_amdgcn_s_setprio(3);
    if (!C1E) c_chunk(Cre, Cim, lb, gb, pt, lw, lane, 1);
    wait_vm<C1E ? 0 : 8>();
    t_done(mkrsrc(ws, WS_END), fb, gb, tag, lane);
    TS(44);
    await<2>(fb + F_HC, 4u);
    c_chunk(Cre, Cim, lb, gb, pt, lw, lane, 2);
    wait_vm<8>();
    sig(fb + F_FULL, 0x11u);
    c_chunk(Cre, Cim, lb, gb, pt, lw, lane, 3);
    wait_vm<0>();
    sig(fb + F_FULL, 0x1100u);
    TS(47);
    if (lw == 1) {
        unsigned m = await<2>(fb + F_FULL, 8u) & 0x88888888u;
#pragma unroll 1
        for (int spin = 0; spin < (1 << 20) && m != 0x88888888u; ++spin) {
            m = h_poll<2>(mkrsrc(ws, WS_END), gb, pt & 1, lane, tag);
            sig_or(fb + F_FULL, m);
        }
    }
#pragma unroll 1
    for (int j = 4; j < 8; ++j) {
        await<2>(fb + F_FREE, 4u << (4 * (j - 4)));
        if (!(XE & 1)) c_chunk<CSL>(Cre, Cim, lb, gb, pt, lw, lane, (XE & 8) ? j & 3 : j);
        wait_vm<8>();
        if (j > 4) sig(fb + F_FULL, 1u << (4 * (j - 1)));
        TS(44 + j);
    }
    wait_vm<0>();
    sig(fb + F_FULL, 1u << 28);
    asm volatile("gm_end1:");
    TSE(1, lw == 0);
}

DEV void role_last(char* ws, lptr smem, int gb, int w, int lane, u32x2 tag, int fe TSARG) {
    int s2t, dat; tile_of(gb, s2t, dat);
    dat = da_of(dat);
    const int c = lane & 15, q = lane >> 4, kh = dat & 1;
    const rsrc_t rs = mkrsrc(ws, WS_END);
    const unsigned fb = ldsa(smem + A_FLG);
    SCHED();
    u32x4 fc[2], fs[2];
    {
        float dt = (float)(32 * w + 16 * kh + c) * (1.0f / 128.0f); PIN(dt);
        float cc[8], ss[8];
        const float t0 = __builtin_amdgcn_fractf(dt * (float)(4 * q)), t1 = __builtin_amdgcn_fractf(dt * (float)(16 + 4 * q));
#pragma unroll
        for (int e = 0; e < 8; ++e) { const float f = fmaf((float)(e & 3), dt, e < 4 ? t0 : t1); cc[e] = __builtin_amdgcn_cosf(f); ss[e] = __builtin_amdgcn_sinf(f); }
        fc[0] = (u32x4){pkh(cc[0], cc[1]), pkh(cc[2], cc[3]), pkh(cc[4], cc[5]), pkh(cc[6], cc[7])};
        fs[0] = (u32x4){pkh(ss[0], ss[1]), pkh(ss[2], ss[3]), pkh(ss[4], ss[5]), pkh(ss[6], ss[7])};
        fc[1] = ((c & 1) ? fs[0] : fc[0]) ^ (((c + 1) & 2) ? SG : 0u);
        fs[1] = ((c & 1) ? fc[0] : fs[0]) ^ ((c & 2) ? SG : 0u);
        PIN(fc[0]); PIN(fs[0]); PIN(fc[1]); PIN(fs[1]);
        wait_vm<0>(); t_done(rs, fb, gb, tag, lane); TS(44);
    }
    u32x4 cA = fc[0], cB = fc[0], sB = fs[0];
    if (q == 0) { cA.x = (cA.x & 0xffff0000u) | ((c & 1) ? 0xbc00u : 0x3c00u); cB.x = (cB.x & 0xffff0000u) | 0x3800u; sB.x = (sB.x & 0xffff0000u) | ((c & 1) ? 0x3800u : 0xb800u); }
    lptr tw = smem + A_XT + ((lane >> 2) & 1) * 8192 + w * 2048 + ((lane >> 1) & 1) * 1024 + (((lane >> 3) ^ (lane & 6)) << 5) + (lane & 1) * 16;
    lptr tsc = smem + A_MAT + w * 32 + (lane >> 3) * 4, tr[4], rw[4];
#pragma unroll
    for (int i = 0; i < 4; ++i) {
        tr[i] = smem + A_XT + (i >> 1) * 8192 + (i & 1) * 1024 + ((lane * 8) ^ (i * 64));
        rw[i] = smem + A_XT + w * 4096 + c * 128 + ((i ^ ((c >> 1) & 3)) << 5) + q * 8;
        PIN(tr[i]); PIN(rw[i]);
    }
    lptr aw_ = smem + A_OG + (16 * w + c) * 4;
    unsigned o = lane * 16, ft = fb + F_TT;
    PIN(cA); PIN(cB); PIN(sB); PIN(tw); PIN(tsc); PIN(aw_); PIN(o); PIN(ft);
    TS(45);
    __builtin_amdgcn_s_setprio(3);
    if (w == 0) {
        u32x4 fl[2];
        if (TP0) __builtin_amdgcn_s_sleep(TP0);
        TS(50);
#pragma unroll 1
        for (int spin = 0; spin < (1 << 20); ++spin) { if (TSL) __builtin_amdgcn_s_sleep(TSL); flag_get(rs, OFF_FT, gb, lane, fl); if (flag_ok(fl, tag)) break; }
        const unsigned em = wave_pkmax(pkmax(pkmax(fl[0].x, fl[0].z), pkmax(fl[1].x, fl[1].z))) & 0xffu;
#pragma unroll
        for (int i = 0; i < 2; ++i) {
            const float a = ldexpf(1.f, (int)(fl[i].x & 0xffu) - (int)em), b = ldexpf(1.f, (int)(fl[i].z & 0xffu) - (int)em);
            lst<u32x2>(smem + A_MAT + i * 512 + lane * 8, (u32x2){pkh(a, a), pkh(b, b)});
        }
        sig(fb + F_TR, em | 0x8000u);
    }
    fe += await<1>(fb + F_TR, 0x8000u) & 0xffu;
    TS(46);
    u32x4 tv[8];
#pragma unroll
    for (int i = 0; i < 8; ++i) {
        tv[i] = __builtin_amdgcn_raw_buffer_load_b128(rs, o + (i & 3) * 1024, OFF_T + (s2t * 8 + (dat >> 1)) * 32768 + w * 8192 + (i >> 2) * 4096, SC1);
        if (TLS) __builtin_amdgcn_s_sleep(TLS);
        SCHED();
    }
    const u32x2 gg = __builtin_amdgcn_raw_buffer_load_b64(rs, c * 8, OFF_G + s2t * 1024 + w * 256 + kh * 128, SC1);
    unsigned ts[8];
#pragma unroll
    for (int i = 0; i < 8; ++i) ts[i] = lld<unsigned>(tsc + i * 128);
    __builtin_amdgcn_s_setprio(PRIOR);
    auto scale = [&](int h) INL {
#pragma unroll
        for (int i = 4 * h; i < 4 * h + 4; ++i) {
            tv[i] = __builtin_bit_cast(u32x4, __builtin_bit_cast(f16x8, tv[i]) * __builtin_bit_cast(f16x8, (u32x4){ts[i], ts[i], ts[i], ts[i]}));
            PIN(tv[i]);
        }
    };
    auto put = [&](int h) INL {
#pragma unroll
        for (int i = 0; i < 4; ++i) lst<u32x4>(tw + i * 256, tv[4 * h + i]);
        sig(ft, h ? 0x100u : 1u);
    };
    u32x4 fa[2][4], fb_[2][4];
    auto get = [&]() INL {
        constexpr int JO[4] = {1, 3, 0, 2};
#pragma unroll
        for (int i = 0; i < 8; ++i) { fa[i >> 2][JO[i & 3]] = trfrag(tr[(i >> 2) * 2] + JO[i & 3] * 2048, 512); fb_[i >> 2][JO[i & 3]] = trfrag(tr[(i >> 2) * 2 + 1] + JO[i & 3] * 2048, 512); SCHED(); }
    };
    f32x4 P0[2], P1[2], P2[2], P3a[2], P3b[2];
#pragma unroll
    for (int n = 0; n < 2; ++n) { P0[n] = Z4; P1[n] = Z4; P2[n] = Z4; P3a[n] = Z4; P3b[n] = Z4; }
    auto mm = [&](u32x4 c1, u32x4 s1, u32x4 c3, u32x4 c0, u32x4 s0) INL {
#pragma unroll
        for (int n = 0; n < 2; ++n) {
            P1[n] = mfh(fa[n][1], c1, P1[n]); P3a[n] = mfh(fa[n][1], s1, P3a[n]); P3b[n] = mfh(fb_[n][1], c1, P3b[n]); P1[n] = mfh(fb_[n][1], s1, P1[n]);
            SCHED();
            P3b[n] = mfh(fa[n][3], s1, P3b[n]); P1[n] = mfh(fa[n][3], c3, P1[n]); P3a[n] = mfh(fb_[n][3], c3, P3a[n]); P1[n] = mfh(fb_[n][3], s1, P1[n]);
            SCHED();
            P0[n] = mfh(fa[n][0], c0, P0[n]); P0[n] = mfh(fb_[n][0], s0, P0[n]); P2[n] = mfh(fa[n][2], c0, P2[n]); P2[n] = mfh(fb_[n][2], s0, P2[n]);
            SCHED();
        }
    };
    scale(0); put(0);
    TS(47);
    scale(1);
    __builtin_amdgcn_s_setprio(PRX);
    await<0>(ft, 4u);
    get();
    sig(ft, 0x10u);
    await<0>(ft, 0x40u);
    put(1);
    SCHED();
    mm(fc[0], fs[0], cA, cB, sB);
    await<0>(ft, 0x400u);
    get();
    sig(ft, 0x1000u);
    mm(fc[1], fs[1], fc[1], fc[1], fs[1]);
    TS(53);
    __builtin_amdgcn_s_setprio(PRV);
    u32x2 ga[2][4];
    const float g = ldexpf(__uint_as_float(gg.x), fe);
    lst<float>(aw_, 2.0f * __uint_as_float(gg.y));
#pragma unroll
    for (int n = 0; n < 2; ++n) {
        const f32x4 sp = P0[n] + P2[n], sm = P0[n] - P2[n], p3 = P3a[n] - P3b[n];
        const f32x4 av[4] = {sp + P1[n], sm - p3, sp - P1[n], sm + p3};
#pragma unroll
        for (int m = 0; m < 4; ++m) {
            const unsigned b0 = pkb(av[m][0], av[m][1]), b1 = pkb(av[m][2], av[m][3]);
            ga[n][m] = (u32x2){pkb(g * lo2f(b0), g * hi2f(b0)), pkb(g * lo2f(b1), g * hi2f(b1))};
        }
    }
    await<0>(ft, 0x4000u);
#pragma unroll
    for (int i = 0; i < 8; ++i) lst<u32x2>(rw[i & 3] + (i >> 2) * 2048, ga[i & 1][i >> 1]);
    sig(fb + F_R0 + w * 4, 1u);
    TS(48);
    asm volatile("gm_end2:");
    TSE(w ? 4 : 5, w == 0 || w == 2);
}

NOPK __global__ __launch_bounds__(768, 3) void gm_kernel(const u16* __restrict__ x, const u16* __restrict__ w_r, const u16* __restrict__ w_i,
                                                         const u16* __restrict__ Bre, const u16* __restrict__ Bim, const u16* __restrict__ Cre,
                                                         const u16* __restrict__ Cim, const u16* __restrict__ lam_re,
                                                         const u16* __restrict__ lam_im, const u16* __restrict__ log_dt,
                                                         const u16* __restrict__ Dskip, const u16* __restrict__ gate_w, char* ws, float* __restrict__ out) {
    extern __shared__ __attribute__((aligned(16))) char smem_[];
    lptr smem = (lptr)smem_;
    u64 pc; int len;
    asm volatile("s_getpc_b64 %0\ngm_beg:\n\ts_mov_b32 %1, gm_end0 - gm_beg + 128\n\ts_max_i32 %1, %1, gm_end1 - gm_beg + 128\n\ts_max_i32 %1, %1, gm_end2 - gm_beg + 128"
                 : "=s"(pc), "=&s"(len) :: "scc");
    const int wid = __builtin_amdgcn_readfirstlane(threadIdx.x >> 6), lane = threadIdx.x & 63;
    int w = wid & 7, gb = blockIdx.x;
    asm volatile("" : "+s"(w), "+s"(gb));
    if (wid < 4) role_mma(x, Dskip, ws, out, smem, blockIdx.x, wid, lane, pc, len);
    else {
        TSD();
        u32x2 tag;
        Tab tab;
        if (wid < 8) role_load(x, Bre, Bim, Cre, Cim, gate_w, ws, smem, blockIdx.x, wid - 4, lane, tab TSPASS);
        else role_aux(w_r, w_i, lam_re, lam_im, log_dt, ws, smem, blockIdx.x, wid - 8, lane, tab TSPASS);
        const int fe = role_mid(ws, smem, gb, w, lane, tag, tab TSPASS);
        if (w >= 4) role_cload(Cre, Cim, ws, smem, gb, w - 4, lane, tag TSPASS);
        else role_last(ws, smem, gb, w, lane, tag, fe TSPASS);
    }
}

extern "C" void kernel_launch(void* const* d_in, const int* in_sizes, int n_in, void* d_out, int out_size, void* d_ws, size_t ws_size,
                              hipStream_t stream) {
    (void)hipFuncSetAttribute((const void*)gm_kernel, hipFuncAttributeMaxDynamicSharedMemorySize, SMEM);
    gm_kernel<<<256, 768, SMEM, stream>>>((const u16*)d_in[0], (const u16*)d_in[1], (const u16*)d_in[2], (const u16*)d_in[6], (const u16*)d_in[7],
                                          (const u16*)d_in[8], (const u16*)d_in[9], (const u16*)d_in[4], (const u16*)d_in[5], (const u16*)d_in[10],
                                          (const u16*)d_in[11], (const u16*)d_in[12], (char*)d_ws, (float*)d_out);
}
```

```cpp
#define GDL 10
#include <hip/hip_runtime.h>
#include <stdint.h>

typedef __attribute__((ext_vector_type(8))) __bf16 bf16x8;
typedef __attribute__((ext_vector_type(2))) __bf16 bf16x2;
typedef __attribute__((ext_vector_type(8))) _Float16 f16x8;
typedef __attribute__((ext_vector_type(2))) _Float16 f16x2;
typedef __attribute__((ext_vector_type(4))) float f32x4;
typedef __attribute__((ext_vector_type(2))) float f32x2;
typedef __attribute__((ext_vector_type(4))) unsigned u32x4;
typedef __attribute__((ext_vector_type(3))) unsigned u32x3;
typedef __attribute__((ext_vector_type(2))) unsigned u32x2;
typedef __attribute__((ext_vector_type(4))) short s16x4;
typedef __attribute__((ext_vector_type(2))) unsigned short u16x2;
typedef unsigned short u16;
typedef unsigned long long u64;
#define GAS __attribute__((address_space(1)))
#define LAS __attribute__((address_space(3)))
#define NOPK __attribute__((target("no-packed-fp32-ops")))
#define DEV __device__ __forceinline__

constexpr int S_ = 2048, D_ = 1024, KF = 513;

constexpr unsigned OFF_H = 0;
constexpr unsigned OFF_U = OFF_H + (8u << 20);
constexpr unsigned OFF_T = OFF_U + (4u << 20);
constexpr unsigned OFF_G = OFF_T + (4u << 20);
constexpr unsigned OFF_FU = OFF_G + 16384u;
constexpr unsigned OFF_FT = OFF_FU + 16384u;
constexpr unsigned OFF_FH = OFF_FT + 16384u;
constexpr unsigned OFF_GRAN = OFF_FH + 16384u;
constexpr unsigned OFF_E = OFF_GRAN + 16u * 512u * 16u;
constexpr unsigned WS_END = OFF_E + 128u;

#define TL(i)
#define TSD()
#define TS(i)
#define TC(i)
#define TSE(slot, on)

DEV unsigned pkb(float a, float b) { f32x2 v = {a, b}; return __builtin_bit_cast(unsigned, __builtin_convertvector(v, bf16x2)); }
DEV unsigned pkh(float a, float b) { f32x2 v = {a, b}; return __builtin_bit_cast(unsigned, __builtin_convertvector(v, f16x2)); }
DEV float lo2f(unsigned p) { return __uint_as_float(p << 16); }
DEV float hi2f(unsigned p) { return __uint_as_float(p & 0xffff0000u); }
DEV float bfr(float f) { return lo2f(pkb(f, f)); }
DEV float2 cmul(float2 a, float2 b) { return make_float2(a.x * b.x - a.y * b.y, a.x * b.y + a.y * b.x); }
DEV float2 cfma(float2 a, float2 b, float2 c) { return make_float2(fmaf(a.x, b.x, fmaf(-a.y, b.y, c.x)), fmaf(a.x, b.y, fmaf(a.y, b.x, c.y))); }
DEV f32x4 mfb(u32x4 a, u32x4 b, f32x4 c) { return __builtin_amdgcn_mfma_f32_16x16x32_bf16(__builtin_bit_cast(bf16x8, a), __builtin_bit_cast(bf16x8, b), c, 0, 0, 0); }
DEV f32x4 mfh(u32x4 a, u32x4 b, f32x4 c) { return __builtin_amdgcn_mfma_f32_16x16x32_f16(__builtin_bit_cast(f16x8, a), __builtin_bit_cast(f16x8, b), c, 0, 0, 0); }
DEV unsigned pkmax(unsigned a, unsigned b) { return __builtin_bit_cast(unsigned, __builtin_elementwise_max(__builtin_bit_cast(u16x2, a), __builtin_bit_cast(u16x2, b))); }
typedef LAS char* lptr;
DEV unsigned ldsa(lptr p) { return (unsigned)(uintptr_t)p; }
template <class T> DEV T lld(lptr p) { return *(LAS const T*)p; }
template <class T> DEV void lst(lptr p, T v) { *(LAS T*)p = v; }
DEV u32x2 trread(lptr p) { return __builtin_bit_cast(u32x2, __builtin_amdgcn_ds_read_tr16_b64_v4i16((LAS s16x4*)p)); }
DEV u32x4 trfrag(lptr p, int second) { const u32x2 a = trread(p), b = trread(p + second); return (u32x4){a.x, a.y, b.x, b.y}; }
typedef __amdgpu_buffer_rsrc_t rsrc_t;
DEV u32x4 ldfrag(lptr tile, int row, int chunk) { return lld<u32x4>(tile + row * 128 + ((chunk ^ (row & 7)) << 4)); }
template <int N> DEV void wait_vm() { asm volatile("s_waitcnt vmcnt(%0)" :: "i"(N) : "memory"); }
#define LDS_WAIT() asm volatile("s_waitcnt lgkmcnt(0)" ::: "memory")
#define SCHED() __builtin_amdgcn_sched_barrier(0)
#define BAR() do { __builtin_amdgcn_s_barrier(); asm volatile("" ::: "memory"); } while (0)
#define PIN(x) asm volatile("" : "+v"(x))
template <int I> struct IC { static constexpr int v = I; };
template <int I, int N, class F> DEV void sfor(F&& f) { if constexpr (I < N) { f(IC<I>{}); sfor<I + 1, N>(f); } }
#define INL __attribute__((always_inline))
DEV rsrc_t mkrsrc(const void* p, unsigned n) { return __builtin_amdgcn_make_buffer_rsrc((void*)p, 0, n, 0x00020000); }
constexpr int SC1 = 16;
#define WTO 16
template <int AUX> DEV void st128(u32x4 v, rsrc_t rs, unsigned off) { __builtin_amdgcn_raw_buffer_store_b128(v, rs, off, 0, AUX); }
DEV void lds_add(unsigned a) { asm volatile("ds_add_u32 %0, %1" :: "v"(a), "v"(1u) : "memory"); }
DEV unsigned lds_peek(unsigned a) {
    unsigned v;
    asm volatile("ds_read_b32 %0, %1\n\ts_waitcnt lgkmcnt(0)" : "=v"(v) : "v"(a) : "memory");
    return __builtin_amdgcn_readfirstlane(v);
}
DEV void tile_of(int gb, int& ta, int& tb) {
    const int xcd = gb & 7, loc = gb >> 3;
    ta = (xcd >> 1) * 4 + (loc >> 3);
    tb = (xcd & 1) * 8 + (loc & 7);
}
#define DMAP 1
DEV int da_of(int pt) { return DMAP ? ((pt & 7) << 1) | (pt >> 3) : pt; }
DEV void rows4(float a, float& a0, float& a1, float& a2, float& a3) {
    const unsigned u = __float_as_uint(a);
    const auto r = __builtin_amdgcn_permlane32_swap(u, u, false, false);
    const auto p = __builtin_amdgcn_permlane16_swap(r[0], r[0], false, false);
    const auto s = __builtin_amdgcn_permlane16_swap(r[1], r[1], false, false);
    a0 = __uint_as_float(p[0]); a1 = __uint_as_float(p[1]); a2 = __uint_as_float(s[0]); a3 = __uint_as_float(s[1]);
}
DEV float rowsum4(float a) {
    const unsigned u = __float_as_uint(a);
    const auto r = __builtin_amdgcn_permlane32_swap(u, u, false, false);
    const unsigned t = __float_as_uint(__uint_as_float(r[0]) + __uint_as_float(r[1]));
    const auto p = __builtin_amdgcn_permlane16_swap(t, t, false, false);
    return __uint_as_float(p[0]) + __uint_as_float(p[1]);
}
DEV float2 sel4(int q, float2 a, float2 b, float2 c, float2 d) {
    PIN(d.x); PIN(d.y);
    const float2 e = (q & 1) ? b : a, f = (q & 1) ? d : c;
    return (q & 2) ? f : e;
}
template <int R> DEV unsigned ror16(unsigned v) { return __builtin_amdgcn_update_dpp(0u, v, 0x120 + R, 0xf, 0xf, false); }
DEV unsigned wave_pkmax(unsigned m) {
    m = pkmax(m, ror16<8>(m)); m = pkmax(m, ror16<4>(m)); m = pkmax(m, ror16<2>(m)); m = pkmax(m, ror16<1>(m));
    const auto r = __builtin_amdgcn_permlane32_swap(m, m, false, false);
    m = pkmax(r[0], r[1]);
    const auto p = __builtin_amdgcn_permlane16_swap(m, m, false, false);
    return pkmax(p[0], p[1]);
}
DEV float half_sum(float v) {
    v += __uint_as_float(ror16<8>(__float_as_uint(v))); v += __uint_as_float(ror16<4>(__float_as_uint(v)));
    v += __uint_as_float(ror16<2>(__float_as_uint(v))); v += __uint_as_float(ror16<1>(__float_as_uint(v)));
    const auto p = __builtin_amdgcn_permlane16_swap(__float_as_uint(v), __float_as_uint(v), false, false);
    return __uint_as_float(p[0]) + __uint_as_float(p[1]);
}
DEV u32x4 quads(lptr buf, int lane, u32x2 p, u32x2 n) {
    lptr wp = buf + (lane & 15) * 64 + ((lane >> 4) & 1) * 8;
    const int v = (lane >> 5) ^ ((lane >> 2) & 3);
    lst<u32x2>(wp + (v << 4), p); lst<u32x2>(wp + ((v ^ 2) << 4), n);
    asm volatile("" ::: "memory");
    return lld<u32x4>(buf + ((lane ^ (lane >> 4)) << 4));
}

#define PRIOA 2
#define NREP 8
#define PRIOM 1
#define PRIOT 2
#define PRIOG 2
#define AD 8
#define NB 12
#define NP 3
#define AM 1
#define CP 0
#define HP 0
#define XE 0
#define TSL 16
#define CSL 1
#define WG2 2
#define WG1 1
#define SPC 1
#define PRV 0
#define PRX 0
#define TLS 0
#define RDL 0
#define WROW {0, 0, 0, 0, 8}
#define ROT (lt & 12)
#define PRIOL 3
#define XM 0
#define UFS 6
#define PRIOR 0
#define PRS 2
#define WXS 13
#define CPF 1
#define FST 8
#define HREP 8
#define NP1 1
#define PDL 8
#define PDE 0
#define PDM 0x80
#define NB2 NB
#define XP 4
#define PU 0
#define BAL -1
#define BA2 0
#define FTC 1
#define LBS 0
#define PT 0
#define PTS 4
#define FTB 768
#define XTP 1
#define TWH 4
#define ACI 0
#define CE 0
#define CES 0
#define CEL 1
#define HPL 0
#define HPW 0
#define CW 0
#define GSP 1
#define PV0 "144"
#define PV0E "147"
#define PV1 "148"
#define PV1E "151"
#define PV2 "152"
#define PV2E "155"
#define PV3 "156"
#define PV3E "159"
#define UDL 0
#define CGW 0
#define C0S 0
#define GSL 0
#define C0L 0
#define C0P 0
#define BSL 0
#define BS2 0
#define PZ1 0
#define PZ2 0
#define PZ3 0
#define PZA 0
#define PZB 48
#define XLP 0
#define PRIOI 0
#define RST (NST - 5)
#define SIL 1
#define CPM 7
#define PRIOE 2
#define MSL 1
#define URS 6
#define UPRE 1
#define UCK 13
#define PRIOD 3
#define PRIOW 0
#define CDL 0
#define ATS 14
#define UPL 10
#define C1E 1
#define TP0 24
#define PD0 0
#define CXM 6
#define CX(g) ((g) & CXM)
#define GSTEP {0, 0, 0, 0}
#define XS 1
#define B1S 0
#define US 1
#define UPS 1
#define DS 8
#define P0S 8
#define VL 1
#define PN -1
#define PG -1
#define SNK 1
#define HFIRST 8u
#define RDPAT {0, 1, 2, -1, 3, 4, -1, 5, 6, -1, 7, -1, -1, -1, -1, -1}
#define Z4 ((f32x4){0.f, 0.f, 0.f, 0.f})
constexpr unsigned SG = 0x80008000u;
struct CM { u32x4 re, im; };
DEV u32x4 cmat1(float t0, float dt) {
    float cc[4], ss[4];
    t0 = __builtin_amdgcn_fractf(t0);
#pragma unroll
    for (int e = 0; e < 4; ++e) { const float f = fmaf((float)e, dt, t0); cc[e] = __builtin_amdgcn_cosf(f); ss[e] = __builtin_amdgcn_sinf(f); }
    return (u32x4){pkh(ss[0], ss[1]), pkh(ss[2], ss[3]), pkh(cc[0], cc[1]), pkh(cc[2], cc[3])};
}
DEV CM cexp(u32x4 v) { CM m; m.re = (u32x4){v.z, v.w, v.x ^ SG, v.y ^ SG}; m.im = v; PIN(m.re); PIN(m.im); return m; }
DEV u32x4 pk8(f32x4 r, f32x4 i) { return (u32x4){pkh(r[0], r[1]), pkh(r[2], r[3]), pkh(i[0], i[1]), pkh(i[2], i[3])}; }
DEV u32x4 wmul(f32x4 r, f32x4 i, u32x4 w) {
    float yr[4], yi[4];
#pragma unroll
    for (int e = 0; e < 4; ++e) {
        const unsigned wu = w[e];
        const f16x2 h = __builtin_bit_cast(f16x2, wu);
        yr[e] = fmaf(r[e], (float)h[0], -i[e] * (float)h[1]); yi[e] = fmaf(r[e], (float)h[1], i[e] * (float)h[0]);
    }
    return (u32x4){pkh(yr[0], yr[1]), pkh(yr[2], yr[3]), pkh(yi[0], yi[1]), pkh(yi[2], yi[3])};
}
DEV u32x4 wcvt(u32x4 w, float sr, float si) {
#pragma unroll
    for (int e = 0; e < 4; ++e) w[e] = pkh(lo2f(w[e]) * sr, hi2f(w[e]) * si);
    PIN(w);
    return w;
}
DEV u32x4 mkdesc(const void* p, unsigned n) { const u64 a = (u64)p; return (u32x4){(unsigned)a, (unsigned)(a >> 32) & 0xffffu, n, 0x00020000u}; }
DEV void dma16(u32x4 d, unsigned l, unsigned vo, unsigned so) {
    asm volatile("s_nop 0\n\tbuffer_load_dwordx4 %0, %1, %2 offen lds" :: "v"(vo), "s"(d), "s"(so), "{m0}"(l) : "memory");
}
DEV void dma16s(u32x4 d, unsigned l, unsigned vo, unsigned so) {
    asm volatile("s_nop 0\n\tbuffer_load_dwordx4 %0, %1, %2 offen sc1 lds" :: "v"(vo), "s"(d), "s"(so), "{m0}"(l) : "memory");
}
DEV void sig(unsigned a, unsigned v) { asm volatile("s_mov_b64 exec, 1\n\tds_add_u32 %0, %1\n\ts_mov_b64 exec, -1" :: "v"(a), "v"(v) : "memory"); }
template <int O> DEV void sig_o(unsigned a, unsigned v) { asm volatile("s_mov_b64 exec, 1\n\tds_add_u32 %0, %1 offset:%2\n\ts_mov_b64 exec, -1" :: "v"(a), "v"(v), "i"(O) : "memory"); }
DEV unsigned sig_rtn(unsigned a) {
    unsigned v;
    asm volatile("s_mov_b64 exec, 1\n\tds_add_rtn_u32 %0, %1, %2\n\ts_mov_b64 exec, -1\n\ts_waitcnt lgkmcnt(0)" : "=&v"(v) : "v"(a), "v"(1u) : "memory");
    return __builtin_amdgcn_readfirstlane(v);
}
template <int SLP> DEV unsigned await(unsigned a, unsigned mask) {
    unsigned v, n, r, t;
    asm volatile("s_mov_b32 %1, 0x400000\n"
                 "1:\tds_read_b32 %0, %4\n\ts_waitcnt lgkmcnt(0)\n\tv_readfirstlane_b32 %2, %0\n\ts_and_b32 %3, %2, %5\n\ts_cbranch_scc1 2f\n\t"
                 "s_sleep %6\n\ts_sub_u32 %1, %1, 1\n\ts_cbranch_scc0 1b\n"
                 "2:" : "=&v"(v), "=&s"(n), "=&s"(r), "=&s"(t) : "v"(a), "s"(mask), "i"(SLP) : "memory", "scc");
    return r;
}
#define BMF "v_mfma_f32_16x16x32_bf16 %0, %2, %3, %0\n\tv_mfma_f32_16x16x32_bf16 %1, %3, %2, %1\n\t"
template <int SLP> DEV void burn(int n, u32x4& a, u32x4& b) {
    f32x4 d0, d1;
    asm volatile("1:\t" BMF BMF "s_nop %5\n\ts_sub_u32 %4, %4, 1\n\ts_cbranch_scc0 1b\n\ts_nop 7\n\ts_nop 3"
                 : "=&v"(d0), "=&v"(d1), "+v"(a), "+v"(b), "+s"(n) : "i"(SLP) : "scc");
}
template <int SLP> DEV unsigned await_burn(unsigned ad, unsigned mask, u32x4 a, u32x4 b) {
    unsigned v, n, r, t; f32x4 d0, d1;
    asm volatile("s_mov_b32 %5, 0x400000\n"
                 "1:\tds_read_b32 %4, %8\n\t" BMF BMF "s_waitcnt lgkmcnt(0)\n\tv_readfirstlane_b32 %6, %4\n\ts_and_b32 %7, %6, %9\n\ts_cbranch_scc1 2f\n\t"
                 ".rept %10\n\t" BMF ".endr\n\ts_sub_u32 %5, %5, 1\n\ts_cbranch_scc0 1b\n"
                 "2:\ts_nop 7\n\ts_nop 3" : "=&v"(d0), "=&v"(d1), "+v"(a), "+v"(b), "=&v"(v), "=&s"(n), "=&s"(r), "=&s"(t) : "v"(ad), "s"(mask), "i"(SLP) : "memory", "scc");
    return r;
}
DEV void need_bit(unsigned a, unsigned& r, unsigned mask) {
    unsigned v, n, t;
    asm volatile("s_and_b32 %3, %2, %5\n\ts_cbranch_scc1 2f\n\ts_mov_b32 %1, 0x400000\n"
                 "1:\tds_read_b32 %0, %4\n\ts_waitcnt lgkmcnt(0)\n\tv_readfirstlane_b32 %2, %0\n\ts_and_b32 %3, %2, %5\n\ts_cbranch_scc1 2f\n\t"
                 "s_sub_u32 %1, %1, 1\n\ts_cbranch_scc0 1b\n"
                 "2:" : "=&v"(v), "=&s"(n), "+s"(r), "=&s"(t) : "v"(a), "i"(mask) : "memory", "scc");
}
#define MF2(a, b, c) asm("v_mfma_f32_16x16x32_bf16 %0, %1, %2, %0" : "+v"(c) : "v"(a), "v"(b))
DEV void flag_put(rsrc_t rs, unsigned off, int gb, u32x2 v, int lane) {
    if (lane < NREP) __builtin_amdgcn_raw_buffer_store_b64(v, rs, lane * 2048, off + gb * 8, SC1);
}
DEV void flag_get(rsrc_t rs, unsigned off, int gb, int lane, u32x4 (&fl)[2]) {
    unsigned o = lane * 16; PIN(o);
#pragma unroll
    for (int i = 0; i < 2; ++i) fl[i] = __builtin_amdgcn_raw_buffer_load_b128(rs, o + i * 1024, off + (gb & (NREP - 1)) * 2048, SC1);
}
DEV bool flag_ok(const u32x4 (&fl)[2], u32x2 tag) {
    bool ok = true;
#pragma unroll
    for (int i = 0; i < 2; ++i) ok = ok & ((fl[i].x ^ tag.x) < 0x10000u) & (fl[i].y == tag.y) & ((fl[i].z ^ tag.x) < 0x10000u) & (fl[i].w == tag.y);
    return __all(ok);
}
template <int N> DEV unsigned h_poll(rsrc_t rs, int gb, int kh, int lane, u32x2 tag) {
    unsigned o = (lane ^ ((CX(gb) & 3) << 4)) * FST, m = 0; PIN(o);
    u32x2 fl[N];
#pragma unroll
    for (int i = 0; i < N; ++i) fl[i] = __builtin_amdgcn_raw_buffer_load_b64(rs, o, OFF_FH + ((gb & (HREP - 1)) * 256 + kh * 128 + (i ^ (CX(gb) >> 2)) * 64) * FST, SC1);
#pragma unroll
    for (int i = 0; i < N; ++i) {
        const u64 b = __ballot(((fl[i].x ^ tag.x) < 0x10000u) & (fl[i].y == tag.y));
#pragma unroll
        for (int j = 0; j < 4; ++j) m |= ((unsigned)(b >> (16 * j)) & 0xffffu) == 0xffffu ? 8u << (16 * i + 4 * j) : 0u;
    }
    return m;
}
DEV void sig_or(unsigned a, unsigned v) { asm volatile("s_mov_b64 exec, 1\n\tds_or_b32 %0, %1\n\ts_mov_b64 exec, -1" :: "v"(a), "v"(v) : "memory"); }

constexpr int A_STAGE = 32768, A_XT = 4 * A_STAGE, A_PRM = A_XT + 16384, A_AGG = A_PRM + 2048, A_FLG = A_AGG + 512, A_GW = A_FLG + 128,
              A_MAT = A_GW + 2048, SMEM = A_MAT + 11264;
constexpr int E1P = 1040, A_EX = A_GW, A_E1 = 0, A_PW = 18 * E1P + 224, A_W = A_STAGE, A_HS = 2 * A_STAGE - 8192, A_OG = A_MAT + 9216;
static_assert(A_PW % 256 == 0 && A_W + 18432 + 512 <= A_HS, "");
constexpr int F_MID = 8, F_EM = 12, F_MAX = 16, F_TAG = 32, F_FULL = 40, F_FREE = 44, F_HC = 48, F_HR = 52, F_TC = 56, F_TR = 60, F_TT = 64, F_ST = 76, F_WM = 80, F_R0 = 96;

constexpr int XG = 0xFF;
DEV void slot_of(int gb, int& mp, int& j1) { gb ^= XG; mp = (gb & 7) * 8 + (gb >> 5); j1 = (gb >> 3) & 3; }
DEV u32x4 mat_sf(int k1, int c, int q) { const float dt = -(float)(k1 + 128 * c) * (1.0f / 2048.0f); return cmat1(dt * (float)(4 * q), dt); }
DEV u32x4 mat_si(int k1, int c, int q) { return cmat1((float)((k1 + 512 * q) * c) * (1.0f / 2048.0f), (float)c * (1.0f / 16.0f)); }

DEV void mk_tables(lptr smem, int gb, int wid, int lane) {
    int mp, j1; slot_of(gb, mp, j1);
    const int c = lane & 15, q = lane >> 4;
    const bool spc = mp == 0 && !(j1 & 1);
    const int k1 = mp ? mp : (j1 == 3 ? 64 : 0);
    lptr mt = smem + A_MAT + lane * 16;
    if (wid == 0) {
        const float dt = -(float)(j1 + 4 * (spc ? c & 7 : c)) * (1.0f / 64.0f), t0 = __builtin_amdgcn_fractf(dt * (float)(4 * q));
        float cc[4], ss[4];
#pragma unroll
        for (int e = 0; e < 4; ++e) { const float f = fmaf((float)e, dt, t0); cc[e] = __builtin_amdgcn_cosf(f); ss[e] = __builtin_amdgcn_sinf(f); }
        const unsigned c0 = pkb(cc[0], cc[1]), c1 = pkb(cc[2], cc[3]), s0 = pkb(ss[0], ss[1]), s1 = pkb(ss[2], ss[3]);
        const unsigned c0l = pkb(cc[0] - lo2f(c0), cc[1] - hi2f(c0)), c1l = pkb(cc[2] - lo2f(c1), cc[3] - hi2f(c1));
        const unsigned s0l = pkb(ss[0] - lo2f(s0), ss[1] - hi2f(s0)), s1l = pkb(ss[2] - lo2f(s1), ss[3] - hi2f(s1));
        u32x4 m[4] = {(u32x4){c0, c1, s0 ^ SG, s1 ^ SG}, (u32x4){s0, s1, c0, c1}, (u32x4){c0l, c1l, s0l ^ SG, s1l ^ SG}, (u32x4){s0l, s1l, c0l, c1l}};
        if (spc) {
            m[0] = c < 8 ? (u32x4){c0, c1, 0, 0} : (u32x4){0, 0, c0, c1}; m[1] = c < 8 ? (u32x4){s0, s1, 0, 0} : (u32x4){0, 0, s0, s1};
            m[2] = c < 8 ? (u32x4){c0l, c1l, 0, 0} : (u32x4){0, 0, c0l, c1l}; m[3] = c < 8 ? (u32x4){s0l, s1l, 0, 0} : (u32x4){0, 0, s0l, s1l};
        }
#pragma unroll
        for (int i = 0; i < 4; ++i) lst<u32x4>(mt + i * 1024, m[i]);
    } else if (wid == 1) {
        const int jq = spc ? (4 * q) & 7 : 4 * q;
        float cc[4], ss[4];
        const float t0 = __builtin_amdgcn_fractf((float)(c * (j1 + 4 * jq)) * (1.0f / 64.0f));
#pragma unroll
        for (int e = 0; e < 4; ++e) {
            const float f = fmaf((float)c, (float)e * (1.0f / 16.0f), t0);
            const float wg = !spc || (gb == XG && jq + e == 0) ? 0.125f : 0.25f;
            cc[e] = __builtin_amdgcn_cosf(f) * wg; ss[e] = -__builtin_amdgcn_sinf(f) * wg;
        }
        const unsigned c0 = pkh(cc[0], cc[1]), c1 = pkh(cc[2], cc[3]), n0 = pkh(ss[0], ss[1]), n1 = pkh(ss[2], ss[3]);
        u32x4 mia = (u32x4){c0, n0, c1, n1}, mib = (u32x4){n0, c0 ^ SG, n1, c1 ^ SG};
        if (spc) { mib = q < 2 ? (u32x4){0, 0, 0, 0} : mia ^ SG; if (q >= 2) mia = (u32x4){0, 0, 0, 0}; }
        lst<u32x4>(mt + 6 * 1024, mia); lst<u32x4>(mt + 7 * 1024, mib);
        lst<u32x4>(mt + 8 * 1024, cmat1((float)(c * q) * -0.25f, (float)c * (-1.0f / 16.0f)));
    } else {
        lst<u32x4>(mt + (2 + wid) * 1024, wid == 2 ? mat_sf(k1, c, q) : mat_si(k1, c, q));
        const int ja = (spc ? 0 : 8 * (wid - 2)) + (lane >> 3);
        const float dt = -(float)(j1 + 4 * ja) * (1.0f / 1024.0f), t0 = dt * (float)(2 * (lane & 7)), t1 = t0 + dt;
        lst<f32x4>(mt + (7 + wid) * 1024, (f32x4){__builtin_amdgcn_cosf(t0), __builtin_amdgcn_sinf(t0), __builtin_amdgcn_cosf(t1), __builtin_amdgcn_sinf(t1)});
    }
}

template <int SLP = 0> DEV void c_chunk(const u16* __restrict__ Cre, const u16* __restrict__ Cim, unsigned lb, int gb, int da, int lw, int lane, int j) {
    const u32x4 rc = mkdesc(lw < 2 ? Cre : Cim, D_ * D_ * 2);
    const unsigned vo = (lane >> 3) * 8192 + (((lane & 7) ^ (lane >> 3)) << 4);
#pragma unroll
    for (int i = 0; i < 8; ++i) {
        if (SLP && i) __builtin_amdgcn_s_sleep(SLP);
        dma16(rc, lb + ((j + 2) & 3) * A_STAGE + lw * 8192 + i * 1024, vo, (512 * (lw & 1) + 256 * (i & 1) + 32 * (da >> 1) + (i >> 1)) * 2048 + (8 * (da & 1) + (j ^ CX(gb))) * 128);
    }
}
DEV void touch(u32x4 d, unsigned l, unsigned vo, unsigned so) {
    asm volatile("s_nop 0\n\tbuffer_load_dword %0, %1, %2 offen sc1 lds" :: "v"(vo), "s"(d), "s"(so), "{m0}"(l) : "memory");
}
DEV void c_pref(const u16* __restrict__ Cre, const u16* __restrict__ Cim, unsigned lb, int gb, int lt, int da, int lane, int j) {
    touch(mkdesc((lt & 2) ? Cim : Cre, D_ * D_ * 2), lb + A_PRM, ((lane >> 5) * 256 + (lane & 31)) * 2048,
          (512 * (lt & 1) + 32 * (da >> 1)) * 2048 + (8 * (da & 1) + (j ^ CX(gb))) * 128);
}
constexpr int KS1 = 32 - 4 * NP, KS2 = 4 * NP, NST = KS1 + 2 * KS2;
constexpr int st_k(int st) { return st < 32 ? st : st - KS2; }
constexpr int b_st(int b) { return b < 2 * KS1 ? b >> 1 : b - KS1; }
constexpr int b_n(int b) { return b < 2 * KS1 ? b & 1 : b >= KS1 + 32; }
constexpr int b_nx(int b) { return b < 2 * KS1 ? b + NB : b + NB2 < 64 && b + NB2 >= 2 * KS1 + NB ? b + NB2 : -1; }
constexpr bool b_new(int b) { return b < KS1 + 32 && !b_n(b) && !(b_st(b) & 3); }

DEV void role_mma(const u16* __restrict__ x, const u16* __restrict__ Dskip, char* ws, float* __restrict__ out, lptr smem, int gb, int wid, int lane, u64 pc, int len) {
    int lt, pt; tile_of(gb, lt, pt);
    const int wr = wid >> 1, wc = wid & 1, c = lane & 15, q = lane >> 4;
    const rsrc_t rs = mkrsrc(ws, WS_END);
    const unsigned fb = ldsa(smem + A_FLG);
    TSD(); TS(0);
    u64 tag = __hip_atomic_load((GAS u64*)(ws + OFF_E), __ATOMIC_RELAXED, __HIP_MEMORY_SCOPE_WORKGROUP) + 0x9E3779B97F4A7C15ull;
    unsigned cpf = 0;
    if (CPF == 1) { if (wid == 3) cpf = __builtin_amdgcn_raw_buffer_load_b32(mkrsrc((const void*)pc, max(len, 0)), lane * 128, ((gb >> 3) % CPM) * 8192, 0); }
    if (FTC == 1) { if (wid == 2) cpf = __builtin_amdgcn_raw_buffer_load_b32(mkrsrc(ws + OFF_G + gb * FTB, FTB), lane * 64, 0, SC1); }
    SCHED();
    if (wid == 0 && lane < 32) lst<unsigned>(smem + A_FLG + lane * 4, 0u);
    mk_tables(smem, gb, wid, lane);
    f32x4 acc[4][4];
#pragma unroll
    for (int m = 0; m < 4; ++m)
#pragma unroll
        for (int n = 0; n < 4; ++n) acc[m][n] = Z4;
    u32x4 af[2][4], bf_[2][4];
#pragma unroll
    for (int m = 0; m < 4; ++m) { af[1][m] = (u32x4){0, 0, 0, 0}; bf_[1][m] = af[1][m]; }
    lptr fp[8];
#pragma unroll
    for (int i = 0; i < 8; ++i) { fp[i] = smem + (i & 2) * A_STAGE + ((i & 4 ? 128 + wc * 64 : wr * 64) + c) * 128 + (((4 * (i & 1) + q) ^ (c & 7)) << 4); PIN(fp[i]); }
    PIN(tag); PIN(cpf);
    if (wid == 0 && lane == 0) lst<u64>(smem + A_FLG + F_TAG, tag);
    tag = (u64)(unsigned)__builtin_amdgcn_readfirstlane((unsigned)tag) | (u64)(unsigned)__builtin_amdgcn_readfirstlane((unsigned)(tag >> 32)) << 32;
    __builtin_amdgcn_s_waitcnt(0xC07F);
    TS(13);
    unsigned fst = fb + F_ST, sv = 0;
    auto step = [&](int sl, bool pf) INL {
#pragma unroll
        for (int hf = 0; hf < 2; ++hf)
#pragma unroll
            for (int i = 0; i < 16; ++i) {
                const int nn = SNK && (i & 4) ? 3 - (i & 3) : i & 3;
                acc[i >> 2][nn] = mfb(af[1 - hf][i >> 2], bf_[1 - hf][nn], acc[i >> 2][nn]);
                constexpr int RD[16] = RDPAT;
                const int r = RD[i];
                if (r == 0) af[hf][0] = lld<u32x4>(fp[(sl & 2) + hf] + (sl & 1) * A_STAGE);
                else if (r > 0 && r < 5) bf_[hf][r - 1] = lld<u32x4>(fp[4 + (sl & 2) + hf] + (sl & 1) * A_STAGE + (r - 1) * 2048);
                else if (r >= 5) af[hf][r - 4] = lld<u32x4>(fp[(sl & 2) + hf] + (sl & 1) * A_STAGE + (r - 4) * 2048);
                if (pf && hf && i == 11) sv = *(volatile LAS unsigned*)(lptr)(uintptr_t)fst;
                SCHED();
            }
        LDS_WAIT();
    };
#pragma unroll 1
    for (int kt = 0; kt < 3; ++kt) {
#pragma unroll
        for (int i = 0; i < 4; ++i) { BAR(); step(i, false); }
    }
    PIN(fst);
    BAR(); step(0, false);
    BAR(); __builtin_amdgcn_s_setprio(PRIOE); step(1, true);
#pragma unroll
    for (int i = 0; i < 2; ++i) {
        unsigned ss = __builtin_amdgcn_readfirstlane(sv);
        need_bit(fst, ss, 4u << (4 * i));
        TL(30 + i);
        if (i) need_bit(fst, ss, 0x100u);
        step(2 + i, i == 0);
    }
    __builtin_amdgcn_s_setprio(PRIOT);
    sig(fb + F_FREE, 1u << 20);
    float2 lam[2], coef[2], l16[2], l64[2], l512[2], cw[2], wq[2], lq[2];
#pragma unroll
    for (int n = 0; n < 2; ++n) {
        const f32x4 a = lld<f32x4>(smem + A_PRM + (wc * 32 + 2 * c + n) * 32), b = lld<f32x4>(smem + A_PRM + (wc * 32 + 2 * c + n) * 32 + 16);
        lam[n] = make_float2(a[0], a[1]); l16[n] = make_float2(a[2], a[3]); l64[n] = make_float2(b[0], b[1]); l512[n] = make_float2(b[2], b[3]);
    }
    float2 h[2];
#pragma unroll
    for (int m = 0; m < 5; ++m) {
#pragma unroll
        for (int n = 0; n < 4; ++n) {
            if (m < 4) acc[m][n] = mfb(af[1][m], bf_[1][n], acc[m][n]);
            if (m == 1 && n == 0) { h[0] = make_float2(acc[0][0][0], acc[0][2][0]); h[1] = make_float2(acc[0][1][0], acc[0][3][0]); }
            else if (m) {
#pragma unroll
                for (int i = 0; i < 2; ++i) h[i] = cfma(lam[i], h[i], make_float2(acc[m - 1][i][n], acc[m - 1][2 + i][n]));
            }
            if (SIL) SCHED();
        }
        if (m == 3) {
#pragma unroll
            for (int n = 0; n < 2; ++n) {
                const f32x4 a = lld<f32x4>(smem + A_PW + 4096 + (wc * 32 + 2 * c + n) * 16), b = lld<f32x4>(smem + A_PW + q * 1024 + (wc * 32 + 2 * c + n) * 16);
                coef[n] = make_float2(a[0], a[1]); cw[n] = wr ? make_float2(a[2], a[3]) : coef[n]; wq[n] = make_float2(b[0], b[1]); lq[n] = make_float2(b[2], b[3]);
            }
        }
    }
    TS(1);
    float2 loc[2], wag[2];
#pragma unroll
    for (int n = 0; n < 2; ++n) {
        float2 a0, a1, a2, a3;
        rows4(h[n].x, a0.x, a1.x, a2.x, a3.x);
        rows4(h[n].y, a0.y, a1.y, a2.y, a3.y);
        const float2 s2 = cfma(l16[n], a0, a1), s3 = cfma(l16[n], s2, a2);
        wag[n] = cfma(l16[n], s3, a3);
        loc[n] = sel4(q, make_float2(0.f, 0.f), a0, s2, s3);
    }
    lptr aggp = smem + A_AGG + (wc * 16 + c) * 16;
    const unsigned flg = ldsa(smem + A_FLG) + wc * 4;
    float2 W0[2] = {make_float2(0.f, 0.f), make_float2(0.f, 0.f)};
    if (wr == 0) {
        if (q == 0) lst<f32x4>(aggp, (f32x4){wag[0].x, wag[0].y, wag[1].x, wag[1].y});
        asm volatile("" ::: "memory");
        if (lane == 0) lds_add(flg);
    } else {
#pragma unroll 1
        for (int spin = 0; spin < (1 << 20); ++spin) { if (lds_peek(flg)) break; }
        const f32x4 w0 = lld<f32x4>(aggp);
        W0[0] = make_float2(w0[0], w0[1]); W0[1] = make_float2(w0[2], w0[3]);
        const float2 g0 = cfma(l64[0], W0[0], wag[0]), g1 = cfma(l64[1], W0[1], wag[1]);
        if (lt < 15 && q == 0) {
            auto tb_ = [&](float v, unsigned i) INL { return __builtin_amdgcn_perm(__float_as_uint(v), (unsigned)tag, 0x07060500u + i); };
            st128<SC1>((u32x4){tb_(g0.x, 0), tb_(g0.y, 1), tb_(g1.x, 2), tb_(g1.y, 3)}, rs, c * 16 + OFF_GRAN + ((15 - lt) * 512 + pt * 32 + wc * 16) * 16);
        }
    }
    TS(2);
    if (CE || CGW) sig(fst, 0x1000u);
    __builtin_amdgcn_s_setprio(PRIOW);
    const int da = da_of(pt), kh = da & 1;
    u32x2 xi[8], dk[2];
    auto xload = [&]() INL {
        const unsigned co = (256 * ((lane >> 3) & 1) + 4 * (lane & 7)) * 2;
        SCHED();
#pragma unroll
        for (int i = 0; i < 8; ++i)
            xi[i] = __builtin_amdgcn_raw_buffer_load_b64(mkrsrc(x, S_ * D_ * 2), (lane >> 4) * 64 * D_ * 2 + co + (i >> 2) * 1024, ((512 * wid + 256 * kh + 16 * (i & 3) + lt) * D_ + 32 * (da >> 1)) * 2, 0);
#pragma unroll
        for (int i = 0; i < 2; ++i) dk[i] = __builtin_amdgcn_raw_buffer_load_b64(mkrsrc(Dskip, D_ * 2), co + i * 1024, 64 * (da >> 1), 0);
        SCHED();
    };
    if (XP == 0) xload();
#pragma unroll
    for (int n = 0; n < 2; ++n) {
        PIN(coef[n].x); PIN(coef[n].y);
        loc[n] = cmul(coef[n], cfma(wq[n], W0[n], loc[n]));
        wq[n] = cmul(wq[n], cw[n]);
        PIN(wq[n].x); PIN(wq[n].y); PIN(loc[n].x); PIN(loc[n].y);
    }
    float2 u[2][16];
#pragma unroll
    for (int t = 0; t < 16; ++t) {
#pragma unroll
        for (int n = 0; n < 2; ++n) { u[n][t] = cmul(coef[n], make_float2(acc[t >> 2][n][t & 3], acc[t >> 2][2 + n][t & 3])); PIN(u[n][t].x); PIN(u[n][t].y); }
        SCHED();
    }
    lptr tb = smem + A_HS + wid * 2048 + lane * 4, rb = smem + A_HS + wid * 2048 + (lane >> 4) * 256 + (lane & 3) * 64 + ((lane >> 2) & 3) * 16;
    unsigned hoff = OFF_H + ((lane >> 4) * 64 + pt * 2 + wc) * 8192 + lt * 512 + wr * 256 + (lane & 15) * 16;
    PIN(tb); PIN(rb); PIN(hoff);
    __builtin_amdgcn_s_setprio(PRIOT);
    await<0>(fb + F_FREE, 8u << 20);
    if (XP == 1) xload();
    if (GDL) __builtin_amdgcn_s_sleep(GDL);
    TS(3);
    {
        u32x4 gr[4];
        unsigned off = ((16 - lt + q) * 512 + pt * 32 + wc * 16 + c) * 16;
        auto gload = [&]() INL {
            PIN(off);
#pragma unroll
            for (int j = 0; j < 4; ++j) gr[j] = __builtin_amdgcn_raw_buffer_load_b128(mkrsrc(ws + OFF_GRAN + j * 32768, (4 - j) * 32768), off, 0, SC1);
        };
        auto gok = [&]() INL {
            bool ok = true;
#pragma unroll
            for (int j = 0; j < 4; ++j) {
                const unsigned t = __builtin_amdgcn_perm(__builtin_amdgcn_perm(gr[j].w, gr[j].z, 0x04000400u), __builtin_amdgcn_perm(gr[j].y, gr[j].x, 0x04000400u), 0x05040100u);
                ok = ok & ((lt - 1 - (4 * j + q) < 0) | (t == (unsigned)tag));
            }
            return (bool)__all(ok);
        };
        gload();
        if (XP == 4) xload();
        if (!gok()) {
            int spin;
#pragma unroll 1
            for (spin = 1; spin < (1 << 20); ++spin) {
                asm volatile("" ::: "memory");
                if (GSL) __builtin_amdgcn_s_sleep(GSL);
                gload();
                if (gok()) break;
            }
        }
        TS(25);
        if (XP == 5) xload();
#pragma unroll
        for (int j = 0; j < 4; ++j) gr[j] &= 0xffffff00u;
#pragma unroll
        for (int n = 0; n < 2; ++n) {
            float2 cs = make_float2(__uint_as_float(gr[3][2 * n]), __uint_as_float(gr[3][2 * n + 1]));
#pragma unroll
            for (int j = 2; j >= 0; --j) cs = cfma(l512[n], cs, make_float2(__uint_as_float(gr[j][2 * n]), __uint_as_float(gr[j][2 * n + 1])));
            cs = cmul(cs, lq[n]);
            cs.x = rowsum4(cs.x); cs.y = rowsum4(cs.y);
            loc[n] = cfma(wq[n], cs, loc[n]);
        }
    }
    TS(4);
#pragma unroll
    for (int r = 0; r < 4; ++r) {
#pragma unroll
        for (int t = 0; t < 4; ++t) {
            loc[0] = cfma(lam[0], loc[0], u[0][4 * r + t]); loc[1] = cfma(lam[1], loc[1], u[1][4 * r + t]);
            lst<unsigned>(tb + t * 256, pkb(loc[0].x, loc[1].x));
            lst<unsigned>(tb + 1024 + t * 256, pkb(-loc[0].y, -loc[1].y));
        }
        asm volatile("" ::: "memory");
#pragma unroll
        for (int pl = 0; pl < 2; ++pl) st128<SC1>(lld<u32x4>(rb + pl * 1024), rs, hoff + (r * 256 + pl * 32) * 8192);
        asm volatile("" ::: "memory");
    }
    TS(5);
    PIN(lane);
    auto cp2 = [&]() INL { if (wid == 3) cpf = __builtin_amdgcn_raw_buffer_load_b32(mkrsrc((const void*)pc, max(len, 0)), lane * 128, ((gb >> 3) % CPM) * 8192, 0); };
    if (XLP == 0) cp2();
    if (XP == 2) xload();
    wait_vm<XP == 2 ? 10 : 0>();
    TS(6);
    const bool pub = sig_rtn(fb + F_HC) == 3u;
    const u32x2 tg = {(unsigned)tag, (unsigned)(tag >> 32)};
    if (pub) {
        if (lane < HREP) __builtin_amdgcn_raw_buffer_store_b64(tg, rs, lane * 256 * FST, OFF_FH + (pt * 16 + lt) * FST, SC1);
        TS(15);
    }
    if (XLP == 1) cp2();
    if (XP == 3 || XP == 6) xload();
    __builtin_amdgcn_s_setprio(PRIOI);
    f32x4 ac[8][2];
    auto acin = [&]() INL {
#pragma unroll
        for (int i = 0; i < 8; ++i) {
            PIN(xi[i]);
            const u32x2 d = dk[i >> 2], v = xi[i];
            const unsigned p0 = pkb(lo2f(v.x) * (0.5f * lo2f(d.x)), hi2f(v.x) * (0.5f * hi2f(d.x))), p1 = pkb(lo2f(v.y) * (0.5f * lo2f(d.y)), hi2f(v.y) * (0.5f * hi2f(d.y)));
            ac[i & 4][1][i & 3] = lo2f(p0); ac[(i & 4) + 1][1][i & 3] = hi2f(p0); ac[(i & 4) + 2][1][i & 3] = lo2f(p1); ac[(i & 4) + 3][1][i & 3] = hi2f(p1);
            ac[i][0] = Z4;
        }
#pragma unroll
        for (int i = 0; i < 16; ++i) PIN(ac[i >> 1][i & 1]);
    };
    if (ACI == 0) acin();
    unsigned vo[2], so[32];
    vo[0] = ((lane & 15) >> 2) * 256 + (lane >> 4) * 64 + (lane & 3) * 16 + (kh ^ 1) * 1024; vo[1] = vo[0] ^ 1024;
    PIN(vo[0]); PIN(vo[1]);
#pragma unroll
    for (int k = 0; k < 32; ++k) {
        so[k] = OFF_H + lt * 524288 + kh * 131072 + wid * 2048 + (((XE & 2) ? 0 : k >> 2) ^ CX(gb)) * 16384 + ((k & 1) + (k & 2) * 16) * 8192;
        if (SPC || k < 8) asm volatile("" : "+s"(so[k]));
    }
    auto ldB = [&](int b) { return __builtin_amdgcn_raw_buffer_load_b128(rs, vo[b_n(b)], so[st_k(b_st(b))], SC1); };
    lptr ab[4];
#pragma unroll
    for (int i = 0; i < 4; ++i) { ab[i] = smem + (1 - (i >> 1)) * 2 * A_STAGE + (lane & 15) * 128 + (((4 * (i & 1) + (lane >> 4)) ^ (lane & 7)) << 4); PIN(ab[i]); }
    auto ldA = [&](int f) {
        const int k = st_k(f >> 3);
        return lld<u32x4>(ab[(k & 1) + ((k >> 2) & 2)] + ((k >> 1) & 3) * 16384 + (f & 7) * 2048);
    };
    unsigned fv = fb + F_FULL; PIN(fv);
    asm volatile("" :: "v"(cpf));
    __builtin_amdgcn_s_setprio(PRIOG);
    TS(27);
    u32x4 ba = {xi[0].x, xi[1].y, xi[2].x, xi[3].y}, bb = {xi[4].x, xi[5].y, xi[6].x, xi[7].y};
    if (pub && !HPL) {
        int spin; unsigned m = 0;
        if (BAL >= 0) burn<0>(PDL, ba, bb); else
        if (PDL) __builtin_amdgcn_s_sleep(PDL);
        if (PDE && !(PDM >> (gb & 7) & 1)) __builtin_amdgcn_s_sleep(PDE);
        if (PD0 && lt == 0) __builtin_amdgcn_s_sleep(PD0);
        TS(19);
#pragma unroll 1
        for (spin = 0; spin < (1 << 20); ++spin) {
            m = h_poll<NP1>(rs, gb, kh, lane, tg);
            if (spin == 0) TS(16);
            if (spin == 1) TS(17);
            if (spin == 2) TS(18);
            if ((m & HFIRST) == HFIRST) break;
        }
        sig_or(fb + F_FULL, m);
    }
    unsigned fe = 0, fc = 0, fs = BAL >= 0 ? await_burn<BAL < 0 ? 0 : BAL>(fb + F_FULL, 8u, ba, bb) : await<1>(fb + F_FULL, 8u);
    TS(7);
    auto need = [&](unsigned bit) INL { need_bit(fv, fs, bit); };
    u32x4 A[AD], B[64];
#pragma unroll
    for (int b = 0; b < NB; ++b) { if (b && b_new(b)) need(8u << (b_st(b) & 28)); B[b] = ldB(b); SCHED(); }
    SCHED();
    if (ACI == 1) { acin(); SCHED(); }
    need(4u);
#pragma unroll
    for (int f = 0; f < AD; ++f) A[f] = ldA(f);
    SCHED();
    if (BSL) { for (int i = 0; i < BSL; ++i) __builtin_amdgcn_s_sleep(16); }
    if (BA2) burn<BS2>(BA2, A[0], A[1]);
    const unsigned po = OFF_U + ((lt * 16 + da) * 4 + wid) * 4096;
    unsigned pk[16], oo[4], fr, pa, t0, t1;
    const rsrc_t ro = mkrsrc(out, S_ * D_ * 4);
    lptr og, rp[2];
    u32x4 pv[4], sl[2]; u32x2 ga[8]; f32x4 o2, r0[8];
    auto put = [&](int i) INL {
        const int e = i & 3, m = i & 4;
#pragma unroll
        for (int j = 0; j < 4; ++j) r0[i][j] = fmaf(o2[e], ac[m + j][1][e], r0[i][j]);
        st128<WTO>(__builtin_bit_cast(u32x4, r0[i]), ro, oo[e] + 512 * m);
    };
    sfor<0, NST>([&](auto ic) INL {
        constexpr int st = decltype(ic)::v, b0 = st < KS1 ? 2 * st : st + KS1, n0 = st >= 32;
        auto nextB = [&](int b) INL {
            if (b_nx(b) < 0) return;
            if (b_new(b_nx(b))) { fs = __builtin_amdgcn_readfirstlane(fe); need(8u << (b_st(b_nx(b)) & 28)); TL(20 + (b_st(b_nx(b)) >> 2)); }
            B[b_nx(b)] = ldB(b_nx(b));
        };
#pragma unroll
        for (int k = 0; k < 8; ++k) {
            const int f = 8 * st + k;
            constexpr int WG = st < KS1 ? WG1 : WG2;
            if (WG > 1 && k % WG == 0 && f + AD < 8 * NST) { __builtin_amdgcn_s_waitcnt(0xC07F | (AD - WG) << 8); SCHED(); }
            MF2(B[b0], A[f % AD], ac[k][n0]);
            SCHED();
            if (k == (st < KS1 ? 3 : 0) && b_nx(b0) >= 0 && (b_new(b_nx(b0)) || (st < KS1 && b_new(b_nx(b0 + 1))))) fe = *(volatile LAS unsigned*)(lptr)(uintptr_t)fv;
            if (k == 7) nextB(b0);
            if (k == 1 && st > 0 && st <= KS1) nextB(b0 - 1);
            if (f == 242) fe = *(volatile LAS unsigned*)(lptr)(uintptr_t)(fv + F_TR - F_FULL);
            if (f == 250) { fs = __builtin_amdgcn_readfirstlane(fe); need_bit(fv + F_TR - F_FULL, fs, 0x8000u); }
            if (f == 251) { PIN(lane); pa = lane * 16; t0 = pa + po; PIN(pa); PIN(t0); }
            if (f >= 252 && f < 260) {
                const int m = f - 252;
                pk[2 * m] = pkb(ac[m][0][0], ac[m][0][1]); pk[2 * m + 1] = pkb(ac[m][0][2], ac[m][0][3]);
                if (!(m & 1)) pk[2 * m] = __builtin_amdgcn_perm(pk[2 * m], __float_as_uint(ac[m][0][0]), 0x07060302u) | 1u;
                PIN(pk[2 * m]); PIN(pk[2 * m + 1]);
                if (m & 1) st128<SC1>((u32x4){pk[2 * m - 2], pk[2 * m - 1], pk[2 * m], pk[2 * m + 1]}, rs, t0 + (m >> 1) * 1024);
                if (m == 7) TS(29);
            }
            if (f == 262) { t0 = lane >> 4; t1 = (lane >> 2) & 3; PIN(t0); PIN(t1); }
            if (f == 263) { oo[0] = (pa & 0x70) | t0 << 18; PIN(oo[0]); }
            if (f == 264) { oo[0] |= (lane & 8) << 7; PIN(oo[0]); }
            if (f == 275) { oo[0] += ((512 * wid + 256 * kh + lt) * D_ + 32 * (da >> 1)) * 4; PIN(oo[0]); }
            if (f > 275 && f < 279) { oo[f - 275] = oo[0] + (f - 275) * 16 * D_ * 4; PIN(oo[f - 275]); }
            if (f == 265) { og = smem + A_OG + 64 * wid + (lane & 48); fr = fb + F_R0 + wid * 4; PIN(og); PIN(fr); }
            if (f == 266) { sl[0].x = t0 == t1 ? 0x3f80u : 0u; PIN(sl[0].x); }
            if (f == 267) { sl[0].x <<= pa & 16; PIN(sl[0].x); }
            if (f == 268) { sl[0].y = (lane & 2) ? sl[0].x : 0u; sl[0].x ^= sl[0].y; PIN(sl[0].x); PIN(sl[0].y); }
            if (f == 269) { sl[0].z = 0; sl[0].w = 0; PIN(sl[0]); }
            if (f == 270) { sl[1] = (u32x4){0, 0, sl[0].x, sl[0].y}; PIN(sl[1]); }
            if (f == 271) { t1 = (t1 ^ (2 * t0)) & 3; PIN(t1); }
            if (f == 272) { t0 = t0 * 512 + (lane & 3) * 8; PIN(t0); }
            if (f == 273) { t0 += t1 << 5; rp[0] = smem + A_XT + wid * 4096 + t0; PIN(t0); PIN(rp[0]); }
            if (f == 274) { rp[1] = smem + A_XT + wid * 4096 + 256 + (t0 ^ 32); PIN(rp[1]); }
            if (st == RST && k < 4) pv[k] = __builtin_amdgcn_raw_buffer_load_b128(rs, pa + k * 1024, po ^ 16384, SC1);
            if (k == 4 && st >= PZA && st < PZB) { constexpr int z = st < KS1 ? PZ1 : st < 32 ? PZ2 : PZ3; if (z) asm volatile(".rept %0\n\ts_nop 15\n\t.endr\n\ts_nop %1" :: "i"((z - 1) / 16), "i"((z - 1) % 16)); }
            constexpr int GF = 8 * NST - 4 - 16 * GSP;
            if (f == GF - 16) fe = *(volatile LAS unsigned*)(lptr)(uintptr_t)fr;
            if (f == GF - 8) { fs = __builtin_amdgcn_readfirstlane(fe); need_bit(fr, fs, 1u); TS(26); }
            if (f >= GF - 8 && f < GF - 4) {
#pragma unroll
                for (int i = 2 * (f - GF + 8); i < 2 * (f - GF + 9); ++i) ga[i] = lld<u32x2>(rp[(i >> 1) & 1] + (i >> 2) * 2048 + (i & 1) * 128);
            }
            if (f == GF - 4) o2 = lld<f32x4>(og);
            if (f >= GF && f < GF + 16 * GSP && (f - GF) % GSP == 0) {
                const int j = (f - GF) / GSP;
                const unsigned v = ga[j >> 1][j & 1];
                float lo = lo2f(v), hi = hi2f(v);
                PIN(lo); PIN(hi);
                r0[j >> 1][2 * (j & 1)] = lo; r0[j >> 1][2 * (j & 1) + 1] = hi;
            }
            SCHED();
            if (st < KS1) { MF2(B[b0 + 1], A[f % AD], ac[k][1]); SCHED(); }
            if (f + AD < 8 * NST) {
                const int g = f + AD;
                if (!(g & 31) && g < 256) { if (g > 64) fs = __builtin_amdgcn_readfirstlane(fc); need(4u << (g >> 5) * 4); }
                if (!((g + AD) & 31) && g + AD < 256 && g + AD > 64) fc = *(volatile LAS unsigned*)(lptr)(uintptr_t)fv;
                A[f % AD] = ldA(g);
                if ((g & 31) == 31 && st_k(g >> 3) < 16 && ((g >> 3) < KS1 || g >= 256)) sig_o<F_FREE - F_FULL>(fv, 1u << (st_k(g >> 3) & 28));
            }
            SCHED();
            if (st == NST - 1 && (k & 3) == 3) {
                if (k == 3) {
                    TS(10);
                    unsigned n;
                    asm volatile("s_mov_b32 %5, 0x100000\n"
                                 "1:\ts_waitcnt vmcnt(0)\n\tv_and_b32 %4, v" PV0 ", v" PV1 "\n\tv_and_b32 %4, v" PV2 ", %4\n\tv_and_b32 %4, v" PV3 ", %4\n\tv_and_b32 %4, 1, %4\n\t"
                                 "v_cmp_eq_u32 vcc, 0, %4\n\ts_cbranch_vccz 2f\n\t"
                                 "buffer_load_dwordx4 %0, %6, %7, %8 offen sc1\n\tbuffer_load_dwordx4 %1, %6, %7, %8 offen offset:1024 sc1\n\t"
                                 "buffer_load_dwordx4 %2, %6, %7, %8 offen offset:2048 sc1\n\tbuffer_load_dwordx4 %3, %6, %7, %8 offen offset:3072 sc1\n\t"
                                 "s_sub_u32 %5, %5, 1\n\ts_cbranch_scc0 1b\n\ts_waitcnt vmcnt(0)\n"
                                 "2:" : "+{v[" PV0 ":" PV0E "]}"(pv[0]), "+{v[" PV1 ":" PV1E "]}"(pv[1]), "+{v[" PV2 ":" PV2E "]}"(pv[2]), "+{v[" PV3 ":" PV3E "]}"(pv[3]), "=&v"(t0), "=&s"(n)
                                 : "v"(pa), "s"(mkdesc(ws, WS_END)), "s"(po ^ 16384) : "memory", "scc", "vcc");
                    TS(11);
                    SCHED();
                }
#pragma unroll
                for (int m = k - 3; m <= k; ++m) { MF2(sl[m & 1], pv[m >> 1], ac[m][1]); SCHED(); if (k == 7) { put(m - 4); SCHED(); } }
            }
        }
        if (st == 0) { TS(8); TC(60); }
        if (st == KS1 - 1) { TS(9); TC(55); }
        if (st == 31) { TS(28); TC(56); }
        if (st == NST - 3) { TS(14); TC(61); }
    });
    asm volatile("s_nop 7");
    SCHED();
#pragma unroll
    for (int i = 4; i < 8; ++i) { put(i); SCHED(); }
    asm volatile("gm_end0:");
    TS(12);
    TSE(0, wid == 0);
    TSE(2, pub);
}

#define TSARG
#define TSPASS
struct Gate {
    unsigned xa, wa, xv, gw; float sum; int rot;
    DEV void rd(int kt) { asm volatile("ds_read_b32 %0, %2\n\tds_read_b32 %1, %3" : "=&v"(xv), "=&v"(gw) : "v"(xa + (kt & 3) * A_STAGE), "v"(wa + (kt ^ rot) * 128) : "memory"); }
    DEV void acc() {
        asm volatile("s_waitcnt lgkmcnt(0)" : "+v"(xv), "+v"(gw) :: "memory");
        const unsigned pr = pkb(lo2f(xv) * lo2f(gw), hi2f(xv) * hi2f(gw));
        sum += lo2f(pr); sum += hi2f(pr); PIN(sum);
    }
};
struct Tab { CM sf, si, ff, fi; u32x4 mt[2], xf, xb; f32x2 ti[2]; };
DEV void mid_tables(lptr smem, int gb, int w, int lane, Tab& t) {
    int mp, j1; slot_of(gb, mp, j1);
    const int c = lane & 15, q = lane >> 4;
    lptr mb = smem + A_MAT + lane * 16;
#pragma unroll
    for (int i = 0; i < 2; ++i) { t.ti[i] = lld<f32x2>(smem + A_MAT + 9216 + (2 * w + i) * 128 + c * 8); t.mt[i] = lld<u32x4>(mb + (6 + i) * 1024); }
    u32x4 sf0 = lld<u32x4>(mb + 4096), si0 = lld<u32x4>(mb + 5120);
    const u32x4 f0 = lld<u32x4>(mb + 8192);
    SCHED();
    asm volatile("" : "=v"(t.xf), "=v"(t.xb));
    if (w >= 4 && mp == 0 && !(j1 & 1)) {
        sf0 = mat_sf(64, c, q); si0 = mat_si(64, c, q);
        const float dt = -(float)(32 + 64 * c) * (1.0f / 1024.0f);
        t.xf = cmat1(dt * (float)(4 * q), dt); t.xb = cmat1((float)(c * (32 + 256 * q)) * (1.0f / 1024.0f), (float)c * (1.0f / 16.0f));
    }
    PIN(t.xf); PIN(t.xb);
    t.sf = cexp(sf0); t.si = cexp(si0); t.ff = cexp(f0); t.fi = cexp((u32x4){f0.x ^ SG, f0.y ^ SG, f0.z, f0.w});
    PIN(t.mt[0]); PIN(t.mt[1]); PIN(t.ti[0]); PIN(t.ti[1]);
}
DEV int u_exp(unsigned em) { return min(max((int)(em & 0xffu), 4), 254); }

DEV void role_load(const u16* __restrict__ x, const u16* __restrict__ Bre, const u16* __restrict__ Bim, const u16* __restrict__ Cre, const u16* __restrict__ Cim,
                   const u16* __restrict__ gate_w, char* ws, lptr smem, int gb, int lw, int lane, Tab& tab TSARG) {
    __builtin_amdgcn_s_waitcnt(0x0F70);
    Gate g;
    int lt, pt; tile_of(gb, lt, pt);
    const int srow = lane >> 3, schunk = (lane & 7) ^ srow;
    const int rot = ROT;
    const u32x4 rx = mkdesc(x, S_ * D_ * 2), rb = mkdesc((lw & 1) ? Bim : Bre, D_ * D_ * 2);
    const unsigned lb = ldsa(smem);
    const unsigned vA = (16 * (srow >> 2) + (srow & 3)) * 2048 + schunk * 16, sA = (lt * 128 + 64 * (lw >> 1) + 8 * (lw & 1)) * 2048;
    const unsigned vB = 2 * srow * 2048 + schunk * 16, sB = (pt * 64 + 32 * (lw >> 1)) * 2048;
    auto stA = [&](int kk) {
#pragma unroll
        for (int i = 0; i < 4; ++i)
            dma16(rx, lb + (kk & 3) * A_STAGE + lw * 4096 + i * 1024, vA, sA + (32 * (i & 1) + 4 * (i >> 1)) * 2048 + (kk ^ rot) * 128);
    };
    auto stB = [&](int kk) {
#pragma unroll
        for (int i = 0; i < 4; ++i)
            dma16(rb, lb + (kk & 3) * A_STAGE + 16384 + lw * 4096 + i * 1024, vB, sB + (16 * (i & 1) + (i >> 1)) * 2048 + (kk ^ rot) * 128);
    };
    TS(0);
    __builtin_amdgcn_s_setprio(PRIOL);
    stA(0); stB(0);
    dma16(mkdesc(gate_w, D_ * 2), lb + A_GW + (lw & 1) * 1024, lane * 16, (lw & 1) * 1024);
#pragma unroll
    for (int i = 0; i < 4; ++i)
        dma16(rx, lb + A_XT + lw * 4096 + i * 1024, (lane >> 1) * 32768 + (lane & 1) * 16, (i * 512 + lt) * 2048 + lw * 512 + pt * 32);
    const int gi = 8 * pt + 2 * lw + (lane >> 5);
    g.xa = lb + gi * 128 + ((((lane & 31) >> 2) ^ (gi & 7)) << 4) + (lane & 3) * 4;
    g.wa = lb + A_GW + (lane & 31) * 4; g.sum = 0.f; g.rot = rot;
    stA(1); stB(1); stA(2); stB(2);
    TS(6);
    const int c = lane & 15, q = lane >> 4;
    const unsigned fb = lb + A_FLG;
    lptr ub = smem + A_XT + lw * 4096 + (lane >> 2) * 64 + (lane & 3) * 8, ea = smem + A_E1 + c * E1P + q * 256 + lw * 64;
    lptr mb = smem + A_MAT + lane * 16, tb = smem + A_MAT + 9216 + c * 128 + q * 32;
    PIN(ub); PIN(ea); PIN(mb); PIN(tb);
#pragma unroll 1
    for (int kt = 0; kt < 13; ++kt) { wait_vm<16>(); TL(8 + kt); BAR(); TL(28 + kt); if (kt == 0) { TS(28); TC(60); } g.rd(kt); stA(kt + 3); stB(kt + 3); g.acc(); }
    u32x4 mt[4], ut[4];
    f32x4 tf[2];
    wait_vm<16>(); TL(21); BAR(); TL(41); g.rd(13);
#pragma unroll
    for (int i = 0; i < 4; ++i) mt[i] = lld<u32x4>(mb + i * 1024);
#pragma unroll
    for (int i = 0; i < 2; ++i) tf[i] = lld<f32x4>(tb + i * 16);
    g.acc();
#pragma unroll
    for (int i = 0; i < 4; ++i) PIN(mt[i]);
    PIN(tf[0]); PIN(tf[1]);
    wait_vm<8>(); sig(fb + F_ST, 1u); TL(42);
    wait_vm<0>(); sig(fb + F_ST, 0x10u);
    __builtin_amdgcn_s_setprio(PRIOD);
    await<0>(fb + F_ST, 0x40u);
    TS(43); TC(61);
    g.rd(14); g.acc(); g.rd(15); g.acc();
    sig(fb + F_FREE, 1u << 20);
    TS(2);
    __builtin_amdgcn_s_setprio(PRIOM);
    mid_tables(smem, gb, lw + 4, lane, tab);
    float gg = bfr(1.0f / (1.0f + expf(-bfr(half_sum(g.sum))))), og = bfr(1.0f - gg);
    const int tau = (gi & 64) | ((gi & 12) << 2) | ((gi & 48) >> 2) | (gi & 3);
    unsigned go = (lane & 31) ? ~0u : OFF_G + (tau & 15) * 1024 + (lt * 8 + (tau >> 4)) * 8;
    PIN(gg); PIN(og); PIN(go);
    __builtin_amdgcn_s_setprio(PRIOD);
    TS(53);
    const unsigned em = await<1>(fb + F_EM, 0x10000u << lw);
    TS(3);
#pragma unroll
    for (int i = 0; i < 4; ++i) ut[i] = trfrag(ub + i * 1024, 32);
    const float su = __uint_as_float((unsigned)(258 - u_exp(em)) << 23);
    tf[0] *= su; tf[1] *= su; PIN(tf[0]); PIN(tf[1]);
#pragma unroll
    for (int i = 0; i < 4; ++i) {
        f32x4 r = mfb(ut[i], mt[2], Z4), m = mfb(ut[i], mt[3], Z4);
        r = mfb(ut[i], mt[0], r); m = mfb(ut[i], mt[1], m);
        f32x4 yr, yi;
#pragma unroll
        for (int e = 0; e < 4; ++e) {
            const float tc = tf[e >> 1][2 * (e & 1)], ts = tf[e >> 1][2 * (e & 1) + 1];
            yr[e] = fmaf(r[e], tc, -m[e] * ts); yi[e] = fmaf(r[e], ts, m[e] * tc);
        }
        lst<u32x4>(ea + i * 16, pk8(yr, yi));
    }
    if (gb == XG) {
        const unsigned sg = 0xbf803f80u;
#pragma unroll
        for (int i = 0; i < 4; ++i) {
            const f32x4 e = mfb(ut[i], c == 0 ? (u32x4){sg, sg, 0, 0} : (u32x4){0, 0, sg, sg}, Z4);
            if (c < 2) lst<u32x4>(ea + 16 * E1P + i * 16, pk8(e * su, Z4));
        }
    }
    sig(fb + F_MID, 1u);
    TS(54);
    __builtin_amdgcn_s_setprio(PRIOM);
    if (C0L < 2) await<1>(fb + F_FREE, 8u << 20);
    if (CGW) await<1>(fb + F_ST, 0x4000u);
    if (C0L < 2) c_chunk<C0S>(Cre, Cim, lb, gb, da_of(pt), lw, lane, 0);
    if (C0L < 1) c_chunk<C0S>(Cre, Cim, lb, gb, da_of(pt), lw, lane, 1);
    __builtin_amdgcn_raw_buffer_store_b64((u32x2){__float_as_uint(gg), __float_as_uint(og)}, mkrsrc(ws, WS_END), go, 0, SC1);
    TS(52);
}

DEV unsigned ldw(rsrc_t r, unsigned vo, unsigned so) { return __builtin_amdgcn_raw_buffer_load_b32(r, vo, so, 0); }
DEV unsigned amax4(u32x4 p) { return pkmax(pkmax(p.x & 0x7fff7fffu, p.y & 0x7fff7fffu), pkmax(p.z & 0x7fff7fffu, p.w & 0x7fff7fffu)); }

DEV void role_aux(const u16* __restrict__ w_r, const u16* __restrict__ w_i, const u16* __restrict__ lam_re, const u16* __restrict__ lam_im,
                  const u16* __restrict__ log_dt, char* ws, lptr smem, int gb, int aw, int lane, Tab& tab TSARG) {
    int lt, pt; tile_of(gb, lt, pt);
    int mp, j1; slot_of(gb, mp, j1);
    const int c = lane & 15, q = lane >> 4;
    const bool spc = mp == 0 && !(j1 & 1);
    const rsrc_t rs = mkrsrc(ws, WS_END), rr = mkrsrc(w_r, S_ * KF * 2), ri = mkrsrc(w_i, S_ * KF * 2);
    TS(0);
    __builtin_amdgcn_s_setprio(PRIOA);
    u32x4 fch, fsh;
    const bool m0 = 16 * aw + c == 0;
    {
        const float th = (float)(16 * aw + c) * (1.0f / 128.0f);
#pragma unroll
        for (int i = 0; i < 4; ++i) {
            const float t0 = __builtin_amdgcn_fractf(th * (float)(16 * (i >> 1) + 2 * (i & 1) + 4 * q)), t1 = t0 + th;
            const float c0 = __builtin_amdgcn_cosf(t0), c1 = __builtin_amdgcn_cosf(t1);
            const float s0 = m0 ? 1.f : -__builtin_amdgcn_sinf(t0), s1 = m0 ? -1.f : -__builtin_amdgcn_sinf(t1);
            fch[i] = pkb(c0, c1); fsh[i] = pkb(s0, s1);
        }
        PIN(fch); PIN(fsh);
    }
    unsigned sg64 = (c & 1) ? SG : 0u, sgA = (c & 2) ? SG : 0u, sgB = ((c + 1) & 2) ? SG : 0u;
    constexpr unsigned WR = 128 * KF * 2;
    const bool xh = gb == XG && aw >= 2;
    const int c0 = 2 * (lane & 7) + xh, jb = (lane >> 3) + 4 * (aw & 2);
    const int k1 = mp ? mp : spc ? (lane & 4) * 16 : (j1 == 1 ? 0 : 64);
    const int kd = j1 + 4 * (spc ? c0 & 7 : c0) + 64 * jb;
    unsigned wo = (aw < 2 ? (k1 * KF + kd) * 2 + aw * 8 * WR : ((128 - k1) * KF + 1020 - kd) * 2 + (3 - aw) * 8 * WR) & ~3u;
    unsigned wo0 = aw < 2 ? wo : k1 || aw == 3 ? wo + 7 * WR : wo - 9 * WR;
    unsigned uoff = (lane >> 3) * 131072 + (lane & 7) * 16 + OFF_U + aw * 64 * 16384 + lt * 2048 + pt * 128;
    PIN(sg64); PIN(sgA); PIN(sgB); PIN(wo); PIN(wo0); PIN(uoff);
    unsigned lru, liu, ldu, um = 0;
    u32x2 tag;
    u32x4 fl[2];
    asm volatile("" : "=v"(fl[0]), "=v"(fl[1]));
    u32x3 wre[8], wim[8];
    u32x4 wsc = {0, 0, 0, 0};
    f32x4 vr[4], vi[4];
    float dt = 0.f, er = 0.f; float2 lam = make_float2(0.f, 0.f);
#pragma unroll
    for (int d1 = 0; d1 < 4; ++d1) { vr[d1] = Z4; vi[d1] = Z4; }
    auto wx = [&]() INL {
        if (j1 == 0 && aw == 3) {
            const int ks = (spc ? (lane >> 5) * 64 : mp) + 128 * c, ng = (S_ - ks) & (S_ - 1);
            unsigned o0 = (q & 1) ? (ng * KF + 511) * 2u : ks * KF * 2u, o1 = (q & 1) ? (ks * KF + 511) * 2u : ng * KF * 2u;
            PIN(o0); PIN(o1);
            wsc = (u32x4){ldw(rr, o0, 0), ldw(rr, o1, 0), ldw(ri, o0, 0), ldw(ri, o1, 0)};
        }
    };
    auto prm = [&]() INL {
        lru = ldw(mkrsrc(lam_re, 2048), (lane >> 1) * 4, pt * 128); liu = ldw(mkrsrc(lam_im, 2048), (lane >> 1) * 4, pt * 128);
        ldu = ldw(mkrsrc(log_dt, 2048), (lane >> 1) * 4, pt * 128);
    };
    if (PRS < 0) prm();
    const unsigned fb = ldsa(smem + A_FLG);
    bool uok = false;
    auto upub = [&]() INL { sig(fb + F_EM, (wave_pkmax(pkmax(pkmax(fl[0].x, fl[0].z), pkmax(fl[1].x, fl[1].z))) & 0xffu) | 0x8000u); };
    u32x4 uo[4];
    f32x4 pq[4];
    asm volatile("" : "=v"(pq[0]), "=v"(pq[1]), "=v"(pq[2]), "=v"(pq[3]));
    sfor<0, 14>([&](auto ic) INL {
        constexpr int kt = decltype(ic)::v;
        constexpr int GS[4] = GSTEP, KSO[4] = {0, 2, 3, 1}, JO[4] = {0, 2, 1, 3}, WRW[5] = WROW;
        auto ust = [&](int k) INL {
#pragma unroll
            for (int u = 0; u < 4; ++u) if (US + u / UPS == k && !(XM & 2)) st128<SC1>(uo[u], rs, uoff + JO[u] * 32768);
        };
        auto mem = [&]() INL {
            if (kt >= 10) {
#pragma unroll
                for (int r = 0; r < 8; ++r) {
                    if (r < WRW[kt < 10 ? 0 : kt - 10] || r >= WRW[kt < 10 ? 0 : kt - 9]) continue;
                    const unsigned so = r ? (aw < 2 ? r : 7 - r) * WR : 0u;
                    wre[r] = __builtin_amdgcn_raw_buffer_load_b96(rr, r ? wo : wo0, so, 0); wim[r] = __builtin_amdgcn_raw_buffer_load_b96(ri, r ? wo : wo0, so, 0);
                    if (r + 1 == URS) { SCHED(); wait_vm<2 * (URS - WRW[UCK - 10])>(); sig(fb + F_EM, 0x10000u << aw); TS(6); SCHED(); }
                }
            }
            if (kt == PRS) prm();
            if (kt == WXS) wx();
            if (kt == UPL && UPL < 13 && aw == 2) flag_get(rs, OFF_FU, gb, lane, fl);
        };
        if (kt == UFS) wait_vm<0>();
        if (kt == UFS + 1) { uoff = (lane >> 2) * 128 + (lane & 3) * 16; PIN(uoff); }
        if (kt == UCK && UPRE && aw == 2) { uok = flag_ok(fl, tag); if (uok) { upub(); LDS_WAIT(); } }
        TL(8 + kt);
        BAR();
        if (VL == 2) ust(kt - 1);
        if (kt == 13 && aw == 1) {
            lst<f32x4>(smem + A_PW + lane * 16, (f32x4){1.f, 0.f, 1.f, 0.f});
#pragma unroll
            for (int i = 0; i < 4; ++i) lst<f32x4>(smem + A_PW + (i + 1) * 1024 + lane * 16, pq[i]);
            sig(fb + F_ST, 0x100u);
        }
        if (kt == UCK) {
            if (aw == 2 && !uok) {
                int spin;
                if (UPL > 12) { if (UDL) __builtin_amdgcn_s_sleep(UDL); flag_get(rs, OFF_FU, gb, lane, fl); }
#pragma unroll 1
                for (spin = 0; spin < (1 << 20); ++spin) { if (flag_ok(fl, tag)) break; flag_get(rs, OFF_FU, gb, lane, fl); }
                upub();
            }
            await<0>(fb + F_EM, 0x8000u);
            const u32x4 rw = mkdesc(ws, WS_END);
#pragma unroll
            for (int i = 0; i < 4; ++i)
                dma16s(rw, ldsa(smem + A_XT) + aw * 4096 + i * 1024, uoff, OFF_U + ((mp >> 1) * 4 + j1) * 32768 + (mp & 1) * 64 + aw * 8192 + i * 2048);
            TS(2);
        }
        if (kt == ATS) mid_tables(smem, gb, aw, lane, tab);
        u32x4 xa[4][4];
#pragma unroll
        for (int g = 0; g < 4; ++g) {
            if (GS[g] != kt) continue;
#pragma unroll
            for (int i = 0; i < 4; ++i) xa[g][i] = trfrag(smem + A_XT + i * 4096 + KSO[g] * 1024 + lane * 8, 512);
        }
        if (VL != 1) mem();
        SCHED();
#pragma unroll
        for (int g = 0; g < 4; ++g) {
            if (GS[g] != kt) continue;
#pragma unroll
            for (int i = 0; i < 4; ++i) { vr[i] = mfb(xa[g][i], fch, vr[i]); vi[i] = mfb(xa[g][i], fsh, vi[i]); PIN(vr[i]); PIN(vi[i]); }
            if (g == 0 || g == 2) { fch ^= sg64; fsh ^= sg64; PIN(fch); PIN(fsh); }
            if (g == 1) {
                const bool od = c & 1;
                const u32x4 a = (od ? fsh : fch) ^ sgA, b = (od ? fch : fsh) ^ sgB;
                fch = a; fsh = b; PIN(fch); PIN(fsh);
            }
        }
        if (kt == B1S) {
            const f32x4 t0r = vr[0] + vr[2], t1r = vr[0] - vr[2], t2r = vr[1] + vr[3], t3r = vr[1] - vr[3];
            const f32x4 t0i = vi[0] + vi[2], t1i = vi[0] - vi[2], t2i = vi[1] + vi[3], t3i = vi[1] - vi[3];
            vr[0] = t0r; vr[1] = t1r; vr[2] = t2r; vr[3] = t3r; vi[0] = t0i; vi[1] = t1i; vi[2] = t2i; vi[3] = t3i;
#pragma unroll
            for (int i = 0; i < 4; ++i) { PIN(vr[i]); PIN(vi[i]); }
        }
#pragma unroll
        for (int u = 0; u < 4; ++u) {
            if (US + u / UPS != kt) continue;
            const int j = JO[u];
            f32x4 a, b;
            if (j == 0) { a = vr[0] + vr[2]; b = vi[0] + vi[2]; }
            if (j == 2) { a = vr[0] - vr[2]; b = vi[0] - vi[2]; }
            if (j == 1) { a = vr[1] + vi[3]; b = vi[1] - vr[3]; if (aw == 0) { if (m0) { a = vr[1]; b = -vr[3]; } } }
            if (j == 3) { a = vr[1] - vi[3]; b = vi[1] + vr[3]; if (aw == 0) { if (m0) { a = vi[1]; b = vi[3]; } } }
            uo[u] = quads(smem + A_XT + aw * 4096 + j * 1024, lane, (u32x2){pkb(a[0], a[1]), pkb(a[2], a[3])}, (u32x2){pkb(b[0], b[1]), pkb(b[2], b[3])});
            uo[u].x = (uo[u].x + ((uo[u].x >> 1) & 1u)) & ~1u;
            um = pkmax(um, amax4(uo[u]));
            PIN(um); PIN(uo[u]);
#pragma unroll
            for (int i = 0; i < 4; ++i) if (u < 3 && ((i & 1) || u == 0)) { PIN(vr[i]); PIN(vi[i]); }
        }
        if (kt == UFS - 1) {
            um = wave_pkmax(pkmax(um, um >> 16));
            if (lane == 0) lst<unsigned>(smem + A_FLG + F_MAX + aw * 4, um);
            tag = lld<u32x2>(smem + A_FLG + F_TAG); PIN(tag);
        }
        if (kt == P0S) {
            PIN(lru); PIN(liu); PIN(ldu);
            if (lane & 1) { lru >>= 16; liu >>= 16; ldu >>= 16; }
            PIN(lru); PIN(liu); PIN(ldu);
        }
        if (kt >= P0S && kt < P0S + 5 && aw == 1) {
            const float lr = lo2f(lru), li = lo2f(liu);
            if (kt == P0S) { dt = bfr(expf(lo2f(ldu))); er = expf(dt * lr); PIN(dt); PIN(er); }
            if (kt == P0S + 1) { float sn, cs; sincosf(dt * li, &sn, &cs); lam = make_float2(er * cs, er * sn); PIN(lam.x); PIN(lam.y); }
            if (kt == P0S + 2) {
                const float nr = lam.x - 1.0f, ni = lam.y, den = lr * lr + li * li;
                pq[3][0] = (nr * lr + ni * li) / den; pq[3][1] = (ni * lr - nr * li) / den;
                PIN(pq[3]);
            }
            if (kt == P0S + 3) {
                float2 l16 = lam;
#pragma unroll
                for (int i = 0; i < 4; ++i) l16 = cmul(l16, l16);
                const float2 l32 = cmul(l16, l16), l48 = cmul(l32, l16);
                lst<f32x4>(smem + A_PRM + lane * 32, (f32x4){lam.x, lam.y, l16.x, l16.y});
                lam = cmul(l32, l32);
                pq[0][0] = l16.x; pq[0][1] = l16.y; pq[1][0] = l32.x; pq[1][1] = l32.y; pq[2][0] = l48.x; pq[2][1] = l48.y;
                PIN(pq[0]); PIN(pq[1]); PIN(pq[2]); PIN(lam.x); PIN(lam.y);
            }
            if (kt == P0S + 4) {
                const float2 l128 = cmul(lam, lam), l256 = cmul(l128, l128), l384 = cmul(l256, l128), l512 = cmul(l256, l256), cl = cmul(make_float2(pq[3][0], pq[3][1]), lam);
                lst<f32x4>(smem + A_PRM + lane * 32 + 16, (f32x4){lam.x, lam.y, l512.x, l512.y});
                pq[0][2] = l128.x; pq[0][3] = l128.y; pq[1][2] = l256.x; pq[1][3] = l256.y; pq[2][2] = l384.x; pq[2][3] = l384.y; pq[3][2] = cl.x; pq[3][3] = cl.y;
                PIN(pq[0]); PIN(pq[1]); PIN(pq[2]); PIN(pq[3]);
            }
        }
        SCHED();
        if (kt == UFS && aw == 0) {
            const u32x4 m4 = lld<u32x4>(smem + A_FLG + F_MAX);
            const unsigned m = pkmax(pkmax(m4.x, m4.y), pkmax(m4.z, m4.w));
            flag_put(rs, OFF_FU, gb, (u32x2){(tag.x & 0xffff0000u) | ((m & 0xffffu) >> 7), tag.y}, lane);
        }
        if (VL != 2) ust(kt);
        if (VL == 1) mem();
        LDS_WAIT();
        SCHED();
    });
    TS(54);
    if (gb == XG && aw == 0 && lane == 0) __builtin_amdgcn_raw_buffer_store_b64(tag, rs, 0, OFF_E, SC1);
    __builtin_amdgcn_s_setprio(PRIOM);
    if (ATS == 14) mid_tables(smem, gb, aw, lane, tab);
    await<1>(fb + F_FREE, 8u << 20);
    TS(3);
    unsigned mx = 0;
    const bool skip = j1 == 0 && (lane & (spc ? 59 : 63)) == 0 && !xh, x8 = xh && (lane & 3) == 3;
    lptr wd[2];
    wd[0] = smem + A_W + (aw & 1) * 512 + c0 * 1024 + ((jb ^ c0) << 4);
    wd[1] = smem + A_W + (aw & 1) * 512 + (x8 ? 16384 + (lane & 4) * 256 + (jb << 4) : (c0 + 1) * 1024 + ((jb ^ (c0 + 1)) << 4));
    if (skip) wd[0] = smem + A_W + 18432;
    if (aw >= 2) { lptr t = wd[0]; wd[0] = wd[1]; wd[1] = t; }
    const unsigned wsel = (mp + j1) & 1 ? 0x07060302u : 0x05040100u;
    PIN(wd[0]); PIN(wd[1]);
    u32x4 wp[2][2];
#pragma unroll
    for (int i = 0; i < 2; ++i) {
#pragma unroll
        for (int e = 0; e < 4; ++e) {
            wp[0][i][e] = __builtin_amdgcn_perm(wim[4 * i + e].x, wre[4 * i + e].x, wsel);
            wp[1][i][e] = __builtin_amdgcn_perm(wim[4 * i + e].z, wre[4 * i + e].z, wsel);
        }
        lst<u32x4>(wd[0] + i * 256, wp[0][i]); lst<u32x4>(wd[1] + i * 256, wp[1][i]);
        mx = pkmax(mx, pkmax(amax4(wp[0][i]), amax4(wp[1][i])));
    }
    if (j1 == 0) {
        if (aw == 3) {
            PIN(wsc);
            const unsigned sel = (q & 1) ? 0x07060302u : 0x05040100u, p0 = __builtin_amdgcn_perm(wsc.z, wsc.x, sel), p1 = __builtin_amdgcn_perm(wsc.w, wsc.y, sel);
            mx = pkmax(mx, pkmax(p0 & 0x7fff7fffu, p1 & 0x7fff7fffu));
            const int it = spc ? (lane >> 5) * 8 : 0;
            lst<unsigned>(smem + A_W + it * 1024 + (c >> 2) * 256 + ((((q & 1) * 8) ^ it) << 4) + (c & 3) * 4,
                          pkb(0.5f * (lo2f(p0) + lo2f(p1)), 0.5f * (hi2f(p0) - hi2f(p1))));
        }
        if (gb == XG && (xh ? x8 : !(lane & 3) && !skip)) {
            lptr wm = smem + A_W + (xh ? 16384 + (lane & 4) * 256 + ((15 - jb) << 4) : c0 * 1024 + (((16 - jb) ^ c0) << 4));
#pragma unroll
            for (int r = 0; r < 8; ++r) {
                const int km = (16 - ((lane >> 2) & 1) - 8 * (aw & 1) - r) & 15;
                lst<unsigned>(wm + (km >> 2) * 256 + (km & 3) * 4, wp[0][r >> 2][r & 3]);
            }
        }
    }
    mx = wave_pkmax(pkmax(mx, mx >> 16));
    if (lane == 0) lst<unsigned>(smem + A_FLG + F_WM + aw * 4, mx);
    sig(ldsa(smem + A_FLG + F_MID), 1u);
    wait_vm<0>();
    SCHED();
}

DEV int role_mid(const u16* __restrict__ Cre, const u16* __restrict__ Cim, char* ws, lptr smem, int gb, int w, int lane, u32x2& tag, const Tab& tab TSARG) {
    int mp, j1; slot_of(gb, mp, j1);
    const int c = lane & 15, q = lane >> 4;
    const bool xtr = gb == XG, ld = w >= 4;
    const rsrc_t rs = mkrsrc(ws, WS_END);
    const unsigned lb = ldsa(smem), fb = lb + A_FLG;
    const CM &sf = tab.sf, &si = tab.si, &ff = tab.ff, &fi = tab.fi;
    const f32x2 (&ti)[2] = tab.ti;
    lptr mb = smem + A_MAT + lane * 16;
    u32x4 wv[2];
    TS(4);
    await<MSL>(fb + F_MID, 24u);
    u32x4 dv[2];
#pragma unroll
    for (int i = 0; i < 2; ++i) dv[i] = lld<u32x4>(smem + A_E1 + (2 * w + i) * E1P + lane * 16);
    const u32x4 m4 = lld<u32x4>(smem + A_FLG + F_WM);
    tag = lld<u32x2>(smem + A_FLG + F_TAG);
    const unsigned em = lld<unsigned>(smem + A_FLG + F_EM);
#pragma unroll
    for (int i = 0; i < 2; ++i) wv[i] = lld<u32x4>(smem + A_W + (2 * w + i) * 1024 + ((lane ^ (2 * w + i)) << 4));
#pragma unroll
    for (int i = 0; i < 2; ++i) dv[i] = pk8(mfh(dv[i], ff.re, Z4), mfh(dv[i], ff.im, Z4));
    const unsigned ew = min((pkmax(pkmax(m4.x, m4.y), pkmax(m4.z, m4.w)) & 0xffffu) >> 7, 245u);
    const float sw = __uint_as_float((246u - ew) << 23), swi = c < 8 ? sw : -sw;
    wv[0] = wcvt(wv[0], sw, swi); wv[1] = wcvt(wv[1], sw, swi);
#pragma unroll
    for (int i = 0; i < 2; ++i) dv[i] = wmul(mfh(sf.re, dv[i], Z4), mfh(sf.im, dv[i], Z4), wv[i]);
#pragma unroll
    for (int i = 0; i < 2; ++i) dv[i] = pk8(mfh(dv[i], si.re, Z4), mfh(dv[i], si.im, Z4));
    {
        f32x4 r[2], m[2];
#pragma unroll
        for (int i = 0; i < 2; ++i) {
            const f32x4 a = mfh(dv[i], fi.re, Z4), b = mfh(dv[i], fi.im, Z4);
#pragma unroll
            for (int e = 0; e < 4; ++e) { r[i][e] = fmaf(a[e], ti[i][0], b[e] * ti[i][1]); m[i][e] = fmaf(b[e], ti[i][0], -a[e] * ti[i][1]); }
        }
#pragma unroll
        for (int e = 0; e < 4; ++e)
            lst<u32x2>(smem + A_XT + (4 * q + e) * 1024 + (w >> 1) * 256 + c * 16 + (w & 1) * 8, (u32x2){pkh(r[0][e], r[1][e]), pkh(m[0][e], m[1][e])});
    }
    if (xtr && (w & 6) == 4) {
        const int it = 5 - w;
        const CM f = cexp(tab.xf), sf2 = cexp(it ? sf.im : lld<u32x4>(mb + 4096)), si2 = cexp(it ? si.im : lld<u32x4>(mb + 5120));
        u32x4 d = lld<u32x4>(smem + A_E1 + (16 + it) * E1P + lane * 16);
        d = pk8(mfh(d, f.re, Z4), mfh(d, f.im, Z4));
        d = wmul(mfh(sf2.re, d, Z4), mfh(sf2.im, d, Z4), wcvt(lld<u32x4>(smem + A_W + 16384 + it * 1024 + lane * 16), sw, swi));
        d = pk8(mfh(d, si2.re, Z4), mfh(d, si2.im, Z4));
        const f32x4 r = mfh(d, cexp(tab.xb).re, Z4);
#pragma unroll
        for (int e = 0; e < 4; ++e) lst<float>(smem + A_EX + (it * 256 + (4 * q + e) * 16 + c) * 4, r[e]);
    }
    TS(5);
    sig(fb + F_MID, 1u);
    auto clate = [&]() INL {
        if (ld) {
            int lt, pt; tile_of(gb, lt, pt);
            if (C0L > 1) c_chunk<C0S>(Cre, Cim, lb, gb, da_of(pt), w - 4, lane, 0);
            c_chunk<C0S>(Cre, Cim, lb, gb, da_of(pt), w - 4, lane, 1);
        }
    };
    if (C0L && C0P == 0) clate();
    await<0>(fb + F_MID, 16u);
    auto cearly = [&]() INL {
        if (ld) {
            int lt, pt; tile_of(gb, lt, pt);
            await<0>(fb + F_ST, 0x4000u);
            c_chunk<CES>(Cre, Cim, lb, gb, da_of(pt), w - 4, lane, 2);
            if (CE > 1 && w < 7) c_chunk<CES>(Cre, Cim, lb, gb, da_of(pt), w - 4, lane, 3);
        }
    };
    if (CE && !CEL) cearly();
    {
        f32x4 xa[2], xe[2];
#pragma unroll
        for (int i = 0; i < 2; ++i) { xa[i] = Z4; xe[i] = Z4; }
        if (xtr) {
            const float sg = (c & 1) ? -0.125f : 0.125f;
#pragma unroll
            for (int i = 0; i < 2; ++i) {
                xa[i] = lld<f32x4>(smem + A_EX + (2 * w + i) * 64 + q * 16) * sg;
                xe[i] = lld<f32x4>(smem + A_EX + 1024 + (2 * w + i) * 64 + q * 16) * -sg;
            }
        }
#pragma unroll
        for (int i = 0; i < 2; ++i) {
            const int s2 = 2 * w + i;
            const u32x4 ea = lld<u32x4>(smem + A_XT + s2 * 1024 + lane * 16);
            const f32x4 ta = mfh(ea, tab.mt[0], xa[i]), tb = mfh(ea, tab.mt[1], xe[i]);
            u32x4 tq = quads(smem + A_XT + s2 * 1024, lane, (u32x2){pkh(ta[0], ta[1]), pkh(ta[2], ta[3])}, (u32x2){pkh(tb[0], tb[1]), pkh(tb[2], tb[3])});
            PIN(tq);
            if (C0L && C0P == 1 && i == 0) clate();
            if (CE && CEL && i == 0) cearly();
            if (TWH && i == 0) await<1>(fb + F_HC, TWH);
            st128<SC1>(tq, rs, (lane >> 3) * 32768 + (lane & 7) * 16 + OFF_T + s2 * 262144 + j1 * 8192 + mp * 128);
        }
    }
    TS(7);
    tag.x = (tag.x & 0xffff0000u) | ew;
    return u_exp(em) - 267;
}

DEV void t_done(rsrc_t rs, unsigned fb, int gb, u32x2 tag, int lane) {
    int mp, j1; slot_of(gb, mp, j1);
    if (sig_rtn(fb + F_TC) == 7u) flag_put(rs, OFF_FT, (mp >> 3) * 32 + j1 * 8 + (mp & 7), tag, lane);
}

DEV void role_cload(const u16* __restrict__ Cre, const u16* __restrict__ Cim, char* ws, lptr smem, int gb, int lw, int lane, u32x2 tag TSARG) {
    int lt, pt; tile_of(gb, lt, pt);
    pt = da_of(pt);
    const unsigned lb = ldsa(smem), fb = lb + A_FLG;
    __builtin_amdgcn_s_setprio(3);
    wait_vm<0>();
    t_done(mkrsrc(ws, WS_END), fb, gb, tag, lane);
    TS(44);
    sig(fb + F_FULL, CE > 1 && lw < 3 ? 0x1111u : CE ? 0x111u : 0x11u);
    auto hpoll = [&](unsigned m, unsigned end) INL {
        int spin;
#pragma unroll 1
        for (spin = 0; spin < (1 << 20) && (m & end) != end; ++spin) {
            m = h_poll<2>(mkrsrc(ws, WS_END), gb, pt & 1, lane, tag);
            sig_or(fb + F_FULL, m);
            if (spin == 0) TS(16);
        }
    };
    if (TWH != 4) await<2>(fb + F_HC, 4u);
    if (HPL == 1 && lw == HPW) { if (PDL) __builtin_amdgcn_s_sleep(PDL); TS(19); hpoll(0, CE > 1 ? 0x88888888u : 8u); }
    if (HPL == 1 && CW) await<1>(fb + F_FULL, 8u);
    if (!CE) c_chunk(Cre, Cim, lb, gb, pt, lw, lane, 2);
    if (CE < 2 || lw == 3) {
        c_chunk(Cre, Cim, lb, gb, pt, lw, lane, 3);
        wait_vm<0>();
        sig(fb + F_FULL, CE ? 0x1000u : 0x1100u);
    }
    TS(47);
    if (lw == HPW && !(HPL == 1 && CE > 1)) hpoll(await<2>(fb + F_FULL, 8u) & 0x88888888u, 0x88888888u);
#pragma unroll 1
    for (int j = 4; j < 8; ++j) {
        await<2>(fb + F_FREE, 4u << (4 * (j - 4)));
        if (!(XE & 1)) c_chunk<CSL>(Cre, Cim, lb, gb, pt, lw, lane, (XE & 8) ? j & 3 : j);
        wait_vm<8>();
        if (j > 4) sig(fb + F_FULL, 1u << (4 * (j - 1)));
        TS(44 + j);
    }
    wait_vm<0>();
    sig(fb + F_FULL, 1u << 28);
    asm volatile("gm_end1:");
    TSE(1, lw == 0);
}

DEV void role_last(char* ws, lptr smem, int gb, int w, int lane, u32x2 tag, int fe TSARG) {
    int s2t, dat; tile_of(gb, s2t, dat);
    dat = da_of(dat);
    const int c = lane & 15, q = lane >> 4, kh = dat & 1;
    const rsrc_t rs = mkrsrc(ws, WS_END);
    const unsigned fb = ldsa(smem + A_FLG);
    SCHED();
    u32x4 fc[2], fs[2];
    {
        float dt = (float)(32 * w + 16 * kh + c) * (1.0f / 128.0f); PIN(dt);
        float cc[8], ss[8];
        const float t0 = __builtin_amdgcn_fractf(dt * (float)(4 * q)), t1 = __builtin_amdgcn_fractf(dt * (float)(16 + 4 * q));
#pragma unroll
        for (int e = 0; e < 8; ++e) { const float f = fmaf((float)(e & 3), dt, e < 4 ? t0 : t1); cc[e] = __builtin_amdgcn_cosf(f); ss[e] = __builtin_amdgcn_sinf(f); }
        fc[0] = (u32x4){pkh(cc[0], cc[1]), pkh(cc[2], cc[3]), pkh(cc[4], cc[5]), pkh(cc[6], cc[7])};
        fs[0] = (u32x4){pkh(ss[0], ss[1]), pkh(ss[2], ss[3]), pkh(ss[4], ss[5]), pkh(ss[6], ss[7])};
        fc[1] = ((c & 1) ? fs[0] : fc[0]) ^ (((c + 1) & 2) ? SG : 0u);
        fs[1] = ((c & 1) ? fc[0] : fs[0]) ^ ((c & 2) ? SG : 0u);
        PIN(fc[0]); PIN(fs[0]); PIN(fc[1]); PIN(fs[1]);
        wait_vm<0>(); t_done(rs, fb, gb, tag, lane); TS(44);
    }
    u32x4 cA = fc[0], cB = fc[0], sB = fs[0];
    if (q == 0) { cA.x = (cA.x & 0xffff0000u) | ((c & 1) ? 0xbc00u : 0x3c00u); cB.x = (cB.x & 0xffff0000u) | 0x3800u; sB.x = (sB.x & 0xffff0000u) | ((c & 1) ? 0x3800u : 0xb800u); }
    lptr tw = smem + A_XT + ((lane >> 2) & 1) * 8192 + w * 2048 + ((lane >> 1) & 1) * 1024 + (((lane >> 3) ^ (lane & 6)) << 5) + (lane & 1) * 16;
    lptr tsc = smem + A_MAT + w * 32 + (lane >> 3) * 4, tr[4], rw[4];
#pragma unroll
    for (int i = 0; i < 4; ++i) {
        tr[i] = smem + A_XT + (i >> 1) * 8192 + (i & 1) * 1024 + ((lane * 8) ^ (i * 64));
        rw[i] = smem + A_XT + w * 4096 + c * 128 + ((i ^ ((c >> 1) & 3)) << 5) + q * 8;
        PIN(tr[i]); PIN(rw[i]);
    }
    lptr aw_ = smem + A_OG + (16 * w + c) * 4;
    unsigned o = lane * 16, ft = fb + F_TT;
    PIN(cA); PIN(cB); PIN(sB); PIN(tw); PIN(tsc); PIN(aw_); PIN(o); PIN(ft);
    TS(45);
    __builtin_amdgcn_s_setprio(3);
    if (w == 0) {
        u32x4 fl[2];
        if (TP0 && !(XTP && gb == XG)) __builtin_amdgcn_s_sleep(TP0);
        TS(50);
#pragma unroll 1
        for (int spin = 0; spin < (1 << 20); ++spin) { if (TSL) __builtin_amdgcn_s_sleep(TSL); flag_get(rs, OFF_FT, gb, lane, fl); if (flag_ok(fl, tag)) break; }
        const unsigned em = wave_pkmax(pkmax(pkmax(fl[0].x, fl[0].z), pkmax(fl[1].x, fl[1].z))) & 0xffu;
#pragma unroll
        for (int i = 0; i < 2; ++i) {
            const float a = ldexpf(1.f, (int)(fl[i].x & 0xffu) - (int)em), b = ldexpf(1.f, (int)(fl[i].z & 0xffu) - (int)em);
            lst<u32x2>(smem + A_MAT + i * 512 + lane * 8, (u32x2){pkh(a, a), pkh(b, b)});
        }
        sig(fb + F_TR, em | 0x8000u);
    }
    fe += await<1>(fb + F_TR, 0x8000u) & 0xffu;
    TS(46);
    u32x4 tv[8];
#pragma unroll
    for (int i = 0; i < 8; ++i) {
        tv[i] = __builtin_amdgcn_raw_buffer_load_b128(rs, o + (i & 3) * 1024, OFF_T + (s2t * 8 + (dat >> 1)) * 32768 + w * 8192 + (i >> 2) * 4096, SC1);
        if (TLS) __builtin_amdgcn_s_sleep(TLS);
        SCHED();
    }
    const u32x2 gg = __builtin_amdgcn_raw_buffer_load_b64(rs, c * 8, OFF_G + s2t * 1024 + w * 256 + kh * 128, SC1);
    unsigned ts[8];
#pragma unroll
    for (int i = 0; i < 8; ++i) ts[i] = lld<unsigned>(tsc + i * 128);
    __builtin_amdgcn_s_setprio(PRIOR);
    auto scale = [&](int h) INL {
#pragma unroll
        for (int i = 4 * h; i < 4 * h + 4; ++i) {
            tv[i] = __builtin_bit_cast(u32x4, __builtin_bit_cast(f16x8, tv[i]) * __builtin_bit_cast(f16x8, (u32x4){ts[i], ts[i], ts[i], ts[i]}));
            PIN(tv[i]);
        }
    };
    auto put = [&](int h) INL {
#pragma unroll
        for (int i = 0; i < 4; ++i) lst<u32x4>(tw + i * 256, tv[4 * h + i]);
        sig(ft, h ? 0x100u : 1u);
    };
    u32x4 fa[2][4], fb_[2][4];
    auto get = [&]() INL {
        constexpr int JO[4] = {1, 3, 0, 2};
#pragma unroll
        for (int i = 0; i < 8; ++i) { fa[i >> 2][JO[i & 3]] = trfrag(tr[(i >> 2) * 2] + JO[i & 3] * 2048, 512); fb_[i >> 2][JO[i & 3]] = trfrag(tr[(i >> 2) * 2 + 1] + JO[i & 3] * 2048, 512); SCHED(); }
    };
    f32x4 P0[2], P1[2], P2[2], P3a[2], P3b[2];
#pragma unroll
    for (int n = 0; n < 2; ++n) { P0[n] = Z4; P1[n] = Z4; P2[n] = Z4; P3a[n] = Z4; P3b[n] = Z4; }
    auto mm = [&](u32x4 c1, u32x4 s1, u32x4 c3, u32x4 c0, u32x4 s0) INL {
#pragma unroll
        for (int n = 0; n < 2; ++n) {
            P1[n] = mfh(fa[n][1], c1, P1[n]); P3a[n] = mfh(fa[n][1], s1, P3a[n]); P3b[n] = mfh(fb_[n][1], c1, P3b[n]); P1[n] = mfh(fb_[n][1], s1, P1[n]);
            SCHED();
            P3b[n] = mfh(fa[n][3], s1, P3b[n]); P1[n] = mfh(fa[n][3], c3, P1[n]); P3a[n] = mfh(fb_[n][3], c3, P3a[n]); P1[n] = mfh(fb_[n][3], s1, P1[n]);
            SCHED();
            P0[n] = mfh(fa[n][0], c0, P0[n]); P0[n] = mfh(fb_[n][0], s0, P0[n]); P2[n] = mfh(fa[n][2], c0, P2[n]); P2[n] = mfh(fb_[n][2], s0, P2[n]);
            SCHED();
        }
    };
    scale(0); put(0);
    TS(47);
    scale(1);
    __builtin_amdgcn_s_setprio(PRX);
    await<0>(ft, 4u);
    get();
    sig(ft, 0x10u);
    await<0>(ft, 0x40u);
    put(1);
    SCHED();
    mm(fc[0], fs[0], cA, cB, sB);
    await<0>(ft, 0x400u);
    get();
    sig(ft, 0x1000u);
    mm(fc[1], fs[1], fc[1], fc[1], fs[1]);
    TS(53);
    __builtin_amdgcn_s_setprio(PRV);
    u32x2 ga[2][4];
    const float g = ldexpf(__uint_as_float(gg.x), fe);
    lst<float>(aw_, 2.0f * __uint_as_float(gg.y));
#pragma unroll
    for (int n = 0; n < 2; ++n) {
        const f32x4 sp = P0[n] + P2[n], sm = P0[n] - P2[n], p3 = P3a[n] - P3b[n];
        const f32x4 av[4] = {sp + P1[n], sm - p3, sp - P1[n], sm + p3};
#pragma unroll
        for (int m = 0; m < 4; ++m) {
            const unsigned b0 = pkb(av[m][0], av[m][1]), b1 = pkb(av[m][2], av[m][3]);
            ga[n][m] = (u32x2){pkb(g * lo2f(b0), g * hi2f(b0)), pkb(g * lo2f(b1), g * hi2f(b1))};
        }
    }
    await<0>(ft, 0x4000u);
#pragma unroll
    for (int i = 0; i < 8; ++i) lst<u32x2>(rw[i & 3] + (i >> 2) * 2048, ga[i & 1][i >> 1]);
    sig(fb + F_R0 + w * 4, 1u);
    TS(48);
    asm volatile("gm_end2:");
    TSE(w ? 4 : 5, w == 0 || w == 2);
}

NOPK __global__ __launch_bounds__(768, 3) void gm_kernel(const u16* __restrict__ x, const u16* __restrict__ w_r, const u16* __restrict__ w_i,
                                                         const u16* __restrict__ Bre, const u16* __restrict__ Bim, const u16* __restrict__ Cre,
                                                         const u16* __restrict__ Cim, const u16* __restrict__ lam_re,
                                                         const u16* __restrict__ lam_im, const u16* __restrict__ log_dt,
                                                         const u16* __restrict__ Dskip, const u16* __restrict__ gate_w, char* ws, float* __restrict__ out) {
    extern __shared__ __attribute__((aligned(16))) char smem_[];
    lptr smem = (lptr)smem_;
    u64 pc; int len;
    asm volatile("s_getpc_b64 %0\ngm_beg:\n\ts_mov_b32 %1, gm_end0 - gm_beg + 128\n\ts_max_i32 %1, %1, gm_end1 - gm_beg + 128\n\ts_max_i32 %1, %1, gm_end2 - gm_beg + 128"
                 : "=s"(pc), "=&s"(len) :: "scc");
    const int wid = __builtin_amdgcn_readfirstlane(threadIdx.x >> 6), lane = threadIdx.x & 63;
    int w = wid & 7, gb = blockIdx.x;
    asm volatile("" : "+s"(w), "+s"(gb));
    if (wid < 4) role_mma(x, Dskip, ws, out, smem, blockIdx.x, wid, lane, pc, len);
    else {
        TSD();
        u32x2 tag;
        Tab tab;
        if (wid < 8) role_load(x, Bre, Bim, Cre, Cim, gate_w, ws, smem, blockIdx.x, wid - 4, lane, tab TSPASS);
        else role_aux(w_r, w_i, lam_re, lam_im, log_dt, ws, smem, blockIdx.x, wid - 8, lane, tab TSPASS);
        const int fe = role_mid(Cre, Cim, ws, smem, gb, w, lane, tag, tab TSPASS);
        if (w >= 4) role_cload(Cre, Cim, ws, smem, gb, w - 4, lane, tag TSPASS);
        else role_last(ws, smem, gb, w, lane, tag, fe TSPASS);
    }
}

extern "C" void kernel_launch(void* const* d_in, const int* in_sizes, int n_in, void* d_out, int out_size, void* d_ws, size_t ws_size,
                              hipStream_t stream) {
    (void)hipFuncSetAttribute((const void*)gm_kernel, hipFuncAttributeMaxDynamicSharedMemorySize, SMEM);
    gm_kernel<<<256, 768, SMEM, stream>>>((const u16*)d_in[0], (const u16*)d_in[1], (const u16*)d_in[2], (const u16*)d_in[6], (const u16*)d_in[7],
                                          (const u16*)d_in[8], (const u16*)d_in[9], (const u16*)d_in[4], (const u16*)d_in[5], (const u16*)d_in[10],
                                          (const u16*)d_in[11], (const u16*)d_in[12], (char*)d_ws, (float*)d_out);
}
```

```cpp
#include <hip/hip_runtime.h>
#include <stdint.h>

typedef __attribute__((ext_vector_type(8))) __bf16 bf16x8;
typedef __attribute__((ext_vector_type(2))) __bf16 bf16x2;
typedef __attribute__((ext_vector_type(8))) _Float16 f16x8;
typedef __attribute__((ext_vector_type(2))) _Float16 f16x2;
typedef __attribute__((ext_vector_type(4))) float f32x4;
typedef __attribute__((ext_vector_type(2))) float f32x2;
typedef __attribute__((ext_vector_type(4))) unsigned u32x4;
typedef __attribute__((ext_vector_type(3))) unsigned u32x3;
typedef __attribute__((ext_vector_type(2))) unsigned u32x2;
typedef __attribute__((ext_vector_type(4))) short s16x4;
typedef __attribute__((ext_vector_type(2))) unsigned short u16x2;
typedef unsigned short u16;
typedef unsigned long long u64;
#define GAS __attribute__((address_space(1)))
#define LAS __attribute__((address_space(3)))
#define NOPK __attribute__((target("no-packed-fp32-ops")))
#define DEV __device__ __forceinline__

constexpr int S_ = 2048, D_ = 1024, KF = 513;

constexpr unsigned OFF_H = 0;
constexpr unsigned OFF_U = OFF_H + (8u << 20);
constexpr unsigned OFF_T = OFF_U + (4u << 20);
constexpr unsigned OFF_G = OFF_T + (4u << 20);
constexpr unsigned OFF_FU = OFF_G + 16384u;
constexpr unsigned OFF_FT = OFF_FU + 16384u;
constexpr unsigned OFF_FH = OFF_FT + 16384u;
constexpr unsigned OFF_GRAN = OFF_FH + 16384u;
constexpr unsigned OFF_E = OFF_GRAN + 16u * 512u * 16u;
constexpr unsigned WS_END = OFF_E + 128u;

#define TL(i)
#define TSD()
#define TS(i)
#define TS0()
#define TC(i)
#define TSE(slot, on)

DEV unsigned pkb(float a, float b) { f32x2 v = {a, b}; return __builtin_bit_cast(unsigned, __builtin_convertvector(v, bf16x2)); }
DEV unsigned pkh(float a, float b) { f32x2 v = {a, b}; return __builtin_bit_cast(unsigned, __builtin_convertvector(v, f16x2)); }
DEV float lo2f(unsigned p) { return __uint_as_float(p << 16); }
DEV float hi2f(unsigned p) { return __uint_as_float(p & 0xffff0000u); }
DEV float bfr(float f) { return lo2f(pkb(f, f)); }
DEV float2 cmul(float2 a, float2 b) { return make_float2(a.x * b.x - a.y * b.y, a.x * b.y + a.y * b.x); }
DEV float2 cfma(float2 a, float2 b, float2 c) { return make_float2(fmaf(a.x, b.x, fmaf(-a.y, b.y, c.x)), fmaf(a.x, b.y, fmaf(a.y, b.x, c.y))); }
DEV f32x4 mfb(u32x4 a, u32x4 b, f32x4 c) { return __builtin_amdgcn_mfma_f32_16x16x32_bf16(__builtin_bit_cast(bf16x8, a), __builtin_bit_cast(bf16x8, b), c, 0, 0, 0); }
DEV f32x4 mfh(u32x4 a, u32x4 b, f32x4 c) { return __builtin_amdgcn_mfma_f32_16x16x32_f16(__builtin_bit_cast(f16x8, a), __builtin_bit_cast(f16x8, b), c, 0, 0, 0); }
DEV unsigned pkmax(unsigned a, unsigned b) { return __builtin_bit_cast(unsigned, __builtin_elementwise_max(__builtin_bit_cast(u16x2, a), __builtin_bit_cast(u16x2, b))); }
typedef LAS char* lptr;
DEV unsigned ldsa(lptr p) { return (unsigned)(uintptr_t)p; }
template <class T> DEV T lld(lptr p) { return *(LAS const T*)p; }
template <class T> DEV void lst(lptr p, T v) { *(LAS T*)p = v; }
DEV u32x2 trread(lptr p) { return __builtin_bit_cast(u32x2, __builtin_amdgcn_ds_read_tr16_b64_v4i16((LAS s16x4*)p)); }
DEV u32x4 trfrag(lptr p, int second) { const u32x2 a = trread(p), b = trread(p + second); return (u32x4){a.x, a.y, b.x, b.y}; }
typedef __amdgpu_buffer_rsrc_t rsrc_t;
DEV u32x4 ldfrag(lptr tile, int row, int chunk) { return lld<u32x4>(tile + row * 128 + ((chunk ^ (row & 7)) << 4)); }
template <int N> DEV void wait_vm() { asm volatile("s_waitcnt vmcnt(%0)" :: "i"(N) : "memory"); }
#define LDS_WAIT() asm volatile("s_waitcnt lgkmcnt(0)" ::: "memory")
#define SCHED() __builtin_amdgcn_sched_barrier(0)
#define BAR() do { __builtin_amdgcn_s_barrier(); asm volatile("" ::: "memory"); } while (0)
#define PIN(x) asm volatile("" : "+v"(x))
template <int I> struct IC { static constexpr int v = I; };
template <int I, int N, class F> DEV void sfor(F&& f) { if constexpr (I < N) { f(IC<I>{}); sfor<I + 1, N>(f); } }
#define INL __attribute__((always_inline))
DEV rsrc_t mkrsrc(const void* p, unsigned n) { return __builtin_amdgcn_make_buffer_rsrc((void*)p, 0, n, 0x00020000); }
constexpr int SC1 = 16;
#define WTO 16
template <int AUX> DEV void st128(u32x4 v, rsrc_t rs, unsigned off) { __builtin_amdgcn_raw_buffer_store_b128(v, rs, off, 0, AUX); }
DEV void lds_add(unsigned a) { asm volatile("ds_add_u32 %0, %1" :: "v"(a), "v"(1u) : "memory"); }
DEV unsigned lds_peek(unsigned a) {
    unsigned v;
    asm volatile("ds_read_b32 %0, %1\n\ts_waitcnt lgkmcnt(0)" : "=v"(v) : "v"(a) : "memory");
    return __builtin_amdgcn_readfirstlane(v);
}
DEV void tile_of(int gb, int& ta, int& tb) {
    const int xcd = gb & 7, loc = gb >> 3;
    ta = (xcd >> 1) * 4 + (loc >> 3);
    tb = (xcd & 1) * 8 + (loc & 7);
}
#define DMAP 1
DEV int da_of(int pt) { return DMAP ? ((pt & 7) << 1) | (pt >> 3) : pt; }
DEV void rows4(float a, float& a0, float& a1, float& a2, float& a3) {
    const unsigned u = __float_as_uint(a);
    const auto r = __builtin_amdgcn_permlane32_swap(u, u, false, false);
    const auto p = __builtin_amdgcn_permlane16_swap(r[0], r[0], false, false);
    const auto s = __builtin_amdgcn_permlane16_swap(r[1], r[1], false, false);
    a0 = __uint_as_float(p[0]); a1 = __uint_as_float(p[1]); a2 = __uint_as_float(s[0]); a3 = __uint_as_float(s[1]);
}
DEV float rowsum4(float a) {
    const unsigned u = __float_as_uint(a);
    const auto r = __builtin_amdgcn_permlane32_swap(u, u, false, false);
    const unsigned t = __float_as_uint(__uint_as_float(r[0]) + __uint_as_float(r[1]));
    const auto p = __builtin_amdgcn_permlane16_swap(t, t, false, false);
    return __uint_as_float(p[0]) + __uint_as_float(p[1]);
}
DEV float2 sel4(int q, float2 a, float2 b, float2 c, float2 d) {
    PIN(d.x); PIN(d.y);
    const float2 e = (q & 1) ? b : a, f = (q & 1) ? d : c;
    return (q & 2) ? f : e;
}
template <int R> DEV unsigned ror16(unsigned v) { return __builtin_amdgcn_update_dpp(0u, v, 0x120 + R, 0xf, 0xf, false); }
DEV unsigned wave_pkmax(unsigned m) {
    m = pkmax(m, ror16<8>(m)); m = pkmax(m, ror16<4>(m)); m = pkmax(m, ror16<2>(m)); m = pkmax(m, ror16<1>(m));
    const auto r = __builtin_amdgcn_permlane32_swap(m, m, false, false);
    m = pkmax(r[0], r[1]);
    const auto p = __builtin_amdgcn_permlane16_swap(m, m, false, false);
    return pkmax(p[0], p[1]);
}
DEV float half_sum(float v) {
    v += __uint_as_float(ror16<8>(__float_as_uint(v))); v += __uint_as_float(ror16<4>(__float_as_uint(v)));
    v += __uint_as_float(ror16<2>(__float_as_uint(v))); v += __uint_as_float(ror16<1>(__float_as_uint(v)));
    const auto p = __builtin_amdgcn_permlane16_swap(__float_as_uint(v), __float_as_uint(v), false, false);
    return __uint_as_float(p[0]) + __uint_as_float(p[1]);
}
DEV u32x4 quads(lptr buf, int lane, u32x2 p, u32x2 n) {
    lptr wp = buf + (lane & 15) * 64 + ((lane >> 4) & 1) * 8;
    const int v = (lane >> 5) ^ ((lane >> 2) & 3);
    lst<u32x2>(wp + (v << 4), p); lst<u32x2>(wp + ((v ^ 2) << 4), n);
    asm volatile("" ::: "memory");
    return lld<u32x4>(buf + ((lane ^ (lane >> 4)) << 4));
}

#define PRIOA 2
#define NREP 8
#define PRIOM 1
#define PRIOT 2
#define PRIOG 2
#define AD 8
#define NB 12
#define AD2 4
#define AG2 168
#define AD3 8
#define AG3 200
#define AD4 6
#define AG4 244
#define ENT 1
#define STG 1
#define FTS 1
#define FLN 2
#define FSL 0
#define PKR 1
#define GS0 474
#define US0 10
#define WGL 2
#define RS0 20
#define RSP 6
#define WS0 0
#define WS1 4
#define RSD 80
#define CPD 4
#define CB 2
#define AWS 0
#define MWT 0
#define TDR 0
#define SA0 24
#define AM 1
#define CP 0
#define HP 0
#define XE 0
#define TSL 16
#define CSL 1
#define WG2 2
#define WG1 1
#define SPC 1
#define PRV 0
#define PRX 3
#define TLS 4
#define RDL 0
#define WROW {0, 0, 0, 0, 8}
#define ROT (lt & 12)
#define PRIOL 3
#define XM 0
#define UFS 6
#define PRIOR 0
#define PRS 2
#define WXS 13
#define CPF 1
#define FST 8
#define HREP 8
#define NP1 1
#define PDL 8
#define PDE 0
#define PDM 0x80
#define NB2 NB
#define XP 4
#define PU 0
#define GDL 8
#define BAL -1
#define BA2 0
#define FTC 1
#define LBS 0
#define PT 0
#define PTS 4
#define FTB 768
#define XTP 1
#define TWH 4
#define ACI 0
#define CE 0
#define CES 0
#define CEL 1
#define HPL 0
#define HPW 0
#define CW 0
#define GSP 1
#define PV0 "144"
#define PV0E "147"
#define PV1 "148"
#define PV1E "151"
#define PV2 "152"
#define PV2E "155"
#define PV3 "156"
#define PV3E "159"
#define UDL 0
#define CGW 0
#define C0S 0
#define GSL 0
#define C0L 0
#define C0P 0
#define BSL 0
#define BS2 0
#define PZ1 0
#define PZ2 0
#define PZ3 0
#define PZA 0
#define PZB 48
#define XLP 0
#define PRIOI 0
#define RST (NST - 5)
#define SIL 1
#define CPM 7
#define PRIOE 2
#define MSL 1
#define URS 6
#define UPRE 1
#define UCK 13
#define PRIOD 3
#define PRIOW 0
#define CDL 0
#define ATS 14
#define UPL 10
#define C1E 1
#define TP0 0
#define PD0 0
#define CXM 6
#define CX(g) ((g) & CXM)
#define GSTEP {0, 0, 0, 0}
#define XS 1
#define B1S 0
#define US 1
#define UPS 1
#define DS 8
#define P0S 8
#define VL 1
#define PN -1
#define PG -1
#define SNK 1
#define HFIRST 8u
#define RDPAT {0, 1, 2, -1, 3, 4, -1, 5, 6, -1, 7, -1, -1, -1, -1, -1}
#define Z4 ((f32x4){0.f, 0.f, 0.f, 0.f})
constexpr unsigned SG = 0x80008000u;
struct CM { u32x4 re, im; };
DEV u32x4 cmat1(float t0, float dt) {
    float cc[4], ss[4];
    t0 = __builtin_amdgcn_fractf(t0);
#pragma unroll
    for (int e = 0; e < 4; ++e) { const float f = fmaf((float)e, dt, t0); cc[e] = __builtin_amdgcn_cosf(f); ss[e] = __builtin_amdgcn_sinf(f); }
    return (u32x4){pkh(ss[0], ss[1]), pkh(ss[2], ss[3]), pkh(cc[0], cc[1]), pkh(cc[2], cc[3])};
}
DEV CM cexp(u32x4 v) { CM m; m.re = (u32x4){v.z, v.w, v.x ^ SG, v.y ^ SG}; m.im = v; PIN(m.re); PIN(m.im); return m; }
DEV u32x4 pk8(f32x4 r, f32x4 i) { return (u32x4){pkh(r[0], r[1]), pkh(r[2], r[3]), pkh(i[0], i[1]), pkh(i[2], i[3])}; }
DEV u32x4 wmul(f32x4 r, f32x4 i, u32x4 w) {
    float yr[4], yi[4];
#pragma unroll
    for (int e = 0; e < 4; ++e) {
        const unsigned wu = w[e];
        const f16x2 h = __builtin_bit_cast(f16x2, wu);
        yr[e] = fmaf(r[e], (float)h[0], -i[e] * (float)h[1]); yi[e] = fmaf(r[e], (float)h[1], i[e] * (float)h[0]);
    }
    return (u32x4){pkh(yr[0], yr[1]), pkh(yr[2], yr[3]), pkh(yi[0], yi[1]), pkh(yi[2], yi[3])};
}
DEV u32x4 wcvt(u32x4 w, float sr, float si) {
#pragma unroll
    for (int e = 0; e < 4; ++e) w[e] = pkh(lo2f(w[e]) * sr, hi2f(w[e]) * si);
    PIN(w);
    return w;
}
DEV u32x4 mkdesc(const void* p, unsigned n) { const u64 a = (u64)p; return (u32x4){(unsigned)a, (unsigned)(a >> 32) & 0xffffu, n, 0x00020000u}; }
DEV void dma16(u32x4 d, unsigned l, unsigned vo, unsigned so) {
    asm volatile("s_nop 0\n\tbuffer_load_dwordx4 %0, %1, %2 offen lds" :: "v"(vo), "s"(d), "s"(so), "{m0}"(l) : "memory");
}
DEV void dma64(u32x4 d, unsigned l, u32x4 vo, unsigned so) {
    asm volatile("s_nop 0\n\tbuffer_load_dwordx4 %0, %4, %5 offen lds\n\tbuffer_load_dwordx4 %1, %4, %5 offen offset:1024 lds\n\t"
                 "buffer_load_dwordx4 %2, %4, %5 offen offset:2048 lds\n\tbuffer_load_dwordx4 %3, %4, %5 offen offset:3072 lds"
                 :: "v"(vo.x), "v"(vo.y), "v"(vo.z), "v"(vo.w), "s"(d), "s"(so), "{m0}"(l) : "memory");
}
#define SPIN(x) asm volatile("" : "+s"(x))
DEV void dma16s(u32x4 d, unsigned l, unsigned vo, unsigned so) {
    asm volatile("s_nop 0\n\tbuffer_load_dwordx4 %0, %1, %2 offen sc1 lds" :: "v"(vo), "s"(d), "s"(so), "{m0}"(l) : "memory");
}
DEV void sig(unsigned a, unsigned v) { asm volatile("s_mov_b64 exec, 1\n\tds_add_u32 %0, %1\n\ts_mov_b64 exec, -1" :: "v"(a), "v"(v) : "memory"); }
template <int O> DEV void sig_o(unsigned a, unsigned v) { asm volatile("s_mov_b64 exec, 1\n\tds_add_u32 %0, %1 offset:%2\n\ts_mov_b64 exec, -1" :: "v"(a), "v"(v), "i"(O) : "memory"); }
DEV unsigned sig_rtn(unsigned a) {
    unsigned v;
    asm volatile("s_mov_b64 exec, 1\n\tds_add_rtn_u32 %0, %1, %2\n\ts_mov_b64 exec, -1\n\ts_waitcnt lgkmcnt(0)" : "=&v"(v) : "v"(a), "v"(1u) : "memory");
    return __builtin_amdgcn_readfirstlane(v);
}
template <int SLP> DEV unsigned await(unsigned a, unsigned mask) {
    unsigned v, n, r, t;
    asm volatile("s_mov_b32 %1, 0x400000\n"
                 "1:\tds_read_b32 %0, %4\n\ts_waitcnt lgkmcnt(0)\n\tv_readfirstlane_b32 %2, %0\n\ts_and_b32 %3, %2, %5\n\ts_cbranch_scc1 2f\n\t"
                 "s_sleep %6\n\ts_sub_u32 %1, %1, 1\n\ts_cbranch_scc0 1b\n"
                 "2:" : "=&v"(v), "=&s"(n), "=&s"(r), "=&s"(t) : "v"(a), "s"(mask), "i"(SLP) : "memory", "scc");
    return r;
}
#define BMF "v_mfma_f32_16x16x32_bf16 %0, %2, %3, %0\n\tv_mfma_f32_16x16x32_bf16 %1, %3, %2, %1\n\t"
template <int SLP> DEV void burn(int n, u32x4& a, u32x4& b) {
    f32x4 d0, d1;
    asm volatile("1:\t" BMF BMF "s_nop %5\n\ts_sub_u32 %4, %4, 1\n\ts_cbranch_scc0 1b\n\ts_nop 7\n\ts_nop 3"
                 : "=&v"(d0), "=&v"(d1), "+v"(a), "+v"(b), "+s"(n) : "i"(SLP) : "scc");
}
template <int SLP> DEV unsigned await_burn(unsigned ad, unsigned mask, u32x4 a, u32x4 b) {
    unsigned v, n, r, t; f32x4 d0, d1;
    asm volatile("s_mov_b32 %5, 0x400000\n"
                 "1:\tds_read_b32 %4, %8\n\t" BMF BMF "s_waitcnt lgkmcnt(0)\n\tv_readfirstlane_b32 %6, %4\n\ts_and_b32 %7, %6, %9\n\ts_cbranch_scc1 2f\n\t"
                 ".rept %10\n\t" BMF ".endr\n\ts_sub_u32 %5, %5, 1\n\ts_cbranch_scc0 1b\n"
                 "2:\ts_nop 7\n\ts_nop 3" : "=&v"(d0), "=&v"(d1), "+v"(a), "+v"(b), "=&v"(v), "=&s"(n), "=&s"(r), "=&s"(t) : "v"(ad), "s"(mask), "i"(SLP) : "memory", "scc");
    return r;
}
DEV void need_bit(unsigned a, unsigned& r, unsigned mask, int off = 0) {
    unsigned v, n, t;
    asm volatile("s_andn2_b32 %3, %5, %2\n\ts_cbranch_scc1 1f\n2:\n\t.subsection 1\n"
                 "1:\ts_mov_b32 %1, 0x400000\n"
                 "3:\tds_read_b32 %0, %4 offset:%6\n\ts_waitcnt lgkmcnt(0)\n\tv_readfirstlane_b32 %2, %0\n\ts_andn2_b32 %3, %5, %2\n\ts_cbranch_scc0 2b\n\t"
                 "s_sub_u32 %1, %1, 1\n\ts_cbranch_scc0 3b\n\ts_branch 2b\n\t.subsection 0"
                 : "=&v"(v), "=&s"(n), "+s"(r), "=&s"(t) : "v"(a), "i"(mask), "i"(off) : "memory", "scc");
}
template <int SLP> DEV void await_all(unsigned a, unsigned mask) {
    unsigned v, n, r;
    asm volatile("s_mov_b32 %1, 0x400000\n"
                 "1:\tds_read_b32 %0, %3\n\ts_waitcnt lgkmcnt(0)\n\tv_readfirstlane_b32 %2, %0\n\ts_andn2_b32 %2, %4, %2\n\ts_cbranch_scc0 2f\n\t"
                 "s_sleep %5\n\ts_sub_u32 %1, %1, 1\n\ts_cbranch_scc0 1b\n"
                 "2:" : "=&v"(v), "=&s"(n), "=&s"(r) : "v"(a), "s"(mask), "i"(SLP) : "memory", "scc");
}
#define MF2(a, b, c) asm("v_mfma_f32_16x16x32_bf16 %0, %1, %2, %0" : "+v"(c) : "v"(a), "v"(b))
DEV void flag_put(rsrc_t rs, unsigned off, int gb, u32x2 v, int lane) {
    if (lane < NREP) __builtin_amdgcn_raw_buffer_store_b64(v, rs, lane * 2048, off + gb * 8, SC1);
}
DEV void flag_get(rsrc_t rs, unsigned off, int gb, int lane, u32x4 (&fl)[2]) {
    unsigned o = lane * 16; PIN(o);
#pragma unroll
    for (int i = 0; i < 2; ++i) fl[i] = __builtin_amdgcn_raw_buffer_load_b128(rs, o + i * 1024, off + (gb & (NREP - 1)) * 2048, SC1);
}
DEV bool flag_ok(const u32x4 (&fl)[2], u32x2 tag) {
    bool ok = true;
#pragma unroll
    for (int i = 0; i < 2; ++i) ok = ok & ((fl[i].x ^ tag.x) < 0x10000u) & (fl[i].y == tag.y) & ((fl[i].z ^ tag.x) < 0x10000u) & (fl[i].w == tag.y);
    return __all(ok);
}
template <int N> DEV unsigned h_poll(rsrc_t rs, int gb, int kh, int lane, u32x2 tag) {
    unsigned o = (lane ^ ((CX(gb) & 3) << 4)) * FST, m = 0; PIN(o);
    u32x2 fl[N];
#pragma unroll
    for (int i = 0; i < N; ++i) fl[i] = __builtin_amdgcn_raw_buffer_load_b64(rs, o, OFF_FH + ((gb & (HREP - 1)) * 256 + kh * 128 + (i ^ (CX(gb) >> 2)) * 64) * FST, SC1);
#pragma unroll
    for (int i = 0; i < N; ++i) {
        const u64 b = __ballot(((fl[i].x ^ tag.x) < 0x10000u) & (fl[i].y == tag.y));
#pragma unroll
        for (int j = 0; j < 4; ++j) m |= ((unsigned)(b >> (16 * j)) & 0xffffu) == 0xffffu ? 8u << (16 * i + 4 * j) : 0u;
    }
    return m;
}
DEV void sig_or(unsigned a, unsigned v) { asm volatile("s_mov_b64 exec, 1\n\tds_or_b32 %0, %1\n\ts_mov_b64 exec, -1" :: "v"(a), "v"(v) : "memory"); }

constexpr int A_STAGE = 32768, A_XT = 4 * A_STAGE, A_PRM = A_XT + 16384, A_AGG = A_PRM + 2048, A_FLG = A_AGG + 512, A_GW = A_FLG + 128,
              A_MAT = A_GW + 2048, SMEM = A_MAT + 11264;
constexpr int E1P = 1040, A_EX = A_GW, A_E1 = 0, A_PW = 18 * E1P + 224, A_W = A_STAGE, A_HS = 2 * A_STAGE - 8192, A_OG = A_MAT + 9216, A_CN = A_MAT + 1024;
static_assert(A_PW % 256 == 0 && A_W + 18432 + 512 <= A_HS, "");
constexpr int F_MID = 8, F_EM = 12, F_MAX = 16, F_TAG = 32, F_FULL = 40, F_FREE = 44, F_HC = 48, F_FB = 52, F_TC = 56, F_TR = 60, F_TT = 64, F_ST = 76, F_WM = 80, F_RD = 96;
static_assert((A_FLG + F_RD & 0xff) == 0x60, "");

constexpr int XG = 0xFF;
DEV void slot_of(int gb, int& mp, int& j1) { gb ^= XG; mp = (gb & 7) * 8 + (gb >> 5); j1 = (gb >> 3) & 3; }
DEV u32x4 mat_sf(int k1, int c, int q) { const float dt = -(float)(k1 + 128 * c) * (1.0f / 2048.0f); return cmat1(dt * (float)(4 * q), dt); }
DEV u32x4 mat_si(int k1, int c, int q) { return cmat1((float)((k1 + 512 * q) * c) * (1.0f / 2048.0f), (float)c * (1.0f / 16.0f)); }

DEV void mk_tables(lptr smem, int gb, int wid, int lane) {
    int mp, j1; slot_of(gb, mp, j1);
    const int c = lane & 15, q = lane >> 4;
    const bool spc = mp == 0 && !(j1 & 1);
    const int k1 = mp ? mp : (j1 == 3 ? 64 : 0);
    lptr mt = smem + A_MAT + lane * 16;
    if (wid == 0) {
        const float dt = -(float)(j1 + 4 * (spc ? c & 7 : c)) * (1.0f / 64.0f), t0 = __builtin_amdgcn_fractf(dt * (float)(4 * q));
        float cc[4], ss[4];
#pragma unroll
        for (int e = 0; e < 4; ++e) { const float f = fmaf((float)e, dt, t0); cc[e] = __builtin_amdgcn_cosf(f); ss[e] = __builtin_amdgcn_sinf(f); }
        const unsigned c0 = pkb(cc[0], cc[1]), c1 = pkb(cc[2], cc[3]), s0 = pkb(ss[0], ss[1]), s1 = pkb(ss[2], ss[3]);
        const unsigned c0l = pkb(cc[0] - lo2f(c0), cc[1] - hi2f(c0)), c1l = pkb(cc[2] - lo2f(c1), cc[3] - hi2f(c1));
        const unsigned s0l = pkb(ss[0] - lo2f(s0), ss[1] - hi2f(s0)), s1l = pkb(ss[2] - lo2f(s1), ss[3] - hi2f(s1));
        u32x4 m[4] = {(u32x4){c0, c1, s0 ^ SG, s1 ^ SG}, (u32x4){s0, s1, c0, c1}, (u32x4){c0l, c1l, s0l ^ SG, s1l ^ SG}, (u32x4){s0l, s1l, c0l, c1l}};
        if (spc) {
            m[0] = c < 8 ? (u32x4){c0, c1, 0, 0} : (u32x4){0, 0, c0, c1}; m[1] = c < 8 ? (u32x4){s0, s1, 0, 0} : (u32x4){0, 0, s0, s1};
            m[2] = c < 8 ? (u32x4){c0l, c1l, 0, 0} : (u32x4){0, 0, c0l, c1l}; m[3] = c < 8 ? (u32x4){s0l, s1l, 0, 0} : (u32x4){0, 0, s0l, s1l};
        }
#pragma unroll
        for (int i = 0; i < 4; ++i) lst<u32x4>(mt + i * 1024, m[i]);
    } else if (wid == 1) {
        const int jq = spc ? (4 * q) & 7 : 4 * q;
        float cc[4], ss[4];
        const float t0 = __builtin_amdgcn_fractf((float)(c * (j1 + 4 * jq)) * (1.0f / 64.0f));
#pragma unroll
        for (int e = 0; e < 4; ++e) {
            const float f = fmaf((float)c, (float)e * (1.0f / 16.0f), t0);
            const float wg = !spc || (gb == XG && jq + e == 0) ? 0.125f : 0.25f;
            cc[e] = __builtin_amdgcn_cosf(f) * wg; ss[e] = -__builtin_amdgcn_sinf(f) * wg;
        }
        const unsigned c0 = pkh(cc[0], cc[1]), c1 = pkh(cc[2], cc[3]), n0 = pkh(ss[0], ss[1]), n1 = pkh(ss[2], ss[3]);
        u32x4 mia = (u32x4){c0, n0, c1, n1}, mib = (u32x4){n0, c0 ^ SG, n1, c1 ^ SG};
        if (spc) { mib = q < 2 ? (u32x4){0, 0, 0, 0} : mia ^ SG; if (q >= 2) mia = (u32x4){0, 0, 0, 0}; }
        lst<u32x4>(mt + 6 * 1024, mia); lst<u32x4>(mt + 7 * 1024, mib);
        lst<u32x4>(mt + 8 * 1024, cmat1((float)(c * q) * -0.25f, (float)c * (-1.0f / 16.0f)));
    } else {
        lst<u32x4>(mt + (2 + wid) * 1024, wid == 2 ? mat_sf(k1, c, q) : mat_si(k1, c, q));
        const int ja = (spc ? 0 : 8 * (wid - 2)) + (lane >> 3);
        const float dt = -(float)(j1 + 4 * ja) * (1.0f / 1024.0f), t0 = dt * (float)(2 * (lane & 7)), t1 = t0 + dt;
        lst<f32x4>(mt + (7 + wid) * 1024, (f32x4){__builtin_amdgcn_cosf(t0), __builtin_amdgcn_sinf(t0), __builtin_amdgcn_cosf(t1), __builtin_amdgcn_sinf(t1)});
    }
}

template <int SLP = 0> DEV void c_chunk(const u16* __restrict__ Cre, const u16* __restrict__ Cim, unsigned lb, int gb, int da, int lw, int lane, int j, int at = -1) {
    if (at < 0) at = ((j + 2) & 3) * A_STAGE + lw * 8192;
    const u32x4 rc = mkdesc(lw < 2 ? Cre : Cim, D_ * D_ * 2);
    const unsigned vo = (lane >> 3) * 8192 + (((lane & 7) ^ (lane >> 3)) << 4);
#pragma unroll
    for (int i = 0; i < 8; ++i) {
        if (SLP && i) __builtin_amdgcn_s_sleep(SLP);
        dma16(rc, lb + at + i * 1024, vo, (512 * (i & 1) + 256 * ((lw ^ da ^ 1) & 1) + 32 * (da >> 1) + (i >> 1)) * 2048 + (8 * (da & 1) + (j ^ CX(gb))) * 128);
    }
}
constexpr int KS2 = 16, KS1 = 16, F1 = 8 * KS1, FT = F1 + 4 * KS2, RT = 8, NRL = 16;
static_assert(!(NB & 1) && NB <= 16, "");
constexpr int f_k(int f) { return f < F1 ? f >> 3 : f < FT ? KS1 + ((f - F1) >> 2) : 31 - ((f - FT) >> 2); }
constexpr int f_m(int f) { return f < F1 ? f & 7 : f < FT ? (f - F1) & 3 : 4 + ((f - FT) & 3); }
constexpr int f_i(int f) { return f < F1 ? f & 7 : (f - F1) & 3; }
constexpr bool f_hi(int f) { return f_i(f) == (f < F1 ? 7 : 3); }
constexpr bool c_new(int f) { return f < FT && !f_i(f) && !(f_k(f) & 3); }
constexpr bool c_new3(int f) { return f >= FT && f < 256 && !f_i(f) && (f_k(f) & 3) == 3; }
constexpr int k_f(int k) { return k < KS1 ? 8 * k : F1 + 4 * (k - KS1); }
struct SCH {
    int a[512], p[512], w[512]; unsigned m[512];
    constexpr void chk(int s, unsigned mask, int word) { m[s] |= mask; p[s - 2] = 3; p[s - 2 - 2 * CPD] = word; }
    constexpr SCH() : a{}, p{}, w{}, m{} {
        int s = -1, as[256] = {}, cs[9] = {};
        for (int i = AD; i < 256; ++i) { const int t = 2 * (i - (i < AG2 ? AD : i < AG3 ? AD2 : i < AG4 ? AD3 : AD4)) + 1; s = t > s ? t : s + 1; a[s] = i; as[i] = s; }
        for (int c = 1; c < 8; ++c) {
            cs[c] = (as[k_f(4 * c)] - (c < 5 ? 5 : 1)) & ~1;
            if (c > 4 && c < 7 && cs[c] > 2 * k_f(2 * c + 10)) cs[c] = 2 * k_f(2 * c + 10);
            chk(cs[c], 4u << (4 * c), 1);
        }
        for (int j = (NB + 7) / 8; j < 8; ++j) {
            int c = 7;
            while (cs[c] > 2 * k_f((8 * j - NB) / 2 + 1) - 2) --c;
            chk(cs[c], 8u << (4 * j), 1);
        }
        for (int j = 7; j > 3; --j) chk((as[FT + 16 * (7 - j)] - 1) & ~1, 4u << (4 * j) | 4u, 2);
        chk(GS0, 0x40u, 2);
        for (int f = 0; f < 252; f += WGL) {
            int n = 0;
            for (int i = f + WGL; i < 256; ++i) n += i < AD || as[i] < 2 * f;
            if (f && WGL > 1) w[2 * f - 1] = n + 1;
        }
    }
    constexpr bool ok() const {
        int e = 0, r = 0;
        for (int s = 0; s < 512; ++s) {
            if (p[s] == 3) { r = e; e = 0; } else if (p[s]) { if (e) return false; e = p[s]; }
            if (m[s]) { if (r != ((m[s] & 0x44u) && s > 128 ? 2 : 1)) return false; r = 0; }
        }
        return true;
    }
};
constexpr SCH ST;
static_assert(ST.ok(), "");
DEV void wait_lds(int n) { sfor<0, 16>([&](auto ic) INL { if (n == decltype(ic)::v) __builtin_amdgcn_s_waitcnt(0xC07F | decltype(ic)::v << 8); }); }
constexpr int s_f(int s) { return s < 504 ? s >> 1 : 252 + (s & 3); }
constexpr int s_n(int s) { return s < 504 ? s & 1 : (s >> 2) & 1; }
constexpr int s_b(int s) { return s >= 2 * FT && f_k(s_f(s)) < 32 - RT ? 64 + 2 * (31 - RT - f_k(s_f(s))) + s_n(s) : 2 * f_k(s_f(s)) + s_n(s); }
constexpr int h_off(int k, int n) { return ((k >> 1) & 1) * A_STAGE + (k & 1) * 16384 + n * 1024; }

DEV void role_mma(const u16* __restrict__ x, const u16* __restrict__ Dskip, char* ws, float* __restrict__ out, lptr smem, int gb, int wid, int lane, u64 pc, int len, unsigned cpf, unsigned t00) {
    int lt, pt; tile_of(gb, lt, pt);
    const int wr = wid >> 1, wc = wid & 1, c = lane & 15, q = lane >> 4;
    const rsrc_t rs = mkrsrc(ws, WS_END);
    const unsigned fb = ldsa(smem + A_FLG);
    TSD(); TS0();
    u64 tag;
    if (STG) tag = *(const __attribute__((address_space(4))) u64*)(uintptr_t)(ws + OFF_E);
    else tag = __hip_atomic_load((GAS u64*)(ws + OFF_E), __ATOMIC_RELAXED, __HIP_MEMORY_SCOPE_WORKGROUP);
    if (!ENT) { if (wid == 3) cpf = __builtin_amdgcn_raw_buffer_load_b32(mkrsrc((const void*)pc, max(len, 0)), lane * 128, ((gb >> 3) % CPM) * 8192, 0); }
    if (!STG) { if (wid == 2) cpf = __builtin_amdgcn_raw_buffer_load_b32(mkrsrc(ws + OFF_G + gb * FTB, FTB), lane * 64, 0, SC1); }
    else if (FTS < 0 && wid == 2) asm volatile("s_nop 0\n\tbuffer_load_dword %0, %1, 0 offen sc1 lds" :: "v"(lane * 64), "s"(mkdesc(ws + OFF_G + gb * FTB, FTB)), "{m0}"(ldsa(smem + A_AGG + 256)) : "memory");
    SCHED();
    if (wid == 0 && lane < 32) lst<unsigned>(smem + A_FLG + lane * 4, 0u);
    mk_tables(smem, gb, wid, lane);
    f32x4 acc[4][4];
#pragma unroll
    for (int m = 0; m < 4; ++m)
#pragma unroll
        for (int n = 0; n < 4; ++n) acc[m][n] = Z4;
    u32x4 af[2][4], bf_[2][4];
#pragma unroll
    for (int m = 0; m < 4; ++m) { af[1][m] = (u32x4){0, 0, 0, 0}; bf_[1][m] = af[1][m]; }
    lptr fp[8];
#pragma unroll
    for (int i = 0; i < 8; ++i) { fp[i] = smem + (i & 2) * A_STAGE + ((i & 4 ? 128 + wc * 64 : wr * 64) + c) * 128 + (((4 * (i & 1) + q) ^ (c & 7)) << 4); PIN(fp[i]); }
    tag += 0x9E3779B97F4A7C15ull;
    PIN(tag); if (!STG || !ENT) PIN(cpf);
    if (wid == 0 && lane == 0) lst<u64>(smem + A_FLG + F_TAG, tag);
    tag = (u64)(unsigned)__builtin_amdgcn_readfirstlane((unsigned)tag) | (u64)(unsigned)__builtin_amdgcn_readfirstlane((unsigned)(tag >> 32)) << 32;
    __builtin_amdgcn_s_waitcnt(0xC07F);
    TS(13);
    unsigned fst = fb + F_ST, sv = 0;
    auto step = [&](int sl, bool pf) INL {
#pragma unroll
        for (int hf = 0; hf < 2; ++hf)
#pragma unroll
            for (int i = 0; i < 16; ++i) {
                const int nn = SNK && (i & 4) ? 3 - (i & 3) : i & 3;
                acc[i >> 2][nn] = mfb(af[1 - hf][i >> 2], bf_[1 - hf][nn], acc[i >> 2][nn]);
                constexpr int RD[16] = RDPAT;
                const int r = RD[i];
                if (r == 0) af[hf][0] = lld<u32x4>(fp[(sl & 2) + hf] + (sl & 1) * A_STAGE);
                else if (r > 0 && r < 5) bf_[hf][r - 1] = lld<u32x4>(fp[4 + (sl & 2) + hf] + (sl & 1) * A_STAGE + (r - 1) * 2048);
                else if (r >= 5) af[hf][r - 4] = lld<u32x4>(fp[(sl & 2) + hf] + (sl & 1) * A_STAGE + (r - 4) * 2048);
                if (pf && hf && i == 11) sv = *(volatile LAS unsigned*)(lptr)(uintptr_t)fst;
                SCHED();
            }
        LDS_WAIT();
    };
#pragma unroll 1
    for (int kt = 0; kt < 3; ++kt) {
#pragma unroll
        for (int i = 0; i < 4; ++i) { BAR(); step(i, false); }
    }
    PIN(fst);
    wait_vm<0>();
    BAR(); step(0, false);
    BAR(); __builtin_amdgcn_s_setprio(PRIOE); step(1, true);
#pragma unroll
    for (int i = 0; i < 2; ++i) {
        unsigned ss = __builtin_amdgcn_readfirstlane(sv);
        need_bit(fst, ss, 4u << (4 * i));
        TL(30 + i);
        if (i) need_bit(fst, ss, 0x100u);
        step(2 + i, i == 0);
    }
    __builtin_amdgcn_s_setprio(PRIOT);
    sig(fb + F_FREE, 1u << 20);
    float2 lam[2], coef[2], l16[2], l64[2], l512[2], cw[2], wq[2], lq[2];
#pragma unroll
    for (int n = 0; n < 2; ++n) {
        const f32x4 a = lld<f32x4>(smem + A_PRM + (wc * 32 + 2 * c + n) * 32), b = lld<f32x4>(smem + A_PRM + (wc * 32 + 2 * c + n) * 32 + 16);
        lam[n] = make_float2(a[0], a[1]); l16[n] = make_float2(a[2], a[3]); l64[n] = make_float2(b[0], b[1]); l512[n] = make_float2(b[2], b[3]);
    }
    float2 h[2];
#pragma unroll
    for (int m = 0; m < 5; ++m) {
#pragma unroll
        for (int n = 0; n < 4; ++n) {
            if (m < 4) acc[m][n] = mfb(af[1][m], bf_[1][n], acc[m][n]);
            if (m == 1 && n == 0) { h[0] = make_float2(acc[0][0][0], acc[0][2][0]); h[1] = make_float2(acc[0][1][0], acc[0][3][0]); }
            else if (m) {
#pragma unroll
                for (int i = 0; i < 2; ++i) h[i] = cfma(lam[i], h[i], make_float2(acc[m - 1][i][n], acc[m - 1][2 + i][n]));
            }
            if (SIL) SCHED();
        }
        if (m == 3) {
#pragma unroll
            for (int n = 0; n < 2; ++n) {
                const f32x4 a = lld<f32x4>(smem + A_PW + 4096 + (wc * 32 + 2 * c + n) * 16), b = lld<f32x4>(smem + A_PW + q * 1024 + (wc * 32 + 2 * c + n) * 16);
                coef[n] = make_float2(a[0], a[1]); cw[n] = wr ? make_float2(a[2], a[3]) : coef[n]; wq[n] = make_float2(b[0], b[1]); lq[n] = make_float2(b[2], b[3]);
            }
        }
    }
    TS(1);
    float2 loc[2], wag[2];
#pragma unroll
    for (int n = 0; n < 2; ++n) {
        float2 a0, a1, a2, a3;
        rows4(h[n].x, a0.x, a1.x, a2.x, a3.x);
        rows4(h[n].y, a0.y, a1.y, a2.y, a3.y);
        const float2 s2 = cfma(l16[n], a0, a1), s3 = cfma(l16[n], s2, a2);
        wag[n] = cfma(l16[n], s3, a3);
        loc[n] = sel4(q, make_float2(0.f, 0.f), a0, s2, s3);
    }
    lptr aggp = smem + A_AGG + (wc * 16 + c) * 16;
    const unsigned flg = ldsa(smem + A_FLG) + wc * 4;
    float2 W0[2] = {make_float2(0.f, 0.f), make_float2(0.f, 0.f)};
    if (wr == 0) {
        if (q == 0) lst<f32x4>(aggp, (f32x4){wag[0].x, wag[0].y, wag[1].x, wag[1].y});
        asm volatile("" ::: "memory");
        if (lane == 0) lds_add(flg);
    } else {
#pragma unroll 1
        for (int spin = 0; spin < (1 << 20); ++spin) { if (lds_peek(flg)) break; }
        const f32x4 w0 = lld<f32x4>(aggp);
        W0[0] = make_float2(w0[0], w0[1]); W0[1] = make_float2(w0[2], w0[3]);
        const float2 g0 = cfma(l64[0], W0[0], wag[0]), g1 = cfma(l64[1], W0[1], wag[1]);
        if (lt < 15 && q == 0) {
            auto tb_ = [&](float v, unsigned i) INL { return __builtin_amdgcn_perm(__float_as_uint(v), (unsigned)tag, 0x07060500u + i); };
            st128<SC1>((u32x4){tb_(g0.x, 0), tb_(g0.y, 1), tb_(g1.x, 2), tb_(g1.y, 3)}, rs, c * 16 + OFF_GRAN + ((15 - lt) * 512 + pt * 32 + wc * 16) * 16);
        }
    }
    TS(2);
    if (CE || CGW) sig(fst, 0x1000u);
    __builtin_amdgcn_s_setprio(PRIOW);
    const int da = da_of(pt), kh = da & 1;
    u32x2 xi[8], dk;
    auto xload = [&]() INL {
        const unsigned co = (512 * ((lane >> 3) & 1) + 4 * (lane & 7)) * 2;
        SCHED();
#pragma unroll
        for (int i = 0; i < 8; ++i)
            xi[i] = __builtin_amdgcn_raw_buffer_load_b64(mkrsrc(x, S_ * D_ * 2), (lane >> 4) * 64 * D_ * 2 + co, ((512 * wid + 256 * (i >> 2) + 16 * (i & 3) + lt) * D_ + 256 * kh + 32 * (da >> 1)) * 2, 0);
        dk = __builtin_amdgcn_raw_buffer_load_b64(mkrsrc(Dskip, D_ * 2), co, 512 * kh + 64 * (da >> 1), 0);
        SCHED();
    };
    if (XP == 0) xload();
#pragma unroll
    for (int n = 0; n < 2; ++n) {
        PIN(coef[n].x); PIN(coef[n].y);
        loc[n] = cmul(coef[n], cfma(wq[n], W0[n], loc[n]));
        wq[n] = cmul(wq[n], cw[n]);
        PIN(wq[n].x); PIN(wq[n].y); PIN(loc[n].x); PIN(loc[n].y);
    }
    float2 u[2][16];
#pragma unroll
    for (int t = 0; t < 16; ++t) {
#pragma unroll
        for (int n = 0; n < 2; ++n) { u[n][t] = cmul(coef[n], make_float2(acc[t >> 2][n][t & 3], acc[t >> 2][2 + n][t & 3])); PIN(u[n][t].x); PIN(u[n][t].y); }
        SCHED();
    }
    lptr tb = smem + A_HS + wid * 2048 + lane * 4, rb = smem + A_HS + wid * 2048 + (lane >> 4) * 256 + (lane & 3) * 64 + ((lane >> 2) & 3) * 16;
    unsigned hoff = OFF_H + ((lane >> 4) * 64 + pt * 2 + wc) * 8192 + lt * 512 + wr * 256 + (lane & 15) * 16;
    PIN(tb); PIN(rb); PIN(hoff);
    __builtin_amdgcn_s_setprio(PRIOT);
    await<0>(fb + F_FREE, 8u << 20);
    if (XP == 1) xload();
    if (GDL) __builtin_amdgcn_s_sleep(GDL);
    TS(3);
    {
        u32x4 gr[4];
        unsigned off = ((16 - lt + q) * 512 + pt * 32 + wc * 16 + c) * 16;
        auto gload = [&]() INL {
            PIN(off);
#pragma unroll
            for (int j = 0; j < 4; ++j) gr[j] = __builtin_amdgcn_raw_buffer_load_b128(mkrsrc(ws + OFF_GRAN + j * 32768, (4 - j) * 32768), off, 0, SC1);
        };
        auto gok = [&]() INL {
            bool ok = true;
#pragma unroll
            for (int j = 0; j < 4; ++j) {
                const unsigned t = __builtin_amdgcn_perm(__builtin_amdgcn_perm(gr[j].w, gr[j].z, 0x04000400u), __builtin_amdgcn_perm(gr[j].y, gr[j].x, 0x04000400u), 0x05040100u);
                ok = ok & ((lt - 1 - (4 * j + q) < 0) | (t == (unsigned)tag));
            }
            return (bool)__all(ok);
        };
        gload();
        if (XP == 4) xload();
        if (!gok()) {
            int spin;
#pragma unroll 1
            for (spin = 1; spin < (1 << 20); ++spin) {
                asm volatile("" ::: "memory");
                if (GSL) __builtin_amdgcn_s_sleep(GSL);
                gload();
                if (gok()) break;
            }
        }
        TS(25);
        if (XP == 5) xload();
#pragma unroll
        for (int j = 0; j < 4; ++j) gr[j] &= 0xffffff00u;
#pragma unroll
        for (int n = 0; n < 2; ++n) {
            float2 cs = make_float2(__uint_as_float(gr[3][2 * n]), __uint_as_float(gr[3][2 * n + 1]));
#pragma unroll
            for (int j = 2; j >= 0; --j) cs = cfma(l512[n], cs, make_float2(__uint_as_float(gr[j][2 * n]), __uint_as_float(gr[j][2 * n + 1])));
            cs = cmul(cs, lq[n]);
            cs.x = rowsum4(cs.x); cs.y = rowsum4(cs.y);
            loc[n] = cfma(wq[n], cs, loc[n]);
        }
    }
    TS(4);
#pragma unroll
    for (int r = 0; r < 4; ++r) {
#pragma unroll
        for (int t = 0; t < 4; ++t) {
            loc[0] = cfma(lam[0], loc[0], u[0][4 * r + t]); loc[1] = cfma(lam[1], loc[1], u[1][4 * r + t]);
            lst<unsigned>(tb + t * 256, pkb(loc[0].x, loc[1].x));
            lst<unsigned>(tb + 1024 + t * 256, pkb(-loc[0].y, -loc[1].y));
        }
        asm volatile("" ::: "memory");
#pragma unroll
        for (int pl = 0; pl < 2; ++pl) st128<SC1>(lld<u32x4>(rb + pl * 1024), rs, hoff + (r * 256 + pl * 32) * 8192);
        asm volatile("" ::: "memory");
    }
    TS(5);
    PIN(lane);
    auto cp2 = [&]() INL { if (wid == 3) cpf = __builtin_amdgcn_raw_buffer_load_b32(mkrsrc((const void*)pc, max(len, 0)), lane * 128, ((gb >> 3) % CPM) * 8192, 0); };
    if (XLP == 0) cp2();
    if (XP == 2) xload();
    wait_vm<XP == 2 ? 9 : 0>();
    TS(6);
    const bool pub = sig_rtn(fb + F_HC) == 3u;
    const u32x2 tg = {(unsigned)tag, (unsigned)(tag >> 32)};
    if (pub) {
        if (lane < HREP) __builtin_amdgcn_raw_buffer_store_b64(tg, rs, lane * 256 * FST, OFF_FH + (pt * 16 + lt) * FST, SC1);
        TS(15);
    }
    if (XLP == 1) cp2();
    if (XP == 3 || XP == 6) xload();
    __builtin_amdgcn_s_setprio(PRIOI);
    f32x4 ac[8][2];
    auto acin = [&]() INL {
#pragma unroll
        for (int i = 0; i < 8; ++i) {
            PIN(xi[i]);
            const u32x2 d = dk, v = xi[i];
            const unsigned p0 = pkb(lo2f(v.x) * (0.5f * lo2f(d.x)), hi2f(v.x) * (0.5f * hi2f(d.x))), p1 = pkb(lo2f(v.y) * (0.5f * lo2f(d.y)), hi2f(v.y) * (0.5f * hi2f(d.y)));
            ac[4][i >> 2][i & 3] = lo2f(p0); ac[5][i >> 2][i & 3] = hi2f(p0); ac[6][i >> 2][i & 3] = lo2f(p1); ac[7][i >> 2][i & 3] = hi2f(p1);
            ac[i >> 1][i & 1] = Z4;
        }
#pragma unroll
        for (int i = 0; i < 16; ++i) PIN(ac[i >> 1][i & 1]);
    };
    if (ACI == 0) acin();
    unsigned vo = ((lane & 15) >> 2) * 256 + (lane >> 4) * 64 + (lane & 3) * 16, so[32];
    PIN(vo);
#pragma unroll
    for (int k = 0; k < 32; ++k) {
        so[k] = OFF_H + lt * 524288 + kh * 131072 + wid * 2048 + (((XE & 2) ? 0 : k >> 2) ^ CX(gb)) * 16384 + ((k & 1) + (k & 2) * 16) * 8192;
        if (SPC || k < 8) asm volatile("" : "+s"(so[k]));
    }
    auto ldB = [&](int b) { return __builtin_amdgcn_raw_buffer_load_b128(rs, vo + (b & 1) * 1024, so[b >> 1], SC1); };
    lptr ab[2];
#pragma unroll
    for (int i = 0; i < 2; ++i) { ab[i] = smem + 2 * A_STAGE + (lane & 15) * 128 + (((4 * i + (lane >> 4)) ^ (lane & 7)) << 4); PIN(ab[i]); }
    auto ldA = [&](int f) INL {
        const int k = f < FT ? f_k(f) : f_k(f) ^ 12;
        if (f > 8 && !f_i(f) && (k & 6) == (f < FT ? 0 : 2)) { ab[k & 1] += (k & 8) ? -2 * A_STAGE : 2 * A_STAGE; PIN(ab[k & 1]); }
        return lld<u32x4>(ab[k & 1] + ((k >> 1) & 3) * 16384 + f_i(f) * 2048);
    };
    const unsigned hb = ldsa(smem) + 2 * A_STAGE + 8192 + wid * 2048;
    unsigned fv = fb + F_FULL, fw = fb + F_RD + wid, pa = hb + lane * 16; PIN(fv); PIN(fw); PIN(pa);
    u64 ob = (u64)(out + (512 * wid + lt) * D_ + 256 * kh + 32 * (da >> 1)); SPIN(ob);
    asm volatile("" :: "v"(cpf));
    __builtin_amdgcn_s_setprio(PRIOG);
    TS(27);
    u32x4 ba = {xi[0].x, xi[1].y, xi[2].x, xi[3].y}, bb = {xi[4].x, xi[5].y, xi[6].x, xi[7].y};
    if (pub && !HPL) {
        int spin; unsigned m = 0;
        if (BAL >= 0) burn<0>(PDL, ba, bb); else
        if (PDL) __builtin_amdgcn_s_sleep(PDL);
        if (PDE && !(PDM >> (gb & 7) & 1)) __builtin_amdgcn_s_sleep(PDE);
        if (PD0 && lt == 0) __builtin_amdgcn_s_sleep(PD0);
        TS(19);
#pragma unroll 1
        for (spin = 0; spin < (1 << 20); ++spin) {
            m = h_poll<NP1>(rs, gb, kh, lane, tg);
            if (spin == 0) TS(16);
            if (spin == 1) TS(17);
            if (spin == 2) TS(18);
            if ((m & HFIRST) == HFIRST) break;
        }
        sig_or(fb + F_FULL, m);
    }
    unsigned fe = 0, fs = BAL >= 0 ? await_burn<BAL < 0 ? 0 : BAL>(fb + F_FULL, 8u, ba, bb) : await<1>(fb + F_FULL, 8u);
    TS(7);
    auto need = [&](unsigned bit) INL { need_bit(fv, fs, bit); };
    u32x4 A[256], B[64 + NRL];
#pragma unroll
    for (int b = 0; b < NB; ++b) { if (b && !(b & 7)) need(8u << (b >> 1)); B[b] = ldB(b); SCHED(); }
    SCHED();
    if (ACI == 1) { acin(); SCHED(); }
    need(4u);
#pragma unroll
    for (int f = 0; f < AD; ++f) A[f] = ldA(f);
    SCHED();
    if (BSL) { for (int i = 0; i < BSL; ++i) __builtin_amdgcn_s_sleep(16); }
    if (BA2) burn<BS2>(BA2, A[0], A[1]);
    const unsigned po = OFF_U + ((lt * 16 + da) * 4 + wid) * 4096;
    unsigned pk[16], t0, pq = (po ^ 16384) - hb, pr = po - hb + 2 * A_STAGE, gc = ldsa(smem) + A_XT + wid * 4096 - hb, cc = ldsa(smem) + A_CN - hb, oc = ldsa(smem) + A_OG + 256 * wid, ks = 0x07060302u;
    SPIN(pq); SPIN(pr); SPIN(gc); SPIN(cc); SPIN(oc); SPIN(ks);
    lptr og, cp;
    unsigned gp[2];
    u32x4 pv[4], sl[2], ga[4], oo; f32x4 o2[2], r0[8];
    auto put = [&](int i) INL {
        const int e = i & 3, n = i >> 2;
#pragma unroll
        for (int j = 0; j < 4; ++j) r0[i][j] = fmaf(o2[n][e], ac[4 + j][n][e], r0[i][j]);
        st128<WTO>(__builtin_bit_cast(u32x4, r0[i]), mkrsrc((const char*)ob + n * 256 * D_ * 4, 241 * D_ * 4), oo[e]);
    };
    auto nextB = [&](int b) INL {
        if (b + NB >= 64) return;
        if (XE & 16) { B[b + NB] = B[b]; PIN(B[b + NB]); } else B[b + NB] = ldB(b + NB);
    };
    auto unp = [&](int j) INL {
        if (j < 0 || j > 31) return;
        const unsigned v = ga[j >> 3][(j >> 1) & 3];
        float t = (j & 1) ? hi2f(v) : lo2f(v);
        PIN(t);
        r0[j >> 2][j & 3] = t;
    };
    static_assert(GS0 + US0 + 32 <= 516 && US0 >= 8, "");
    sfor<0, 64>([&](auto ic) INL {
#define T8(x) {ST.x[s0], ST.x[s0 + 1], ST.x[s0 + 2], ST.x[s0 + 3], ST.x[s0 + 4], ST.x[s0 + 5], ST.x[s0 + 6], ST.x[s0 + 7]}
        constexpr int s0 = 8 * decltype(ic)::v, TA_[8] = T8(a), TP_[8] = T8(p), TW_[8] = T8(w);
        constexpr unsigned TM_[8] = T8(m);
#pragma unroll
        for (int u = 0; u < 8; ++u) {
            const int s = s0 + u, f = s_f(s), n = s_n(s), k = f_k(f), z = s - 2 * FT;
            MF2(B[s_b(s)], A[f], ac[f_m(f)][n]);
            SCHED();
            if (TP_[u] == 3) { fs = __builtin_amdgcn_readfirstlane(fe); SPIN(fs); }
            if (TP_[u] == 1 || TP_[u] == 2) fe = *(volatile LAS unsigned*)(lptr)(uintptr_t)(fv + (TP_[u] == 2 ? F_FB - F_FULL : 0));
            if (TM_[u]) { need_bit(fv, fs, TM_[u], (TM_[u] & 0x44u) && s > 128 ? F_FB - F_FULL : 0); if (s == GS0 && !(XE & 32)) TS(26); }
            if (z < 0 && !n) {
                if (f_hi(f)) nextB(2 * k);
                if (f_i(f) == 1 && k) nextB(2 * k - 1);
            }
            if (z < 0 && k >= 16 && k < 24) {
                const int v = s & 7;
                if (v == 6 && k == 19) { pa -= 2 * A_STAGE; PIN(pa); }
                if (v == WS0) lst<u32x4>((lptr)(uintptr_t)pa + h_off(k, 0), B[2 * k]);
                if (v == WS1) lst<u32x4>((lptr)(uintptr_t)pa + h_off(k, 1), B[2 * k + 1]);
            }
            if (z == -6) { t0 = pa + pr; PIN(t0); }
#pragma unroll
            for (int i = 0; i < 24; ++i) {
                const int j = i / 6, o = i % 6;
                if (z != (PKR == 1 ? i - 4 - j : PKR == 2 ? 3 * j + o / 2 - 4 : 2 * j + o / 3 - 4)) continue;
                if (o == 0) pk[4 * j] = __builtin_amdgcn_perm(__float_as_uint(ac[j][0][1]), __float_as_uint(ac[j][0][0]), ks);
                if (o == 1) pk[4 * j + 1] = pkb(ac[j][0][2], ac[j][0][3]);
                if (o == 2) pk[4 * j] |= 0x10001u;
                if (o == 3) pk[4 * j + 2] = pkb(ac[j][1][0], ac[j][1][1]);
                if (o == 4) pk[4 * j + 3] = pkb(ac[j][1][2], ac[j][1][3]);
                if (o < 5) PIN(pk[4 * j + (o == 2 ? 0 : o > 2 ? o - 1 : o)]);
                else { st128<SC1>((u32x4){pk[4 * j], pk[4 * j + 1], pk[4 * j + 2], pk[4 * j + 3]}, rs, t0 + j * 1024); if (j == 3 && !(XE & 32)) TS(29); }
            }
            if (z == RS0 + RSP * 8 - 2) { pa += 2 * A_STAGE; PIN(pa); }
#pragma unroll
            for (int r = 0; r < NRL; ++r) if (z == RS0 + RSP * r) B[64 + r] = lld<u32x4>((lptr)(uintptr_t)pa + h_off(23 - (r >> 1), r & 1));
            constexpr int RS = 2 * FT + RSD;
            static_assert(RSD >= RS0 + RSP * 8 - 2 && RS + 6 <= 507 && GS0 - 14 - 2 * FT >= RS0 + RSP * 8 - 2, "");
            if (s >= RS && s < RS + 8 && !((s - RS) & 1)) pv[(s - RS) >> 1] = __builtin_amdgcn_raw_buffer_load_b128(rs, pa + (s - RS) * 512, pq, SC1);
            if (s == GS0 - 20) { gp[0] = pa >> 2; PIN(gp[0]); }
            if (s == GS0 - 18) { gp[0] &= 0xc0u; PIN(gp[0]); }
            if (s == GS0 - 16) { og = (lptr)(uintptr_t)(gp[0] + oc); PIN(og); }
            if (s == GS0 - 14) { gp[0] = (gp[0] ^ pa) + gc; PIN(gp[0]); }
            if (s == GS0 - 8) { gp[1] = gp[0] ^ 32u; PIN(gp[1]); }
            if (s == GS0 - 6) { cp = (lptr)(uintptr_t)(pa + cc); PIN(cp); }
            if (s > GS0 && s <= GS0 + 4) ga[s - GS0 - 1] = lld<u32x4>((lptr)(uintptr_t)gp[(s - GS0 - 1) & 1] + (s - GS0 - 1) * 1024);
            if (s == 496) sl[0] = lld<u32x4>(cp);
            if (s == 498) sl[1] = lld<u32x4>(cp + 1024);
            if (s == 500) oo = lld<u32x4>(cp + 2048);
            if (s == 501) o2[0] = lld<f32x4>(og);
            if (s == 502) o2[1] = lld<f32x4>(og + 16);
            unp(s + (s > 507 ? 4 : 0) - GS0 - US0);
            if (const int g = TA_[u]; g) {
                A[g] = ldA(g);
                if (g < FT && f_hi(g) && (f_k(g) & 3) == 3) asm volatile("ds_write_b8 %0, %0 offset:%1" :: "v"(fw), "i"(f_k(g) & 28) : "memory");
            }
            if (TW_[u]) wait_lds(TW_[u] - 1);
            SCHED();
            if (s == 507 || s == 511) {
                if (s == 507) {
                    TS(10);
                    unsigned n;
                    asm volatile("s_mov_b32 %5, 0x100000\n"
                                 "1:\ts_waitcnt vmcnt(0)\n\tv_and_b32 %4, v" PV0 ", v" PV1 "\n\tv_and_b32 %4, v" PV2 ", %4\n\tv_and_b32 %4, v" PV3 ", %4\n\tv_and_b32 %4, 1, %4\n\t"
                                 "v_cmp_eq_u32 vcc, 0, %4\n\ts_cbranch_vccz 2f\n\t"
                                 "buffer_load_dwordx4 %0, %6, %7, %8 offen sc1\n\tbuffer_load_dwordx4 %1, %6, %7, %8 offen offset:1024 sc1\n\t"
                                 "buffer_load_dwordx4 %2, %6, %7, %8 offen offset:2048 sc1\n\tbuffer_load_dwordx4 %3, %6, %7, %8 offen offset:3072 sc1\n\t"
                                 "s_sub_u32 %5, %5, 1\n\ts_cbranch_scc0 1b\n\ts_waitcnt vmcnt(0)\n"
                                 "2:" : "+{v[" PV0 ":" PV0E "]}"(pv[0]), "+{v[" PV1 ":" PV1E "]}"(pv[1]), "+{v[" PV2 ":" PV2E "]}"(pv[2]), "+{v[" PV3 ":" PV3E "]}"(pv[3]), "=&v"(t0), "=&s"(n)
                                 : "v"(pa), "s"(mkdesc(ws, WS_END)), "s"(pq) : "memory", "scc", "vcc");
                    TS(11);
                    SCHED();
                }
#pragma unroll
                for (int m = 0; m < 4; ++m) { MF2(sl[n], pv[m], ac[4 + m][n]); SCHED(); if (n) put(m); else unp(508 + m - GS0 - US0); SCHED(); }
            }
            if (s == 15) { TS(8); TC(60); }
            if (s == 2 * F1 - 1) { if (!(XE & 32)) TS(9); TC(55); }
            if (z == -1) { if (!(XE & 32)) TS(28); TC(56); }
            if (s == 495) { TC(61); TS(14); }
        }
    });
    asm volatile("s_nop 7");
    SCHED();
#pragma unroll
    for (int i = 4; i < 8; ++i) { put(i); SCHED(); }
    TS(12);
    TSE(0, wid == 0);
    TSE(2, pub);
}

#define TSARG
#define TSPASS
struct Gate {
    unsigned xa, wa, xv, gw; float sum; int rot;
    DEV void rd(int kt) { asm volatile("ds_read_b32 %0, %2\n\tds_read_b32 %1, %3" : "=&v"(xv), "=&v"(gw) : "v"(xa + (kt & 3) * A_STAGE), "v"(wa + (kt ^ rot) * 128) : "memory"); }
    DEV void acc() {
        asm volatile("s_waitcnt lgkmcnt(0)" : "+v"(xv), "+v"(gw) :: "memory");
        const unsigned pr = pkb(lo2f(xv) * lo2f(gw), hi2f(xv) * hi2f(gw));
        sum += lo2f(pr); sum += hi2f(pr); PIN(sum);
    }
};
struct Tab { CM sf, si, ff, fi; u32x4 mt[2], xf, xb; f32x2 ti[2]; };
DEV void mid_tables(lptr smem, int gb, int w, int lane, Tab& t) {
    int mp, j1; slot_of(gb, mp, j1);
    const int c = lane & 15, q = lane >> 4;
    lptr mb = smem + A_MAT + lane * 16;
#pragma unroll
    for (int i = 0; i < 2; ++i) { t.ti[i] = lld<f32x2>(smem + A_MAT + 9216 + (2 * w + i) * 128 + c * 8); t.mt[i] = lld<u32x4>(mb + (6 + i) * 1024); }
    u32x4 sf0 = lld<u32x4>(mb + 4096), si0 = lld<u32x4>(mb + 5120);
    const u32x4 f0 = lld<u32x4>(mb + 8192);
    SCHED();
    asm volatile("" : "=v"(t.xf), "=v"(t.xb));
    if (w >= 4 && mp == 0 && !(j1 & 1)) {
        sf0 = mat_sf(64, c, q); si0 = mat_si(64, c, q);
        const float dt = -(float)(32 + 64 * c) * (1.0f / 1024.0f);
        t.xf = cmat1(dt * (float)(4 * q), dt); t.xb = cmat1((float)(c * (32 + 256 * q)) * (1.0f / 1024.0f), (float)c * (1.0f / 16.0f));
    }
    PIN(t.xf); PIN(t.xb);
    t.sf = cexp(sf0); t.si = cexp(si0); t.ff = cexp(f0); t.fi = cexp((u32x4){f0.x ^ SG, f0.y ^ SG, f0.z, f0.w});
    PIN(t.mt[0]); PIN(t.mt[1]); PIN(t.ti[0]); PIN(t.ti[1]);
}
DEV int u_exp(unsigned em) { return min(max((int)(em & 0xffu), 4), 254); }

struct Ld {
    u32x4 rx, rb, vA, vB; unsigned lb, sA, sB; int rot;
    DEV Ld(const u16* x, const u16* Bre, const u16* Bim, lptr smem, int gb, int lw, int lane) {
        int lt, pt; tile_of(gb, lt, pt);
        const int srow = lane >> 3, schunk = (lane & 7) ^ srow;
        rot = ROT;
        lb = ldsa(smem) + lw * 4096;
        const unsigned a = (16 * (srow >> 2) + (srow & 3)) * 2048 + schunk * 16, b = 2 * srow * 2048 + schunk * 16;
        vA = (u32x4){a, a + 65536 - 1024, a + 8192 - 2048, a + 73728 - 3072}; vB = (u32x4){b, b + 32768 - 1024, b, b + 34816 - 3072};
        sA = (lt * 128 + 64 * (lw >> 1) + 8 * (lw & 1)) * 2048; sB = (pt * 64 + 32 * (lw >> 1)) * 2048;
        PIN(vA.x); PIN(vA.y); PIN(vA.z); PIN(vA.w); PIN(vB.x); PIN(vB.y); vB.z = vB.x; vB.w = vB.y;
        SPIN(sA); SPIN(sB); SPIN(lb); SPIN(rot);
        SCHED();
        rx = mkdesc(x, S_ * D_ * 2); rb = mkdesc((lw & 1) ? Bim : Bre, D_ * D_ * 2);
    }
    DEV void st(int kk) const {
        dma64(rx, lb + (kk & 3) * A_STAGE, vA, sA + (kk ^ rot) * 128);
        dma64(rb, lb + (kk & 3) * A_STAGE + 16384, vB, sB + (kk ^ rot) * 128);
    }
};
DEV void first_loads(const u16* __restrict__ x, const u16* __restrict__ Bre, const u16* __restrict__ Bim, const u16* __restrict__ gate_w, lptr smem, int gb, int lw, int lane) {
    __builtin_amdgcn_s_setprio(PRIOL);
    const Ld ld(x, Bre, Bim, smem, gb, lw, lane);
    ld.st(0);
    SCHED();
    int lt, pt; tile_of(gb, lt, pt);
    dma16(mkdesc(gate_w, D_ * 2), ldsa(smem) + A_GW + (lw & 1) * 1024, lane * 16, (lw & 1) * 1024);
    const unsigned t = (lane >> 1) * 32768 + (lane & 1) * 16;
    dma64(ld.rx, ld.lb + A_XT, (u32x4){t, t + 1047552, t + 2 * 1047552, t + 3 * 1047552}, lt * 2048 + lw * 512 + pt * 32);
    if (FSL) __builtin_amdgcn_s_sleep(FSL);
    if (FLN > 1) ld.st(1);
    if (FLN > 2) ld.st(2);
}
DEV void role_load(const u16* __restrict__ x, const u16* __restrict__ Bre, const u16* __restrict__ Bim, const u16* __restrict__ Cre, const u16* __restrict__ Cim,
                   const u16* __restrict__ gate_w, char* ws, lptr smem, int gb, int lw, int lane, Tab& tab TSARG) {
    if (!ENT) __builtin_amdgcn_s_waitcnt(0x0F70);
    Gate g;
    int lt, pt; tile_of(gb, lt, pt);
    TS(0);
    if (!ENT) first_loads(x, Bre, Bim, gate_w, smem, gb, lw, lane);
    const Ld ld(x, Bre, Bim, smem, gb, lw, lane);
    if (FLN < 2) ld.st(1);
    if (FLN < 3) ld.st(2);
    const unsigned lb = ldsa(smem);
    const int gi = 8 * pt + 2 * lw + (lane >> 5);
    g.xa = lb + gi * 128 + ((((lane & 31) >> 2) ^ (gi & 7)) << 4) + (lane & 3) * 4;
    g.wa = lb + A_GW + (lane & 31) * 4; g.sum = 0.f; g.rot = ld.rot;
    TS(6);
    const int c = lane & 15, q = lane >> 4;
    const unsigned fb = lb + A_FLG;
    lptr ub = smem + A_XT + lw * 4096 + (lane >> 2) * 64 + (lane & 3) * 8, ea = smem + A_E1 + c * E1P + q * 256 + lw * 64;
    lptr mb = smem + A_MAT + lane * 16, tb = smem + A_MAT + 9216 + c * 128 + q * 32;
    PIN(ub); PIN(ea); PIN(mb); PIN(tb);
#pragma unroll 1
    for (int kt = 0; kt < 13; ++kt) { wait_vm<16>(); TL(8 + kt); BAR(); TL(28 + kt); if (kt == 0) { TS(28); TC(60); } g.rd(kt); ld.st(kt + 3); g.acc(); }
    u32x4 mt[4], ut[4];
    f32x4 tf[2];
    wait_vm<16>(); TL(21); BAR(); TL(41); g.rd(13);
#pragma unroll
    for (int i = 0; i < 4; ++i) mt[i] = lld<u32x4>(mb + i * 1024);
#pragma unroll
    for (int i = 0; i < 2; ++i) tf[i] = lld<f32x4>(tb + i * 16);
    g.acc();
#pragma unroll
    for (int i = 0; i < 4; ++i) PIN(mt[i]);
    PIN(tf[0]); PIN(tf[1]);
    wait_vm<8>(); sig(fb + F_ST, 1u); TL(42);
    wait_vm<0>(); sig(fb + F_ST, 0x10u);
    __builtin_amdgcn_s_setprio(PRIOD);
    await<0>(fb + F_ST, 0x40u);
    TS(43); TC(61);
    g.rd(14); g.acc(); g.rd(15); g.acc();
    sig(fb + F_FREE, 1u << 20);
    TS(2);
    __builtin_amdgcn_s_setprio(PRIOM);
    mid_tables(smem, gb, lw + 4, lane, tab);
    float gg = bfr(1.0f / (1.0f + expf(-bfr(half_sum(g.sum))))), og = bfr(1.0f - gg);
    const int tau = (gi & 64) | ((gi & 12) << 2) | ((gi & 48) >> 2) | (gi & 3);
    unsigned go = (lane & 31) ? ~0u : OFF_G + (tau & 15) * 1024 + (lt * 8 + (tau >> 4)) * 8;
    PIN(gg); PIN(og); PIN(go);
    __builtin_amdgcn_s_setprio(PRIOD);
    TS(53);
    const unsigned em = await<1>(fb + F_EM, 0x10000u << lw);
    TS(3);
#pragma unroll
    for (int i = 0; i < 4; ++i) ut[i] = trfrag(ub + i * 1024, 32);
    const float su = __uint_as_float((unsigned)(258 - u_exp(em)) << 23);
    tf[0] *= su; tf[1] *= su; PIN(tf[0]); PIN(tf[1]);
#pragma unroll
    for (int i = 0; i < 4; ++i) {
        f32x4 r = mfb(ut[i], mt[2], Z4), m = mfb(ut[i], mt[3], Z4);
        r = mfb(ut[i], mt[0], r); m = mfb(ut[i], mt[1], m);
        f32x4 yr, yi;
#pragma unroll
        for (int e = 0; e < 4; ++e) {
            const float tc = tf[e >> 1][2 * (e & 1)], ts = tf[e >> 1][2 * (e & 1) + 1];
            yr[e] = fmaf(r[e], tc, -m[e] * ts); yi[e] = fmaf(r[e], ts, m[e] * tc);
        }
        lst<u32x4>(ea + i * 16, pk8(yr, yi));
    }
    if (gb == XG) {
        const unsigned sg = 0xbf803f80u;
#pragma unroll
        for (int i = 0; i < 4; ++i) {
            const f32x4 e = mfb(ut[i], c == 0 ? (u32x4){sg, sg, 0, 0} : (u32x4){0, 0, sg, sg}, Z4);
            if (c < 2) lst<u32x4>(ea + 16 * E1P + i * 16, pk8(e * su, Z4));
        }
    }
    sig(fb + F_MID, 1u);
    TS(54);
    __builtin_amdgcn_s_setprio(PRIOM);
    if (C0L < 2) await<1>(fb + F_FREE, 8u << 20);
    if (CGW) await<1>(fb + F_ST, 0x4000u);
    if (C0L < 2) c_chunk<C0S>(Cre, Cim, lb, gb, da_of(pt), lw, lane, 0);
    if (C0L < 1) c_chunk<C0S>(Cre, Cim, lb, gb, da_of(pt), lw, lane, 1);
    __builtin_amdgcn_raw_buffer_store_b64((u32x2){__float_as_uint(gg), __float_as_uint(og)}, mkrsrc(ws, WS_END), go, 0, SC1);
    TS(52);
}

DEV unsigned ldw(rsrc_t r, unsigned vo, unsigned so) { return __builtin_amdgcn_raw_buffer_load_b32(r, vo, so, 0); }
DEV unsigned amax4(u32x4 p) { return pkmax(pkmax(p.x & 0x7fff7fffu, p.y & 0x7fff7fffu), pkmax(p.z & 0x7fff7fffu, p.w & 0x7fff7fffu)); }

DEV void role_aux(const u16* __restrict__ w_r, const u16* __restrict__ w_i, const u16* __restrict__ lam_re, const u16* __restrict__ lam_im,
                  const u16* __restrict__ log_dt, char* ws, lptr smem, int gb, int aw, int lane, Tab& tab TSARG) {
    int lt, pt; tile_of(gb, lt, pt);
    int mp, j1; slot_of(gb, mp, j1);
    const int c = lane & 15, q = lane >> 4;
    const bool spc = mp == 0 && !(j1 & 1);
    const rsrc_t rs = mkrsrc(ws, WS_END), rr = mkrsrc(w_r, S_ * KF * 2), ri = mkrsrc(w_i, S_ * KF * 2);
    TS(0);
    __builtin_amdgcn_s_setprio(PRIOA);
    u32x4 fch, fsh;
    const bool m0 = 16 * aw + c == 0;
    {
        const float th = (float)(16 * aw + c) * (1.0f / 128.0f);
#pragma unroll
        for (int i = 0; i < 4; ++i) {
            const float t0 = __builtin_amdgcn_fractf(th * (float)(16 * (i >> 1) + 2 * (i & 1) + 4 * q)), t1 = t0 + th;
            const float c0 = __builtin_amdgcn_cosf(t0), c1 = __builtin_amdgcn_cosf(t1);
            const float s0 = m0 ? 1.f : -__builtin_amdgcn_sinf(t0), s1 = m0 ? -1.f : -__builtin_amdgcn_sinf(t1);
            fch[i] = pkb(c0, c1); fsh[i] = pkb(s0, s1);
        }
        PIN(fch); PIN(fsh);
    }
    unsigned sg64 = (c & 1) ? SG : 0u, sgA = (c & 2) ? SG : 0u, sgB = ((c + 1) & 2) ? SG : 0u;
    constexpr unsigned WR = 128 * KF * 2;
    const bool xh = gb == XG && aw >= 2;
    const int c0 = 2 * (lane & 7) + xh, jb = (lane >> 3) + 4 * (aw & 2);
    const int k1 = mp ? mp : spc ? (lane & 4) * 16 : (j1 == 1 ? 0 : 64);
    const int kd = j1 + 4 * (spc ? c0 & 7 : c0) + 64 * jb;
    unsigned wo = (aw < 2 ? (k1 * KF + kd) * 2 + aw * 8 * WR : ((128 - k1) * KF + 1020 - kd) * 2 + (3 - aw) * 8 * WR) & ~3u;
    unsigned wo0 = aw < 2 ? wo : k1 || aw == 3 ? wo + 7 * WR : wo - 9 * WR;
    unsigned uoff = (lane >> 3) * 131072 + (lane & 7) * 16 + OFF_U + aw * 64 * 16384 + lt * 2048 + pt * 128;
    PIN(sg64); PIN(sgA); PIN(sgB); PIN(wo); PIN(wo0); PIN(uoff);
    unsigned lru, liu, ldu, um = 0;
    u32x2 tag;
    u32x4 fl[2];
    asm volatile("" : "=v"(fl[0]), "=v"(fl[1]));
    u32x3 wre[8], wim[8];
    u32x4 wsc = {0, 0, 0, 0};
    f32x4 vr[4], vi[4];
    float dt = 0.f, er = 0.f; float2 lam = make_float2(0.f, 0.f);
#pragma unroll
    for (int d1 = 0; d1 < 4; ++d1) { vr[d1] = Z4; vi[d1] = Z4; }
    auto wx = [&]() INL {
        if (j1 == 0 && aw == 3) {
            const int ks = (spc ? (lane >> 5) * 64 : mp) + 128 * c, ng = (S_ - ks) & (S_ - 1);
            unsigned o0 = (q & 1) ? (ng * KF + 511) * 2u : ks * KF * 2u, o1 = (q & 1) ? (ks * KF + 511) * 2u : ng * KF * 2u;
            PIN(o0); PIN(o1);
            wsc = (u32x4){ldw(rr, o0, 0), ldw(rr, o1, 0), ldw(ri, o0, 0), ldw(ri, o1, 0)};
        }
    };
    auto prm = [&]() INL {
        lru = ldw(mkrsrc(lam_re, 2048), (lane >> 1) * 4, pt * 128); liu = ldw(mkrsrc(lam_im, 2048), (lane >> 1) * 4, pt * 128);
        ldu = ldw(mkrsrc(log_dt, 2048), (lane >> 1) * 4, pt * 128);
    };
    if (PRS < 0) prm();
    const unsigned fb = ldsa(smem + A_FLG);
    bool uok = false;
    auto upub = [&]() INL { sig(fb + F_EM, (wave_pkmax(pkmax(pkmax(fl[0].x, fl[0].z), pkmax(fl[1].x, fl[1].z))) & 0xffu) | 0x8000u); };
    u32x4 uo[4];
    f32x4 pq[4];
    asm volatile("" : "=v"(pq[0]), "=v"(pq[1]), "=v"(pq[2]), "=v"(pq[3]));
    sfor<0, 14>([&](auto ic) INL {
        constexpr int kt = decltype(ic)::v;
        constexpr int GS[4] = GSTEP, KSO[4] = {0, 2, 3, 1}, JO[4] = {0, 2, 1, 3}, WRW[5] = WROW;
        auto ust = [&](int k) INL {
#pragma unroll
            for (int u = 0; u < 4; ++u) if (US + u / UPS == k && !(XM & 2)) st128<SC1>(uo[u], rs, uoff + JO[u] * 32768);
        };
        auto mem = [&]() INL {
            if (kt >= 10) {
#pragma unroll
                for (int r = 0; r < 8; ++r) {
                    if (r < WRW[kt < 10 ? 0 : kt - 10] || r >= WRW[kt < 10 ? 0 : kt - 9]) continue;
                    const unsigned so = r ? (aw < 2 ? r : 7 - r) * WR : 0u;
                    wre[r] = __builtin_amdgcn_raw_buffer_load_b96(rr, r ? wo : wo0, so, 0); wim[r] = __builtin_amdgcn_raw_buffer_load_b96(ri, r ? wo : wo0, so, 0);
                    if (r + 1 == URS) { SCHED(); wait_vm<2 * (URS - WRW[UCK - 10])>(); sig(fb + F_EM, 0x10000u << aw); TS(6); SCHED(); }
                }
            }
            if (kt == PRS) prm();
            if (kt == FTS && aw == 3)
                asm volatile("s_nop 0\n\tbuffer_load_dword %0, %1, 0 offen sc1 lds" :: "v"(lane * 64), "s"(mkdesc(ws + OFF_G + gb * FTB, FTB)), "{m0}"(ldsa(smem + A_AGG + 256)) : "memory");
            if (kt == WXS) wx();
            if (kt == UPL && UPL < 13 && aw == 2) flag_get(rs, OFF_FU, gb, lane, fl);
        };
        if (kt == UFS) wait_vm<0>();
        if (kt == UFS + 1) { uoff = (lane >> 2) * 128 + (lane & 3) * 16; PIN(uoff); }
        if (kt == UCK && UPRE && aw == 2) { uok = flag_ok(fl, tag); if (uok) { upub(); LDS_WAIT(); } }
        TL(8 + kt);
        BAR();
        if (VL == 2) ust(kt - 1);
        if (kt == 13 && aw == 1) {
            lst<f32x4>(smem + A_PW + lane * 16, (f32x4){1.f, 0.f, 1.f, 0.f});
#pragma unroll
            for (int i = 0; i < 4; ++i) lst<f32x4>(smem + A_PW + (i + 1) * 1024 + lane * 16, pq[i]);
            sig(fb + F_ST, 0x100u);
        }
        if (kt == UCK) {
            if (aw == 2 && !uok) {
                int spin;
                if (UPL > 12) { if (UDL) __builtin_amdgcn_s_sleep(UDL); flag_get(rs, OFF_FU, gb, lane, fl); }
#pragma unroll 1
                for (spin = 0; spin < (1 << 20); ++spin) { if (flag_ok(fl, tag)) break; flag_get(rs, OFF_FU, gb, lane, fl); }
                upub();
            }
            await<0>(fb + F_EM, 0x8000u);
            const u32x4 rw = mkdesc(ws, WS_END);
#pragma unroll
            for (int i = 0; i < 4; ++i)
                dma16s(rw, ldsa(smem + A_XT) + aw * 4096 + i * 1024, uoff, OFF_U + ((mp >> 1) * 4 + j1) * 32768 + (mp & 1) * 64 + aw * 8192 + i * 2048);
            TS(2);
        }
        if (kt == ATS) mid_tables(smem, gb, aw, lane, tab);
        u32x4 xa[4][4];
#pragma unroll
        for (int g = 0; g < 4; ++g) {
            if (GS[g] != kt) continue;
#pragma unroll
            for (int i = 0; i < 4; ++i) xa[g][i] = trfrag(smem + A_XT + i * 4096 + KSO[g] * 1024 + lane * 8, 512);
        }
        if (VL != 1) mem();
        SCHED();
#pragma unroll
        for (int g = 0; g < 4; ++g) {
            if (GS[g] != kt) continue;
#pragma unroll
            for (int i = 0; i < 4; ++i) { vr[i] = mfb(xa[g][i], fch, vr[i]); vi[i] = mfb(xa[g][i], fsh, vi[i]); PIN(vr[i]); PIN(vi[i]); }
            if (g == 0 || g == 2) { fch ^= sg64; fsh ^= sg64; PIN(fch); PIN(fsh); }
            if (g == 1) {
                const bool od = c & 1;
                const u32x4 a = (od ? fsh : fch) ^ sgA, b = (od ? fch : fsh) ^ sgB;
                fch = a; fsh = b; PIN(fch); PIN(fsh);
            }
        }
        if (kt == B1S) {
            const f32x4 t0r = vr[0] + vr[2], t1r = vr[0] - vr[2], t2r = vr[1] + vr[3], t3r = vr[1] - vr[3];
            const f32x4 t0i = vi[0] + vi[2], t1i = vi[0] - vi[2], t2i = vi[1] + vi[3], t3i = vi[1] - vi[3];
            vr[0] = t0r; vr[1] = t1r; vr[2] = t2r; vr[3] = t3r; vi[0] = t0i; vi[1] = t1i; vi[2] = t2i; vi[3] = t3i;
#pragma unroll
            for (int i = 0; i < 4; ++i) { PIN(vr[i]); PIN(vi[i]); }
        }
#pragma unroll
        for (int u = 0; u < 4; ++u) {
            if (US + u / UPS != kt) continue;
            const int j = JO[u];
            f32x4 a, b;
            if (j == 0) { a = vr[0] + vr[2]; b = vi[0] + vi[2]; }
            if (j == 2) { a = vr[0] - vr[2]; b = vi[0] - vi[2]; }
            if (j == 1) { a = vr[1] + vi[3]; b = vi[1] - vr[3]; if (aw == 0) { if (m0) { a = vr[1]; b = -vr[3]; } } }
            if (j == 3) { a = vr[1] - vi[3]; b = vi[1] + vr[3]; if (aw == 0) { if (m0) { a = vi[1]; b = vi[3]; } } }
            uo[u] = quads(smem + A_XT + aw * 4096 + j * 1024, lane, (u32x2){pkb(a[0], a[1]), pkb(a[2], a[3])}, (u32x2){pkb(b[0], b[1]), pkb(b[2], b[3])});
            uo[u].x = (uo[u].x + ((uo[u].x >> 1) & 1u)) & ~1u;
            um = pkmax(um, amax4(uo[u]));
            PIN(um); PIN(uo[u]);
#pragma unroll
            for (int i = 0; i < 4; ++i) if (u < 3 && ((i & 1) || u == 0)) { PIN(vr[i]); PIN(vi[i]); }
        }
        if (kt == UFS - 1) {
            um = wave_pkmax(pkmax(um, um >> 16));
            if (lane == 0) lst<unsigned>(smem + A_FLG + F_MAX + aw * 4, um);
            tag = lld<u32x2>(smem + A_FLG + F_TAG); PIN(tag);
        }
        if (kt == P0S) {
            PIN(lru); PIN(liu); PIN(ldu);
            if (lane & 1) { lru >>= 16; liu >>= 16; ldu >>= 16; }
            PIN(lru); PIN(liu); PIN(ldu);
        }
        if (kt >= P0S && kt < P0S + 5 && aw == 1) {
            const float lr = lo2f(lru), li = lo2f(liu);
            if (kt == P0S) { dt = bfr(expf(lo2f(ldu))); er = expf(dt * lr); PIN(dt); PIN(er); }
            if (kt == P0S + 1) { float sn, cs; sincosf(dt * li, &sn, &cs); lam = make_float2(er * cs, er * sn); PIN(lam.x); PIN(lam.y); }
            if (kt == P0S + 2) {
                const float nr = lam.x - 1.0f, ni = lam.y, den = lr * lr + li * li;
                pq[3][0] = (nr * lr + ni * li) / den; pq[3][1] = (ni * lr - nr * li) / den;
                PIN(pq[3]);
            }
            if (kt == P0S + 3) {
                float2 l16 = lam;
#pragma unroll
                for (int i = 0; i < 4; ++i) l16 = cmul(l16, l16);
                const float2 l32 = cmul(l16, l16), l48 = cmul(l32, l16);
                lst<f32x4>(smem + A_PRM + lane * 32, (f32x4){lam.x, lam.y, l16.x, l16.y});
                lam = cmul(l32, l32);
                pq[0][0] = l16.x; pq[0][1] = l16.y; pq[1][0] = l32.x; pq[1][1] = l32.y; pq[2][0] = l48.x; pq[2][1] = l48.y;
                PIN(pq[0]); PIN(pq[1]); PIN(pq[2]); PIN(lam.x); PIN(lam.y);
            }
            if (kt == P0S + 4) {
                const float2 l128 = cmul(lam, lam), l256 = cmul(l128, l128), l384 = cmul(l256, l128), l512 = cmul(l256, l256), cl = cmul(make_float2(pq[3][0], pq[3][1]), lam);
                lst<f32x4>(smem + A_PRM + lane * 32 + 16, (f32x4){lam.x, lam.y, l512.x, l512.y});
                pq[0][2] = l128.x; pq[0][3] = l128.y; pq[1][2] = l256.x; pq[1][3] = l256.y; pq[2][2] = l384.x; pq[2][3] = l384.y; pq[3][2] = cl.x; pq[3][3] = cl.y;
                PIN(pq[0]); PIN(pq[1]); PIN(pq[2]); PIN(pq[3]);
            }
        }
        SCHED();
        if (kt == UFS && aw == 0) {
            const u32x4 m4 = lld<u32x4>(smem + A_FLG + F_MAX);
            const unsigned m = pkmax(pkmax(m4.x, m4.y), pkmax(m4.z, m4.w));
            flag_put(rs, OFF_FU, gb, (u32x2){(tag.x & 0xffff0000u) | ((m & 0xffffu) >> 7), tag.y}, lane);
        }
        if (VL != 2) ust(kt);
        if (VL == 1) mem();
        LDS_WAIT();
        SCHED();
    });
    TS(54);
    if (gb == XG && aw == 0 && lane == 0) __builtin_amdgcn_raw_buffer_store_b64(tag, rs, 0, OFF_E, SC1);
    __builtin_amdgcn_s_setprio(PRIOM);
    if (ATS == 14) mid_tables(smem, gb, aw, lane, tab);
    await<1>(fb + F_FREE, 8u << 20);
    TS(3);
    unsigned mx = 0;
    const bool skip = j1 == 0 && (lane & (spc ? 59 : 63)) == 0 && !xh, x8 = xh && (lane & 3) == 3;
    lptr wd[2];
    wd[0] = smem + A_W + (aw & 1) * 512 + c0 * 1024 + ((jb ^ c0) << 4);
    wd[1] = smem + A_W + (aw & 1) * 512 + (x8 ? 16384 + (lane & 4) * 256 + (jb << 4) : (c0 + 1) * 1024 + ((jb ^ (c0 + 1)) << 4));
    if (skip) wd[0] = smem + A_W + 18432;
    if (aw >= 2) { lptr t = wd[0]; wd[0] = wd[1]; wd[1] = t; }
    const unsigned wsel = (mp + j1) & 1 ? 0x07060302u : 0x05040100u;
    PIN(wd[0]); PIN(wd[1]);
    u32x4 wp[2][2];
#pragma unroll
    for (int i = 0; i < 2; ++i) {
#pragma unroll
        for (int e = 0; e < 4; ++e) {
            wp[0][i][e] = __builtin_amdgcn_perm(wim[4 * i + e].x, wre[4 * i + e].x, wsel);
            wp[1][i][e] = __builtin_amdgcn_perm(wim[4 * i + e].z, wre[4 * i + e].z, wsel);
        }
        lst<u32x4>(wd[0] + i * 256, wp[0][i]); lst<u32x4>(wd[1] + i * 256, wp[1][i]);
        mx = pkmax(mx, pkmax(amax4(wp[0][i]), amax4(wp[1][i])));
    }
    if (j1 == 0) {
        if (aw == 3) {
            PIN(wsc);
            const unsigned sel = (q & 1) ? 0x07060302u : 0x05040100u, p0 = __builtin_amdgcn_perm(wsc.z, wsc.x, sel), p1 = __builtin_amdgcn_perm(wsc.w, wsc.y, sel);
            mx = pkmax(mx, pkmax(p0 & 0x7fff7fffu, p1 & 0x7fff7fffu));
            const int it = spc ? (lane >> 5) * 8 : 0;
            lst<unsigned>(smem + A_W + it * 1024 + (c >> 2) * 256 + ((((q & 1) * 8) ^ it) << 4) + (c & 3) * 4,
                          pkb(0.5f * (lo2f(p0) + lo2f(p1)), 0.5f * (hi2f(p0) - hi2f(p1))));
        }
        if (gb == XG && (xh ? x8 : !(lane & 3) && !skip)) {
            lptr wm = smem + A_W + (xh ? 16384 + (lane & 4) * 256 + ((15 - jb) << 4) : c0 * 1024 + (((16 - jb) ^ c0) << 4));
#pragma unroll
            for (int r = 0; r < 8; ++r) {
                const int km = (16 - ((lane >> 2) & 1) - 8 * (aw & 1) - r) & 15;
                lst<unsigned>(wm + (km >> 2) * 256 + (km & 3) * 4, wp[0][r >> 2][r & 3]);
            }
        }
    }
    mx = wave_pkmax(pkmax(mx, mx >> 16));
    if (lane == 0) lst<unsigned>(smem + A_FLG + F_WM + aw * 4, mx);
    sig(ldsa(smem + A_FLG + F_MID), 1u);
    wait_vm<0>();
    SCHED();
}

DEV int role_mid(const u16* __restrict__ Cre, const u16* __restrict__ Cim, char* ws, lptr smem, int gb, int w, int lane, u32x2& tag, const Tab& tab TSARG) {
    int mp, j1; slot_of(gb, mp, j1);
    const int c = lane & 15, q = lane >> 4;
    const bool xtr = gb == XG, ld = w >= 4;
    const rsrc_t rs = mkrsrc(ws, WS_END);
    const unsigned lb = ldsa(smem), fb = lb + A_FLG;
    const CM &sf = tab.sf, &si = tab.si, &ff = tab.ff, &fi = tab.fi;
    const f32x2 (&ti)[2] = tab.ti;
    lptr mb = smem + A_MAT + lane * 16;
    u32x4 wv[2];
    TS(4);
    await<MSL>(fb + F_MID, 24u);
    u32x4 dv[2];
#pragma unroll
    for (int i = 0; i < 2; ++i) dv[i] = lld<u32x4>(smem + A_E1 + (2 * w + i) * E1P + lane * 16);
    const u32x4 m4 = lld<u32x4>(smem + A_FLG + F_WM);
    tag = lld<u32x2>(smem + A_FLG + F_TAG);
    const unsigned em = lld<unsigned>(smem + A_FLG + F_EM);
#pragma unroll
    for (int i = 0; i < 2; ++i) wv[i] = lld<u32x4>(smem + A_W + (2 * w + i) * 1024 + ((lane ^ (2 * w + i)) << 4));
#pragma unroll
    for (int i = 0; i < 2; ++i) dv[i] = pk8(mfh(dv[i], ff.re, Z4), mfh(dv[i], ff.im, Z4));
    const unsigned ew = min((pkmax(pkmax(m4.x, m4.y), pkmax(m4.z, m4.w)) & 0xffffu) >> 7, 245u);
    const float sw = __uint_as_float((246u - ew) << 23), swi = c < 8 ? sw : -sw;
    wv[0] = wcvt(wv[0], sw, swi); wv[1] = wcvt(wv[1], sw, swi);
#pragma unroll
    for (int i = 0; i < 2; ++i) dv[i] = wmul(mfh(sf.re, dv[i], Z4), mfh(sf.im, dv[i], Z4), wv[i]);
#pragma unroll
    for (int i = 0; i < 2; ++i) dv[i] = pk8(mfh(dv[i], si.re, Z4), mfh(dv[i], si.im, Z4));
    {
        f32x4 r[2], m[2];
#pragma unroll
        for (int i = 0; i < 2; ++i) {
            const f32x4 a = mfh(dv[i], fi.re, Z4), b = mfh(dv[i], fi.im, Z4);
#pragma unroll
            for (int e = 0; e < 4; ++e) { r[i][e] = fmaf(a[e], ti[i][0], b[e] * ti[i][1]); m[i][e] = fmaf(b[e], ti[i][0], -a[e] * ti[i][1]); }
        }
#pragma unroll
        for (int e = 0; e < 4; ++e)
            lst<u32x2>(smem + A_XT + (4 * q + e) * 1024 + (w >> 1) * 256 + c * 16 + (w & 1) * 8, (u32x2){pkh(r[0][e], r[1][e]), pkh(m[0][e], m[1][e])});
    }
    if (xtr && (w & 6) == 4) {
        const int it = 5 - w;
        const CM f = cexp(tab.xf), sf2 = cexp(it ? sf.im : lld<u32x4>(mb + 4096)), si2 = cexp(it ? si.im : lld<u32x4>(mb + 5120));
        u32x4 d = lld<u32x4>(smem + A_E1 + (16 + it) * E1P + lane * 16);
        d = pk8(mfh(d, f.re, Z4), mfh(d, f.im, Z4));
        d = wmul(mfh(sf2.re, d, Z4), mfh(sf2.im, d, Z4), wcvt(lld<u32x4>(smem + A_W + 16384 + it * 1024 + lane * 16), sw, swi));
        d = pk8(mfh(d, si2.re, Z4), mfh(d, si2.im, Z4));
        const f32x4 r = mfh(d, cexp(tab.xb).re, Z4);
#pragma unroll
        for (int e = 0; e < 4; ++e) lst<float>(smem + A_EX + (it * 256 + (4 * q + e) * 16 + c) * 4, r[e]);
    }
    TS(5);
    sig(fb + F_MID, 1u);
    auto clate = [&]() INL {
        if (ld) {
            int lt, pt; tile_of(gb, lt, pt);
            if (C0L > 1) c_chunk<C0S>(Cre, Cim, lb, gb, da_of(pt), w - 4, lane, 0);
            c_chunk<C0S>(Cre, Cim, lb, gb, da_of(pt), w - 4, lane, 1);
        }
    };
    if (C0L && C0P == 0) clate();
    await<0>(fb + F_MID, 16u);
    auto cearly = [&]() INL {
        if (ld) {
            int lt, pt; tile_of(gb, lt, pt);
            await<0>(fb + F_ST, 0x4000u);
            c_chunk<CES>(Cre, Cim, lb, gb, da_of(pt), w - 4, lane, 2);
            if (CE > 1 && w < 7) c_chunk<CES>(Cre, Cim, lb, gb, da_of(pt), w - 4, lane, 3);
        }
    };
    if (CE && !CEL) cearly();
    {
        f32x4 xa[2], xe[2];
#pragma unroll
        for (int i = 0; i < 2; ++i) { xa[i] = Z4; xe[i] = Z4; }
        if (xtr) {
            const float sg = (c & 1) ? -0.125f : 0.125f;
#pragma unroll
            for (int i = 0; i < 2; ++i) {
                xa[i] = lld<f32x4>(smem + A_EX + (2 * w + i) * 64 + q * 16) * sg;
                xe[i] = lld<f32x4>(smem + A_EX + 1024 + (2 * w + i) * 64 + q * 16) * -sg;
            }
        }
#pragma unroll
        for (int i = 0; i < 2; ++i) {
            const int s2 = 2 * w + i;
            const u32x4 ea = lld<u32x4>(smem + A_XT + s2 * 1024 + lane * 16);
            const f32x4 ta = mfh(ea, tab.mt[0], xa[i]), tb = mfh(ea, tab.mt[1], xe[i]);
            u32x4 tq = quads(smem + A_XT + s2 * 1024, lane, (u32x2){pkh(ta[0], ta[1]), pkh(ta[2], ta[3])}, (u32x2){pkh(tb[0], tb[1]), pkh(tb[2], tb[3])});
            PIN(tq);
            if (C0L && C0P == 1 && i == 0) clate();
            if (CE && CEL && i == 0) cearly();
            if (TWH && i == 0) await<1>(fb + F_HC, TWH);
            if (CB >= 2 && i == 0 && ld) {
                int lt, pt; tile_of(gb, lt, pt);
                c_chunk(Cre, Cim, lb, gb, da_of(pt), w - 4, lane, 2);
                if (CB == 2) c_chunk(Cre, Cim, lb, gb, da_of(pt), w - 4, lane, 3);
            }
            st128<SC1>(tq, rs, (lane >> 3) * 32768 + (lane & 7) * 16 + OFF_T + s2 * 262144 + j1 * 8192 + mp * 128);
        }
    }
    TS(7);
    tag.x = (tag.x & 0xffff0000u) | ew;
    return u_exp(em) - 267;
}

DEV void t_done(rsrc_t rs, unsigned fb, int gb, u32x2 tag, int lane) {
    int mp, j1; slot_of(gb, mp, j1);
    if (sig_rtn(fb + F_TC) == 7u) flag_put(rs, OFF_FT, (mp >> 3) * 32 + j1 * 8 + (mp & 7), tag, lane);
}

DEV void role_cload(const u16* __restrict__ Cre, const u16* __restrict__ Cim, char* ws, lptr smem, int gb, int lw, int lane, u32x2 tag TSARG) {
    int lt, pt; tile_of(gb, lt, pt);
    pt = da_of(pt);
    const unsigned lb = ldsa(smem), fb = lb + A_FLG;
    __builtin_amdgcn_s_setprio(3);
    static_assert(!CB || (TWH == 4 && !CE), "");
    if (CB == 1) c_chunk(Cre, Cim, lb, gb, pt, lw, lane, 2);
    if (CB & 1) c_chunk(Cre, Cim, lb, gb, pt, lw, lane, 3);
    wait_vm<CB == 1 ? 16 : CB == 3 ? 8 : 0>();
    t_done(mkrsrc(ws, WS_END), fb, gb, tag, lane);
    TS(44);
    sig(fb + F_FULL, CE > 1 && lw < 3 ? 0x1111u : CE ? 0x111u : 0x11u);
    auto hpoll = [&](unsigned m, unsigned end) INL {
        int spin;
#pragma unroll 1
        for (spin = 0; spin < (1 << 20) && (m & end) != end; ++spin) {
            m = h_poll<2>(mkrsrc(ws, WS_END), gb, pt & 1, lane, tag);
            sig_or(fb + F_FULL, m);
            if (spin == 0) TS(16);
        }
    };
    if (TWH != 4) await<2>(fb + F_HC, 4u);
    if (HPL == 1 && lw == HPW) { if (PDL) __builtin_amdgcn_s_sleep(PDL); TS(19); hpoll(0, CE > 1 ? 0x88888888u : 8u); }
    if (HPL == 1 && CW) await<1>(fb + F_FULL, 8u);
    if (!CE && !CB) c_chunk(Cre, Cim, lb, gb, pt, lw, lane, 2);
    if (CE < 2 || lw == 3) {
        if (!CB) c_chunk(Cre, Cim, lb, gb, pt, lw, lane, 3);
        wait_vm<0>();
        sig(fb + F_FULL, CE ? 0x1000u : 0x1100u);
    }
    TS(47);
    if (lw == HPW && !(HPL == 1 && CE > 1)) hpoll(await<2>(fb + F_FULL, 8u) & 0x88888888u, 0x88888888u);
    const unsigned sa = fb + ((lw & 1) ? F_FB : F_FULL);
    unsigned sv = 0;
#pragma unroll 1
    for (int i = 4; i < 8; ++i) {
        const int j = (lw & 1) ? 11 - i : i;
        await_all<2>(fb + F_RD + 4 * (i - 4) + 16 * (lw & 1), 0x40404040u);
        if (!(XE & 1)) c_chunk<CSL>(Cre, Cim, lb, gb, pt, lw, lane, j, ((i + 2) & 3) * A_STAGE + (lw & 2) * 8192);
        wait_vm<8>();
        sig(sa, sv);
        sv = 2u << (4 * j);
        TS(44 + i);
    }
    wait_vm<0>();
    sig(sa, sv);
    TSE(1, lw == 0);
}

DEV void role_last(char* ws, lptr smem, int gb, int w, int lane, u32x2 tag, int fe TSARG) {
    int s2t, dat; tile_of(gb, s2t, dat);
    dat = da_of(dat);
    const int c = lane & 15, q = lane >> 4, kh = dat & 1;
    const rsrc_t rs = mkrsrc(ws, WS_END);
    const unsigned fb = ldsa(smem + A_FLG);
    SCHED();
    u32x4 fc[2][2], fs[2][2];
    unsigned ce[2], se[2];
#pragma unroll
    for (int r = 0; r < 2; ++r) {
        float dt = (float)(32 * w + 16 * r + c) * (1.0f / 128.0f); PIN(dt);
        float cc[8], ss[8];
        const float t0 = __builtin_amdgcn_fractf(dt * (float)(4 * q)), t1 = __builtin_amdgcn_fractf(dt * (float)(16 + 4 * q));
#pragma unroll
        for (int e = 0; e < 8; ++e) { const float f = fmaf((float)(e & 3), dt, e < 4 ? t0 : t1); cc[e] = __builtin_amdgcn_cosf(f); ss[e] = __builtin_amdgcn_sinf(f); }
        fc[0][r] = (u32x4){pkh(cc[0], cc[1]), pkh(cc[2], cc[3]), pkh(cc[4], cc[5]), pkh(cc[6], cc[7])};
        fs[0][r] = (u32x4){pkh(ss[0], ss[1]), pkh(ss[2], ss[3]), pkh(ss[4], ss[5]), pkh(ss[6], ss[7])};
        PIN(fc[0][r]); PIN(fs[0][r]);
        if (r == TDR) { wait_vm<0>(); t_done(rs, fb, gb, tag, lane); TS(44); }
    }
#pragma unroll
    for (int r = 0; r < 2; ++r) {
        fc[1][r] = ((c & 1) ? fs[0][r] : fc[0][r]) ^ (((c + 1) & 2) ? SG : 0u);
        fs[1][r] = ((c & 1) ? fc[0][r] : fs[0][r]) ^ ((c & 2) ? SG : 0u);
        ce[r] = fc[0][r].x; se[r] = fs[0][r].x;
        if (q == 0) {
            ce[r] = (ce[r] & 0xffff0000u) | 0x3800u; se[r] = (se[r] & 0xffff0000u) | ((c & 1) ? 0x3800u : 0xb800u);
            fs[0][r].x = (se[r] & 0xffff0000u) | ((c & 1) ? 0xbc00u : 0x3c00u);
        }
        PIN(fc[1][r]); PIN(fs[1][r]); PIN(fs[0][r]); PIN(ce[r]); PIN(se[r]);
    }
    const int ls = lane ^ (2 * (kh & w));
    const bool sp = w == 1 && lane < 8;
    lptr tw = smem + A_XT + ((lane >> 2) & 1) * 8192 + w * 2048 + ((ls >> 1) & 1) * 1024 + (((lane >> 3) ^ (ls & 6)) << 5) + (lane & 1) * 16;
    lptr tsc = smem + A_MAT + (w >> 1) * 512 + (w & 1) * 32 + (lane >> 3) * 4, tr[4];
#pragma unroll
    for (int i = 0; i < 4; ++i) {
        tr[i] = smem + A_XT + (i >> 1) * 8192 + (i & 1) * 1024 + ((lane * 8) ^ (i * 64));
        PIN(tr[i]);
    }
    lptr aw_ = smem + A_OG + (c >> 2) * 64 + 256 * w + (c & 3) * 4, rw[4];
#pragma unroll
    for (int i = 0; i < 4; ++i) { rw[i] = smem + A_XT + w * 4096 + (c & 2) * 512 + (c >> 2) * 256 + (((4 * i + q) ^ (c & 14)) << 4) + (c & 1) * 8; PIN(rw[i]); }
    {
        const unsigned v = q == (c >> 2) ? 0x3f80u << (16 * (c & 1)) : 0u, x = (c & 2) ? 0u : v, y = v ^ x, o = q * 64 * D_ * 4 + (c >> 3) * 2048 + (c & 7) * 16;
        lst<u32x4>(smem + A_CN + w * 1024 + lane * 16, w == 0 ? (u32x4){x, y, 0, 0} : w == 1 ? (u32x4){0, 0, x, y} : (u32x4){o, o + 16 * D_ * 4, o + 32 * D_ * 4, o + 48 * D_ * 4});
    }
    unsigned o = lane * 16, o4 = (sp ? lane ^ (ls & 2) : lane) * 16, ft = fb + F_TT;
    unsigned n0 = kh && (((w & 1) + 1 - ((lane >> 1) & 1)) & 2) ? SG : 0u, n1 = kh ? n0 ^ SG : 0u, n4 = kh && (((w & 1) + 3 - ((o4 >> 5) & 1)) & 2) ? SG : 0u;
    unsigned m0 = sp && (ls & 2) ? 0u : ~0u, m4 = sp && !(ls & 2) ? 0u : ~0u;
    PIN(tw); PIN(tsc); PIN(aw_); PIN(o); PIN(o4); PIN(ft); PIN(n0); PIN(n1); PIN(n4); PIN(m0); PIN(m4);
    TS(45);
    __builtin_amdgcn_s_setprio(3);
    if (w == 0) {
        u32x4 fl[2];
        if (TP0 && !(XTP && gb == XG)) __builtin_amdgcn_s_sleep(TP0);
        TS(50);
#pragma unroll 1
        for (int spin = 0; spin < (1 << 20); ++spin) { if (TSL) __builtin_amdgcn_s_sleep(TSL); flag_get(rs, OFF_FT, gb, lane, fl); if (flag_ok(fl, tag)) break; }
        const unsigned em = wave_pkmax(pkmax(pkmax(fl[0].x, fl[0].z), pkmax(fl[1].x, fl[1].z))) & 0xffu;
#pragma unroll
        for (int i = 0; i < 2; ++i) {
            const float a = ldexpf(1.f, (int)(fl[i].x & 0xffu) - (int)em - 1), b = ldexpf(1.f, (int)(fl[i].z & 0xffu) - (int)em - 1);
            lst<u32x2>(smem + A_MAT + i * 512 + lane * 8, (u32x2){pkh(a, a), pkh(b, b)});
        }
        sig(fb + F_TR, em | 0x8000u);
        sig(fb + F_FB, 4u);
    }
    fe += 1 + (await<1>(fb + F_TR, 0x8000u) & 0xffu);
    TS(46);
    if (XE & 4) { sig(fb + F_FB, 0x10u); TSE(w ? 4 : 5, w == 0 || w == 2); asm volatile("s_endpgm"); }
    u32x4 tv[8];
#pragma unroll
    for (int i = 0; i < 8; ++i) {
        tv[i] = __builtin_amdgcn_raw_buffer_load_b128(rs, (i == 1 ? o4 : o) + (i >> 1) * 1024, OFF_T + (s2t * 8 + (dat >> 1)) * 32768 + ((w & 1) + (i & 1) * 2) * 8192 + (w >> 1) * 4096, SC1);
        if (XE & 64) { tv[i] = fc[0][0]; PIN(tv[i]); }
        if (TLS) __builtin_amdgcn_s_sleep(TLS);
        SCHED();
    }
    if (XE & 64) __builtin_amdgcn_s_sleep(32);
    if (XE & 128) { for (int i = 0; i < 8; ++i) PIN(tv[i]); sig(fb + F_FB, 0x10u); TSE(w ? 4 : 5, w == 0 || w == 2); asm volatile("s_endpgm"); }
    u32x2 gg[2];
#pragma unroll
    for (int r = 0; r < 2; ++r) gg[r] = __builtin_amdgcn_raw_buffer_load_b64(rs, c * 8 + r * 128, OFF_G + s2t * 1024 + w * 256, SC1);
    __builtin_amdgcn_s_setprio(PRIOR);
    unsigned ts[8];
#pragma unroll
    for (int i = 0; i < 8; ++i) {
        ts[i] = lld<unsigned>(tsc + (i >> 1) * 128 + (i & 1) * 64) ^ (i == 1 ? n4 : (i & 1) ? n1 : n0);
        if (i < 2) ts[i] &= i ? m4 : m0;
        PIN(ts[i]);
    }
    auto h8 = [&](u32x4 v) INL { return __builtin_bit_cast(f16x8, v); };
#pragma unroll
    for (int i = 0; i < 8; i += 2) {
        const f16x8 t = h8(tv[i]) * h8((u32x4){ts[i], ts[i], ts[i], ts[i]});
        lst<f16x8>(tw + i * 128, __builtin_elementwise_fma(h8(tv[i + 1]), h8((u32x4){ts[i + 1], ts[i + 1], ts[i + 1], ts[i + 1]}), t));
        SCHED();
    }
    sig(ft, 1u);
    TS(47);
    float g[2];
#pragma unroll
    for (int r = 0; r < 2; ++r) { g[r] = ldexpf(__uint_as_float(gg[r].x), fe); PIN(g[r]); lst<float>(aw_ + 16 * r, 2.0f * __uint_as_float(gg[r].y)); }
    await<AWS>(ft, 4u);
    if (XE & 256) { sig(fb + F_FB, 0x10u); TSE(w ? 4 : 5, w == 0 || w == 2); asm volatile("s_endpgm"); }
    u32x4 tf[16];
#pragma unroll
    for (int i = 0; i < 16; ++i) { tf[i] = trfrag(tr[(i & 1) * 2 + ((i >> 1) & 1)] + ((i >> 2) & 1) * 4096 + (1 - (i >> 3)) * 2048, 512); SCHED(); }
    sig(ft, 0x10u);
    f32x4 P[2][2], O[2][2];
#pragma unroll
    for (int i = 0; i < 4; ++i) O[i >> 1][i & 1] = Z4;
    if (XE & 512) { for (int i = 0; i < 16; ++i) PIN(tf[i]); sig(fb + F_FB, 0x10u); TSE(w ? 4 : 5, w == 0 || w == 2); asm volatile("s_endpgm"); }
    if (MWT) wait_lds(MWT - 1);
    __builtin_amdgcn_s_setprio(PRX);
    SCHED();
#pragma unroll
    for (int i = 0; i < 8; ++i) {
#pragma unroll
        for (int r = 0; r < 2; ++r) O[r][i & 1] = mfh(tf[i], (i & 2) ? fs[i >> 2][r] : fc[i >> 2][r], O[r][i & 1]);
        SCHED();
        if (i == 5) {
#pragma unroll
            for (int r = 0; r < 2; ++r) { fc[0][r].x = ce[r]; fs[0][r].x = se[r]; PIN(fc[0][r]); PIN(fs[0][r]); }
            SCHED();
        }
    }
#pragma unroll
    for (int i = 0; i < 8; ++i) {
#pragma unroll
        for (int r = 0; r < 2; ++r) P[r][i & 1] = mfh(tf[8 + i], (i & 2) ? fs[i >> 2][r] : fc[i >> 2][r], i < 2 ? O[r][i] : P[r][i & 1]);
        SCHED();
    }
    __builtin_amdgcn_s_setprio(PRV);
    TS(53);
    if (XE & 1024) { for (int i = 0; i < 4; ++i) { PIN(P[i >> 1][i & 1]); PIN(O[i >> 1][i & 1]); } sig(fb + F_FB, 0x10u); TSE(w ? 4 : 5, w == 0 || w == 2); asm volatile("s_endpgm"); }
    await<0>(ft, 0x40u);
#pragma unroll
    for (int i = 0; i < 8; ++i) {
        const int r = i >> 2;
        float av[4];
#pragma unroll
        for (int e = 0; e < 4; ++e) av[e] = g[r] * __uint_as_float(pkb(0.f, (i & 2) ? fmaf(O[r][i & 1][e], -2.0f, P[r][i & 1][e]) : P[r][i & 1][e]));
        lst<u32x2>(rw[i & 3] + r * 2048, (u32x2){pkb(av[0], av[1]), pkb(av[2], av[3])});
    }
    sig(fb + F_FB, 0x10u);
    TS(48);
    asm volatile(".subsection 2\ngm_end:\n\t.fill 64, 4, 0xbf800000\n\t.subsection 0");
    TSE(w ? 4 : 5, w == 0 || w == 2);
}

NOPK __global__ __launch_bounds__(768, 3) void gm_kernel(const u16* __restrict__ x, const u16* __restrict__ Bre, const u16* __restrict__ Bim,
                                                         const u16* __restrict__ gate_w, char* ws, const u16* __restrict__ lam_re,
                                                         const u16* __restrict__ lam_im, const u16* __restrict__ log_dt,
                                                         const u16* __restrict__ w_r, const u16* __restrict__ w_i, const u16* __restrict__ Cre,
                                                         const u16* __restrict__ Cim, const u16* __restrict__ Dskip, float* __restrict__ out) {
    extern __shared__ __attribute__((aligned(16))) char smem_[];
    lptr smem = (lptr)smem_;
    unsigned t00 = 0;
    const int wid = __builtin_amdgcn_readfirstlane(threadIdx.x >> 6), lane = threadIdx.x & 63;
    u32x4 cd; unsigned cpf = 0, ct;
    asm volatile("s_getpc_b64 s[40:41]\ngm_beg:\n\ts_mov_b32 s42, gm_end - gm_beg\n\ts_mov_b32 s43, 0x20000\n\ts_and_b32 s41, s41, 0xffff\n\t"
                 "s_cmp_lg_u32 %3, %6\n\ts_cbranch_scc1 1f\n\t"
                 "s_bfe_u32 %2, %4, 0x30003\n\ts_mov_b32 m0, %7\n\ts_lshl_b32 %2, %2, 13\n\tv_lshlrev_b32 %1, 7, %5\n\tbuffer_load_dword %1, s[40:43], %2 offen lds\n1:"
                 : "=&{s[40:43]}"(cd), "=&v"(cpf), "=&s"(ct) : "s"(wid), "s"(blockIdx.x), "v"(lane), "i"(ENT ? 3 : 99), "i"(A_AGG) : "scc", "m0", "memory");
    if (ENT && (wid & 12) == 4) first_loads(x, Bre, Bim, gate_w, smem, blockIdx.x, wid - 4, lane);
    int w = wid & 7, gb = blockIdx.x;
    asm volatile("" : "+s"(w), "+s"(gb));
    if (wid < 4) role_mma(x, Dskip, ws, out, smem, blockIdx.x, wid, lane, (u64)cd.x | (u64)cd.y << 32, cd.z, 0, t00);
    else {
        TSD();
        u32x2 tag;
        Tab tab;
        if (wid < 8) role_load(x, Bre, Bim, Cre, Cim, gate_w, ws, smem, blockIdx.x, wid - 4, lane, tab TSPASS);
        else role_aux(w_r, w_i, lam_re, lam_im, log_dt, ws, smem, blockIdx.x, wid - 8, lane, tab TSPASS);
        const int fe = role_mid(Cre, Cim, ws, smem, gb, w, lane, tag, tab TSPASS);
        if (w >= 4) role_cload(Cre, Cim, ws, smem, gb, w - 4, lane, tag TSPASS);
        else role_last(ws, smem, gb, w, lane, tag, fe TSPASS);
    }
}

extern "C" void kernel_launch(void* const* d_in, const int* in_sizes, int n_in, void* d_out, int out_size, void* d_ws, size_t ws_size,
                              hipStream_t stream) {
    (void)hipFuncSetAttribute((const void*)gm_kernel, hipFuncAttributeMaxDynamicSharedMemorySize, SMEM);
    gm_kernel<<<256, 768, SMEM, stream>>>((const u16*)d_in[0], (const u16*)d_in[6], (const u16*)d_in[7], (const u16*)d_in[12], (char*)d_ws,
                                          (const u16*)d_in[4], (const u16*)d_in[5], (const u16*)d_in[10], (const u16*)d_in[1], (const u16*)d_in[2],
                                          (const u16*)d_in[8], (const u16*)d_in[9], (const u16*)d_in[11], (float*)d_out);
}
```

```cpp
#define PDL 18
#define GDL 12
#define TP0 24
#include <hip/hip_runtime.h>
#include <stdint.h>

typedef __attribute__((ext_vector_type(8))) __bf16 bf16x8;
typedef __attribute__((ext_vector_type(2))) __bf16 bf16x2;
typedef __attribute__((ext_vector_type(8))) _Float16 f16x8;
typedef __attribute__((ext_vector_type(2))) _Float16 f16x2;
typedef __attribute__((ext_vector_type(4))) float f32x4;
typedef __attribute__((ext_vector_type(2))) float f32x2;
typedef __attribute__((ext_vector_type(4))) unsigned u32x4;
typedef __attribute__((ext_vector_type(3))) unsigned u32x3;
typedef __attribute__((ext_vector_type(2))) unsigned u32x2;
typedef __attribute__((ext_vector_type(4))) short s16x4;
typedef __attribute__((ext_vector_type(2))) unsigned short u16x2;
typedef unsigned short u16;
typedef unsigned long long u64;
#define GAS __attribute__((address_space(1)))
#define LAS __attribute__((address_space(3)))
#define NOPK __attribute__((target("no-packed-fp32-ops")))
#define DEV __device__ __forceinline__

constexpr int S_ = 2048, D_ = 1024, KF = 513;

constexpr unsigned OFF_H = 0;
constexpr unsigned OFF_U = OFF_H + (8u << 20);
constexpr unsigned OFF_T = OFF_U + (4u << 20);
constexpr unsigned OFF_G = OFF_T + (4u << 20);
constexpr unsigned OFF_FU = OFF_G + 16384u;
constexpr unsigned OFF_FT = OFF_FU + 16384u;
constexpr unsigned OFF_FH = OFF_FT + 16384u;
constexpr unsigned OFF_GRAN = OFF_FH + 16384u;
constexpr unsigned OFF_E = OFF_GRAN + 16u * 512u * 16u;
constexpr unsigned WS_END = OFF_E + 128u;

#define TL(i)
#define TSD()
#define TS(i)
#define TS0()
#define TC(i)
#define TSE(slot, on)

DEV unsigned pkb(float a, float b) { f32x2 v = {a, b}; return __builtin_bit_cast(unsigned, __builtin_convertvector(v, bf16x2)); }
DEV unsigned pkh(float a, float b) { f32x2 v = {a, b}; return __builtin_bit_cast(unsigned, __builtin_convertvector(v, f16x2)); }
DEV float lo2f(unsigned p) { return __uint_as_float(p << 16); }
DEV float hi2f(unsigned p) { return __uint_as_float(p & 0xffff0000u); }
DEV float bfr(float f) { return lo2f(pkb(f, f)); }
DEV float2 cmul(float2 a, float2 b) { return make_float2(a.x * b.x - a.y * b.y, a.x * b.y + a.y * b.x); }
DEV float2 cfma(float2 a, float2 b, float2 c) { return make_float2(fmaf(a.x, b.x, fmaf(-a.y, b.y, c.x)), fmaf(a.x, b.y, fmaf(a.y, b.x, c.y))); }
DEV f32x4 mfb(u32x4 a, u32x4 b, f32x4 c) { return __builtin_amdgcn_mfma_f32_16x16x32_bf16(__builtin_bit_cast(bf16x8, a), __builtin_bit_cast(bf16x8, b), c, 0, 0, 0); }
DEV f32x4 mfh(u32x4 a, u32x4 b, f32x4 c) { return __builtin_amdgcn_mfma_f32_16x16x32_f16(__builtin_bit_cast(f16x8, a), __builtin_bit_cast(f16x8, b), c, 0, 0, 0); }
DEV unsigned pkmax(unsigned a, unsigned b) { return __builtin_bit_cast(unsigned, __builtin_elementwise_max(__builtin_bit_cast(u16x2, a), __builtin_bit_cast(u16x2, b))); }
typedef LAS char* lptr;
DEV unsigned ldsa(lptr p) { return (unsigned)(uintptr_t)p; }
template <class T> DEV T lld(lptr p) { return *(LAS const T*)p; }
template <class T> DEV void lst(lptr p, T v) { *(LAS T*)p = v; }
DEV u32x2 trread(lptr p) { return __builtin_bit_cast(u32x2, __builtin_amdgcn_ds_read_tr16_b64_v4i16((LAS s16x4*)p)); }
DEV u32x4 trfrag(lptr p, int second) { const u32x2 a = trread(p), b = trread(p + second); return (u32x4){a.x, a.y, b.x, b.y}; }
typedef __amdgpu_buffer_rsrc_t rsrc_t;
DEV u32x4 ldfrag(lptr tile, int row, int chunk) { return lld<u32x4>(tile + row * 128 + ((chunk ^ (row & 7)) << 4)); }
template <int N> DEV void wait_vm() { asm volatile("s_waitcnt vmcnt(%0)" :: "i"(N) : "memory"); }
#define LDS_WAIT() asm volatile("s_waitcnt lgkmcnt(0)" ::: "memory")
#define SCHED() __builtin_amdgcn_sched_barrier(0)
#define BAR() do { __builtin_amdgcn_s_barrier(); asm volatile("" ::: "memory"); } while (0)
#define PIN(x) asm volatile("" : "+v"(x))
template <int I> struct IC { static constexpr int v = I; };
template <int I, int N, class F> DEV void sfor(F&& f) { if constexpr (I < N) { f(IC<I>{}); sfor<I + 1, N>(f); } }
#define INL __attribute__((always_inline))
DEV rsrc_t mkrsrc(const void* p, unsigned n) { return __builtin_amdgcn_make_buffer_rsrc((void*)p, 0, n, 0x00020000); }
constexpr int SC1 = 16;
#define WTO 16
template <int AUX> DEV void st128(u32x4 v, rsrc_t rs, unsigned off) { __builtin_amdgcn_raw_buffer_store_b128(v, rs, off, 0, AUX); }
DEV void lds_add(unsigned a) { asm volatile("ds_add_u32 %0, %1" :: "v"(a), "v"(1u) : "memory"); }
DEV unsigned lds_peek(unsigned a) {
    unsigned v;
    asm volatile("ds_read_b32 %0, %1\n\ts_waitcnt lgkmcnt(0)" : "=v"(v) : "v"(a) : "memory");
    return __builtin_amdgcn_readfirstlane(v);
}
DEV void tile_of(int gb, int& ta, int& tb) {
    const int xcd = gb & 7, loc = gb >> 3;
    ta = (xcd >> 1) * 4 + (loc >> 3);
    tb = (xcd & 1) * 8 + (loc & 7);
}
#define DMAP 1
DEV int da_of(int pt) { return DMAP ? ((pt & 7) << 1) | (pt >> 3) : pt; }
DEV void rows4(float a, float& a0, float& a1, float& a2, float& a3) {
    const unsigned u = __float_as_uint(a);
    const auto r = __builtin_amdgcn_permlane32_swap(u, u, false, false);
    const auto p = __builtin_amdgcn_permlane16_swap(r[0], r[0], false, false);
    const auto s = __builtin_amdgcn_permlane16_swap(r[1], r[1], false, false);
    a0 = __uint_as_float(p[0]); a1 = __uint_as_float(p[1]); a2 = __uint_as_float(s[0]); a3 = __uint_as_float(s[1]);
}
DEV void rowsum4(float& a, float& b, float& c, float& d) {
    const auto r = __builtin_amdgcn_permlane32_swap(__float_as_uint(a), __float_as_uint(b), false, false);
    const auto t = __builtin_amdgcn_permlane32_swap(__float_as_uint(c), __float_as_uint(d), false, false);
    const auto p = __builtin_amdgcn_permlane16_swap(__float_as_uint(__uint_as_float(r[0]) + __uint_as_float(r[1])), __float_as_uint(__uint_as_float(t[0]) + __uint_as_float(t[1])), false, false);
    rows4(__uint_as_float(p[0]) + __uint_as_float(p[1]), a, c, b, d);
}
DEV float2 sel4(int q, float2 a, float2 b, float2 c, float2 d) {
    PIN(d.x); PIN(d.y);
    const float2 e = (q & 1) ? b : a, f = (q & 1) ? d : c;
    return (q & 2) ? f : e;
}
template <int R> DEV unsigned ror16(unsigned v) { return __builtin_amdgcn_update_dpp(0u, v, 0x120 + R, 0xf, 0xf, false); }
DEV unsigned wave_pkmax(unsigned m) {
    m = pkmax(m, ror16<8>(m)); m = pkmax(m, ror16<4>(m)); m = pkmax(m, ror16<2>(m)); m = pkmax(m, ror16<1>(m));
    const auto r = __builtin_amdgcn_permlane32_swap(m, m, false, false);
    m = pkmax(r[0], r[1]);
    const auto p = __builtin_amdgcn_permlane16_swap(m, m, false, false);
    return pkmax(p[0], p[1]);
}
DEV float half_sum(float v) {
    v += __uint_as_float(ror16<8>(__float_as_uint(v))); v += __uint_as_float(ror16<4>(__float_as_uint(v)));
    v += __uint_as_float(ror16<2>(__float_as_uint(v))); v += __uint_as_float(ror16<1>(__float_as_uint(v)));
    const auto p = __builtin_amdgcn_permlane16_swap(__float_as_uint(v), __float_as_uint(v), false, false);
    return __uint_as_float(p[0]) + __uint_as_float(p[1]);
}
DEV u32x4 quads(lptr buf, int lane, u32x2 p, u32x2 n) {
    lptr wp = buf + (lane & 15) * 64 + ((lane >> 4) & 1) * 8;
    const int v = (lane >> 5) ^ ((lane >> 2) & 3);
    lst<u32x2>(wp + (v << 4), p); lst<u32x2>(wp + ((v ^ 2) << 4), n);
    asm volatile("" ::: "memory");
    return lld<u32x4>(buf + ((lane ^ (lane >> 4)) << 4));
}

#define PRIOA 2
#define NREP 8
#define PRIOM 1
#define PRIOT 2
#define PRIOG 2
#define AD 8
#define NB 12
#define AD2 4
#define AG2 168
#define AD3 8
#define AG3 200
#define AD4 6
#define AG4 244
#define ENT 1
#define STG 1
#define FTS 1
#define FLN 2
#define FSL 0
#define PKR 1
#define GS0 474
#define US0 10
#define WGL 2
#define RS0 (HD ? 50 : 20)
#define RSP (HD ? 4 : 6)
#define WS0 0
#define WS1 4
#define RSD 88
#define CPD 4
#define CB 4
#define AWS 0
#define HD 0
#define HDT 5
#define HDS 1
#define HD0 0
#define C3D 10
#define C3S 1
#define POW 0
#define AW0 0
#define C0E 1
#define HST 1
#define PRT 0
#define LX 0
#define LXG 1
#define PDX 6
#define C2D 6
#define MWT 0
#define TDR 0
#define SA0 24
#define AM 1
#define CP 0
#define HP 0
#define XE 0
#define TSL 4
#define CSL 1
#define WG2 2
#define WG1 1
#define SPC 1
#define PRV 0
#define PRX 3
#define TLS 4
#define RDL 0
#define WROW {0, 0, 0, 0, 8}
#define ROT (lt & 12)
#define PRIOL 3
#define XM 0
#define UFS 6
#define PRIOR 0
#define PRS 2
#define WXS 13
#define CPF 1
#define FST 8
#define HREP 8
#define NP1 1
#define PDE 0
#define PDM 0x80
#define NB2 NB
#define XP 4
#define PU 0
#define BAL -1
#define BA2 0
#define FTC 1
#define LBS 0
#define PT 0
#define PTS 4
#define FTB 768
#define XTP 1
#define TWH 4
#define ACI 0
#define CE 0
#define CES 0
#define CEL 1
#define HPL 0
#define HPW 0
#define CW 0
#define GSP 1
#define PV0 "144"
#define PV0E "147"
#define PV1 "148"
#define PV1E "151"
#define PV2 "152"
#define PV2E "155"
#define PV3 "156"
#define PV3E "159"
#define UDL 0
#define CGW 0
#define C0S 0
#define GSL 0
#define C0L 0
#define C0P 0
#define BSL 0
#define BS2 0
#define PZ1 0
#define PZ2 0
#define PZ3 0
#define PZA 0
#define PZB 48
#define XLP 0
#define PRIOI 0
#define RST (NST - 5)
#define SIL 1
#define CPM 7
#define PRIOE 2
#define MSL 1
#define URS 6
#define UPRE 1
#define UCK 13
#define PRIOD 1
#define PRIOW 1
#define CDL 0
#define ATS 14
#define UPL 10
#define C1E 1
#define PD0 0
#define CXM 6
#define CX(g) ((g) & CXM)
#define GSTEP {0, 0, 0, 0}
#define XS 1
#define B1S 0
#define US 1
#define UPS 1
#define DS 8
#define P0S 8
#define VL 1
#define PN -1
#define PG -1
#define SNK 1
#define HFIRST 8u
#define RDPAT {0, 1, 2, -1, 3, 4, -1, 5, 6, -1, 7, -1, -1, -1, -1, -1}
#define Z4 ((f32x4){0.f, 0.f, 0.f, 0.f})
constexpr unsigned SG = 0x80008000u;
struct CM { u32x4 re, im; };
DEV u32x4 cmat1(float t0, float dt) {
    float cc[4], ss[4];
    t0 = __builtin_amdgcn_fractf(t0);
#pragma unroll
    for (int e = 0; e < 4; ++e) { const float f = fmaf((float)e, dt, t0); cc[e] = __builtin_amdgcn_cosf(f); ss[e] = __builtin_amdgcn_sinf(f); }
    return (u32x4){pkh(ss[0], ss[1]), pkh(ss[2], ss[3]), pkh(cc[0], cc[1]), pkh(cc[2], cc[3])};
}
DEV CM cexp(u32x4 v) { CM m; m.re = (u32x4){v.z, v.w, v.x ^ SG, v.y ^ SG}; m.im = v; PIN(m.re); PIN(m.im); return m; }
DEV u32x4 pk8(f32x4 r, f32x4 i) { return (u32x4){pkh(r[0], r[1]), pkh(r[2], r[3]), pkh(i[0], i[1]), pkh(i[2], i[3])}; }
DEV u32x4 wmul(f32x4 r, f32x4 i, u32x4 w) {
    float yr[4], yi[4];
#pragma unroll
    for (int e = 0; e < 4; ++e) {
        const unsigned wu = w[e];
        const f16x2 h = __builtin_bit_cast(f16x2, wu);
        yr[e] = fmaf(r[e], (float)h[0], -i[e] * (float)h[1]); yi[e] = fmaf(r[e], (float)h[1], i[e] * (float)h[0]);
    }
    return (u32x4){pkh(yr[0], yr[1]), pkh(yr[2], yr[3]), pkh(yi[0], yi[1]), pkh(yi[2], yi[3])};
}
DEV u32x4 wcvt(u32x4 w, float sr, float si) {
#pragma unroll
    for (int e = 0; e < 4; ++e) w[e] = pkh(lo2f(w[e]) * sr, hi2f(w[e]) * si);
    PIN(w);
    return w;
}
DEV u32x4 mkdesc(const void* p, unsigned n) { const u64 a = (u64)p; return (u32x4){(unsigned)a, (unsigned)(a >> 32) & 0xffffu, n, 0x00020000u}; }
DEV void dma16(u32x4 d, unsigned l, unsigned vo, unsigned so) {
    asm volatile("s_nop 0\n\tbuffer_load_dwordx4 %0, %1, %2 offen lds" :: "v"(vo), "s"(d), "s"(so), "{m0}"(l) : "memory");
}
DEV void dma64(u32x4 d, unsigned l, u32x4 vo, unsigned so) {
    asm volatile("s_nop 0\n\tbuffer_load_dwordx4 %0, %4, %5 offen lds\n\tbuffer_load_dwordx4 %1, %4, %5 offen offset:1024 lds\n\t"
                 "buffer_load_dwordx4 %2, %4, %5 offen offset:2048 lds\n\tbuffer_load_dwordx4 %3, %4, %5 offen offset:3072 lds"
                 :: "v"(vo.x), "v"(vo.y), "v"(vo.z), "v"(vo.w), "s"(d), "s"(so), "{m0}"(l) : "memory");
}
#define SPIN(x) asm volatile("" : "+s"(x))
DEV void dma16s(u32x4 d, unsigned l, unsigned vo, unsigned so) {
    asm volatile("s_nop 0\n\tbuffer_load_dwordx4 %0, %1, %2 offen sc1 lds" :: "v"(vo), "s"(d), "s"(so), "{m0}"(l) : "memory");
}
DEV void sig(unsigned a, unsigned v) { asm volatile("s_mov_b64 exec, 1\n\tds_add_u32 %0, %1\n\ts_mov_b64 exec, -1" :: "v"(a), "v"(v) : "memory"); }
template <int O> DEV void sig_o(unsigned a, unsigned v) { asm volatile("s_mov_b64 exec, 1\n\tds_add_u32 %0, %1 offset:%2\n\ts_mov_b64 exec, -1" :: "v"(a), "v"(v), "i"(O) : "memory"); }
DEV unsigned sig_rtn(unsigned a) {
    unsigned v;
    asm volatile("s_mov_b64 exec, 1\n\tds_add_rtn_u32 %0, %1, %2\n\ts_mov_b64 exec, -1\n\ts_waitcnt lgkmcnt(0)" : "=&v"(v) : "v"(a), "v"(1u) : "memory");
    return __builtin_amdgcn_readfirstlane(v);
}
template <int SLP> DEV unsigned await(unsigned a, unsigned mask) {
    unsigned v, n, r, t;
    asm volatile("s_mov_b32 %1, 0x400000\n"
                 "1:\tds_read_b32 %0, %4\n\ts_waitcnt lgkmcnt(0)\n\tv_readfirstlane_b32 %2, %0\n\ts_and_b32 %3, %2, %5\n\ts_cbranch_scc1 2f\n\t"
                 "s_sleep %6\n\ts_sub_u32 %1, %1, 1\n\ts_cbranch_scc0 1b\n"
                 "2:" : "=&v"(v), "=&s"(n), "=&s"(r), "=&s"(t) : "v"(a), "s"(mask), "i"(SLP) : "memory", "scc");
    return r;
}
#define BMF "v_mfma_f32_16x16x32_bf16 %0, %2, %3, %0\n\tv_mfma_f32_16x16x32_bf16 %1, %3, %2, %1\n\t"
template <int SLP> DEV void burn(int n, u32x4& a, u32x4& b) {
    f32x4 d0, d1;
    asm volatile("1:\t" BMF BMF "s_nop %5\n\ts_sub_u32 %4, %4, 1\n\ts_cbranch_scc0 1b\n\ts_nop 7\n\ts_nop 3"
                 : "=&v"(d0), "=&v"(d1), "+v"(a), "+v"(b), "+s"(n) : "i"(SLP) : "scc");
}
template <int SLP> DEV unsigned await_burn(unsigned ad, unsigned mask, u32x4 a, u32x4 b) {
    unsigned v, n, r, t; f32x4 d0, d1;
    asm volatile("s_mov_b32 %5, 0x400000\n"
                 "1:\tds_read_b32 %4, %8\n\t" BMF BMF "s_waitcnt lgkmcnt(0)\n\tv_readfirstlane_b32 %6, %4\n\ts_and_b32 %7, %6, %9\n\ts_cbranch_scc1 2f\n\t"
                 ".rept %10\n\t" BMF ".endr\n\ts_sub_u32 %5, %5, 1\n\ts_cbranch_scc0 1b\n"
                 "2:\ts_nop 7\n\ts_nop 3" : "=&v"(d0), "=&v"(d1), "+v"(a), "+v"(b), "=&v"(v), "=&s"(n), "=&s"(r), "=&s"(t) : "v"(ad), "s"(mask), "i"(SLP) : "memory", "scc");
    return r;
}
DEV void need_bit(unsigned a, unsigned& r, unsigned mask, int off = 0) {
    unsigned v, n, t;
    asm volatile("s_andn2_b32 %3, %5, %2\n\ts_cbranch_scc1 1f\n2:\n\t.subsection 1\n"
                 "1:\ts_mov_b32 %1, 0x400000\n"
                 "3:\tds_read_b32 %0, %4 offset:%6\n\ts_waitcnt lgkmcnt(0)\n\tv_readfirstlane_b32 %2, %0\n\ts_andn2_b32 %3, %5, %2\n\ts_cbranch_scc0 2b\n\t"
                 "s_sub_u32 %1, %1, 1\n\ts_cbranch_scc0 3b\n\ts_branch 2b\n\t.subsection 0"
                 : "=&v"(v), "=&s"(n), "+s"(r), "=&s"(t) : "v"(a), "i"(mask), "i"(off) : "memory", "scc");
}
template <int SLP> DEV void await_all(unsigned a, unsigned mask) {
    unsigned v, n, r;
    asm volatile("s_mov_b32 %1, 0x400000\n"
                 "1:\tds_read_b32 %0, %3\n\ts_waitcnt lgkmcnt(0)\n\tv_readfirstlane_b32 %2, %0\n\ts_andn2_b32 %2, %4, %2\n\ts_cbranch_scc0 2f\n\t"
                 "s_sleep %5\n\ts_sub_u32 %1, %1, 1\n\ts_cbranch_scc0 1b\n"
                 "2:" : "=&v"(v), "=&s"(n), "=&s"(r) : "v"(a), "s"(mask), "i"(SLP) : "memory", "scc");
}
#define MF2(a, b, c) asm("v_mfma_f32_16x16x32_bf16 %0, %1, %2, %0" : "+v"(c) : "v"(a), "v"(b))
DEV void flag_put(rsrc_t rs, unsigned off, int gb, u32x2 v, int lane) {
    if (lane < NREP) __builtin_amdgcn_raw_buffer_store_b64(v, rs, lane * 2048, off + gb * 8, SC1);
}
DEV void flag_get(rsrc_t rs, unsigned off, int gb, int lane, u32x4 (&fl)[2]) {
    unsigned o = lane * 16; PIN(o);
#pragma unroll
    for (int i = 0; i < 2; ++i) fl[i] = __builtin_amdgcn_raw_buffer_load_b128(rs, o + i * 1024, off + (gb & (NREP - 1)) * 2048, SC1);
}
DEV bool flag_ok(const u32x4 (&fl)[2], u32x2 tag) {
    bool ok = true;
#pragma unroll
    for (int i = 0; i < 2; ++i) ok = ok & ((fl[i].x ^ tag.x) < 0x10000u) & (fl[i].y == tag.y) & ((fl[i].z ^ tag.x) < 0x10000u) & (fl[i].w == tag.y);
    return __all(ok);
}
template <int N> DEV unsigned h_poll(rsrc_t rs, int gb, int kh, int lane, u32x2 tag) {
    unsigned o = (lane ^ ((CX(gb) & 3) << 4)) * FST, m = 0; PIN(o);
    u32x2 fl[N];
#pragma unroll
    for (int i = 0; i < N; ++i) fl[i] = __builtin_amdgcn_raw_buffer_load_b64(rs, o, OFF_FH + ((gb & (HREP - 1)) * 256 + kh * 128 + (i ^ (CX(gb) >> 2)) * 64) * FST, SC1);
#pragma unroll
    for (int i = 0; i < N; ++i) {
        const u64 b = __ballot(((fl[i].x ^ tag.x) < 0x10000u) & (fl[i].y == tag.y));
#pragma unroll
        for (int j = 0; j < 4; ++j) m |= ((unsigned)(b >> (16 * j)) & 0xffffu) == 0xffffu ? 8u << (16 * i + 4 * j) : 0u;
    }
    return m;
}
DEV void sig_or(unsigned a, unsigned v) { asm volatile("s_mov_b64 exec, 1\n\tds_or_b32 %0, %1\n\ts_mov_b64 exec, -1" :: "v"(a), "v"(v) : "memory"); }

constexpr int A_STAGE = 32768, A_XT = 4 * A_STAGE, A_PRM = A_XT + 16384, A_AGG = A_PRM + 2048, A_FLG = A_AGG + 512, A_GW = A_FLG + 128,
              A_MAT = A_GW + 2048, SMEM = A_MAT + 11264;
constexpr int E1P = 1040, A_EX = A_GW, A_E1 = 0, A_PW = 18 * E1P + 224, A_W = A_STAGE, A_HS = 2 * A_STAGE - 8192, A_OG = A_MAT + 9216, A_CN = A_MAT + 1024;
static_assert(A_PW % 256 == 0 && A_W + 18432 + 512 <= A_HS, "");
constexpr int F_MID = 8, F_EM = 12, F_MAX = 16, F_TAG = 32, F_FULL = 40, F_FREE = 44, F_HC = 48, F_FB = 52, F_TC = 56, F_TR = 60, F_TT = 64, F_ST = 76, F_WM = 80, F_RD = 96;
static_assert((A_FLG + F_RD & 0xff) == 0x60, "");

#define XGV 0xFF
constexpr int XG = XGV;
DEV bool late(int gb) { return (LX >> (gb & 7) & 1) || (LXG && (gb | 16) == (XG | 16)); }
DEV void slot_of(int gb, int& mp, int& j1) { gb ^= XG; mp = (gb & 7) * 8 + (gb >> 5); j1 = (gb >> 3) & 3; }
DEV u32x4 mat_sf(int k1, int c, int q) { const float dt = -(float)(k1 + 128 * c) * (1.0f / 2048.0f); return cmat1(dt * (float)(4 * q), dt); }
DEV u32x4 mat_si(int k1, int c, int q) { return cmat1((float)((k1 + 512 * q) * c) * (1.0f / 2048.0f), (float)c * (1.0f / 16.0f)); }

DEV void mk_tables(lptr smem, int gb, int wid, int lane) {
    int mp, j1; slot_of(gb, mp, j1);
    const int c = lane & 15, q = lane >> 4;
    const bool spc = mp == 0 && !(j1 & 1);
    const int k1 = mp ? mp : (j1 == 3 ? 64 : 0);
    lptr mt = smem + A_MAT + lane * 16;
    if (wid == 0) {
        const float dt = -(float)(j1 + 4 * (spc ? c & 7 : c)) * (1.0f / 64.0f), t0 = __builtin_amdgcn_fractf(dt * (float)(4 * q));
        float cc[4], ss[4];
#pragma unroll
        for (int e = 0; e < 4; ++e) { const float f = fmaf((float)e, dt, t0); cc[e] = __builtin_amdgcn_cosf(f); ss[e] = __builtin_amdgcn_sinf(f); }
        const unsigned c0 = pkb(cc[0], cc[1]), c1 = pkb(cc[2], cc[3]), s0 = pkb(ss[0], ss[1]), s1 = pkb(ss[2], ss[3]);
        const unsigned c0l = pkb(cc[0] - lo2f(c0), cc[1] - hi2f(c0)), c1l = pkb(cc[2] - lo2f(c1), cc[3] - hi2f(c1));
        const unsigned s0l = pkb(ss[0] - lo2f(s0), ss[1] - hi2f(s0)), s1l = pkb(ss[2] - lo2f(s1), ss[3] - hi2f(s1));
        u32x4 m[4] = {(u32x4){c0, c1, s0 ^ SG, s1 ^ SG}, (u32x4){s0, s1, c0, c1}, (u32x4){c0l, c1l, s0l ^ SG, s1l ^ SG}, (u32x4){s0l, s1l, c0l, c1l}};
        if (spc) {
            m[0] = c < 8 ? (u32x4){c0, c1, 0, 0} : (u32x4){0, 0, c0, c1}; m[1] = c < 8 ? (u32x4){s0, s1, 0, 0} : (u32x4){0, 0, s0, s1};
            m[2] = c < 8 ? (u32x4){c0l, c1l, 0, 0} : (u32x4){0, 0, c0l, c1l}; m[3] = c < 8 ? (u32x4){s0l, s1l, 0, 0} : (u32x4){0, 0, s0l, s1l};
        }
#pragma unroll
        for (int i = 0; i < 4; ++i) lst<u32x4>(mt + i * 1024, m[i]);
    } else if (wid == 1) {
        const int jq = spc ? (4 * q) & 7 : 4 * q;
        float cc[4], ss[4];
        const float t0 = __builtin_amdgcn_fractf((float)(c * (j1 + 4 * jq)) * (1.0f / 64.0f));
#pragma unroll
        for (int e = 0; e < 4; ++e) {
            const float f = fmaf((float)c, (float)e * (1.0f / 16.0f), t0);
            const float wg = !spc || (gb == XG && jq + e == 0) ? 0.125f : 0.25f;
            cc[e] = __builtin_amdgcn_cosf(f) * wg; ss[e] = -__builtin_amdgcn_sinf(f) * wg;
        }
        const unsigned c0 = pkh(cc[0], cc[1]), c1 = pkh(cc[2], cc[3]), n0 = pkh(ss[0], ss[1]), n1 = pkh(ss[2], ss[3]);
        u32x4 mia = (u32x4){c0, n0, c1, n1}, mib = (u32x4){n0, c0 ^ SG, n1, c1 ^ SG};
        if (spc) { mib = q < 2 ? (u32x4){0, 0, 0, 0} : mia ^ SG; if (q >= 2) mia = (u32x4){0, 0, 0, 0}; }
        lst<u32x4>(mt + 6 * 1024, mia); lst<u32x4>(mt + 7 * 1024, mib);
        lst<u32x4>(mt + 8 * 1024, cmat1((float)(c * q) * -0.25f, (float)c * (-1.0f / 16.0f)));
    } else {
        lst<u32x4>(mt + (2 + wid) * 1024, wid == 2 ? mat_sf(k1, c, q) : mat_si(k1, c, q));
        const int ja = (spc ? 0 : 8 * (wid - 2)) + (lane >> 3);
        const float dt = -(float)(j1 + 4 * ja) * (1.0f / 1024.0f), t0 = dt * (float)(2 * (lane & 7)), t1 = t0 + dt;
        lst<f32x4>(mt + (7 + wid) * 1024, (f32x4){__builtin_amdgcn_cosf(t0), __builtin_amdgcn_sinf(t0), __builtin_amdgcn_cosf(t1), __builtin_amdgcn_sinf(t1)});
    }
}

template <int SLP = 0> DEV void c_chunk(const u16* __restrict__ Cre, const u16* __restrict__ Cim, unsigned lb, int gb, int da, int lw, int lane, int j, int at = -1) {
    if (at < 0) at = ((j + 2) & 3) * A_STAGE + lw * 8192;
    const u32x4 rc = mkdesc(lw < 2 ? Cre : Cim, D_ * D_ * 2);
    const unsigned vo = (lane >> 3) * 8192 + (((lane & 7) ^ (lane >> 3)) << 4);
#pragma unroll
    for (int i = 0; i < 8; ++i) {
        if (SLP && i) __builtin_amdgcn_s_sleep(SLP);
        dma16(rc, lb + at + i * 1024, vo, (512 * (i & 1) + 256 * ((lw ^ da ^ 1) & 1) + 32 * (da >> 1) + (i >> 1)) * 2048 + (8 * (da & 1) + (j ^ CX(gb))) * 128);
    }
}
constexpr int KS2 = 16, KS1 = 16, F1 = 8 * KS1, FT = F1 + 4 * KS2, RT = 8, NRL = 16;
static_assert(!(NB & 1) && NB <= 16, "");
constexpr int f_k(int f) { return f < F1 ? f >> 3 : f < FT ? KS1 + ((f - F1) >> 2) : 31 - ((f - FT) >> 2); }
constexpr int f_m(int f) { return f < F1 ? f & 7 : f < FT ? (f - F1) & 3 : 4 + ((f - FT) & 3); }
constexpr int f_i(int f) { return f < F1 ? f & 7 : (f - F1) & 3; }
constexpr bool f_hi(int f) { return f_i(f) == (f < F1 ? 7 : 3); }
constexpr bool c_new(int f) { return f < FT && !f_i(f) && !(f_k(f) & 3); }
constexpr bool c_new3(int f) { return f >= FT && f < 256 && !f_i(f) && (f_k(f) & 3) == 3; }
constexpr int k_f(int k) { return k < KS1 ? 8 * k : F1 + 4 * (k - KS1); }
struct SCH {
    int a[512], p[512], w[512]; unsigned m[512];
    constexpr void chk(int s, unsigned mask, int word) { m[s] |= mask; p[s - 2] = 3; p[s - 2 - 2 * CPD] = word; }
    constexpr SCH() : a{}, p{}, w{}, m{} {
        int s = -1, as[256] = {}, cs[9] = {};
        for (int i = AD; i < 256; ++i) { const int t = 2 * (i - (i < AG2 ? AD : i < AG3 ? AD2 : i < AG4 ? AD3 : AD4)) + 1; s = t > s ? t : s + 1; a[s] = i; as[i] = s; }
        for (int c = 1; c < 8; ++c) {
            cs[c] = (as[k_f(4 * c)] - (c < 5 ? 5 : 1)) & ~1;
            if (!HD && c > 4 && c < 7 && cs[c] > 2 * k_f(2 * c + 10)) cs[c] = 2 * k_f(2 * c + 10);
            chk(cs[c], 4u << (4 * c), 1);
        }
        for (int j = (NB + 7) / 8; j < 8; ++j) {
            int c = 7;
            while (cs[c] > 2 * k_f((8 * j - NB) / 2 + 1) - 2) --c;
            chk(cs[c], 8u << (4 * j), 1);
        }
        for (int j = 7; j > 3; --j) {
            const int t = (as[FT + 16 * (7 - j)] - 1) & ~1;
            chk(t, 4u << (4 * j) | 4u | (HD && j < 6 ? 0x400u << (4 * (5 - j)) : 0u), 2);
            if (HD && j < 6 && t > 2 * FT + RS0 + RSP * 8 * (5 - j)) p[-1] = 0;
        }
        chk(GS0, 0x40u, 2);
        for (int f = 0; f < 252; f += WGL) {
            int n = 0;
            for (int i = f + WGL; i < 256; ++i) n += i < AD || as[i] < 2 * f;
            if (f && WGL > 1) w[2 * f - 1] = n + 1;
        }
    }
    constexpr bool ok() const {
        int e = 0, r = 0;
        for (int s = 0; s < 512; ++s) {
            if (p[s] == 3) { r = e; e = 0; } else if (p[s]) { if (e) return false; e = p[s]; }
            if (m[s]) { if (r != ((m[s] & 0x44u) && s > 128 ? 2 : 1)) return false; r = 0; }
        }
        return true;
    }
};
constexpr SCH ST;
static_assert(ST.ok(), "");
DEV void wait_lds(int n) { sfor<0, 16>([&](auto ic) INL { if (n == decltype(ic)::v) __builtin_amdgcn_s_waitcnt(0xC07F | decltype(ic)::v << 8); }); }
constexpr int s_f(int s) { return s < 504 ? s >> 1 : 252 + (s & 3); }
constexpr int s_n(int s) { return s < 504 ? s & 1 : (s >> 2) & 1; }
constexpr int s_b(int s) { return s >= 2 * FT && f_k(s_f(s)) < 32 - RT ? 64 + 2 * (31 - RT - f_k(s_f(s))) + s_n(s) : 2 * f_k(s_f(s)) + s_n(s); }
constexpr int h_off(int k, int n) { return ((k >> 1) & 1) * A_STAGE + (k & 1) * 16384 + n * 1024; }

DEV void role_mma(const u16* __restrict__ x, const u16* __restrict__ Dskip, char* ws, float* __restrict__ out, lptr smem, int gb, int wid, int lane, u32x4 cd, unsigned t00) {
    int lt, pt; tile_of(gb, lt, pt);
    const int wr = wid >> 1, wc = wid & 1, c = lane & 15, q = lane >> 4;
    const rsrc_t rs = mkrsrc(ws, WS_END);
    const unsigned fb = ldsa(smem + A_FLG);
    TSD(); TS0();
    u64 tag;
    if (STG) tag = *(const __attribute__((address_space(4))) u64*)(uintptr_t)(ws + OFF_E);
    else tag = __hip_atomic_load((GAS u64*)(ws + OFF_E), __ATOMIC_RELAXED, __HIP_MEMORY_SCOPE_WORKGROUP);
    static_assert(ENT && STG, "");
    if (FTS < 0 && wid == 2) asm volatile("s_nop 0\n\tbuffer_load_dword %0, %1, 0 offen sc1 lds" :: "v"(lane * 64), "s"(mkdesc(ws + OFF_G + gb * FTB, FTB)), "{m0}"(ldsa(smem + A_AGG + 256)) : "memory");
    SCHED();
    if (wid == 0 && lane < 32) lst<unsigned>(smem + A_FLG + lane * 4, 0u);
    mk_tables(smem, gb, wid, lane);
    f32x4 acc[4][4];
#pragma unroll
    for (int m = 0; m < 4; ++m)
#pragma unroll
        for (int n = 0; n < 4; ++n) acc[m][n] = Z4;
    u32x4 af[2][4], bf_[2][4];
#pragma unroll
    for (int m = 0; m < 4; ++m) { af[1][m] = (u32x4){0, 0, 0, 0}; bf_[1][m] = af[1][m]; }
    lptr fp[8];
#pragma unroll
    for (int i = 0; i < 8; ++i) { fp[i] = smem + (i & 2) * A_STAGE + ((i & 4 ? 128 + wc * 64 : wr * 64) + c) * 128 + (((4 * (i & 1) + q) ^ (c & 7)) << 4); PIN(fp[i]); }
    tag += 0x9E3779B97F4A7C15ull;
    PIN(tag);
    if (wid == 0 && lane == 0) lst<u64>(smem + A_FLG + F_TAG, tag);
    tag = (u64)(unsigned)__builtin_amdgcn_readfirstlane((unsigned)tag) | (u64)(unsigned)__builtin_amdgcn_readfirstlane((unsigned)(tag >> 32)) << 32;
    __builtin_amdgcn_s_waitcnt(0xC07F);
    TS(13);
    unsigned fst = fb + F_ST, sv = 0;
    auto step = [&](int sl, bool pf) INL {
#pragma unroll
        for (int hf = 0; hf < 2; ++hf)
#pragma unroll
            for (int i = 0; i < 16; ++i) {
                const int nn = SNK && (i & 4) ? 3 - (i & 3) : i & 3;
                acc[i >> 2][nn] = mfb(af[1 - hf][i >> 2], bf_[1 - hf][nn], acc[i >> 2][nn]);
                constexpr int RD[16] = RDPAT;
                const int r = RD[i];
                if (r == 0) af[hf][0] = lld<u32x4>(fp[(sl & 2) + hf] + (sl & 1) * A_STAGE);
                else if (r > 0 && r < 5) bf_[hf][r - 1] = lld<u32x4>(fp[4 + (sl & 2) + hf] + (sl & 1) * A_STAGE + (r - 1) * 2048);
                else if (r >= 5) af[hf][r - 4] = lld<u32x4>(fp[(sl & 2) + hf] + (sl & 1) * A_STAGE + (r - 4) * 2048);
                if (pf && hf && i == 11) sv = *(volatile LAS unsigned*)(lptr)(uintptr_t)fst;
                SCHED();
            }
        LDS_WAIT();
    };
#pragma unroll 1
    for (int kt = 0; kt < 3; ++kt) {
#pragma unroll
        for (int i = 0; i < 4; ++i) { BAR(); step(i, false); }
    }
    PIN(fst);
    unsigned agp = ldsa(smem + A_AGG) + (wc * 16 + c) * 16, tgv = (unsigned)tag, sel = 0x07060500u;
    unsigned gro = lt < 15 && q == 0 ? c * 16 + OFF_GRAN + ((15 - lt) * 512 + pt * 32 + wc * 16) * 16 : ~15u;
    PIN(agp); PIN(tgv); PIN(gro);
    wait_vm<0>();
    BAR();
    if (wr) lst<u32x4>((lptr)(uintptr_t)agp, (u32x4){0, 0, 0, 0});
    step(0, false);
    BAR(); __builtin_amdgcn_s_setprio(PRIOE); step(1, true);
#pragma unroll
    for (int i = 0; i < 2; ++i) {
        unsigned ss = __builtin_amdgcn_readfirstlane(sv);
        need_bit(fst, ss, 4u << (4 * i));
        TL(30 + i);
        if (i) need_bit(fst, ss, 0x100u);
        step(2 + i, i == 0);
    }
    __builtin_amdgcn_s_setprio(PRIOT);
    sig(fb + F_FREE, 1u << 20);
    float2 lam[2], coef[2], l16[2], l64[2], l512[2], cw[2], wq[2], lq[2];
#pragma unroll
    for (int n = 0; n < 2; ++n) {
        const f32x4 a = lld<f32x4>(smem + A_PRM + (wc * 32 + 2 * c + n) * 32), b = lld<f32x4>(smem + A_PRM + (wc * 32 + 2 * c + n) * 32 + 16);
        lam[n] = make_float2(a[0], a[1]); l16[n] = make_float2(a[2], a[3]); l64[n] = make_float2(b[0], b[1]); l512[n] = make_float2(b[2], b[3]);
    }
    float2 h[2];
#pragma unroll
    for (int m = 0; m < 5; ++m) {
#pragma unroll
        for (int n = 0; n < 4; ++n) {
            if (m < 4) acc[m][n] = mfb(af[1][m], bf_[1][n], acc[m][n]);
            if (m == 1 && n == 0) { h[0] = make_float2(acc[0][0][0], acc[0][2][0]); h[1] = make_float2(acc[0][1][0], acc[0][3][0]); }
            else if (m) {
#pragma unroll
                for (int i = 0; i < 2; ++i) h[i] = cfma(lam[i], h[i], make_float2(acc[m - 1][i][n], acc[m - 1][2 + i][n]));
            }
            if (SIL) SCHED();
        }
        if (m == 3) {
#pragma unroll
            for (int n = 0; n < 2; ++n) {
                const f32x4 a = lld<f32x4>(smem + A_PW + 4096 + (wc * 32 + 2 * c + n) * 16), b = lld<f32x4>(smem + A_PW + q * 1024 + (wc * 32 + 2 * c + n) * 16);
                coef[n] = make_float2(a[0], a[1]); cw[n] = wr ? make_float2(a[2], a[3]) : coef[n]; wq[n] = make_float2(b[0], b[1]); lq[n] = make_float2(b[2], b[3]);
            }
        }
    }
    TS(1);
    float2 loc[2], wag[2];
#pragma unroll
    for (int n = 0; n < 2; ++n) {
        float2 a0, a1, a2, a3;
        rows4(h[n].x, a0.x, a1.x, a2.x, a3.x);
        rows4(h[n].y, a0.y, a1.y, a2.y, a3.y);
        const float2 s2 = cfma(l16[n], a0, a1), s3 = cfma(l16[n], s2, a2);
        wag[n] = cfma(l16[n], s3, a3);
        loc[n] = sel4(q, make_float2(0.f, 0.f), a0, s2, s3);
    }
    float2 W0[2] = {make_float2(0.f, 0.f), make_float2(0.f, 0.f)};
    if (wr == 0) lst<u32x4>((lptr)(uintptr_t)agp, (u32x4){__float_as_uint(wag[0].x) | 1u, __float_as_uint(wag[0].y), __float_as_uint(wag[1].x), __float_as_uint(wag[1].y) | 1u});
    else {
        f32x4 w0; unsigned t, n;
        asm volatile("s_mov_b32 %2, 0x400000\n"
                     "1:\tds_read_b128 %0, %3\n\ts_waitcnt lgkmcnt(0)\n\tv_and_b32 %1, v" PV0 ", v" PV0E "\n\tv_and_b32 %1, 1, %1\n\tv_cmp_eq_u32 vcc, 0, %1\n\ts_cbranch_vccz 2f\n\t"
                     "s_sub_u32 %2, %2, 1\n\ts_cbranch_scc0 1b\n"
                     "2:" : "=&{v[" PV0 ":" PV0E "]}"(w0), "=&v"(t), "=&s"(n) : "v"(agp) : "memory", "scc", "vcc");
        W0[0] = make_float2(w0[0], w0[1]); W0[1] = make_float2(w0[2], w0[3]);
        const float2 g0 = cfma(l64[0], W0[0], wag[0]), g1 = cfma(l64[1], W0[1], wag[1]);
        auto tb_ = [&](float v, unsigned i) INL { unsigned k = sel + i; SPIN(k); return __builtin_amdgcn_perm(__float_as_uint(v), tgv, k); };
        st128<SC1>((u32x4){tb_(g0.x, 0), tb_(g0.y, 1), tb_(g1.x, 2), tb_(g1.y, 3)}, rs, gro);
    }
    TS(2);
    if (CE || CGW) sig(fst, 0x1000u);
    __builtin_amdgcn_s_setprio(PRIOW);
    const int da = da_of(pt), kh = da & 1;
    u32x2 xi[8], dk;
    auto xload = [&]() INL {
        const unsigned co = (512 * ((lane >> 3) & 1) + 4 * (lane & 7)) * 2;
        SCHED();
#pragma unroll
        for (int i = 0; i < 8; ++i)
            xi[i] = __builtin_amdgcn_raw_buffer_load_b64(mkrsrc(x, S_ * D_ * 2), (lane >> 4) * 64 * D_ * 2 + co, ((512 * wid + 256 * (i >> 2) + 16 * (i & 3) + lt) * D_ + 256 * kh + 32 * (da >> 1)) * 2, 0);
        dk = __builtin_amdgcn_raw_buffer_load_b64(mkrsrc(Dskip, D_ * 2), co, 512 * kh + 64 * (da >> 1), 0);
        SCHED();
    };
    if (XP == 0) xload();
#pragma unroll
    for (int n = 0; n < 2; ++n) {
        PIN(coef[n].x); PIN(coef[n].y);
        loc[n] = cmul(coef[n], cfma(wq[n], W0[n], loc[n]));
        wq[n] = cmul(wq[n], cw[n]);
        loc[n].y = -loc[n].y;
        PIN(wq[n].x); PIN(wq[n].y); PIN(loc[n].x); PIN(loc[n].y);
    }
    float2 u[2][16];
#pragma unroll
    for (int t = 0; t < 16; ++t) {
#pragma unroll
        for (int n = 0; n < 2; ++n) { u[n][t] = cmul(make_float2(coef[n].x, -coef[n].y), make_float2(acc[t >> 2][n][t & 3], -acc[t >> 2][2 + n][t & 3])); PIN(u[n][t].x); PIN(u[n][t].y); }
        SCHED();
    }
    lptr tb = smem + A_HS + wid * 2048 + lane * 4, rb = smem + A_HS + wid * 2048 + (lane >> 4) * 256 + (lane & 3) * 64 + ((lane >> 2) & 3) * 16;
    unsigned hoff = OFF_H + ((lane >> 4) * 64 + pt * 2 + wc) * 8192 + lt * 512 + wr * 256 + (lane & 15) * 16;
    PIN(tb); PIN(rb); PIN(hoff);
    unsigned ex[4], s0 = 0x04000400u, s1 = 0x05040100u, km = 0xffffff00u;
#pragma unroll
    for (int j = 0; j < 4; ++j) { ex[j] = 4 * j + q < lt ? (unsigned)tag : 0u; PIN(ex[j]); }
    SPIN(s0); SPIN(s1); SPIN(km);
    __builtin_amdgcn_s_setprio(PRIOT);
    await<0>(fb + F_FREE, 8u << 20);
    if (XP == 1) xload();
    if (GDL) __builtin_amdgcn_s_sleep(GDL);
    TS(3);
    {
        u32x4 gr[4];
        unsigned off = ((16 - lt + q) * 512 + pt * 32 + wc * 16 + c) * 16;
        auto gload = [&]() INL {
            PIN(off);
#pragma unroll
            for (int j = 0; j < 4; ++j) gr[j] = __builtin_amdgcn_raw_buffer_load_b128(mkrsrc(ws + OFF_GRAN + j * 32768, (4 - j) * 32768), off, 0, SC1);
        };
        auto gok = [&]() INL {
            unsigned d = 0;
#pragma unroll
            for (int j = 0; j < 4; ++j) d |= __builtin_amdgcn_perm(__builtin_amdgcn_perm(gr[j].w, gr[j].z, s0), __builtin_amdgcn_perm(gr[j].y, gr[j].x, s0), s1) ^ ex[j];
            return __builtin_amdgcn_uicmp(d, 0u, 33) == 0ull;
        };
        gload();
        if (XP == 4) xload();
        if (__builtin_expect(!gok(), 0)) {
            int spin;
#pragma unroll 1
            for (spin = 1; spin < (1 << 20); ++spin) {
                asm volatile("" ::: "memory");
                if (GSL) __builtin_amdgcn_s_sleep(GSL);
                gload();
                if (gok()) break;
            }
        }
        TS(25);
        if (XP == 5) xload();
#pragma unroll
        for (int j = 0; j < 4; ++j) gr[j] &= km;
        float2 cs[2];
#pragma unroll
        for (int j = 3; j >= -1; --j) {
#pragma unroll
            for (int n = 0; n < 2; ++n) {
                const float2 g = make_float2(__uint_as_float(gr[j & 3][2 * n]), __uint_as_float(gr[j & 3][2 * n + 1]));
                cs[n] = j == 3 ? g : j < 0 ? cmul(cs[n], lq[n]) : cfma(l512[n], cs[n], g);
            }
        }
        rowsum4(cs[0].x, cs[0].y, cs[1].x, cs[1].y);
#pragma unroll
        for (int n = 0; n < 2; ++n) loc[n] = cfma(make_float2(wq[n].x, -wq[n].y), make_float2(cs[n].x, -cs[n].y), loc[n]);
    }
    TS(4);
    u32x4 hv[2];
#pragma unroll
    for (int r = 0; r < 5; ++r) {
#pragma unroll
        for (int t = 0; t < 4; ++t) {
            if (r < 4) {
#pragma unroll
                for (int n = 0; n < 2; ++n) loc[n] = cfma(make_float2(lam[n].x, -lam[n].y), loc[n], u[n][4 * r + t]);
                lst<unsigned>(tb + t * 256, pkb(loc[0].x, loc[1].x));
                lst<unsigned>(tb + 1024 + t * 256, pkb(loc[0].y, loc[1].y));
            }
            if (r && t >= HST && t < HST + 2) { SCHED(); st128<SC1>(hv[t - HST], rs, hoff + ((r - 1) * 256 + (t - HST) * 32) * 8192); SCHED(); }
        }
        asm volatile("" ::: "memory");
        if (r < 4) { hv[0] = lld<u32x4>(rb); hv[1] = lld<u32x4>(rb + 1024); }
        asm volatile("" ::: "memory");
        SCHED();
    }
    TS(5);
    PIN(lane);
    auto cp2 = [&]() INL {
        if (wid == 3) asm volatile("s_nop 0\n\tbuffer_load_dword %0, %1, %2 offen lds" :: "v"(lane * 128), "s"(cd), "s"(((gb >> 3) & 7) * 8192), "{m0}"(ldsa(smem + A_AGG + 256)) : "memory");
    };
    if (XLP == 0) cp2();
    f32x4 ac[8][2];
    auto acin = [&]() INL {
#pragma unroll
        for (int i = 0; i < 8; ++i) {
            PIN(xi[i]);
            const u32x2 d = dk, v = xi[i];
            const unsigned p0 = pkb(lo2f(v.x) * (0.5f * lo2f(d.x)), hi2f(v.x) * (0.5f * hi2f(d.x))), p1 = pkb(lo2f(v.y) * (0.5f * lo2f(d.y)), hi2f(v.y) * (0.5f * hi2f(d.y)));
            ac[4][i >> 2][i & 3] = lo2f(p0); ac[5][i >> 2][i & 3] = hi2f(p0); ac[6][i >> 2][i & 3] = lo2f(p1); ac[7][i >> 2][i & 3] = hi2f(p1);
            ac[i >> 1][i & 1] = Z4;
        }
#pragma unroll
        for (int i = 0; i < 16; ++i) PIN(ac[i >> 1][i & 1]);
    };
    if (XP == 2) xload();
    if (ACI == 2) { SCHED(); acin(); SCHED(); }
    const u32x2 tg = {(unsigned)tag, (unsigned)(tag >> 32)};
    unsigned ord, tv;
    asm volatile("s_mov_b64 exec, 1\n\ts_waitcnt vmcnt(%8)\n\tds_add_rtn_u32 %1, %2, %3\n\ts_mov_b64 exec, %9\n\ts_waitcnt lgkmcnt(0)\n\tv_readfirstlane_b32 %0, %1\n\t"
                 "s_cmp_lg_u32 %0, 3\n\ts_cbranch_scc1 1f\n\tbuffer_store_dwordx2 %4, %5, %6, %7 offen sc1\n1:\ts_mov_b64 exec, -1"
                 : "=&s"(ord), "=&v"(tv) : "v"(fb + F_HC), "v"(1u), "v"(tg), "v"(lane * 256 * FST), "s"(mkdesc(ws, WS_END)), "s"(OFF_FH + (pt * 16 + lt) * FST),
                   "i"(XP == 2 ? 9 : 0), "i"((1 << HREP) - 1) : "memory", "scc");
    ord = __builtin_amdgcn_readfirstlane(ord);
    const bool pub = ord == 3u;
    TS(15);
    if (XLP == 1) cp2();
    if (XP == 3 || XP == 6) xload();
    __builtin_amdgcn_s_setprio(PRIOI);
    if (ACI == 0) acin();
    unsigned vo = ((lane & 15) >> 2) * 256 + (lane >> 4) * 64 + (lane & 3) * 16, so[32];
    PIN(vo);
#pragma unroll
    for (int k = 0; k < 32; ++k) {
        so[k] = OFF_H + lt * 524288 + kh * 131072 + wid * 2048 + (((XE & 2) ? 0 : k >> 2) ^ CX(gb)) * 16384 + ((k & 1) + (k & 2) * 16) * 8192;
        if (SPC || k < 8) asm volatile("" : "+s"(so[k]));
    }
    auto ldB = [&](int b) { return __builtin_amdgcn_raw_buffer_load_b128(rs, vo + (b & 1) * 1024, so[b >> 1], SC1); };
    lptr ab[2];
#pragma unroll
    for (int i = 0; i < 2; ++i) { ab[i] = smem + 2 * A_STAGE + (lane & 15) * 128 + (((4 * i + (lane >> 4)) ^ (lane & 7)) << 4); PIN(ab[i]); }
    auto ldA = [&](int f) INL {
        const int k = f < FT ? f_k(f) : f_k(f) ^ 12;
        if (f > 8 && !f_i(f) && (k & 6) == (f < FT ? 0 : 2)) { ab[k & 1] += (k & 8) ? -2 * A_STAGE : 2 * A_STAGE; PIN(ab[k & 1]); }
        return lld<u32x4>(ab[k & 1] + ((k >> 1) & 3) * 16384 + f_i(f) * 2048);
    };
    const unsigned hb = ldsa(smem) + 8192 + wid * 2048;
    unsigned fv = fb + F_FULL, fw = fb + F_RD + wid, pa = hb + (HD ? 0 : 2 * A_STAGE) + lane * 16; PIN(fv); PIN(fw); PIN(pa);
    u64 ob = (u64)(out + (512 * wid + lt) * D_ + 256 * kh + 32 * (da >> 1)); SPIN(ob);
    __builtin_amdgcn_s_setprio(PRIOG);
    TS(27);
    unsigned fe = 0, fs;
    static_assert(!HPL && NP1 == 1, "");
    if (ord == POW) {
        int spin = 0;
        unsigned o = (lane ^ ((CX(gb) & 3) << 4)) * FST, all = 0x8888u; PIN(o); PIN(all);
        if (POW != 3) await<1>(fb + F_HC, 4u);
        if (late(gb)) __builtin_amdgcn_s_sleep(PDX); else __builtin_amdgcn_s_sleep(PDL);
        TS(19);
        const u32x2 fl = __builtin_amdgcn_raw_buffer_load_b64(rs, o, OFF_FH + ((gb & (HREP - 1)) * 256 + kh * 128 + (CX(gb) >> 2) * 64) * FST, SC1);
        fs = lds_peek(fv) | 0x8888u;
        const u64 b = __builtin_amdgcn_uicmpl(__builtin_bit_cast(u64, fl), tag, 32);
        if (__builtin_expect(b == ~0ull, 1)) sig_or(fv, all);
        else {
            fs = 0;
#pragma unroll
            for (int j = 0; j < 4; ++j) fs |= ((unsigned)(b >> (16 * j)) & 0xffffu) == 0xffffu ? 8u << (4 * j) : 0u;
#pragma unroll 1
            for (; spin < (1 << 20) && !(fs & 8u); ++spin) fs = h_poll<1>(rs, gb, kh, lane, tg);
            sig_or(fv, fs);
        }
    } else fs = await<AW0>(fv, 8u);
    TS(7);
    auto need = [&](unsigned bit) INL { need_bit(fv, fs, bit); };
    u32x4 A[256], B[64 + NRL];
#pragma unroll
    for (int b = 0; b < NB; ++b) { if (b && !(b & 7)) need(8u << (b >> 1)); B[b] = ldB(b); SCHED(); }
    SCHED();
    if (ACI == 1) { acin(); SCHED(); }
    need(4u);
#pragma unroll
    for (int f = 0; f < AD; ++f) A[f] = ldA(f);
    SCHED();
    if (BSL) { for (int i = 0; i < BSL; ++i) __builtin_amdgcn_s_sleep(16); }
    if (BA2) burn<BS2>(BA2, A[0], A[1]);
    const unsigned po = OFF_U + ((lt * 16 + da) * 4 + wid) * 4096;
    unsigned pk[16], t0, pq = (po ^ 16384) - hb, pr = po - hb, gc = ldsa(smem) + A_XT + wid * 4096 - hb, cc = ldsa(smem) + A_CN - hb, oc = ldsa(smem) + A_OG + 256 * wid, ks = 0x07060302u;
    SPIN(pq); SPIN(pr); SPIN(gc); SPIN(cc); SPIN(oc); SPIN(ks);
    lptr og, cp;
    unsigned gp[2], ph;
    u32x4 pv[4], sl[2], ga[4], oo; f32x4 o2[2], r0[8];
    auto put = [&](int i) INL {
        const int e = i & 3, n = i >> 2;
#pragma unroll
        for (int j = 0; j < 4; ++j) r0[i][j] = fmaf(o2[n][e], ac[4 + j][n][e], r0[i][j]);
        st128<WTO>(__builtin_bit_cast(u32x4, r0[i]), mkrsrc((const char*)ob + n * 256 * D_ * 4, 241 * D_ * 4), oo[e]);
    };
    auto nextB = [&](int b) INL {
        if (b + NB >= 64) return;
        if (XE & 16) { B[b + NB] = B[b]; PIN(B[b + NB]); } else B[b + NB] = ldB(b + NB);
    };
    auto unp = [&](int j) INL {
        if (j < 0 || j > 31) return;
        const unsigned v = ga[j >> 3][(j >> 1) & 3];
        float t = (j & 1) ? hi2f(v) : lo2f(v);
        PIN(t);
        r0[j >> 2][j & 3] = t;
    };
    static_assert(GS0 + US0 + 32 <= 516 && US0 >= 8, "");
    sfor<0, 64>([&](auto ic) INL {
#define T8(x) {ST.x[s0], ST.x[s0 + 1], ST.x[s0 + 2], ST.x[s0 + 3], ST.x[s0 + 4], ST.x[s0 + 5], ST.x[s0 + 6], ST.x[s0 + 7]}
        constexpr int s0 = 8 * decltype(ic)::v, TA_[8] = T8(a), TP_[8] = T8(p), TW_[8] = T8(w);
        constexpr unsigned TM_[8] = T8(m);
#pragma unroll
        for (int u = 0; u < 8; ++u) {
            const int s = s0 + u, f = s_f(s), n = s_n(s), k = f_k(f), z = s - 2 * FT;
            MF2(B[s_b(s)], A[f], ac[f_m(f)][n]);
            SCHED();
            if (TP_[u] == 3) { fs = __builtin_amdgcn_readfirstlane(fe); SPIN(fs); }
            if (TP_[u] == 1 || TP_[u] == 2) fe = *(volatile LAS unsigned*)(lptr)(uintptr_t)(fv + (TP_[u] == 2 ? F_FB - F_FULL : 0));
            if (TM_[u]) { need_bit(fv, fs, TM_[u], (TM_[u] & 0x44u) && s > 128 ? F_FB - F_FULL : 0); if (s == GS0 && !(XE & 32)) TS(26); }
            if (z < 0 && !n) {
                if (f_hi(f)) nextB(2 * k);
                if (f_i(f) == 1 && k) nextB(2 * k - 1);
            }
            if (!HD && z < 0 && k >= 16 && k < 24) {
                const int v = s & 7;
                if (v == 6 && k == 19) { pa -= 2 * A_STAGE; PIN(pa); }
                if (v == WS0) lst<u32x4>((lptr)(uintptr_t)pa + h_off(k, 0), B[2 * k]);
                if (v == WS1) lst<u32x4>((lptr)(uintptr_t)pa + h_off(k, 1), B[2 * k + 1]);
            }
            if (z == -6) { t0 = pa + pr; PIN(t0); }
#pragma unroll
            for (int i = 0; i < 24; ++i) {
                const int j = i / 6, o = i % 6;
                if (z != (PKR == 1 ? i - 4 - j : PKR == 2 ? 3 * j + o / 2 - 4 : 2 * j + o / 3 - 4)) continue;
                if (o == 0) pk[4 * j] = __builtin_amdgcn_perm(__float_as_uint(ac[j][0][1]), __float_as_uint(ac[j][0][0]), ks);
                if (o == 1) pk[4 * j + 1] = pkb(ac[j][0][2], ac[j][0][3]);
                if (o == 2) pk[4 * j] |= 0x10001u;
                if (o == 3) pk[4 * j + 2] = pkb(ac[j][1][0], ac[j][1][1]);
                if (o == 4) pk[4 * j + 3] = pkb(ac[j][1][2], ac[j][1][3]);
                if (o < 5) PIN(pk[4 * j + (o == 2 ? 0 : o > 2 ? o - 1 : o)]);
                else { st128<SC1>((u32x4){pk[4 * j], pk[4 * j + 1], pk[4 * j + 2], pk[4 * j + 3]}, rs, t0 + j * 1024); if (j == 3 && !(XE & 32)) TS(29); }
            }
            if (z == 18) { ph = pa + 2 * A_STAGE; PIN(ph); }
#pragma unroll
            for (int r = 0; r < NRL; ++r) if (z == RS0 + RSP * r) B[64 + r] = lld<u32x4>((lptr)(uintptr_t)(r < 8 ? pa : ph) + h_off(23 - (r >> 1), r & 1));
            constexpr int RS = 2 * FT + RSD;
            static_assert(RS + 6 <= 507, "");
            if (s >= RS && s < RS + 8 && !((s - RS) & 1)) pv[(s - RS) >> 1] = __builtin_amdgcn_raw_buffer_load_b128(rs, pa + (s - RS) * 512, pq, SC1);
            if (z == 22) { gp[0] = pa >> 2; PIN(gp[0]); }
            if (z == 24) { gp[0] &= 0xc0u; PIN(gp[0]); }
            if (z == 28) { og = (lptr)(uintptr_t)(gp[0] + oc); PIN(og); }
            if (z == 30) { gp[0] = (gp[0] ^ pa) + gc; PIN(gp[0]); }
            if (z == 34) { gp[1] = gp[0] ^ 32u; PIN(gp[1]); }
            if (z == 36) { cp = (lptr)(uintptr_t)(pa + cc); PIN(cp); }
            if (s > GS0 && s <= GS0 + 4) ga[s - GS0 - 1] = lld<u32x4>((lptr)(uintptr_t)gp[(s - GS0 - 1) & 1] + (s - GS0 - 1) * 1024);
            if (s == 496) sl[0] = lld<u32x4>(cp);
            if (s == 498) sl[1] = lld<u32x4>(cp + 1024);
            if (s == 500) oo = lld<u32x4>(cp + 2048);
            if (s == 501) o2[0] = lld<f32x4>(og);
            if (s == 502) o2[1] = lld<f32x4>(og + 16);
            unp(s + (s > 507 ? 4 : 0) - GS0 - US0);
            if (const int g = TA_[u]; g) {
                A[g] = ldA(g);
                if (g < FT && f_hi(g) && (f_k(g) & 3) == 3) asm volatile("ds_write_b8 %0, %0 offset:%1" :: "v"(fw), "i"(f_k(g) & 28) : "memory");
            }
            if (TW_[u]) wait_lds(TW_[u] - 1);
            SCHED();
            if (s == 507 || s == 511) {
                if (s == 507) {
                    TS(10);
                    unsigned n;
                    asm volatile("s_mov_b32 %5, 0x100000\n"
                                 "1:\ts_waitcnt vmcnt(0)\n\tv_and_b32 %4, v" PV0 ", v" PV1 "\n\tv_and_b32 %4, v" PV2 ", %4\n\tv_and_b32 %4, v" PV3 ", %4\n\tv_and_b32 %4, 1, %4\n\t"
                                 "v_cmp_eq_u32 vcc, 0, %4\n\ts_cbranch_vccz 2f\n\t"
                                 "buffer_load_dwordx4 %0, %6, %7, %8 offen sc1\n\tbuffer_load_dwordx4 %1, %6, %7, %8 offen offset:1024 sc1\n\t"
                                 "buffer_load_dwordx4 %2, %6, %7, %8 offen offset:2048 sc1\n\tbuffer_load_dwordx4 %3, %6, %7, %8 offen offset:3072 sc1\n\t"
                                 "s_sub_u32 %5, %5, 1\n\ts_cbranch_scc0 1b\n\ts_waitcnt vmcnt(0)\n"
                                 "2:" : "+{v[" PV0 ":" PV0E "]}"(pv[0]), "+{v[" PV1 ":" PV1E "]}"(pv[1]), "+{v[" PV2 ":" PV2E "]}"(pv[2]), "+{v[" PV3 ":" PV3E "]}"(pv[3]), "=&v"(t0), "=&s"(n)
                                 : "v"(pa), "s"(mkdesc(ws, WS_END)), "s"(pq) : "memory", "scc", "vcc");
                    TS(11);
                    SCHED();
                }
#pragma unroll
                for (int m = 0; m < 4; ++m) { MF2(sl[n], pv[m], ac[4 + m][n]); SCHED(); if (n) put(m); else unp(508 + m - GS0 - US0); SCHED(); }
            }
            if (s == 15) { TS(8); TC(60); }
            if (s == 2 * F1 - 1) { if (!(XE & 32)) TS(9); TC(55); }
            if (z == -1) { if (!(XE & 32)) TS(28); TC(56); }
            if (s == 495) { TC(61); TS(14); }
        }
    });
    asm volatile("s_nop 7");
    SCHED();
#pragma unroll
    for (int i = 4; i < 8; ++i) { put(i); SCHED(); }
    TS(12);
    TSE(0, wid == 0);
    TSE(2, pub);
    TSE(3, ord == POW);
}

#define TSARG
#define TSPASS
struct Gate {
    unsigned xa, wa, xv, gw; float sum; int rot;
    DEV void rd(int kt) { asm volatile("ds_read_b32 %0, %2\n\tds_read_b32 %1, %3" : "=&v"(xv), "=&v"(gw) : "v"(xa + (kt & 3) * A_STAGE), "v"(wa + (kt ^ rot) * 128) : "memory"); }
    DEV void acc() {
        asm volatile("s_waitcnt lgkmcnt(0)" : "+v"(xv), "+v"(gw) :: "memory");
        const unsigned pr = pkb(lo2f(xv) * lo2f(gw), hi2f(xv) * hi2f(gw));
        sum += lo2f(pr); sum += hi2f(pr); PIN(sum);
    }
};
struct Tab { CM sf, si, ff, fi; u32x4 mt[2], xf, xb; f32x2 ti[2]; };
DEV void mid_tables(lptr smem, int gb, int w, int lane, Tab& t) {
    int mp, j1; slot_of(gb, mp, j1);
    const int c = lane & 15, q = lane >> 4;
    lptr mb = smem + A_MAT + lane * 16;
#pragma unroll
    for (int i = 0; i < 2; ++i) { t.ti[i] = lld<f32x2>(smem + A_MAT + 9216 + (2 * w + i) * 128 + c * 8); t.mt[i] = lld<u32x4>(mb + (6 + i) * 1024); }
    u32x4 sf0 = lld<u32x4>(mb + 4096), si0 = lld<u32x4>(mb + 5120);
    const u32x4 f0 = lld<u32x4>(mb + 8192);
    SCHED();
    asm volatile("" : "=v"(t.xf), "=v"(t.xb));
    if (w >= 4 && mp == 0 && !(j1 & 1)) {
        sf0 = mat_sf(64, c, q); si0 = mat_si(64, c, q);
        const float dt = -(float)(32 + 64 * c) * (1.0f / 1024.0f);
        t.xf = cmat1(dt * (float)(4 * q), dt); t.xb = cmat1((float)(c * (32 + 256 * q)) * (1.0f / 1024.0f), (float)c * (1.0f / 16.0f));
    }
    PIN(t.xf); PIN(t.xb);
    t.sf = cexp(sf0); t.si = cexp(si0); t.ff = cexp(f0); t.fi = cexp((u32x4){f0.x ^ SG, f0.y ^ SG, f0.z, f0.w});
    PIN(t.mt[0]); PIN(t.mt[1]); PIN(t.ti[0]); PIN(t.ti[1]);
}
DEV int u_exp(unsigned em) { return min(max((int)(em & 0xffu), 4), 254); }

struct Ld {
    u32x4 rx, rb, vA, vB; unsigned lb, sA, sB; int rot;
    DEV Ld(const u16* x, const u16* Bre, const u16* Bim, lptr smem, int gb, int lw, int lane) {
        int lt, pt; tile_of(gb, lt, pt);
        const int srow = lane >> 3, schunk = (lane & 7) ^ srow;
        rot = ROT;
        lb = ldsa(smem) + lw * 4096;
        const unsigned a = (16 * (srow >> 2) + (srow & 3)) * 2048 + schunk * 16, b = 2 * srow * 2048 + schunk * 16;
        vA = (u32x4){a, a + 65536 - 1024, a + 8192 - 2048, a + 73728 - 3072}; vB = (u32x4){b, b + 32768 - 1024, b, b + 34816 - 3072};
        sA = (lt * 128 + 64 * (lw >> 1) + 8 * (lw & 1)) * 2048; sB = (pt * 64 + 32 * (lw >> 1)) * 2048;
        PIN(vA.x); PIN(vA.y); PIN(vA.z); PIN(vA.w); PIN(vB.x); PIN(vB.y); vB.z = vB.x; vB.w = vB.y;
        SPIN(sA); SPIN(sB); SPIN(lb); SPIN(rot);
        SCHED();
        rx = mkdesc(x, S_ * D_ * 2); rb = mkdesc((lw & 1) ? Bim : Bre, D_ * D_ * 2);
    }
    DEV void st(int kk) const {
        dma64(rx, lb + (kk & 3) * A_STAGE, vA, sA + (kk ^ rot) * 128);
        dma64(rb, lb + (kk & 3) * A_STAGE + 16384, vB, sB + (kk ^ rot) * 128);
    }
};
DEV void first_loads(const u16* __restrict__ x, const u16* __restrict__ Bre, const u16* __restrict__ Bim, const u16* __restrict__ gate_w, lptr smem, int gb, int lw, int lane) {
    __builtin_amdgcn_s_setprio(PRIOL);
    const Ld ld(x, Bre, Bim, smem, gb, lw, lane);
    ld.st(0);
    SCHED();
    int lt, pt; tile_of(gb, lt, pt);
    dma16(mkdesc(gate_w, D_ * 2), ldsa(smem) + A_GW + (lw & 1) * 1024, lane * 16, (lw & 1) * 1024);
    const unsigned t = (lane >> 1) * 32768 + (lane & 1) * 16;
    dma64(ld.rx, ld.lb + A_XT, (u32x4){t, t + 1047552, t + 2 * 1047552, t + 3 * 1047552}, lt * 2048 + lw * 512 + pt * 32);
    if (FSL) __builtin_amdgcn_s_sleep(FSL);
    if (FLN > 1) ld.st(1);
    if (FLN > 2) ld.st(2);
}
DEV void role_load(const u16* __restrict__ x, const u16* __restrict__ Bre, const u16* __restrict__ Bim, const u16* __restrict__ Cre, const u16* __restrict__ Cim,
                   const u16* __restrict__ gate_w, char* ws, lptr smem, int gb, int lw, int lane, Tab& tab TSARG) {
    if (!ENT) __builtin_amdgcn_s_waitcnt(0x0F70);
    Gate g;
    int lt, pt; tile_of(gb, lt, pt);
    TS(0);
    if (!ENT) first_loads(x, Bre, Bim, gate_w, smem, gb, lw, lane);
    const Ld ld(x, Bre, Bim, smem, gb, lw, lane);
    if (FLN < 2) ld.st(1);
    if (FLN < 3) ld.st(2);
    const unsigned lb = ldsa(smem);
    const int gi = 8 * pt + 2 * lw + (lane >> 5);
    g.xa = lb + gi * 128 + ((((lane & 31) >> 2) ^ (gi & 7)) << 4) + (lane & 3) * 4;
    g.wa = lb + A_GW + (lane & 31) * 4; g.sum = 0.f; g.rot = ld.rot;
    TS(6);
    const int c = lane & 15, q = lane >> 4;
    const unsigned fb = lb + A_FLG;
    lptr ub = smem + A_XT + lw * 4096 + (lane >> 2) * 64 + (lane & 3) * 8, ea = smem + A_E1 + c * E1P + q * 256 + lw * 64;
    lptr mb = smem + A_MAT + lane * 16, tb = smem + A_MAT + 9216 + c * 128 + q * 32;
    PIN(ub); PIN(ea); PIN(mb); PIN(tb);
#pragma unroll 1
    for (int kt = 0; kt < 13; ++kt) { wait_vm<16>(); TL(8 + kt); BAR(); TL(28 + kt); if (kt == 0) { TS(28); TC(60); } g.rd(kt); ld.st(kt + 3); g.acc(); }
    u32x4 mt[4], ut[4];
    f32x4 tf[2];
    wait_vm<16>(); TL(21); BAR(); TL(41); g.rd(13);
#pragma unroll
    for (int i = 0; i < 4; ++i) mt[i] = lld<u32x4>(mb + i * 1024);
#pragma unroll
    for (int i = 0; i < 2; ++i) tf[i] = lld<f32x4>(tb + i * 16);
    g.acc();
#pragma unroll
    for (int i = 0; i < 4; ++i) PIN(mt[i]);
    PIN(tf[0]); PIN(tf[1]);
    wait_vm<8>(); sig(fb + F_ST, 1u); TL(42);
    wait_vm<0>(); sig(fb + F_ST, 0x10u);
    __builtin_amdgcn_s_setprio(PRIOD);
    await<0>(fb + F_ST, 0x40u);
    TS(43); TC(61);
    g.rd(14); g.acc(); g.rd(15); g.acc();
    sig(fb + F_FREE, 1u << 20);
    TS(2);
    __builtin_amdgcn_s_setprio(PRIOM);
    mid_tables(smem, gb, lw + 4, lane, tab);
    float gg = bfr(1.0f / (1.0f + expf(-bfr(half_sum(g.sum))))), og = bfr(1.0f - gg);
    const int tau = (gi & 64) | ((gi & 12) << 2) | ((gi & 48) >> 2) | (gi & 3);
    unsigned go = (lane & 31) ? ~0u : OFF_G + (tau & 15) * 1024 + (lt * 8 + (tau >> 4)) * 8;
    PIN(gg); PIN(og); PIN(go);
    __builtin_amdgcn_s_setprio(PRIOD);
    TS(53);
    auto c01 = [&]() INL {
        await<1>(fb + F_FREE, 8u << 20);
        c_chunk<C0S>(Cre, Cim, lb, gb, da_of(pt), lw, lane, 0);
        if (C0E != 2) c_chunk<C0S>(Cre, Cim, lb, gb, da_of(pt), lw, lane, 1);
    };
    if (C0E) c01();
    const unsigned em = await<1>(fb + F_EM, 0x10000u << lw);
    TS(3);
#pragma unroll
    for (int i = 0; i < 4; ++i) ut[i] = trfrag(ub + i * 1024, 32);
    const float su = __uint_as_float((unsigned)(258 - u_exp(em)) << 23);
    tf[0] *= su; tf[1] *= su; PIN(tf[0]); PIN(tf[1]);
#pragma unroll
    for (int i = 0; i < 4; ++i) {
        f32x4 r = mfb(ut[i], mt[2], Z4), m = mfb(ut[i], mt[3], Z4);
        r = mfb(ut[i], mt[0], r); m = mfb(ut[i], mt[1], m);
        f32x4 yr, yi;
#pragma unroll
        for (int e = 0; e < 4; ++e) {
            const float tc = tf[e >> 1][2 * (e & 1)], ts = tf[e >> 1][2 * (e & 1) + 1];
            yr[e] = fmaf(r[e], tc, -m[e] * ts); yi[e] = fmaf(r[e], ts, m[e] * tc);
        }
        lst<u32x4>(ea + i * 16, pk8(yr, yi));
    }
    if (gb == XG) {
        const unsigned sg = 0xbf803f80u;
#pragma unroll
        for (int i = 0; i < 4; ++i) {
            const f32x4 e = mfb(ut[i], c == 0 ? (u32x4){sg, sg, 0, 0} : (u32x4){0, 0, sg, sg}, Z4);
            if (c < 2) lst<u32x4>(ea + 16 * E1P + i * 16, pk8(e * su, Z4));
        }
    }
    sig(fb + F_MID, 1u);
    TS(54);
    __builtin_amdgcn_s_setprio(PRIOM);
    if (C0L < 2 && !C0E) await<1>(fb + F_FREE, 8u << 20);
    if (CGW) await<1>(fb + F_ST, 0x4000u);
    if (C0L < 2 && !C0E) c_chunk<C0S>(Cre, Cim, lb, gb, da_of(pt), lw, lane, 0);
    if (C0L < 1 && C0E != 1) c_chunk<C0S>(Cre, Cim, lb, gb, da_of(pt), lw, lane, 1);
    __builtin_amdgcn_raw_buffer_store_b64((u32x2){__float_as_uint(gg), __float_as_uint(og)}, mkrsrc(ws, WS_END), go, 0, SC1);
    TS(52);
}

DEV unsigned ldw(rsrc_t r, unsigned vo, unsigned so) { return __builtin_amdgcn_raw_buffer_load_b32(r, vo, so, 0); }
DEV unsigned amax4(u32x4 p) { return pkmax(pkmax(p.x & 0x7fff7fffu, p.y & 0x7fff7fffu), pkmax(p.z & 0x7fff7fffu, p.w & 0x7fff7fffu)); }

DEV void role_aux(const u16* __restrict__ w_r, const u16* __restrict__ w_i, const u16* __restrict__ lam_re, const u16* __restrict__ lam_im,
                  const u16* __restrict__ log_dt, char* ws, lptr smem, int gb, int aw, int lane, Tab& tab TSARG) {
    int lt, pt; tile_of(gb, lt, pt);
    int mp, j1; slot_of(gb, mp, j1);
    const int c = lane & 15, q = lane >> 4;
    const bool spc = mp == 0 && !(j1 & 1);
    const rsrc_t rs = mkrsrc(ws, WS_END), rr = mkrsrc(w_r, S_ * KF * 2), ri = mkrsrc(w_i, S_ * KF * 2);
    TS(0);
    __builtin_amdgcn_s_setprio(PRIOA);
    u32x4 fch, fsh;
    const bool m0 = 16 * aw + c == 0;
    {
        const float th = (float)(16 * aw + c) * (1.0f / 128.0f);
#pragma unroll
        for (int i = 0; i < 4; ++i) {
            const float t0 = __builtin_amdgcn_fractf(th * (float)(16 * (i >> 1) + 2 * (i & 1) + 4 * q)), t1 = t0 + th;
            const float c0 = __builtin_amdgcn_cosf(t0), c1 = __builtin_amdgcn_cosf(t1);
            const float s0 = m0 ? 1.f : -__builtin_amdgcn_sinf(t0), s1 = m0 ? -1.f : -__builtin_amdgcn_sinf(t1);
            fch[i] = pkb(c0, c1); fsh[i] = pkb(s0, s1);
        }
        PIN(fch); PIN(fsh);
    }
    unsigned sg64 = (c & 1) ? SG : 0u, sgA = (c & 2) ? SG : 0u, sgB = ((c + 1) & 2) ? SG : 0u;
    constexpr unsigned WR = 128 * KF * 2;
    const bool xh = gb == XG && aw >= 2;
    const int c0 = 2 * (lane & 7) + xh, jb = (lane >> 3) + 4 * (aw & 2);
    const int k1 = mp ? mp : spc ? (lane & 4) * 16 : (j1 == 1 ? 0 : 64);
    const int kd = j1 + 4 * (spc ? c0 & 7 : c0) + 64 * jb;
    unsigned wo = (aw < 2 ? (k1 * KF + kd) * 2 + aw * 8 * WR : ((128 - k1) * KF + 1020 - kd) * 2 + (3 - aw) * 8 * WR) & ~3u;
    unsigned wo0 = aw < 2 ? wo : k1 || aw == 3 ? wo + 7 * WR : wo - 9 * WR;
    unsigned uoff = (lane >> 3) * 131072 + (lane & 7) * 16 + OFF_U + aw * 64 * 16384 + lt * 2048 + pt * 128;
    PIN(sg64); PIN(sgA); PIN(sgB); PIN(wo); PIN(wo0); PIN(uoff);
    unsigned lru, liu, ldu, um = 0;
    u32x2 tag;
    u32x4 fl[2];
    asm volatile("" : "=v"(fl[0]), "=v"(fl[1]));
    u32x3 wre[8], wim[8];
    u32x4 wsc = {0, 0, 0, 0};
    f32x4 vr[4], vi[4];
    float dt = 0.f, er = 0.f; float2 lam = make_float2(0.f, 0.f);
#pragma unroll
    for (int d1 = 0; d1 < 4; ++d1) { vr[d1] = Z4; vi[d1] = Z4; }
    auto wx = [&]() INL {
        if (j1 == 0 && aw == 3) {
            const int ks = (spc ? (lane >> 5) * 64 : mp) + 128 * c, ng = (S_ - ks) & (S_ - 1);
            unsigned o0 = (q & 1) ? (ng * KF + 511) * 2u : ks * KF * 2u, o1 = (q & 1) ? (ks * KF + 511) * 2u : ng * KF * 2u;
            PIN(o0); PIN(o1);
            wsc = (u32x4){ldw(rr, o0, 0), ldw(rr, o1, 0), ldw(ri, o0, 0), ldw(ri, o1, 0)};
        }
    };
    auto prm = [&]() INL {
        lru = ldw(mkrsrc(lam_re, 2048), (lane >> 1) * 4, pt * 128); liu = ldw(mkrsrc(lam_im, 2048), (lane >> 1) * 4, pt * 128);
        ldu = ldw(mkrsrc(log_dt, 2048), (lane >> 1) * 4, pt * 128);
    };
    if (PRS < 0) prm();
    const unsigned fb = ldsa(smem + A_FLG);
    bool uok = false;
    auto upub = [&]() INL { sig(fb + F_EM, (wave_pkmax(pkmax(pkmax(fl[0].x, fl[0].z), pkmax(fl[1].x, fl[1].z))) & 0xffu) | 0x8000u); };
    u32x4 uo[4];
    f32x4 pq[4];
    asm volatile("" : "=v"(pq[0]), "=v"(pq[1]), "=v"(pq[2]), "=v"(pq[3]));
    sfor<0, 14>([&](auto ic) INL {
        constexpr int kt = decltype(ic)::v;
        constexpr int GS[4] = GSTEP, KSO[4] = {0, 2, 3, 1}, JO[4] = {0, 2, 1, 3}, WRW[5] = WROW;
        auto ust = [&](int k) INL {
#pragma unroll
            for (int u = 0; u < 4; ++u) if (US + u / UPS == k && !(XM & 2)) st128<SC1>(uo[u], rs, uoff + JO[u] * 32768);
        };
        auto mem = [&]() INL {
            if (kt >= 10) {
#pragma unroll
                for (int r = 0; r < 8; ++r) {
                    if (r < WRW[kt < 10 ? 0 : kt - 10] || r >= WRW[kt < 10 ? 0 : kt - 9]) continue;
                    const unsigned so = r ? (aw < 2 ? r : 7 - r) * WR : 0u;
                    wre[r] = __builtin_amdgcn_raw_buffer_load_b96(rr, r ? wo : wo0, so, 0); wim[r] = __builtin_amdgcn_raw_buffer_load_b96(ri, r ? wo : wo0, so, 0);
                    if (r + 1 == URS) { SCHED(); wait_vm<2 * (URS - WRW[UCK - 10])>(); sig(fb + F_EM, 0x10000u << aw); TS(6); SCHED(); }
                }
            }
            if (kt == PRS) prm();
            if (kt == FTS && aw == 3)
                asm volatile("s_nop 0\n\tbuffer_load_dword %0, %1, 0 offen sc1 lds" :: "v"(lane * 64), "s"(mkdesc(ws + OFF_G + gb * FTB, FTB)), "{m0}"(ldsa(smem + A_AGG + 256)) : "memory");
            if (kt == WXS) wx();
            if (kt == UPL && UPL < 13 && aw == 2) flag_get(rs, OFF_FU, gb, lane, fl);
        };
        if (kt == UFS) wait_vm<0>();
        if (kt == UFS + 1) { uoff = (lane >> 2) * 128 + (lane & 3) * 16; PIN(uoff); }
        if (kt == UCK && UPRE && aw == 2) { uok = flag_ok(fl, tag); if (uok) { upub(); LDS_WAIT(); } }
        TL(8 + kt);
        BAR();
        if (VL == 2) ust(kt - 1);
        if (kt == 13 && aw == 1) {
            lst<f32x4>(smem + A_PW + lane * 16, (f32x4){1.f, 0.f, 1.f, 0.f});
#pragma unroll
            for (int i = 0; i < 4; ++i) lst<f32x4>(smem + A_PW + (i + 1) * 1024 + lane * 16, pq[i]);
            sig(fb + F_ST, 0x100u);
        }
        if (kt == UCK) {
            if (aw == 2 && !uok) {
                int spin;
                if (UPL > 12) { if (UDL) __builtin_amdgcn_s_sleep(UDL); flag_get(rs, OFF_FU, gb, lane, fl); }
#pragma unroll 1
                for (spin = 0; spin < (1 << 20); ++spin) { if (flag_ok(fl, tag)) break; flag_get(rs, OFF_FU, gb, lane, fl); }
                upub();
            }
            await<0>(fb + F_EM, 0x8000u);
            const u32x4 rw = mkdesc(ws, WS_END);
#pragma unroll
            for (int i = 0; i < 4; ++i)
                dma16s(rw, ldsa(smem + A_XT) + aw * 4096 + i * 1024, uoff, OFF_U + ((mp >> 1) * 4 + j1) * 32768 + (mp & 1) * 64 + aw * 8192 + i * 2048);
            TS(2);
        }
        if (kt == ATS) mid_tables(smem, gb, aw, lane, tab);
        u32x4 xa[4][4];
#pragma unroll
        for (int g = 0; g < 4; ++g) {
            if (GS[g] != kt) continue;
#pragma unroll
            for (int i = 0; i < 4; ++i) xa[g][i] = trfrag(smem + A_XT + i * 4096 + KSO[g] * 1024 + lane * 8, 512);
        }
        if (VL != 1) mem();
        SCHED();
#pragma unroll
        for (int g = 0; g < 4; ++g) {
            if (GS[g] != kt) continue;
#pragma unroll
            for (int i = 0; i < 4; ++i) { vr[i] = mfb(xa[g][i], fch, vr[i]); vi[i] = mfb(xa[g][i], fsh, vi[i]); PIN(vr[i]); PIN(vi[i]); }
            if (g == 0 || g == 2) { fch ^= sg64; fsh ^= sg64; PIN(fch); PIN(fsh); }
            if (g == 1) {
                const bool od = c & 1;
                const u32x4 a = (od ? fsh : fch) ^ sgA, b = (od ? fch : fsh) ^ sgB;
                fch = a; fsh = b; PIN(fch); PIN(fsh);
            }
        }
        if (kt == B1S) {
            const f32x4 t0r = vr[0] + vr[2], t1r = vr[0] - vr[2], t2r = vr[1] + vr[3], t3r = vr[1] - vr[3];
            const f32x4 t0i = vi[0] + vi[2], t1i = vi[0] - vi[2], t2i = vi[1] + vi[3], t3i = vi[1] - vi[3];
            vr[0] = t0r; vr[1] = t1r; vr[2] = t2r; vr[3] = t3r; vi[0] = t0i; vi[1] = t1i; vi[2] = t2i; vi[3] = t3i;
#pragma unroll
            for (int i = 0; i < 4; ++i) { PIN(vr[i]); PIN(vi[i]); }
        }
#pragma unroll
        for (int u = 0; u < 4; ++u) {
            if (US + u / UPS != kt) continue;
            const int j = JO[u];
            f32x4 a, b;
            if (j == 0) { a = vr[0] + vr[2]; b = vi[0] + vi[2]; }
            if (j == 2) { a = vr[0] - vr[2]; b = vi[0] - vi[2]; }
            if (j == 1) { a = vr[1] + vi[3]; b = vi[1] - vr[3]; if (aw == 0) { if (m0) { a = vr[1]; b = -vr[3]; } } }
            if (j == 3) { a = vr[1] - vi[3]; b = vi[1] + vr[3]; if (aw == 0) { if (m0) { a = vi[1]; b = vi[3]; } } }
            uo[u] = quads(smem + A_XT + aw * 4096 + j * 1024, lane, (u32x2){pkb(a[0], a[1]), pkb(a[2], a[3])}, (u32x2){pkb(b[0], b[1]), pkb(b[2], b[3])});
            uo[u].x = (uo[u].x + ((uo[u].x >> 1) & 1u)) & ~1u;
            um = pkmax(um, amax4(uo[u]));
            PIN(um); PIN(uo[u]);
#pragma unroll
            for (int i = 0; i < 4; ++i) if (u < 3 && ((i & 1) || u == 0)) { PIN(vr[i]); PIN(vi[i]); }
        }
        if (kt == UFS - 1) {
            um = wave_pkmax(pkmax(um, um >> 16));
            if (lane == 0) lst<unsigned>(smem + A_FLG + F_MAX + aw * 4, um);
            tag = lld<u32x2>(smem + A_FLG + F_TAG); PIN(tag);
        }
        if (kt == P0S) {
            PIN(lru); PIN(liu); PIN(ldu);
            if (lane & 1) { lru >>= 16; liu >>= 16; ldu >>= 16; }
            PIN(lru); PIN(liu); PIN(ldu);
        }
        if (kt >= P0S && kt < P0S + 5 && aw == 1) {
            const float lr = lo2f(lru), li = lo2f(liu);
            if (kt == P0S) { dt = bfr(expf(lo2f(ldu))); er = expf(dt * lr); PIN(dt); PIN(er); }
            if (kt == P0S + 1) { float sn, cs; sincosf(dt * li, &sn, &cs); lam = make_float2(er * cs, er * sn); PIN(lam.x); PIN(lam.y); }
            if (kt == P0S + 2) {
                const float nr = lam.x - 1.0f, ni = lam.y, den = lr * lr + li * li;
                pq[3][0] = (nr * lr + ni * li) / den; pq[3][1] = (ni * lr - nr * li) / den;
                PIN(pq[3]);
            }
            if (kt == P0S + 3) {
                float2 l16 = lam;
#pragma unroll
                for (int i = 0; i < 4; ++i) l16 = cmul(l16, l16);
                const float2 l32 = cmul(l16, l16), l48 = cmul(l32, l16);
                lst<f32x4>(smem + A_PRM + lane * 32, (f32x4){lam.x, lam.y, l16.x, l16.y});
                lam = cmul(l32, l32);
                pq[0][0] = l16.x; pq[0][1] = l16.y; pq[1][0] = l32.x; pq[1][1] = l32.y; pq[2][0] = l48.x; pq[2][1] = l48.y;
                PIN(pq[0]); PIN(pq[1]); PIN(pq[2]); PIN(lam.x); PIN(lam.y);
            }
            if (kt == P0S + 4) {
                const float2 l128 = cmul(lam, lam), l256 = cmul(l128, l128), l384 = cmul(l256, l128), l512 = cmul(l256, l256), cl = cmul(make_float2(pq[3][0], pq[3][1]), lam);
                lst<f32x4>(smem + A_PRM + lane * 32 + 16, (f32x4){lam.x, lam.y, l512.x, l512.y});
                pq[0][2] = l128.x; pq[0][3] = l128.y; pq[1][2] = l256.x; pq[1][3] = l256.y; pq[2][2] = l384.x; pq[2][3] = l384.y; pq[3][2] = cl.x; pq[3][3] = cl.y;
                PIN(pq[0]); PIN(pq[1]); PIN(pq[2]); PIN(pq[3]);
            }
        }
        SCHED();
        if (kt == UFS && aw == 0) {
            const u32x4 m4 = lld<u32x4>(smem + A_FLG + F_MAX);
            const unsigned m = pkmax(pkmax(m4.x, m4.y), pkmax(m4.z, m4.w));
            flag_put(rs, OFF_FU, gb, (u32x2){(tag.x & 0xffff0000u) | ((m & 0xffffu) >> 7), tag.y}, lane);
        }
        if (VL != 2) ust(kt);
        if (VL == 1) mem();
        LDS_WAIT();
        SCHED();
    });
    TS(54);
    if (gb == XG && aw == 0 && lane == 0) __builtin_amdgcn_raw_buffer_store_b64(tag, rs, 0, OFF_E, SC1);
    __builtin_amdgcn_s_setprio(PRIOM);
    if (ATS == 14) mid_tables(smem, gb, aw, lane, tab);
    await<1>(fb + F_FREE, 8u << 20);
    TS(3);
    unsigned mx = 0;
    const bool skip = j1 == 0 && (lane & (spc ? 59 : 63)) == 0 && !xh, x8 = xh && (lane & 3) == 3;
    lptr wd[2];
    wd[0] = smem + A_W + (aw & 1) * 512 + c0 * 1024 + ((jb ^ c0) << 4);
    wd[1] = smem + A_W + (aw & 1) * 512 + (x8 ? 16384 + (lane & 4) * 256 + (jb << 4) : (c0 + 1) * 1024 + ((jb ^ (c0 + 1)) << 4));
    if (skip) wd[0] = smem + A_W + 18432;
    if (aw >= 2) { lptr t = wd[0]; wd[0] = wd[1]; wd[1] = t; }
    const unsigned wsel = (mp + j1) & 1 ? 0x07060302u : 0x05040100u;
    PIN(wd[0]); PIN(wd[1]);
    u32x4 wp[2][2];
#pragma unroll
    for (int i = 0; i < 2; ++i) {
#pragma unroll
        for (int e = 0; e < 4; ++e) {
            wp[0][i][e] = __builtin_amdgcn_perm(wim[4 * i + e].x, wre[4 * i + e].x, wsel);
            wp[1][i][e] = __builtin_amdgcn_perm(wim[4 * i + e].z, wre[4 * i + e].z, wsel);
        }
        lst<u32x4>(wd[0] + i * 256, wp[0][i]); lst<u32x4>(wd[1] + i * 256, wp[1][i]);
        mx = pkmax(mx, pkmax(amax4(wp[0][i]), amax4(wp[1][i])));
    }
    if (j1 == 0) {
        if (aw == 3) {
            PIN(wsc);
            const unsigned sel = (q & 1) ? 0x07060302u : 0x05040100u, p0 = __builtin_amdgcn_perm(wsc.z, wsc.x, sel), p1 = __builtin_amdgcn_perm(wsc.w, wsc.y, sel);
            mx = pkmax(mx, pkmax(p0 & 0x7fff7fffu, p1 & 0x7fff7fffu));
            const int it = spc ? (lane >> 5) * 8 : 0;
            lst<unsigned>(smem + A_W + it * 1024 + (c >> 2) * 256 + ((((q & 1) * 8) ^ it) << 4) + (c & 3) * 4,
                          pkb(0.5f * (lo2f(p0) + lo2f(p1)), 0.5f * (hi2f(p0) - hi2f(p1))));
        }
        if (gb == XG && (xh ? x8 : !(lane & 3) && !skip)) {
            lptr wm = smem + A_W + (xh ? 16384 + (lane & 4) * 256 + ((15 - jb) << 4) : c0 * 1024 + (((16 - jb) ^ c0) << 4));
#pragma unroll
            for (int r = 0; r < 8; ++r) {
                const int km = (16 - ((lane >> 2) & 1) - 8 * (aw & 1) - r) & 15;
                lst<unsigned>(wm + (km >> 2) * 256 + (km & 3) * 4, wp[0][r >> 2][r & 3]);
            }
        }
    }
    mx = wave_pkmax(pkmax(mx, mx >> 16));
    if (lane == 0) lst<unsigned>(smem + A_FLG + F_WM + aw * 4, mx);
    sig(ldsa(smem + A_FLG + F_MID), 1u);
    wait_vm<0>();
    SCHED();
}

DEV int role_mid(const u16* __restrict__ Cre, const u16* __restrict__ Cim, char* ws, lptr smem, int gb, int w, int lane, u32x2& tag, const Tab& tab TSARG) {
    int mp, j1; slot_of(gb, mp, j1);
    const int c = lane & 15, q = lane >> 4;
    const bool xtr = gb == XG, ld = w >= 4;
    const rsrc_t rs = mkrsrc(ws, WS_END);
    const unsigned lb = ldsa(smem), fb = lb + A_FLG;
    const CM &sf = tab.sf, &si = tab.si, &ff = tab.ff, &fi = tab.fi;
    const f32x2 (&ti)[2] = tab.ti;
    lptr mb = smem + A_MAT + lane * 16;
    u32x4 wv[2];
    TS(4);
    await<MSL>(fb + F_MID, 24u);
    u32x4 dv[2];
#pragma unroll
    for (int i = 0; i < 2; ++i) dv[i] = lld<u32x4>(smem + A_E1 + (2 * w + i) * E1P + lane * 16);
    const u32x4 m4 = lld<u32x4>(smem + A_FLG + F_WM);
    tag = lld<u32x2>(smem + A_FLG + F_TAG);
    const unsigned em = lld<unsigned>(smem + A_FLG + F_EM);
#pragma unroll
    for (int i = 0; i < 2; ++i) wv[i] = lld<u32x4>(smem + A_W + (2 * w + i) * 1024 + ((lane ^ (2 * w + i)) << 4));
#pragma unroll
    for (int i = 0; i < 2; ++i) dv[i] = pk8(mfh(dv[i], ff.re, Z4), mfh(dv[i], ff.im, Z4));
    const unsigned ew = min((pkmax(pkmax(m4.x, m4.y), pkmax(m4.z, m4.w)) & 0xffffu) >> 7, 245u);
    const float sw = __uint_as_float((246u - ew) << 23), swi = c < 8 ? sw : -sw;
    wv[0] = wcvt(wv[0], sw, swi); wv[1] = wcvt(wv[1], sw, swi);
#pragma unroll
    for (int i = 0; i < 2; ++i) dv[i] = wmul(mfh(sf.re, dv[i], Z4), mfh(sf.im, dv[i], Z4), wv[i]);
#pragma unroll
    for (int i = 0; i < 2; ++i) dv[i] = pk8(mfh(dv[i], si.re, Z4), mfh(dv[i], si.im, Z4));
    {
        f32x4 r[2], m[2];
#pragma unroll
        for (int i = 0; i < 2; ++i) {
            const f32x4 a = mfh(dv[i], fi.re, Z4), b = mfh(dv[i], fi.im, Z4);
#pragma unroll
            for (int e = 0; e < 4; ++e) { r[i][e] = fmaf(a[e], ti[i][0], b[e] * ti[i][1]); m[i][e] = fmaf(b[e], ti[i][0], -a[e] * ti[i][1]); }
        }
#pragma unroll
        for (int e = 0; e < 4; ++e)
            lst<u32x2>(smem + A_XT + (4 * q + e) * 1024 + (w >> 1) * 256 + c * 16 + (w & 1) * 8, (u32x2){pkh(r[0][e], r[1][e]), pkh(m[0][e], m[1][e])});
    }
    if (xtr && (w & 6) == 4) {
        const int it = 5 - w;
        const CM f = cexp(tab.xf), sf2 = cexp(it ? sf.im : lld<u32x4>(mb + 4096)), si2 = cexp(it ? si.im : lld<u32x4>(mb + 5120));
        u32x4 d = lld<u32x4>(smem + A_E1 + (16 + it) * E1P + lane * 16);
        d = pk8(mfh(d, f.re, Z4), mfh(d, f.im, Z4));
        d = wmul(mfh(sf2.re, d, Z4), mfh(sf2.im, d, Z4), wcvt(lld<u32x4>(smem + A_W + 16384 + it * 1024 + lane * 16), sw, swi));
        d = pk8(mfh(d, si2.re, Z4), mfh(d, si2.im, Z4));
        const f32x4 r = mfh(d, cexp(tab.xb).re, Z4);
#pragma unroll
        for (int e = 0; e < 4; ++e) lst<float>(smem + A_EX + (it * 256 + (4 * q + e) * 16 + c) * 4, r[e]);
    }
    TS(5);
    sig(fb + F_MID, 1u);
    auto clate = [&]() INL {
        if (ld) {
            int lt, pt; tile_of(gb, lt, pt);
            if (C0L > 1) c_chunk<C0S>(Cre, Cim, lb, gb, da_of(pt), w - 4, lane, 0);
            c_chunk<C0S>(Cre, Cim, lb, gb, da_of(pt), w - 4, lane, 1);
        }
    };
    if (C0L && C0P == 0) clate();
    await<0>(fb + F_MID, 16u);
    auto cearly = [&]() INL {
        if (ld) {
            int lt, pt; tile_of(gb, lt, pt);
            await<0>(fb + F_ST, 0x4000u);
            c_chunk<CES>(Cre, Cim, lb, gb, da_of(pt), w - 4, lane, 2);
            if (CE > 1 && w < 7) c_chunk<CES>(Cre, Cim, lb, gb, da_of(pt), w - 4, lane, 3);
        }
    };
    if (CE && !CEL) cearly();
    {
        f32x4 xa[2], xe[2];
#pragma unroll
        for (int i = 0; i < 2; ++i) { xa[i] = Z4; xe[i] = Z4; }
        if (xtr) {
            const float sg = (c & 1) ? -0.125f : 0.125f;
#pragma unroll
            for (int i = 0; i < 2; ++i) {
                xa[i] = lld<f32x4>(smem + A_EX + (2 * w + i) * 64 + q * 16) * sg;
                xe[i] = lld<f32x4>(smem + A_EX + 1024 + (2 * w + i) * 64 + q * 16) * -sg;
            }
        }
#pragma unroll
        for (int i = 0; i < 2; ++i) {
            const int s2 = 2 * w + i;
            const u32x4 ea = lld<u32x4>(smem + A_XT + s2 * 1024 + lane * 16);
            const f32x4 ta = mfh(ea, tab.mt[0], xa[i]), tb = mfh(ea, tab.mt[1], xe[i]);
            u32x4 tq = quads(smem + A_XT + s2 * 1024, lane, (u32x2){pkh(ta[0], ta[1]), pkh(ta[2], ta[3])}, (u32x2){pkh(tb[0], tb[1]), pkh(tb[2], tb[3])});
            PIN(tq);
            if (C0L && C0P == 1 && i == 0) clate();
            if (CE && CEL && i == 0) cearly();
            if (TWH && i == 0) await<1>(fb + F_HC, TWH);
            if (CB >= 2 && i == 0 && ld && !late(gb)) {
                int lt, pt; tile_of(gb, lt, pt);
                c_chunk(Cre, Cim, lb, gb, da_of(pt), w - 4, lane, 2);
                if (CB == 2) c_chunk(Cre, Cim, lb, gb, da_of(pt), w - 4, lane, 3);
            }
            st128<SC1>(tq, rs, (lane >> 3) * 32768 + (lane & 7) * 16 + OFF_T + s2 * 262144 + j1 * 8192 + mp * 128);
        }
    }
    TS(7);
    tag.x = (tag.x & 0xffff0000u) | ew;
    return u_exp(em) - 267;
}

DEV void t_done(rsrc_t rs, unsigned fb, int gb, u32x2 tag, int lane) {
    int mp, j1; slot_of(gb, mp, j1);
    if (sig_rtn(fb + F_TC) == 7u) flag_put(rs, OFF_FT, (mp >> 3) * 32 + j1 * 8 + (mp & 7), tag, lane);
}

DEV void role_cload(const u16* __restrict__ Cre, const u16* __restrict__ Cim, char* ws, lptr smem, int gb, int lw, int lane, u32x2 tag TSARG) {
    int lt, pt; tile_of(gb, lt, pt);
    pt = da_of(pt);
    const unsigned lb = ldsa(smem), fb = lb + A_FLG;
    __builtin_amdgcn_s_setprio(3);
    static_assert(!CB || (TWH == 4 && !CE), "");
    if (CB == 1) c_chunk(Cre, Cim, lb, gb, pt, lw, lane, 2);
    if (CB & 1) c_chunk(Cre, Cim, lb, gb, pt, lw, lane, 3);
    wait_vm<CB == 1 ? 16 : CB == 3 ? 8 : 0>();
    t_done(mkrsrc(ws, WS_END), fb, gb, tag, lane);
    TS(44);
    sig(fb + F_FULL, CE > 1 && lw < 3 ? 0x1111u : CE ? 0x111u : 0x11u);
    auto hpoll = [&](unsigned m, unsigned end) INL {
        int spin;
#pragma unroll 1
        for (spin = 0; spin < (1 << 20) && (m & end) != end; ++spin) {
            m = h_poll<2>(mkrsrc(ws, WS_END), gb, pt & 1, lane, tag);
            sig_or(fb + F_FULL, m);
            if (spin == 0) TS(16);
        }
    };
    if (TWH != 4) await<2>(fb + F_HC, 4u);
    if (HPL == 1 && lw == HPW) { if (PDL) __builtin_amdgcn_s_sleep(PDL); TS(19); hpoll(0, CE > 1 ? 0x88888888u : 8u); }
    if (HPL == 1 && CW) await<1>(fb + F_FULL, 8u);
    if (!CE && !CB) c_chunk(Cre, Cim, lb, gb, pt, lw, lane, 2);
    if (CE < 2 || lw == 3) {
        if (CB == 4) {
            if (!late(gb)) sig(fb + F_FULL, 0x100u);
            await<1>(fb + F_FULL, 8u);
            if (late(gb)) { __builtin_amdgcn_s_sleep(C2D); c_chunk(Cre, Cim, lb, gb, pt, lw, lane, 2); }
            else if (C3D) __builtin_amdgcn_s_sleep(C3D);
        }
        if (!(CB & 3)) c_chunk<C3S>(Cre, Cim, lb, gb, pt, lw, lane, 3);
        if (late(gb)) { wait_vm<8>(); sig(fb + F_FULL, 0x100u); }
        wait_vm<0>();
        sig(fb + F_FULL, CE || CB == 4 ? 0x1000u : 0x1100u);
    }
    TS(47);
    if (lw == HPW && !(HPL == 1 && CE > 1)) hpoll(await<2>(fb + F_FULL, 8u) & 0x88888888u, 0x88888888u);
    const unsigned sa = fb + ((lw & 1) ? F_FB : F_FULL);
    unsigned sv = 0;
#pragma unroll 1
    for (int i = 4; i < 8; ++i) {
        const int j = (lw & 1) ? 11 - i : i;
        await_all<2>(fb + F_RD + 4 * (i - 4) + 16 * (lw & 1), 0x40404040u);
        if (!(XE & 1)) c_chunk<CSL>(Cre, Cim, lb, gb, pt, lw, lane, j, ((i + 2) & 3) * A_STAGE + (lw & 2) * 8192);
        wait_vm<8>();
        sig(sa, sv);
        sv = 2u << (4 * j);
        TS(44 + i);
    }
    wait_vm<0>();
    sig(sa, sv);
    if (HD && !(lw & 1)) {
        const u32x4 rh = mkdesc(ws, WS_END);
        unsigned vo = ((lane & 15) >> 2) * 256 + (lane >> 4) * 64 + (lane & 3) * 16; PIN(vo);
        await_all<2>(fb + F_RD + 4 * HDT, 0x40404040u);
        if (HD0) __builtin_amdgcn_s_sleep(HD0);
#pragma unroll
        for (int i = 0; i < 8; ++i) {
            const int k2 = 11 - (i >> 1), k1 = 1 - (lw >> 1);
            const unsigned so = OFF_H + lt * 524288 + (pt & 1) * 131072 + ((k2 >> 1) ^ CX(gb)) * 16384 + (k1 + (k2 & 1) * 32) * 8192 + (i & 1) * 4096;
            asm volatile("s_nop 0\n\tbuffer_load_dwordx4 %0, %1, %2 offen sc1 lds\n\tbuffer_load_dwordx4 %0, %1, %2 offen offset:1024 sc1 lds\n\t"
                         "buffer_load_dwordx4 %0, %1, %2 offen offset:2048 sc1 lds\n\tbuffer_load_dwordx4 %0, %1, %2 offen offset:3072 sc1 lds"
                         :: "v"(vo), "s"(rh), "s"(so), "{m0}"(lb + ((k2 + 2) & 3) * A_STAGE + 8192 + k1 * 16384 + (i & 1) * 4096) : "memory");
            if (HDS) __builtin_amdgcn_s_sleep(HDS);
            if (i == 5) { wait_vm<8>(); sig(fb + F_FB, 0x200u); TS(52); }
        }
        wait_vm<0>();
        sig(fb + F_FB, 0x2000u);
    }
    TSE(1, lw == 0);
}

DEV void role_last(char* ws, lptr smem, int gb, int w, int lane, u32x2 tag, int fe TSARG) {
    int s2t, dat; tile_of(gb, s2t, dat);
    dat = da_of(dat);
    const int c = lane & 15, q = lane >> 4, kh = dat & 1;
    const rsrc_t rs = mkrsrc(ws, WS_END);
    const unsigned fb = ldsa(smem + A_FLG);
    SCHED();
    u32x4 fc[2][2], fs[2][2];
    unsigned ce[2], se[2];
#pragma unroll
    for (int r = 0; r < 2; ++r) {
        float dt = (float)(32 * w + 16 * r + c) * (1.0f / 128.0f); PIN(dt);
        float cc[8], ss[8];
        const float t0 = __builtin_amdgcn_fractf(dt * (float)(4 * q)), t1 = __builtin_amdgcn_fractf(dt * (float)(16 + 4 * q));
#pragma unroll
        for (int e = 0; e < 8; ++e) { const float f = fmaf((float)(e & 3), dt, e < 4 ? t0 : t1); cc[e] = __builtin_amdgcn_cosf(f); ss[e] = __builtin_amdgcn_sinf(f); }
        fc[0][r] = (u32x4){pkh(cc[0], cc[1]), pkh(cc[2], cc[3]), pkh(cc[4], cc[5]), pkh(cc[6], cc[7])};
        fs[0][r] = (u32x4){pkh(ss[0], ss[1]), pkh(ss[2], ss[3]), pkh(ss[4], ss[5]), pkh(ss[6], ss[7])};
        PIN(fc[0][r]); PIN(fs[0][r]);
        if (r == TDR) { wait_vm<0>(); t_done(rs, fb, gb, tag, lane); TS(44); }
    }
#pragma unroll
    for (int r = 0; r < 2; ++r) {
        fc[1][r] = ((c & 1) ? fs[0][r] : fc[0][r]) ^ (((c + 1) & 2) ? SG : 0u);
        fs[1][r] = ((c & 1) ? fc[0][r] : fs[0][r]) ^ ((c & 2) ? SG : 0u);
        ce[r] = fc[0][r].x; se[r] = fs[0][r].x;
        if (q == 0) {
            ce[r] = (ce[r] & 0xffff0000u) | 0x3800u; se[r] = (se[r] & 0xffff0000u) | ((c & 1) ? 0x3800u : 0xb800u);
            fs[0][r].x = (se[r] & 0xffff0000u) | ((c & 1) ? 0xbc00u : 0x3c00u);
        }
        PIN(fc[1][r]); PIN(fs[1][r]); PIN(fs[0][r]); PIN(ce[r]); PIN(se[r]);
    }
    const int ls = lane ^ (2 * (kh & w));
    const bool sp = w == 1 && lane < 8;
    lptr tw = smem + A_XT + ((lane >> 2) & 1) * 8192 + w * 2048 + ((ls >> 1) & 1) * 1024 + (((lane >> 3) ^ (ls & 6)) << 5) + (lane & 1) * 16;
    lptr tsc = smem + A_MAT + (w >> 1) * 512 + (w & 1) * 32 + (lane >> 3) * 4, tr[4];
#pragma unroll
    for (int i = 0; i < 4; ++i) {
        tr[i] = smem + A_XT + (i >> 1) * 8192 + (i & 1) * 1024 + ((lane * 8) ^ (i * 64));
        PIN(tr[i]);
    }
    lptr aw_ = smem + A_OG + (c >> 2) * 64 + 256 * w + (c & 3) * 4, rw[4];
#pragma unroll
    for (int i = 0; i < 4; ++i) { rw[i] = smem + A_XT + w * 4096 + (c & 2) * 512 + (c >> 2) * 256 + (((4 * i + q) ^ (c & 14)) << 4) + (c & 1) * 8; PIN(rw[i]); }
    {
        const unsigned v = q == (c >> 2) ? 0x3f80u << (16 * (c & 1)) : 0u, x = (c & 2) ? 0u : v, y = v ^ x, o = q * 64 * D_ * 4 + (c >> 3) * 2048 + (c & 7) * 16;
        lst<u32x4>(smem + A_CN + w * 1024 + lane * 16, w == 0 ? (u32x4){x, y, 0, 0} : w == 1 ? (u32x4){0, 0, x, y} : (u32x4){o, o + 16 * D_ * 4, o + 32 * D_ * 4, o + 48 * D_ * 4});
    }
    unsigned o = lane * 16, o4 = (sp ? lane ^ (ls & 2) : lane) * 16, ft = fb + F_TT;
    unsigned n0 = kh && (((w & 1) + 1 - ((lane >> 1) & 1)) & 2) ? SG : 0u, n1 = kh ? n0 ^ SG : 0u, n4 = kh && (((w & 1) + 3 - ((o4 >> 5) & 1)) & 2) ? SG : 0u;
    unsigned m0 = sp && (ls & 2) ? 0u : ~0u, m4 = sp && !(ls & 2) ? 0u : ~0u;
    PIN(tw); PIN(tsc); PIN(aw_); PIN(o); PIN(o4); PIN(ft); PIN(n0); PIN(n1); PIN(n4); PIN(m0); PIN(m4);
    TS(45);
    __builtin_amdgcn_s_setprio(3);
    if (w == 0) {
        u32x4 fl[2];
        if (TP0 && !(XTP && gb == XG)) __builtin_amdgcn_s_sleep(TP0);
        TS(50);
#pragma unroll 1
        for (int spin = 0; spin < (1 << 20); ++spin) { flag_get(rs, OFF_FT, gb, lane, fl); if (flag_ok(fl, tag)) break; if (spin == 0) TS(49); if (TSL) __builtin_amdgcn_s_sleep(TSL); }
        const unsigned em = wave_pkmax(pkmax(pkmax(fl[0].x, fl[0].z), pkmax(fl[1].x, fl[1].z))) & 0xffu;
#pragma unroll
        for (int i = 0; i < 2; ++i) {
            const float a = ldexpf(1.f, (int)(fl[i].x & 0xffu) - (int)em - 1), b = ldexpf(1.f, (int)(fl[i].z & 0xffu) - (int)em - 1);
            lst<u32x2>(smem + A_MAT + i * 512 + lane * 8, (u32x2){pkh(a, a), pkh(b, b)});
        }
        sig(fb + F_TR, em | 0x8000u);
        sig(fb + F_FB, 4u);
    }
    fe += 1 + (await<1>(fb + F_TR, 0x8000u) & 0xffu);
    TS(46);
    if (XE & 4) { sig(fb + F_FB, 0x10u); TSE(w ? 4 : 5, w == 0 || w == 2); asm volatile("s_endpgm"); }
    u32x4 tv[8];
#pragma unroll
    for (int i = 0; i < 8; ++i) {
        tv[i] = __builtin_amdgcn_raw_buffer_load_b128(rs, (i == 1 ? o4 : o) + (i >> 1) * 1024, OFF_T + (s2t * 8 + (dat >> 1)) * 32768 + ((w & 1) + (i & 1) * 2) * 8192 + (w >> 1) * 4096, SC1);
        if (XE & 64) { tv[i] = fc[0][0]; PIN(tv[i]); }
        if (TLS) __builtin_amdgcn_s_sleep(TLS);
        SCHED();
    }
    if (XE & 64) __builtin_amdgcn_s_sleep(32);
    if (XE & 128) { for (int i = 0; i < 8; ++i) PIN(tv[i]); sig(fb + F_FB, 0x10u); TSE(w ? 4 : 5, w == 0 || w == 2); asm volatile("s_endpgm"); }
    u32x2 gg[2];
#pragma unroll
    for (int r = 0; r < 2; ++r) gg[r] = __builtin_amdgcn_raw_buffer_load_b64(rs, c * 8 + r * 128, OFF_G + s2t * 1024 + w * 256, SC1);
    __builtin_amdgcn_s_setprio(PRIOR);
    unsigned ts[8];
#pragma unroll
    for (int i = 0; i < 8; ++i) {
        ts[i] = lld<unsigned>(tsc + (i >> 1) * 128 + (i & 1) * 64) ^ (i == 1 ? n4 : (i & 1) ? n1 : n0);
        if (i < 2) ts[i] &= i ? m4 : m0;
        PIN(ts[i]);
    }
    auto h8 = [&](u32x4 v) INL { return __builtin_bit_cast(f16x8, v); };
#pragma unroll
    for (int i = 0; i < 8; i += 2) {
        const f16x8 t = h8(tv[i]) * h8((u32x4){ts[i], ts[i], ts[i], ts[i]});
        lst<f16x8>(tw + i * 128, __builtin_elementwise_fma(h8(tv[i + 1]), h8((u32x4){ts[i + 1], ts[i + 1], ts[i + 1], ts[i + 1]}), t));
        SCHED();
    }
    sig(ft, 1u);
    TS(47);
    float g[2];
#pragma unroll
    for (int r = 0; r < 2; ++r) { g[r] = ldexpf(__uint_as_float(gg[r].x), fe); PIN(g[r]); lst<float>(aw_ + 16 * r, 2.0f * __uint_as_float(gg[r].y)); }
    __builtin_amdgcn_s_setprio(PRT);
    await<AWS>(ft, 4u);
    TS(51);
    if (XE & 256) { sig(fb + F_FB, 0x10u); TSE(w ? 4 : 5, w == 0 || w == 2); asm volatile("s_endpgm"); }
    u32x4 tf[16];
#pragma unroll
    for (int i = 0; i < 16; ++i) { tf[i] = trfrag(tr[(i & 1) * 2 + ((i >> 1) & 1)] + ((i >> 2) & 1) * 4096 + (1 - (i >> 3)) * 2048, 512); SCHED(); }
    sig(ft, 0x10u);
    f32x4 P[2][2], O[2][2];
#pragma unroll
    for (int i = 0; i < 4; ++i) O[i >> 1][i & 1] = Z4;
    if (XE & 512) { for (int i = 0; i < 16; ++i) PIN(tf[i]); sig(fb + F_FB, 0x10u); TSE(w ? 4 : 5, w == 0 || w == 2); asm volatile("s_endpgm"); }
    if (MWT) wait_lds(MWT - 1);
    __builtin_amdgcn_s_setprio(PRX);
    SCHED();
#pragma unroll
    for (int i = 0; i < 8; ++i) {
#pragma unroll
        for (int r = 0; r < 2; ++r) O[r][i & 1] = mfh(tf[i], (i & 2) ? fs[i >> 2][r] : fc[i >> 2][r], O[r][i & 1]);
        SCHED();
        if (i == 5) {
#pragma unroll
            for (int r = 0; r < 2; ++r) { fc[0][r].x = ce[r]; fs[0][r].x = se[r]; PIN(fc[0][r]); PIN(fs[0][r]); }
            SCHED();
        }
    }
#pragma unroll
    for (int i = 0; i < 8; ++i) {
#pragma unroll
        for (int r = 0; r < 2; ++r) P[r][i & 1] = mfh(tf[8 + i], (i & 2) ? fs[i >> 2][r] : fc[i >> 2][r], i < 2 ? O[r][i] : P[r][i & 1]);
        SCHED();
    }
    __builtin_amdgcn_s_setprio(PRV);
    TS(53);
    if (XE & 1024) { for (int i = 0; i < 4; ++i) { PIN(P[i >> 1][i & 1]); PIN(O[i >> 1][i & 1]); } sig(fb + F_FB, 0x10u); TSE(w ? 4 : 5, w == 0 || w == 2); asm volatile("s_endpgm"); }
    await<0>(ft, 0x40u);
#pragma unroll
    for (int i = 0; i < 8; ++i) {
        const int r = i >> 2;
        float av[4];
#pragma unroll
        for (int e = 0; e < 4; ++e) av[e] = g[r] * __uint_as_float(pkb(0.f, (i & 2) ? fmaf(O[r][i & 1][e], -2.0f, P[r][i & 1][e]) : P[r][i & 1][e]));
        lst<u32x2>(rw[i & 3] + r * 2048, (u32x2){pkb(av[0], av[1]), pkb(av[2], av[3])});
    }
    sig(fb + F_FB, 0x10u);
    TS(48);
    asm volatile(".subsection 2\ngm_end:\n\t.fill 64, 4, 0xbf800000\n\t.subsection 0");
    TSE(w ? 4 : 5, w == 0 || w == 2);
}

NOPK __global__ __launch_bounds__(768, 3) void gm_kernel(const u16* __restrict__ x, const u16* __restrict__ Bre, const u16* __restrict__ Bim,
                                                         const u16* __restrict__ gate_w, char* ws, const u16* __restrict__ lam_re,
                                                         const u16* __restrict__ lam_im, const u16* __restrict__ log_dt,
                                                         const u16* __restrict__ w_r, const u16* __restrict__ w_i, const u16* __restrict__ Cre,
                                                         const u16* __restrict__ Cim, const u16* __restrict__ Dskip, float* __restrict__ out) {
    extern __shared__ __attribute__((aligned(16))) char smem_[];
    lptr smem = (lptr)smem_;
    unsigned t00 = 0;
    const int wid = __builtin_amdgcn_readfirstlane(threadIdx.x >> 6), lane = threadIdx.x & 63;
    u32x4 cd; unsigned cpf = 0, ct;
    asm volatile("s_getpc_b64 s[40:41]\ngm_beg:\n\ts_mov_b32 s42, gm_end - gm_beg\n\ts_mov_b32 s43, 0x20000\n\ts_and_b32 s41, s41, 0xffff\n\t"
                 "s_cmp_lg_u32 %3, %6\n\ts_cbranch_scc1 1f\n\t"
                 "s_bfe_u32 %2, %4, 0x30003\n\ts_mov_b32 m0, %7\n\ts_lshl_b32 %2, %2, 13\n\tv_lshlrev_b32 %1, 7, %5\n\tbuffer_load_dword %1, s[40:43], %2 offen lds\n1:"
                 : "=&{s[40:43]}"(cd), "=&v"(cpf), "=&s"(ct) : "s"(wid), "s"(blockIdx.x), "v"(lane), "i"(ENT ? 3 : 99), "i"(A_AGG) : "scc", "m0", "memory");
    if (ENT && (wid & 12) == 4) first_loads(x, Bre, Bim, gate_w, smem, blockIdx.x, wid - 4, lane);
    int w = wid & 7, gb = blockIdx.x;
    asm volatile("" : "+s"(w), "+s"(gb));
    if (wid < 4) role_mma(x, Dskip, ws, out, smem, blockIdx.x, wid, lane, cd, t00);
    else {
        TSD();
        u32x2 tag;
        Tab tab;
        if (wid < 8) role_load(x, Bre, Bim, Cre, Cim, gate_w, ws, smem, blockIdx.x, wid - 4, lane, tab TSPASS);
        else role_aux(w_r, w_i, lam_re, lam_im, log_dt, ws, smem, blockIdx.x, wid - 8, lane, tab TSPASS);
        const int fe = role_mid(Cre, Cim, ws, smem, gb, w, lane, tag, tab TSPASS);
        if (w >= 4) role_cload(Cre, Cim, ws, smem, gb, w - 4, lane, tag TSPASS);
        else role_last(ws, smem, gb, w, lane, tag, fe TSPASS);
    }
}

extern "C" void kernel_launch(void* const* d_in, const int* in_sizes, int n_in, void* d_out, int out_size, void* d_ws, size_t ws_size,
                              hipStream_t stream) {
    (void)hipFuncSetAttribute((const void*)gm_kernel, hipFuncAttributeMaxDynamicSharedMemorySize, SMEM);
    gm_kernel<<<256, 768, SMEM, stream>>>((const u16*)d_in[0], (const u16*)d_in[6], (const u16*)d_in[7], (const u16*)d_in[12], (char*)d_ws,
                                          (const u16*)d_in[4], (const u16*)d_in[5], (const u16*)d_in[10], (const u16*)d_in[1], (const u16*)d_in[2],
                                          (const u16*)d_in[8], (const u16*)d_in[9], (const u16*)d_in[11], (float*)d_out);
}
```

```cpp
#include <hip/hip_runtime.h>
#include <stdint.h>

typedef __attribute__((ext_vector_type(8))) __bf16 bf16x8;
typedef __attribute__((ext_vector_type(2))) __bf16 bf16x2;
typedef __attribute__((ext_vector_type(8))) _Float16 f16x8;
typedef __attribute__((ext_vector_type(2))) _Float16 f16x2;
typedef __attribute__((ext_vector_type(4))) float f32x4;
typedef __attribute__((ext_vector_type(2))) float f32x2;
typedef __attribute__((ext_vector_type(4))) unsigned u32x4;
typedef __attribute__((ext_vector_type(3))) unsigned u32x3;
typedef __attribute__((ext_vector_type(2))) unsigned u32x2;
typedef __attribute__((ext_vector_type(4))) short s16x4;
typedef __attribute__((ext_vector_type(2))) unsigned short u16x2;
typedef unsigned short u16;
typedef unsigned long long u64;
#define GAS __attribute__((address_space(1)))
#define LAS __attribute__((address_space(3)))
#define NOPK __attribute__((target("no-packed-fp32-ops")))
#define DEV __device__ __forceinline__

constexpr int S_ = 2048, D_ = 1024, KF = 513;

constexpr unsigned OFF_H = 0;
constexpr unsigned OFF_U = OFF_H + (8u << 20);
constexpr unsigned OFF_T = OFF_U + (4u << 20);
constexpr unsigned OFF_G = OFF_T + (4u << 20);
constexpr unsigned OFF_FU = OFF_G + 16384u;
constexpr unsigned OFF_FT = OFF_FU + 16384u;
constexpr unsigned OFF_FH = OFF_FT + 16384u;
constexpr unsigned OFF_GRAN = OFF_FH + 16384u;
constexpr unsigned OFF_E = OFF_GRAN + 16u * 512u * 16u;
constexpr unsigned WS_END = OFF_E + 128u;

#define TL(i)
#define TSD()
#define TS(i)
#define TS0()
#define TC(i)
#define TSE(slot, on)

DEV unsigned pkb(float a, float b) { f32x2 v = {a, b}; return __builtin_bit_cast(unsigned, __builtin_convertvector(v, bf16x2)); }
DEV unsigned pkh(float a, float b) { f32x2 v = {a, b}; return __builtin_bit_cast(unsigned, __builtin_convertvector(v, f16x2)); }
DEV float lo2f(unsigned p) { return __uint_as_float(p << 16); }
DEV float hi2f(unsigned p) { return __uint_as_float(p & 0xffff0000u); }
DEV float bfr(float f) { return lo2f(pkb(f, f)); }
DEV float2 cmul(float2 a, float2 b) { return make_float2(a.x * b.x - a.y * b.y, a.x * b.y + a.y * b.x); }
DEV float2 cfma(float2 a, float2 b, float2 c) { return make_float2(fmaf(a.x, b.x, fmaf(-a.y, b.y, c.x)), fmaf(a.x, b.y, fmaf(a.y, b.x, c.y))); }
DEV f32x4 mfb(u32x4 a, u32x4 b, f32x4 c) { return __builtin_amdgcn_mfma_f32_16x16x32_bf16(__builtin_bit_cast(bf16x8, a), __builtin_bit_cast(bf16x8, b), c, 0, 0, 0); }
DEV f32x4 mfh(u32x4 a, u32x4 b, f32x4 c) { return __builtin_amdgcn_mfma_f32_16x16x32_f16(__builtin_bit_cast(f16x8, a), __builtin_bit_cast(f16x8, b), c, 0, 0, 0); }
DEV unsigned pkmax(unsigned a, unsigned b) { return __builtin_bit_cast(unsigned, __builtin_elementwise_max(__builtin_bit_cast(u16x2, a), __builtin_bit_cast(u16x2, b))); }
typedef LAS char* lptr;
DEV unsigned ldsa(lptr p) { return (unsigned)(uintptr_t)p; }
template <class T> DEV T lld(lptr p) { return *(LAS const T*)p; }
template <class T> DEV void lst(lptr p, T v) { *(LAS T*)p = v; }
DEV u32x2 trread(lptr p) { return __builtin_bit_cast(u32x2, __builtin_amdgcn_ds_read_tr16_b64_v4i16((LAS s16x4*)p)); }
DEV u32x4 trfrag(lptr p, int second) { const u32x2 a = trread(p), b = trread(p + second); return (u32x4){a.x, a.y, b.x, b.y}; }
typedef __amdgpu_buffer_rsrc_t rsrc_t;
DEV u32x4 ldfrag(lptr tile, int row, int chunk) { return lld<u32x4>(tile + row * 128 + ((chunk ^ (row & 7)) << 4)); }
template <int N> DEV void wait_vm() { asm volatile("s_waitcnt vmcnt(%0)" :: "i"(N) : "memory"); }
#define LDS_WAIT() asm volatile("s_waitcnt lgkmcnt(0)" ::: "memory")
#define SCHED() __builtin_amdgcn_sched_barrier(0)
#define BAR() do { __builtin_amdgcn_s_barrier(); asm volatile("" ::: "memory"); } while (0)
#define PIN(x) asm volatile("" : "+v"(x))
template <int I> struct IC { static constexpr int v = I; };
template <int I, int N, class F> DEV void sfor(F&& f) { if constexpr (I < N) { f(IC<I>{}); sfor<I + 1, N>(f); } }
#define INL __attribute__((always_inline))
DEV rsrc_t mkrsrc(const void* p, unsigned n) { return __builtin_amdgcn_make_buffer_rsrc((void*)p, 0, n, 0x00020000); }
constexpr int SC1 = 16;
#define WTO 16
template <int AUX> DEV void st128(u32x4 v, rsrc_t rs, unsigned off) { __builtin_amdgcn_raw_buffer_store_b128(v, rs, off, 0, AUX); }
DEV void lds_add(unsigned a) { asm volatile("ds_add_u32 %0, %1" :: "v"(a), "v"(1u) : "memory"); }
DEV unsigned lds_peek(unsigned a) {
    unsigned v;
    asm volatile("ds_read_b32 %0, %1\n\ts_waitcnt lgkmcnt(0)" : "=v"(v) : "v"(a) : "memory");
    return __builtin_amdgcn_readfirstlane(v);
}
DEV void tile_of(int gb, int& ta, int& tb) {
    const int xcd = gb & 7, loc = gb >> 3;
    ta = (xcd >> 1) * 4 + (loc >> 3);
    tb = (xcd & 1) * 8 + (loc & 7);
}
#define DMAP 1
DEV int da_of(int pt) { return DMAP ? ((pt & 7) << 1) | (pt >> 3) : pt; }
DEV void rows4(float a, float& a0, float& a1, float& a2, float& a3) {
    const unsigned u = __float_as_uint(a);
    const auto r = __builtin_amdgcn_permlane32_swap(u, u, false, false);
    const auto p = __builtin_amdgcn_permlane16_swap(r[0], r[0], false, false);
    const auto s = __builtin_amdgcn_permlane16_swap(r[1], r[1], false, false);
    a0 = __uint_as_float(p[0]); a1 = __uint_as_float(p[1]); a2 = __uint_as_float(s[0]); a3 = __uint_as_float(s[1]);
}
DEV void rowsum4(float& a, float& b, float& c, float& d) {
    const auto r = __builtin_amdgcn_permlane32_swap(__float_as_uint(a), __float_as_uint(b), false, false);
    const auto t = __builtin_amdgcn_permlane32_swap(__float_as_uint(c), __float_as_uint(d), false, false);
    const auto p = __builtin_amdgcn_permlane16_swap(__float_as_uint(__uint_as_float(r[0]) + __uint_as_float(r[1])), __float_as_uint(__uint_as_float(t[0]) + __uint_as_float(t[1])), false, false);
    rows4(__uint_as_float(p[0]) + __uint_as_float(p[1]), a, c, b, d);
}
DEV float2 sel4(int q, float2 a, float2 b, float2 c, float2 d) {
    PIN(d.x); PIN(d.y);
    const float2 e = (q & 1) ? b : a, f = (q & 1) ? d : c;
    return (q & 2) ? f : e;
}
template <int R> DEV unsigned ror16(unsigned v) { return __builtin_amdgcn_update_dpp(0u, v, 0x120 + R, 0xf, 0xf, false); }
DEV unsigned wave_pkmax(unsigned m) {
    m = pkmax(m, ror16<8>(m)); m = pkmax(m, ror16<4>(m)); m = pkmax(m, ror16<2>(m)); m = pkmax(m, ror16<1>(m));
    const auto r = __builtin_amdgcn_permlane32_swap(m, m, false, false);
    m = pkmax(r[0], r[1]);
    const auto p = __builtin_amdgcn_permlane16_swap(m, m, false, false);
    return pkmax(p[0], p[1]);
}
DEV float half_sum(float v) {
    v += __uint_as_float(ror16<8>(__float_as_uint(v))); v += __uint_as_float(ror16<4>(__float_as_uint(v)));
    v += __uint_as_float(ror16<2>(__float_as_uint(v))); v += __uint_as_float(ror16<1>(__float_as_uint(v)));
    const auto p = __builtin_amdgcn_permlane16_swap(__float_as_uint(v), __float_as_uint(v), false, false);
    return __uint_as_float(p[0]) + __uint_as_float(p[1]);
}
DEV u32x4 quads(lptr buf, int lane, u32x2 p, u32x2 n) {
    lptr wp = buf + (lane & 15) * 64 + ((lane >> 4) & 1) * 8;
    const int v = (lane >> 5) ^ ((lane >> 2) & 3);
    lst<u32x2>(wp + (v << 4), p); lst<u32x2>(wp + ((v ^ 2) << 4), n);
    asm volatile("" ::: "memory");
    return lld<u32x4>(buf + ((lane ^ (lane >> 4)) << 4));
}

#define PRIOA 2
#define NREP 8
#define PRIOM 1
#define PRIOT 2
#define PRIOG 2
#define AD 8
#define NB 12
#define AD2 4
#define AG2 168
#define AD3 8
#define AG3 200
#define AD4 6
#define AG4 244
#define ENT 1
#define STG 1
#define FTS 1
#define FLN 2
#define FSL 0
#define PKR 1
#define GS0 474
#define US0 10
#define WGL 2
#define RS0 (HD ? 50 : 20)
#define RSP (HD ? 4 : 6)
#define WS0 0
#define WS1 4
#define RSD 88
#define CPD 4
#define CB 4
#define AWS 0
#define HD 0
#define HDT 5
#define HDS 1
#define HD0 0
#define C3D 10
#define C3S 1
#define POW 0
#define AW0 0
#define C0E 1
#define HST 1
#define PRT 0
#define LX 0
#define LXG 1
#define PDX 12
#define C2D 6
#define CTS 7
#define CTD 0
#define MWT 0
#define TDR 0
#define SA0 24
#define AM 1
#define CP 0
#define HP 0
#define XE 0
#define TSL 8
#define CSL 1
#define WG2 2
#define WG1 1
#define SPC 1
#define PRV 0
#define PRX 3
#define TLS 4
#define RDL 0
#define WROW {0, 0, 0, 0, 8}
#define ROT (lt & 12)
#define PRIOL 3
#define XM 0
#define UFS 6
#define PRIOR 0
#define PRS 2
#define WXS 13
#define CPF 1
#define FST 8
#define HREP 8
#define NP1 1
#define PDL 16
#define PDE 0
#define PDM 0x80
#define NB2 NB
#define XP 4
#define PU 0
#define GDL 16
#define BAL -1
#define BA2 0
#define FTC 1
#define LBS 0
#define PT 0
#define PTS 4
#define FTB 768
#define XTP 1
#define TWH 4
#define ACI 0
#define CE 0
#define CES 0
#define CEL 1
#define HPL 0
#define HPW 0
#define CW 0
#define GSP 1
#define PV0 "144"
#define PV0E "147"
#define PV1 "148"
#define PV1E "151"
#define PV2 "152"
#define PV2E "155"
#define PV3 "156"
#define PV3E "159"
#define UDL 0
#define CGW 0
#define C0S 0
#define GSL 0
#define C0L 0
#define C0P 0
#define BSL 0
#define BS2 0
#define PZ1 0
#define PZ2 0
#define PZ3 0
#define PZA 0
#define PZB 48
#define XLP 0
#define PRIOI 0
#define RST (NST - 5)
#define SIL 1
#define CPM 7
#define PRIOE 2
#define MSL 1
#define URS 6
#define UPRE 1
#define UCK 13
#define PRIOD 1
#define PRIOW 1
#define CDL 0
#define ATS 14
#define UPL 10
#define C1E 1
#define TP0 40
#define PD0 0
#define CXM 6
#define CX(g) ((g) & CXM)
#define GSTEP {0, 0, 0, 0}
#define XS 1
#define B1S 0
#define US 1
#define UPS 1
#define DS 8
#define P0S 8
#define VL 1
#define PN -1
#define PG -1
#define SNK 1
#define HFIRST 8u
#define RDPAT {0, 1, 2, -1, 3, 4, -1, 5, 6, -1, 7, -1, -1, -1, -1, -1}
#define Z4 ((f32x4){0.f, 0.f, 0.f, 0.f})
constexpr unsigned SG = 0x80008000u;
struct CM { u32x4 re, im; };
DEV u32x4 cmat1(float t0, float dt) {
    float cc[4], ss[4];
    t0 = __builtin_amdgcn_fractf(t0);
#pragma unroll
    for (int e = 0; e < 4; ++e) { const float f = fmaf((float)e, dt, t0); cc[e] = __builtin_amdgcn_cosf(f); ss[e] = __builtin_amdgcn_sinf(f); }
    return (u32x4){pkh(ss[0], ss[1]), pkh(ss[2], ss[3]), pkh(cc[0], cc[1]), pkh(cc[2], cc[3])};
}
DEV CM cexp(u32x4 v) { CM m; m.re = (u32x4){v.z, v.w, v.x ^ SG, v.y ^ SG}; m.im = v; PIN(m.re); PIN(m.im); return m; }
DEV u32x4 pk8(f32x4 r, f32x4 i) { return (u32x4){pkh(r[0], r[1]), pkh(r[2], r[3]), pkh(i[0], i[1]), pkh(i[2], i[3])}; }
DEV u32x4 wmul(f32x4 r, f32x4 i, u32x4 w) {
    float yr[4], yi[4];
#pragma unroll
    for (int e = 0; e < 4; ++e) {
        const unsigned wu = w[e];
        const f16x2 h = __builtin_bit_cast(f16x2, wu);
        yr[e] = fmaf(r[e], (float)h[0], -i[e] * (float)h[1]); yi[e] = fmaf(r[e], (float)h[1], i[e] * (float)h[0]);
    }
    return (u32x4){pkh(yr[0], yr[1]), pkh(yr[2], yr[3]), pkh(yi[0], yi[1]), pkh(yi[2], yi[3])};
}
DEV u32x4 wcvt(u32x4 w, float sr, float si) {
#pragma unroll
    for (int e = 0; e < 4; ++e) w[e] = pkh(lo2f(w[e]) * sr, hi2f(w[e]) * si);
    PIN(w);
    return w;
}
DEV u32x4 mkdesc(const void* p, unsigned n) { const u64 a = (u64)p; return (u32x4){(unsigned)a, (unsigned)(a >> 32) & 0xffffu, n, 0x00020000u}; }
DEV void dma16(u32x4 d, unsigned l, unsigned vo, unsigned so) {
    asm volatile("s_nop 0\n\tbuffer_load_dwordx4 %0, %1, %2 offen lds" :: "v"(vo), "s"(d), "s"(so), "{m0}"(l) : "memory");
}
DEV void dma64(u32x4 d, unsigned l, u32x4 vo, unsigned so) {
    asm volatile("s_nop 0\n\tbuffer_load_dwordx4 %0, %4, %5 offen lds\n\tbuffer_load_dwordx4 %1, %4, %5 offen offset:1024 lds\n\t"
                 "buffer_load_dwordx4 %2, %4, %5 offen offset:2048 lds\n\tbuffer_load_dwordx4 %3, %4, %5 offen offset:3072 lds"
                 :: "v"(vo.x), "v"(vo.y), "v"(vo.z), "v"(vo.w), "s"(d), "s"(so), "{m0}"(l) : "memory");
}
#define SPIN(x) asm volatile("" : "+s"(x))
DEV void dma16s(u32x4 d, unsigned l, unsigned vo, unsigned so) {
    asm volatile("s_nop 0\n\tbuffer_load_dwordx4 %0, %1, %2 offen sc1 lds" :: "v"(vo), "s"(d), "s"(so), "{m0}"(l) : "memory");
}
DEV void sig(unsigned a, unsigned v) { asm volatile("s_mov_b64 exec, 1\n\tds_add_u32 %0, %1\n\ts_mov_b64 exec, -1" :: "v"(a), "v"(v) : "memory"); }
template <int O> DEV void sig_o(unsigned a, unsigned v) { asm volatile("s_mov_b64 exec, 1\n\tds_add_u32 %0, %1 offset:%2\n\ts_mov_b64 exec, -1" :: "v"(a), "v"(v), "i"(O) : "memory"); }
DEV unsigned sig_rtn(unsigned a) {
    unsigned v;
    asm volatile("s_mov_b64 exec, 1\n\tds_add_rtn_u32 %0, %1, %2\n\ts_mov_b64 exec, -1\n\ts_waitcnt lgkmcnt(0)" : "=&v"(v) : "v"(a), "v"(1u) : "memory");
    return __builtin_amdgcn_readfirstlane(v);
}
template <int SLP> DEV unsigned await(unsigned a, unsigned mask) {
    unsigned v, n, r, t;
    asm volatile("s_mov_b32 %1, 0x400000\n"
                 "1:\tds_read_b32 %0, %4\n\ts_waitcnt lgkmcnt(0)\n\tv_readfirstlane_b32 %2, %0\n\ts_and_b32 %3, %2, %5\n\ts_cbranch_scc1 2f\n\t"
                 "s_sleep %6\n\ts_sub_u32 %1, %1, 1\n\ts_cbranch_scc0 1b\n"
                 "2:" : "=&v"(v), "=&s"(n), "=&s"(r), "=&s"(t) : "v"(a), "s"(mask), "i"(SLP) : "memory", "scc");
    return r;
}
#define BMF "v_mfma_f32_16x16x32_bf16 %0, %2, %3, %0\n\tv_mfma_f32_16x16x32_bf16 %1, %3, %2, %1\n\t"
template <int SLP> DEV void burn(int n, u32x4& a, u32x4& b) {
    f32x4 d0, d1;
    asm volatile("1:\t" BMF BMF "s_nop %5\n\ts_sub_u32 %4, %4, 1\n\ts_cbranch_scc0 1b\n\ts_nop 7\n\ts_nop 3"
                 : "=&v"(d0), "=&v"(d1), "+v"(a), "+v"(b), "+s"(n) : "i"(SLP) : "scc");
}
template <int SLP> DEV unsigned await_burn(unsigned ad, unsigned mask, u32x4 a, u32x4 b) {
    unsigned v, n, r, t; f32x4 d0, d1;
    asm volatile("s_mov_b32 %5, 0x400000\n"
                 "1:\tds_read_b32 %4, %8\n\t" BMF BMF "s_waitcnt lgkmcnt(0)\n\tv_readfirstlane_b32 %6, %4\n\ts_and_b32 %7, %6, %9\n\ts_cbranch_scc1 2f\n\t"
                 ".rept %10\n\t" BMF ".endr\n\ts_sub_u32 %5, %5, 1\n\ts_cbranch_scc0 1b\n"
                 "2:\ts_nop 7\n\ts_nop 3" : "=&v"(d0), "=&v"(d1), "+v"(a), "+v"(b), "=&v"(v), "=&s"(n), "=&s"(r), "=&s"(t) : "v"(ad), "s"(mask), "i"(SLP) : "memory", "scc");
    return r;
}
DEV void need_bit(unsigned a, unsigned& r, unsigned mask, int off = 0) {
    unsigned v, n, t;
    asm volatile("s_andn2_b32 %3, %5, %2\n\ts_cbranch_scc1 1f\n2:\n\t.subsection 1\n"
                 "1:\ts_mov_b32 %1, 0x400000\n"
                 "3:\tds_read_b32 %0, %4 offset:%6\n\ts_waitcnt lgkmcnt(0)\n\tv_readfirstlane_b32 %2, %0\n\ts_andn2_b32 %3, %5, %2\n\ts_cbranch_scc0 2b\n\t"
                 "s_sub_u32 %1, %1, 1\n\ts_cbranch_scc0 3b\n\ts_branch 2b\n\t.subsection 0"
                 : "=&v"(v), "=&s"(n), "+s"(r), "=&s"(t) : "v"(a), "i"(mask), "i"(off) : "memory", "scc");
}
template <int SLP> DEV void await_all(unsigned a, unsigned mask) {
    unsigned v, n, r;
    asm volatile("s_mov_b32 %1, 0x400000\n"
                 "1:\tds_read_b32 %0, %3\n\ts_waitcnt lgkmcnt(0)\n\tv_readfirstlane_b32 %2, %0\n\ts_andn2_b32 %2, %4, %2\n\ts_cbranch_scc0 2f\n\t"
                 "s_sleep %5\n\ts_sub_u32 %1, %1, 1\n\ts_cbranch_scc0 1b\n"
                 "2:" : "=&v"(v), "=&s"(n), "=&s"(r) : "v"(a), "s"(mask), "i"(SLP) : "memory", "scc");
}
#define MF2(a, b, c) asm("v_mfma_f32_16x16x32_bf16 %0, %1, %2, %0" : "+v"(c) : "v"(a), "v"(b))
DEV void flag_put(rsrc_t rs, unsigned off, int gb, u32x2 v, int lane) {
    if (lane < NREP) __builtin_amdgcn_raw_buffer_store_b64(v, rs, lane * 2048, off + gb * 8, SC1);
}
DEV void flag_get(rsrc_t rs, unsigned off, int gb, int lane, u32x4 (&fl)[2]) {
    unsigned o = lane * 16; PIN(o);
#pragma unroll
    for (int i = 0; i < 2; ++i) fl[i] = __builtin_amdgcn_raw_buffer_load_b128(rs, o + i * 1024, off + (gb & (NREP - 1)) * 2048, SC1);
}
DEV bool flag_ok(const u32x4 (&fl)[2], u32x2 tag) {
    bool ok = true;
#pragma unroll
    for (int i = 0; i < 2; ++i) ok = ok & ((fl[i].x ^ tag.x) < 0x10000u) & (fl[i].y == tag.y) & ((fl[i].z ^ tag.x) < 0x10000u) & (fl[i].w == tag.y);
    return __all(ok);
}
template <int N> DEV unsigned h_poll(rsrc_t rs, int gb, int kh, int lane, u32x2 tag) {
    unsigned o = (lane ^ ((CX(gb) & 3) << 4)) * FST, m = 0; PIN(o);
    u32x2 fl[N];
#pragma unroll
    for (int i = 0; i < N; ++i) fl[i] = __builtin_amdgcn_raw_buffer_load_b64(rs, o, OFF_FH + ((gb & (HREP - 1)) * 256 + kh * 128 + (i ^ (CX(gb) >> 2)) * 64) * FST, SC1);
#pragma unroll
    for (int i = 0; i < N; ++i) {
        const u64 b = __ballot(((fl[i].x ^ tag.x) < 0x10000u) & (fl[i].y == tag.y));
#pragma unroll
        for (int j = 0; j < 4; ++j) m |= ((unsigned)(b >> (16 * j)) & 0xffffu) == 0xffffu ? 8u << (16 * i + 4 * j) : 0u;
    }
    return m;
}
DEV void sig_or(unsigned a, unsigned v) { asm volatile("s_mov_b64 exec, 1\n\tds_or_b32 %0, %1\n\ts_mov_b64 exec, -1" :: "v"(a), "v"(v) : "memory"); }

constexpr int A_STAGE = 32768, A_XT = 4 * A_STAGE, A_PRM = A_XT + 16384, A_AGG = A_PRM + 2048, A_FLG = A_AGG + 512, A_GW = A_FLG + 128,
              A_MAT = A_GW + 2048, SMEM = A_MAT + 11264;
constexpr int E1P = 1040, A_EX = A_GW, A_E1 = 0, A_PW = 18 * E1P + 224, A_W = A_STAGE, A_HS = 2 * A_STAGE - 8192, A_OG = A_MAT + 9216, A_CN = A_MAT + 1024;
static_assert(A_PW % 256 == 0 && A_W + 18432 + 512 <= A_HS, "");
constexpr int F_MID = 8, F_EM = 12, F_MAX = 16, F_TAG = 32, F_FULL = 40, F_FREE = 44, F_HC = 48, F_FB = 52, F_TC = 56, F_TR = 60, F_TT = 64, F_ST = 76, F_WM = 80, F_RD = 96;
static_assert((A_FLG + F_RD & 0xff) == 0x60, "");

#define XGV 0xFF
constexpr int XG = XGV;
DEV bool late(int gb) { return (LX >> (gb & 7) & 1) || (LXG && (gb | 16) == (XG | 16)); }
DEV void slot_of(int gb, int& mp, int& j1) { gb ^= XG; mp = (gb & 7) * 8 + (gb >> 5); j1 = (gb >> 3) & 3; }
DEV u32x4 mat_sf(int k1, int c, int q) { const float dt = -(float)(k1 + 128 * c) * (1.0f / 2048.0f); return cmat1(dt * (float)(4 * q), dt); }
DEV u32x4 mat_si(int k1, int c, int q) { return cmat1((float)((k1 + 512 * q) * c) * (1.0f / 2048.0f), (float)c * (1.0f / 16.0f)); }

DEV void mk_tables(lptr smem, int gb, int wid, int lane) {
    int mp, j1; slot_of(gb, mp, j1);
    const int c = lane & 15, q = lane >> 4;
    const bool spc = mp == 0 && !(j1 & 1);
    const int k1 = mp ? mp : (j1 == 3 ? 64 : 0);
    lptr mt = smem + A_MAT + lane * 16;
    if (wid == 0) {
        const float dt = -(float)(j1 + 4 * (spc ? c & 7 : c)) * (1.0f / 64.0f), t0 = __builtin_amdgcn_fractf(dt * (float)(4 * q));
        float cc[4], ss[4];
#pragma unroll
        for (int e = 0; e < 4; ++e) { const float f = fmaf((float)e, dt, t0); cc[e] = __builtin_amdgcn_cosf(f); ss[e] = __builtin_amdgcn_sinf(f); }
        const unsigned c0 = pkb(cc[0], cc[1]), c1 = pkb(cc[2], cc[3]), s0 = pkb(ss[0], ss[1]), s1 = pkb(ss[2], ss[3]);
        const unsigned c0l = pkb(cc[0] - lo2f(c0), cc[1] - hi2f(c0)), c1l = pkb(cc[2] - lo2f(c1), cc[3] - hi2f(c1));
        const unsigned s0l = pkb(ss[0] - lo2f(s0), ss[1] - hi2f(s0)), s1l = pkb(ss[2] - lo2f(s1), ss[3] - hi2f(s1));
        u32x4 m[4] = {(u32x4){c0, c1, s0 ^ SG, s1 ^ SG}, (u32x4){s0, s1, c0, c1}, (u32x4){c0l, c1l, s0l ^ SG, s1l ^ SG}, (u32x4){s0l, s1l, c0l, c1l}};
        if (spc) {
            m[0] = c < 8 ? (u32x4){c0, c1, 0, 0} : (u32x4){0, 0, c0, c1}; m[1] = c < 8 ? (u32x4){s0, s1, 0, 0} : (u32x4){0, 0, s0, s1};
            m[2] = c < 8 ? (u32x4){c0l, c1l, 0, 0} : (u32x4){0, 0, c0l, c1l}; m[3] = c < 8 ? (u32x4){s0l, s1l, 0, 0} : (u32x4){0, 0, s0l, s1l};
        }
#pragma unroll
        for (int i = 0; i < 4; ++i) lst<u32x4>(mt + i * 1024, m[i]);
    } else if (wid == 1) {
        const int jq = spc ? (4 * q) & 7 : 4 * q;
        float cc[4], ss[4];
        const float t0 = __builtin_amdgcn_fractf((float)(c * (j1 + 4 * jq)) * (1.0f / 64.0f));
#pragma unroll
        for (int e = 0; e < 4; ++e) {
            const float f = fmaf((float)c, (float)e * (1.0f / 16.0f), t0);
            const float wg = !spc || (gb == XG && jq + e == 0) ? 0.125f : 0.25f;
            cc[e] = __builtin_amdgcn_cosf(f) * wg; ss[e] = -__builtin_amdgcn_sinf(f) * wg;
        }
        const unsigned c0 = pkh(cc[0], cc[1]), c1 = pkh(cc[2], cc[3]), n0 = pkh(ss[0], ss[1]), n1 = pkh(ss[2], ss[3]);
        u32x4 mia = (u32x4){c0, n0, c1, n1}, mib = (u32x4){n0, c0 ^ SG, n1, c1 ^ SG};
        if (spc) { mib = q < 2 ? (u32x4){0, 0, 0, 0} : mia ^ SG; if (q >= 2) mia = (u32x4){0, 0, 0, 0}; }
        lst<u32x4>(mt + 6 * 1024, mia); lst<u32x4>(mt + 7 * 1024, mib);
        lst<u32x4>(mt + 8 * 1024, cmat1((float)(c * q) * -0.25f, (float)c * (-1.0f / 16.0f)));
    } else {
        lst<u32x4>(mt + (2 + wid) * 1024, wid == 2 ? mat_sf(k1, c, q) : mat_si(k1, c, q));
        const int ja = (spc ? 0 : 8 * (wid - 2)) + (lane >> 3);
        const float dt = -(float)(j1 + 4 * ja) * (1.0f / 1024.0f), t0 = dt * (float)(2 * (lane & 7)), t1 = t0 + dt;
        lst<f32x4>(mt + (7 + wid) * 1024, (f32x4){__builtin_amdgcn_cosf(t0), __builtin_amdgcn_sinf(t0), __builtin_amdgcn_cosf(t1), __builtin_amdgcn_sinf(t1)});
    }
}

template <int SLP = 0> DEV void c_chunk(const u16* __restrict__ Cre, const u16* __restrict__ Cim, unsigned lb, int gb, int da, int lw, int lane, int j, int at = -1) {
    if (at < 0) at = ((j + 2) & 3) * A_STAGE + lw * 8192;
    const u32x4 rc = mkdesc(lw < 2 ? Cre : Cim, D_ * D_ * 2);
    const unsigned vo = (lane >> 3) * 8192 + (((lane & 7) ^ (lane >> 3)) << 4);
#pragma unroll
    for (int i = 0; i < 8; ++i) {
        if (SLP && i) __builtin_amdgcn_s_sleep(SLP);
        dma16(rc, lb + at + i * 1024, vo, (512 * (i & 1) + 256 * ((lw ^ da ^ 1) & 1) + 32 * (da >> 1) + (i >> 1)) * 2048 + (8 * (da & 1) + (j ^ CX(gb))) * 128);
    }
}
constexpr int KS2 = 16, KS1 = 16, F1 = 8 * KS1, FT = F1 + 4 * KS2, RT = 8, NRL = 16;
static_assert(!(NB & 1) && NB <= 16, "");
constexpr int f_k(int f) { return f < F1 ? f >> 3 : f < FT ? KS1 + ((f - F1) >> 2) : 31 - ((f - FT) >> 2); }
constexpr int f_m(int f) { return f < F1 ? f & 7 : f < FT ? (f - F1) & 3 : 4 + ((f - FT) & 3); }
constexpr int f_i(int f) { return f < F1 ? f & 7 : (f - F1) & 3; }
constexpr bool f_hi(int f) { return f_i(f) == (f < F1 ? 7 : 3); }
constexpr bool c_new(int f) { return f < FT && !f_i(f) && !(f_k(f) & 3); }
constexpr bool c_new3(int f) { return f >= FT && f < 256 && !f_i(f) && (f_k(f) & 3) == 3; }
constexpr int k_f(int k) { return k < KS1 ? 8 * k : F1 + 4 * (k - KS1); }
struct SCH {
    int a[512], p[512], w[512]; unsigned m[512];
    constexpr void chk(int s, unsigned mask, int word) { m[s] |= mask; p[s - 2] = 3; p[s - 2 - 2 * CPD] = word; }
    constexpr SCH() : a{}, p{}, w{}, m{} {
        int s = -1, as[256] = {}, cs[9] = {};
        for (int i = AD; i < 256; ++i) { const int t = 2 * (i - (i < AG2 ? AD : i < AG3 ? AD2 : i < AG4 ? AD3 : AD4)) + 1; s = t > s ? t : s + 1; a[s] = i; as[i] = s; }
        for (int c = 1; c < 8; ++c) {
            cs[c] = (as[k_f(4 * c)] - (c < 5 ? 5 : 1)) & ~1;
            if (!HD && c > 4 && c < 7 && cs[c] > 2 * k_f(2 * c + 10)) cs[c] = 2 * k_f(2 * c + 10);
            chk(cs[c], 4u << (4 * c), 1);
        }
        for (int j = (NB + 7) / 8; j < 8; ++j) {
            int c = 7;
            while (cs[c] > 2 * k_f((8 * j - NB) / 2 + 1) - 2) --c;
            chk(cs[c], 8u << (4 * j), 1);
        }
        for (int j = 7; j > 3; --j) {
            const int t = (as[FT + 16 * (7 - j)] - 1) & ~1;
            chk(t, 4u << (4 * j) | 4u | (HD && j < 6 ? 0x400u << (4 * (5 - j)) : 0u), 2);
            if (HD && j < 6 && t > 2 * FT + RS0 + RSP * 8 * (5 - j)) p[-1] = 0;
        }
        chk(GS0, 0x40u, 2);
        for (int f = 0; f < 252; f += WGL) {
            int n = 0;
            for (int i = f + WGL; i < 256; ++i) n += i < AD || as[i] < 2 * f;
            if (f && WGL > 1) w[2 * f - 1] = n + 1;
        }
    }
    constexpr bool ok() const {
        int e = 0, r = 0;
        for (int s = 0; s < 512; ++s) {
            if (p[s] == 3) { r = e; e = 0; } else if (p[s]) { if (e) return false; e = p[s]; }
            if (m[s]) { if (r != ((m[s] & 0x44u) && s > 128 ? 2 : 1)) return false; r = 0; }
        }
        return true;
    }
};
constexpr SCH ST;
static_assert(ST.ok(), "");
DEV void wait_lds(int n) { sfor<0, 16>([&](auto ic) INL { if (n == decltype(ic)::v) __builtin_amdgcn_s_waitcnt(0xC07F | decltype(ic)::v << 8); }); }
constexpr int s_f(int s) { return s < 504 ? s >> 1 : 252 + (s & 3); }
constexpr int s_n(int s) { return s < 504 ? s & 1 : (s >> 2) & 1; }
constexpr int s_b(int s) { return s >= 2 * FT && f_k(s_f(s)) < 32 - RT ? 64 + 2 * (31 - RT - f_k(s_f(s))) + s_n(s) : 2 * f_k(s_f(s)) + s_n(s); }
constexpr int h_off(int k, int n) { return ((k >> 1) & 1) * A_STAGE + (k & 1) * 16384 + n * 1024; }

DEV void role_mma(const u16* __restrict__ x, const u16* __restrict__ Dskip, char* ws, float* __restrict__ out, lptr smem, int gb, int wid, int lane, u32x4 cd, unsigned t00) {
    int lt, pt; tile_of(gb, lt, pt);
    const int wr = wid >> 1, wc = wid & 1, c = lane & 15, q = lane >> 4;
    const rsrc_t rs = mkrsrc(ws, WS_END);
    const unsigned fb = ldsa(smem + A_FLG);
    TSD(); TS0();
    u64 tag;
    if (STG) tag = *(const __attribute__((address_space(4))) u64*)(uintptr_t)(ws + OFF_E);
    else tag = __hip_atomic_load((GAS u64*)(ws + OFF_E), __ATOMIC_RELAXED, __HIP_MEMORY_SCOPE_WORKGROUP);
    static_assert(ENT && STG, "");
    if (FTS < 0 && wid == 2) asm volatile("s_nop 0\n\tbuffer_load_dword %0, %1, 0 offen sc1 lds" :: "v"(lane * 64), "s"(mkdesc(ws + OFF_G + gb * FTB, FTB)), "{m0}"(ldsa(smem + A_AGG + 256)) : "memory");
    SCHED();
    if (wid == 0 && lane < 32) lst<unsigned>(smem + A_FLG + lane * 4, 0u);
    mk_tables(smem, gb, wid, lane);
    f32x4 acc[4][4];
#pragma unroll
    for (int m = 0; m < 4; ++m)
#pragma unroll
        for (int n = 0; n < 4; ++n) acc[m][n] = Z4;
    u32x4 af[2][4], bf_[2][4];
#pragma unroll
    for (int m = 0; m < 4; ++m) { af[1][m] = (u32x4){0, 0, 0, 0}; bf_[1][m] = af[1][m]; }
    lptr fp[8];
#pragma unroll
    for (int i = 0; i < 8; ++i) { fp[i] = smem + (i & 2) * A_STAGE + ((i & 4 ? 128 + wc * 64 : wr * 64) + c) * 128 + (((4 * (i & 1) + q) ^ (c & 7)) << 4); PIN(fp[i]); }
    tag += 0x9E3779B97F4A7C15ull;
    PIN(tag);
    if (wid == 0 && lane == 0) lst<u64>(smem + A_FLG + F_TAG, tag);
    tag = (u64)(unsigned)__builtin_amdgcn_readfirstlane((unsigned)tag) | (u64)(unsigned)__builtin_amdgcn_readfirstlane((unsigned)(tag >> 32)) << 32;
    __builtin_amdgcn_s_waitcnt(0xC07F);
    TS(13);
    unsigned fst = fb + F_ST, sv = 0;
    auto step = [&](int sl, bool pf) INL {
#pragma unroll
        for (int hf = 0; hf < 2; ++hf)
#pragma unroll
            for (int i = 0; i < 16; ++i) {
                const int nn = SNK && (i & 4) ? 3 - (i & 3) : i & 3;
                acc[i >> 2][nn] = mfb(af[1 - hf][i >> 2], bf_[1 - hf][nn], acc[i >> 2][nn]);
                constexpr int RD[16] = RDPAT;
                const int r = RD[i];
                if (r == 0) af[hf][0] = lld<u32x4>(fp[(sl & 2) + hf] + (sl & 1) * A_STAGE);
                else if (r > 0 && r < 5) bf_[hf][r - 1] = lld<u32x4>(fp[4 + (sl & 2) + hf] + (sl & 1) * A_STAGE + (r - 1) * 2048);
                else if (r >= 5) af[hf][r - 4] = lld<u32x4>(fp[(sl & 2) + hf] + (sl & 1) * A_STAGE + (r - 4) * 2048);
                if (pf && hf && i == 11) sv = *(volatile LAS unsigned*)(lptr)(uintptr_t)fst;
                SCHED();
            }
        LDS_WAIT();
    };
#pragma unroll 1
    for (int kt = 0; kt < 3; ++kt) {
#pragma unroll
        for (int i = 0; i < 4; ++i) { BAR(); step(i, false); }
    }
    PIN(fst);
    unsigned agp = ldsa(smem + A_AGG) + (wc * 16 + c) * 16, tgv = (unsigned)tag, sel = 0x07060500u;
    unsigned gro = lt < 15 && q == 0 ? c * 16 + OFF_GRAN + ((15 - lt) * 512 + pt * 32 + wc * 16) * 16 : ~15u;
    PIN(agp); PIN(tgv); PIN(gro);
    wait_vm<0>();
    BAR();
    if (wr) lst<u32x4>((lptr)(uintptr_t)agp, (u32x4){0, 0, 0, 0});
    step(0, false);
    BAR(); __builtin_amdgcn_s_setprio(PRIOE); step(1, true);
#pragma unroll
    for (int i = 0; i < 2; ++i) {
        unsigned ss = __builtin_amdgcn_readfirstlane(sv);
        need_bit(fst, ss, 4u << (4 * i));
        TL(30 + i);
        if (i) need_bit(fst, ss, 0x100u);
        step(2 + i, i == 0);
    }
    __builtin_amdgcn_s_setprio(PRIOT);
    sig(fb + F_FREE, 1u << 20);
    float2 lam[2], coef[2], l16[2], l64[2], l512[2], cw[2], wq[2], lq[2];
#pragma unroll
    for (int n = 0; n < 2; ++n) {
        const f32x4 a = lld<f32x4>(smem + A_PRM + (wc * 32 + 2 * c + n) * 32), b = lld<f32x4>(smem + A_PRM + (wc * 32 + 2 * c + n) * 32 + 16);
        lam[n] = make_float2(a[0], a[1]); l16[n] = make_float2(a[2], a[3]); l64[n] = make_float2(b[0], b[1]); l512[n] = make_float2(b[2], b[3]);
    }
    float2 h[2];
#pragma unroll
    for (int m = 0; m < 5; ++m) {
#pragma unroll
        for (int n = 0; n < 4; ++n) {
            if (m < 4) acc[m][n] = mfb(af[1][m], bf_[1][n], acc[m][n]);
            if (m == 1 && n == 0) { h[0] = make_float2(acc[0][0][0], acc[0][2][0]); h[1] = make_float2(acc[0][1][0], acc[0][3][0]); }
            else if (m) {
#pragma unroll
                for (int i = 0; i < 2; ++i) h[i] = cfma(lam[i], h[i], make_float2(acc[m - 1][i][n], acc[m - 1][2 + i][n]));
            }
            if (SIL) SCHED();
        }
        if (m == 3) {
#pragma unroll
            for (int n = 0; n < 2; ++n) {
                const f32x4 a = lld<f32x4>(smem + A_PW + 4096 + (wc * 32 + 2 * c + n) * 16), b = lld<f32x4>(smem + A_PW + q * 1024 + (wc * 32 + 2 * c + n) * 16);
                coef[n] = make_float2(a[0], a[1]); cw[n] = wr ? make_float2(a[2], a[3]) : coef[n]; wq[n] = make_float2(b[0], b[1]); lq[n] = make_float2(b[2], b[3]);
            }
        }
    }
    TS(1);
    float2 loc[2], wag[2];
#pragma unroll
    for (int n = 0; n < 2; ++n) {
        float2 a0, a1, a2, a3;
        rows4(h[n].x, a0.x, a1.x, a2.x, a3.x);
        rows4(h[n].y, a0.y, a1.y, a2.y, a3.y);
        const float2 s2 = cfma(l16[n], a0, a1), s3 = cfma(l16[n], s2, a2);
        wag[n] = cfma(l16[n], s3, a3);
        loc[n] = sel4(q, make_float2(0.f, 0.f), a0, s2, s3);
    }
    float2 W0[2] = {make_float2(0.f, 0.f), make_float2(0.f, 0.f)};
    if (wr == 0) lst<u32x4>((lptr)(uintptr_t)agp, (u32x4){__float_as_uint(wag[0].x) | 1u, __float_as_uint(wag[0].y), __float_as_uint(wag[1].x), __float_as_uint(wag[1].y) | 1u});
    else {
        f32x4 w0; unsigned t, n;
        asm volatile("s_mov_b32 %2, 0x400000\n"
                     "1:\tds_read_b128 %0, %3\n\ts_waitcnt lgkmcnt(0)\n\tv_and_b32 %1, v" PV0 ", v" PV0E "\n\tv_and_b32 %1, 1, %1\n\tv_cmp_eq_u32 vcc, 0, %1\n\ts_cbranch_vccz 2f\n\t"
                     "s_sub_u32 %2, %2, 1\n\ts_cbranch_scc0 1b\n"
                     "2:" : "=&{v[" PV0 ":" PV0E "]}"(w0), "=&v"(t), "=&s"(n) : "v"(agp) : "memory", "scc", "vcc");
        W0[0] = make_float2(w0[0], w0[1]); W0[1] = make_float2(w0[2], w0[3]);
        const float2 g0 = cfma(l64[0], W0[0], wag[0]), g1 = cfma(l64[1], W0[1], wag[1]);
        auto tb_ = [&](float v, unsigned i) INL { unsigned k = sel + i; SPIN(k); return __builtin_amdgcn_perm(__float_as_uint(v), tgv, k); };
        st128<SC1>((u32x4){tb_(g0.x, 0), tb_(g0.y, 1), tb_(g1.x, 2), tb_(g1.y, 3)}, rs, gro);
    }
    TS(2);
    if (CE || CGW) sig(fst, 0x1000u);
    __builtin_amdgcn_s_setprio(PRIOW);
    const int da = da_of(pt), kh = da & 1;
    u32x2 xi[8], dk;
    auto xload = [&]() INL {
        const unsigned co = (512 * ((lane >> 3) & 1) + 4 * (lane & 7)) * 2;
        SCHED();
#pragma unroll
        for (int i = 0; i < 8; ++i)
            xi[i] = __builtin_amdgcn_raw_buffer_load_b64(mkrsrc(x, S_ * D_ * 2), (lane >> 4) * 64 * D_ * 2 + co, ((512 * wid + 256 * (i >> 2) + 16 * (i & 3) + lt) * D_ + 256 * kh + 32 * (da >> 1)) * 2, 0);
        dk = __builtin_amdgcn_raw_buffer_load_b64(mkrsrc(Dskip, D_ * 2), co, 512 * kh + 64 * (da >> 1), 0);
        SCHED();
    };
    if (XP == 0) xload();
#pragma unroll
    for (int n = 0; n < 2; ++n) {
        PIN(coef[n].x); PIN(coef[n].y);
        loc[n] = cmul(coef[n], cfma(wq[n], W0[n], loc[n]));
        wq[n] = cmul(wq[n], cw[n]);
        loc[n].y = -loc[n].y;
        PIN(wq[n].x); PIN(wq[n].y); PIN(loc[n].x); PIN(loc[n].y);
    }
    float2 u[2][16];
#pragma unroll
    for (int t = 0; t < 16; ++t) {
#pragma unroll
        for (int n = 0; n < 2; ++n) { u[n][t] = cmul(make_float2(coef[n].x, -coef[n].y), make_float2(acc[t >> 2][n][t & 3], -acc[t >> 2][2 + n][t & 3])); PIN(u[n][t].x); PIN(u[n][t].y); }
        SCHED();
    }
    lptr tb = smem + A_HS + wid * 2048 + lane * 4, rb = smem + A_HS + wid * 2048 + (lane >> 4) * 256 + (lane & 3) * 64 + ((lane >> 2) & 3) * 16;
    unsigned hoff = OFF_H + ((lane >> 4) * 64 + pt * 2 + wc) * 8192 + lt * 512 + wr * 256 + (lane & 15) * 16;
    PIN(tb); PIN(rb); PIN(hoff);
    unsigned ex[4], s0 = 0x04000400u, s1 = 0x05040100u, km = 0xffffff00u;
#pragma unroll
    for (int j = 0; j < 4; ++j) { ex[j] = 4 * j + q < lt ? (unsigned)tag : 0u; PIN(ex[j]); }
    SPIN(s0); SPIN(s1); SPIN(km);
    __builtin_amdgcn_s_setprio(PRIOT);
    await<0>(fb + F_FREE, 8u << 20);
    if (XP == 1) xload();
    if (GDL) __builtin_amdgcn_s_sleep(GDL);
    TS(3);
    {
        u32x4 gr[4];
        unsigned off = ((16 - lt + q) * 512 + pt * 32 + wc * 16 + c) * 16;
        auto gload = [&]() INL {
            PIN(off);
#pragma unroll
            for (int j = 0; j < 4; ++j) gr[j] = __builtin_amdgcn_raw_buffer_load_b128(mkrsrc(ws + OFF_GRAN + j * 32768, (4 - j) * 32768), off, 0, SC1);
        };
        auto gok = [&]() INL {
            unsigned d = 0;
#pragma unroll
            for (int j = 0; j < 4; ++j) d |= __builtin_amdgcn_perm(__builtin_amdgcn_perm(gr[j].w, gr[j].z, s0), __builtin_amdgcn_perm(gr[j].y, gr[j].x, s0), s1) ^ ex[j];
            return __builtin_amdgcn_uicmp(d, 0u, 33) == 0ull;
        };
        gload();
        if (XP == 4) xload();
        if (__builtin_expect(!gok(), 0)) {
            int spin;
#pragma unroll 1
            for (spin = 1; spin < (1 << 20); ++spin) {
                asm volatile("" ::: "memory");
                if (GSL) __builtin_amdgcn_s_sleep(GSL);
                gload();
                if (gok()) break;
            }
        }
        TS(25);
        if (XP == 5) xload();
#pragma unroll
        for (int j = 0; j < 4; ++j) gr[j] &= km;
        float2 cs[2];
#pragma unroll
        for (int j = 3; j >= -1; --j) {
#pragma unroll
            for (int n = 0; n < 2; ++n) {
                const float2 g = make_float2(__uint_as_float(gr[j & 3][2 * n]), __uint_as_float(gr[j & 3][2 * n + 1]));
                cs[n] = j == 3 ? g : j < 0 ? cmul(cs[n], lq[n]) : cfma(l512[n], cs[n], g);
            }
        }
        rowsum4(cs[0].x, cs[0].y, cs[1].x, cs[1].y);
#pragma unroll
        for (int n = 0; n < 2; ++n) loc[n] = cfma(make_float2(wq[n].x, -wq[n].y), make_float2(cs[n].x, -cs[n].y), loc[n]);
    }
    TS(4);
    u32x4 hv[2];
#pragma unroll
    for (int r = 0; r < 5; ++r) {
#pragma unroll
        for (int t = 0; t < 4; ++t) {
            if (r < 4) {
#pragma unroll
                for (int n = 0; n < 2; ++n) loc[n] = cfma(make_float2(lam[n].x, -lam[n].y), loc[n], u[n][4 * r + t]);
                lst<unsigned>(tb + t * 256, pkb(loc[0].x, loc[1].x));
                lst<unsigned>(tb + 1024 + t * 256, pkb(loc[0].y, loc[1].y));
            }
            if (r && t >= HST && t < HST + 2) { SCHED(); st128<SC1>(hv[t - HST], rs, hoff + ((r - 1) * 256 + (t - HST) * 32) * 8192); SCHED(); }
        }
        asm volatile("" ::: "memory");
        if (r < 4) { hv[0] = lld<u32x4>(rb); hv[1] = lld<u32x4>(rb + 1024); }
        asm volatile("" ::: "memory");
        SCHED();
    }
    TS(5);
    PIN(lane);
    auto cp2 = [&]() INL {
        if (wid == 3) asm volatile("s_nop 0\n\tbuffer_load_dword %0, %1, %2 offen lds" :: "v"(lane * 128), "s"(cd), "s"(((gb >> 3) & 7) * 8192), "{m0}"(ldsa(smem + A_AGG + 256)) : "memory");
    };
    if (XLP == 0) cp2();
    f32x4 ac[8][2];
    auto acin = [&]() INL {
#pragma unroll
        for (int i = 0; i < 8; ++i) {
            PIN(xi[i]);
            const u32x2 d = dk, v = xi[i];
            const unsigned p0 = pkb(lo2f(v.x) * (0.5f * lo2f(d.x)), hi2f(v.x) * (0.5f * hi2f(d.x))), p1 = pkb(lo2f(v.y) * (0.5f * lo2f(d.y)), hi2f(v.y) * (0.5f * hi2f(d.y)));
            ac[4][i >> 2][i & 3] = lo2f(p0); ac[5][i >> 2][i & 3] = hi2f(p0); ac[6][i >> 2][i & 3] = lo2f(p1); ac[7][i >> 2][i & 3] = hi2f(p1);
            ac[i >> 1][i & 1] = Z4;
        }
#pragma unroll
        for (int i = 0; i < 16; ++i) PIN(ac[i >> 1][i & 1]);
    };
    if (XP == 2) xload();
    if (ACI == 2) { SCHED(); acin(); SCHED(); }
    const u32x2 tg = {(unsigned)tag, (unsigned)(tag >> 32)};
    unsigned ord, tv;
    asm volatile("s_mov_b64 exec, 1\n\ts_waitcnt vmcnt(%8)\n\tds_add_rtn_u32 %1, %2, %3\n\ts_mov_b64 exec, %9\n\ts_waitcnt lgkmcnt(0)\n\tv_readfirstlane_b32 %0, %1\n\t"
                 "s_cmp_lg_u32 %0, 3\n\ts_cbranch_scc1 1f\n\tbuffer_store_dwordx2 %4, %5, %6, %7 offen sc1\n1:\ts_mov_b64 exec, -1"
                 : "=&s"(ord), "=&v"(tv) : "v"(fb + F_HC), "v"(1u), "v"(tg), "v"(lane * 256 * FST), "s"(mkdesc(ws, WS_END)), "s"(OFF_FH + (pt * 16 + lt) * FST),
                   "i"(XP == 2 ? 9 : 0), "i"((1 << HREP) - 1) : "memory", "scc");
    ord = __builtin_amdgcn_readfirstlane(ord);
    const bool pub = ord == 3u;
    TS(15);
    if (XLP == 1) cp2();
    if (XP == 3 || XP == 6) xload();
    __builtin_amdgcn_s_setprio(PRIOI);
    if (ACI == 0) acin();
    unsigned vo = ((lane & 15) >> 2) * 256 + (lane >> 4) * 64 + (lane & 3) * 16, so[32];
    PIN(vo);
#pragma unroll
    for (int k = 0; k < 32; ++k) {
        so[k] = OFF_H + lt * 524288 + kh * 131072 + wid * 2048 + (((XE & 2) ? 0 : k >> 2) ^ CX(gb)) * 16384 + ((k & 1) + (k & 2) * 16) * 8192;
        if (SPC || k < 8) asm volatile("" : "+s"(so[k]));
    }
    auto ldB = [&](int b) { return __builtin_amdgcn_raw_buffer_load_b128(rs, vo + (b & 1) * 1024, so[b >> 1], SC1); };
    lptr ab[2];
#pragma unroll
    for (int i = 0; i < 2; ++i) { ab[i] = smem + 2 * A_STAGE + (lane & 15) * 128 + (((4 * i + (lane >> 4)) ^ (lane & 7)) << 4); PIN(ab[i]); }
    auto ldA = [&](int f) INL {
        const int k = f < FT ? f_k(f) : f_k(f) ^ 12;
        if (f > 8 && !f_i(f) && (k & 6) == (f < FT ? 0 : 2)) { ab[k & 1] += (k & 8) ? -2 * A_STAGE : 2 * A_STAGE; PIN(ab[k & 1]); }
        return lld<u32x4>(ab[k & 1] + ((k >> 1) & 3) * 16384 + f_i(f) * 2048);
    };
    const unsigned hb = ldsa(smem) + 8192 + wid * 2048;
    unsigned fv = fb + F_FULL, fw = fb + F_RD + wid, pa = hb + (HD ? 0 : 2 * A_STAGE) + lane * 16; PIN(fv); PIN(fw); PIN(pa);
    u64 ob = (u64)(out + (512 * wid + lt) * D_ + 256 * kh + 32 * (da >> 1)); SPIN(ob);
    __builtin_amdgcn_s_setprio(PRIOG);
    TS(27);
    unsigned fe = 0, fs;
    static_assert(!HPL && NP1 == 1, "");
    if (ord == POW) {
        int spin = 0;
        unsigned o = (lane ^ ((CX(gb) & 3) << 4)) * FST, all = 0x8888u; PIN(o); PIN(all);
        if (POW != 3) await<1>(fb + F_HC, 4u);
        if (late(gb)) __builtin_amdgcn_s_sleep(PDX); else __builtin_amdgcn_s_sleep(PDL);
        TS(19);
        const u32x2 fl = __builtin_amdgcn_raw_buffer_load_b64(rs, o, OFF_FH + ((gb & (HREP - 1)) * 256 + kh * 128 + (CX(gb) >> 2) * 64) * FST, SC1);
        fs = lds_peek(fv) | 0x8888u;
        const u64 b = __builtin_amdgcn_uicmpl(__builtin_bit_cast(u64, fl), tag, 32);
        if (__builtin_expect(b == ~0ull, 1)) sig_or(fv, all);
        else {
            fs = 0;
#pragma unroll
            for (int j = 0; j < 4; ++j) fs |= ((unsigned)(b >> (16 * j)) & 0xffffu) == 0xffffu ? 8u << (4 * j) : 0u;
#pragma unroll 1
            for (; spin < (1 << 20) && !(fs & 8u); ++spin) fs = h_poll<1>(rs, gb, kh, lane, tg);
            sig_or(fv, fs);
        }
    } else fs = await<AW0>(fv, 8u);
    TS(7);
    auto need = [&](unsigned bit) INL { need_bit(fv, fs, bit); };
    u32x4 A[256], B[64 + NRL];
#pragma unroll
    for (int b = 0; b < NB; ++b) { if (b && !(b & 7)) need(8u << (b >> 1)); B[b] = ldB(b); SCHED(); }
    SCHED();
    if (ACI == 1) { acin(); SCHED(); }
    need(4u);
#pragma unroll
    for (int f = 0; f < AD; ++f) A[f] = ldA(f);
    SCHED();
    if (BSL) { for (int i = 0; i < BSL; ++i) __builtin_amdgcn_s_sleep(16); }
    if (BA2) burn<BS2>(BA2, A[0], A[1]);
    const unsigned po = OFF_U + ((lt * 16 + da) * 4 + wid) * 4096;
    unsigned pk[16], t0, pq = (po ^ 16384) - hb, pr = po - hb, gc = ldsa(smem) + A_XT + wid * 4096 - hb, cc = ldsa(smem) + A_CN - hb, oc = ldsa(smem) + A_OG + 256 * wid, ks = 0x07060302u;
    SPIN(pq); SPIN(pr); SPIN(gc); SPIN(cc); SPIN(oc); SPIN(ks);
    lptr og, cp;
    unsigned gp[2], ph;
    u32x4 pv[4], sl[2], ga[4], oo; f32x4 o2[2], r0[8];
    auto put = [&](int i) INL {
        const int e = i & 3, n = i >> 2;
#pragma unroll
        for (int j = 0; j < 4; ++j) r0[i][j] = fmaf(o2[n][e], ac[4 + j][n][e], r0[i][j]);
        st128<WTO>(__builtin_bit_cast(u32x4, r0[i]), mkrsrc((const char*)ob + n * 256 * D_ * 4, 241 * D_ * 4), oo[e]);
    };
    auto nextB = [&](int b) INL {
        if (b + NB >= 64) return;
        if (XE & 16) { B[b + NB] = B[b]; PIN(B[b + NB]); } else B[b + NB] = ldB(b + NB);
    };
    auto unp = [&](int j) INL {
        if (j < 0 || j > 31) return;
        const unsigned v = ga[j >> 3][(j >> 1) & 3];
        float t = (j & 1) ? hi2f(v) : lo2f(v);
        PIN(t);
        r0[j >> 2][j & 3] = t;
    };
    static_assert(GS0 + US0 + 32 <= 516 && US0 >= 8, "");
    sfor<0, 64>([&](auto ic) INL {
#define T8(x) {ST.x[s0], ST.x[s0 + 1], ST.x[s0 + 2], ST.x[s0 + 3], ST.x[s0 + 4], ST.x[s0 + 5], ST.x[s0 + 6], ST.x[s0 + 7]}
        constexpr int s0 = 8 * decltype(ic)::v, TA_[8] = T8(a), TP_[8] = T8(p), TW_[8] = T8(w);
        constexpr unsigned TM_[8] = T8(m);
#pragma unroll
        for (int u = 0; u < 8; ++u) {
            const int s = s0 + u, f = s_f(s), n = s_n(s), k = f_k(f), z = s - 2 * FT;
            MF2(B[s_b(s)], A[f], ac[f_m(f)][n]);
            SCHED();
            if (TP_[u] == 3) { fs = __builtin_amdgcn_readfirstlane(fe); SPIN(fs); }
            if (TP_[u] == 1 || TP_[u] == 2) fe = *(volatile LAS unsigned*)(lptr)(uintptr_t)(fv + (TP_[u] == 2 ? F_FB - F_FULL : 0));
            if (TM_[u]) { need_bit(fv, fs, TM_[u], (TM_[u] & 0x44u) && s > 128 ? F_FB - F_FULL : 0); if (s == GS0 && !(XE & 32)) TS(26); }
            if (z < 0 && !n) {
                if (f_hi(f)) nextB(2 * k);
                if (f_i(f) == 1 && k) nextB(2 * k - 1);
            }
            if (!HD && z < 0 && k >= 16 && k < 24) {
                const int v = s & 7;
                if (v == 6 && k == 19) { pa -= 2 * A_STAGE; PIN(pa); }
                if (v == WS0) lst<u32x4>((lptr)(uintptr_t)pa + h_off(k, 0), B[2 * k]);
                if (v == WS1) lst<u32x4>((lptr)(uintptr_t)pa + h_off(k, 1), B[2 * k + 1]);
            }
            if (z == -6) { t0 = pa + pr; PIN(t0); }
#pragma unroll
            for (int i = 0; i < 24; ++i) {
                const int j = i / 6, o = i % 6;
                if (z != (PKR == 1 ? i - 4 - j : PKR == 2 ? 3 * j + o / 2 - 4 : 2 * j + o / 3 - 4)) continue;
                if (o == 0) pk[4 * j] = __builtin_amdgcn_perm(__float_as_uint(ac[j][0][1]), __float_as_uint(ac[j][0][0]), ks);
                if (o == 1) pk[4 * j + 1] = pkb(ac[j][0][2], ac[j][0][3]);
                if (o == 2) pk[4 * j] |= 0x10001u;
                if (o == 3) pk[4 * j + 2] = pkb(ac[j][1][0], ac[j][1][1]);
                if (o == 4) pk[4 * j + 3] = pkb(ac[j][1][2], ac[j][1][3]);
                if (o < 5) PIN(pk[4 * j + (o == 2 ? 0 : o > 2 ? o - 1 : o)]);
                else { st128<SC1>((u32x4){pk[4 * j], pk[4 * j + 1], pk[4 * j + 2], pk[4 * j + 3]}, rs, t0 + j * 1024); if (j == 3 && !(XE & 32)) TS(29); }
            }
            if (z == 18) { ph = pa + 2 * A_STAGE; PIN(ph); }
#pragma unroll
            for (int r = 0; r < NRL; ++r) if (z == RS0 + RSP * r) B[64 + r] = lld<u32x4>((lptr)(uintptr_t)(r < 8 ? pa : ph) + h_off(23 - (r >> 1), r & 1));
            constexpr int RS = 2 * FT + RSD;
            static_assert(RS + 6 <= 507, "");
            if (s >= RS && s < RS + 8 && !((s - RS) & 1)) pv[(s - RS) >> 1] = __builtin_amdgcn_raw_buffer_load_b128(rs, pa + (s - RS) * 512, pq, SC1);
            if (z == 22) { gp[0] = pa >> 2; PIN(gp[0]); }
            if (z == 24) { gp[0] &= 0xc0u; PIN(gp[0]); }
            if (z == 28) { og = (lptr)(uintptr_t)(gp[0] + oc); PIN(og); }
            if (z == 30) { gp[0] = (gp[0] ^ pa) + gc; PIN(gp[0]); }
            if (z == 34) { gp[1] = gp[0] ^ 32u; PIN(gp[1]); }
            if (z == 36) { cp = (lptr)(uintptr_t)(pa + cc); PIN(cp); }
            if (s > GS0 && s <= GS0 + 4) ga[s - GS0 - 1] = lld<u32x4>((lptr)(uintptr_t)gp[(s - GS0 - 1) & 1] + (s - GS0 - 1) * 1024);
            if (s == 496) sl[0] = lld<u32x4>(cp);
            if (s == 498) sl[1] = lld<u32x4>(cp + 1024);
            if (s == 500) oo = lld<u32x4>(cp + 2048);
            if (s == 501) o2[0] = lld<f32x4>(og);
            if (s == 502) o2[1] = lld<f32x4>(og + 16);
            unp(s + (s > 507 ? 4 : 0) - GS0 - US0);
            if (const int g = TA_[u]; g) {
                A[g] = ldA(g);
                if (g < FT && f_hi(g) && (f_k(g) & 3) == 3) asm volatile("ds_write_b8 %0, %0 offset:%1" :: "v"(fw), "i"(f_k(g) & 28) : "memory");
            }
            if (TW_[u]) wait_lds(TW_[u] - 1);
            SCHED();
            if (s == 507 || s == 511) {
                if (s == 507) {
                    TS(10);
                    unsigned n;
                    asm volatile("s_mov_b32 %5, 0x100000\n"
                                 "1:\ts_waitcnt vmcnt(0)\n\tv_and_b32 %4, v" PV0 ", v" PV1 "\n\tv_and_b32 %4, v" PV2 ", %4\n\tv_and_b32 %4, v" PV3 ", %4\n\tv_and_b32 %4, 1, %4\n\t"
                                 "v_cmp_eq_u32 vcc, 0, %4\n\ts_cbranch_vccz 2f\n\t"
                                 "buffer_load_dwordx4 %0, %6, %7, %8 offen sc1\n\tbuffer_load_dwordx4 %1, %6, %7, %8 offen offset:1024 sc1\n\t"
                                 "buffer_load_dwordx4 %2, %6, %7, %8 offen offset:2048 sc1\n\tbuffer_load_dwordx4 %3, %6, %7, %8 offen offset:3072 sc1\n\t"
                                 "s_sub_u32 %5, %5, 1\n\ts_cbranch_scc0 1b\n\ts_waitcnt vmcnt(0)\n"
                                 "2:" : "+{v[" PV0 ":" PV0E "]}"(pv[0]), "+{v[" PV1 ":" PV1E "]}"(pv[1]), "+{v[" PV2 ":" PV2E "]}"(pv[2]), "+{v[" PV3 ":" PV3E "]}"(pv[3]), "=&v"(t0), "=&s"(n)
                                 : "v"(pa), "s"(mkdesc(ws, WS_END)), "s"(pq) : "memory", "scc", "vcc");
                    TS(11);
                    SCHED();
                }
#pragma unroll
                for (int m = 0; m < 4; ++m) { MF2(sl[n], pv[m], ac[4 + m][n]); SCHED(); if (n) put(m); else unp(508 + m - GS0 - US0); SCHED(); }
            }
            if (s == 15) { TS(8); TC(60); }
            if (s == 2 * F1 - 1) { if (!(XE & 32)) TS(9); TC(55); }
            if (z == -1) { if (!(XE & 32)) TS(28); TC(56); }
            if (s == 495) { TC(61); TS(14); }
        }
    });
    asm volatile("s_nop 7");
    SCHED();
#pragma unroll
    for (int i = 4; i < 8; ++i) { put(i); SCHED(); }
    TS(12);
    TSE(0, wid == 0);
    TSE(2, pub);
    TSE(3, ord == POW);
}

#define TSARG
#define TSPASS
struct Gate {
    unsigned xa, wa, xv, gw; float sum; int rot;
    DEV void rd(int kt) { asm volatile("ds_read_b32 %0, %2\n\tds_read_b32 %1, %3" : "=&v"(xv), "=&v"(gw) : "v"(xa + (kt & 3) * A_STAGE), "v"(wa + (kt ^ rot) * 128) : "memory"); }
    DEV void acc() {
        asm volatile("s_waitcnt lgkmcnt(0)" : "+v"(xv), "+v"(gw) :: "memory");
        const unsigned pr = pkb(lo2f(xv) * lo2f(gw), hi2f(xv) * hi2f(gw));
        sum += lo2f(pr); sum += hi2f(pr); PIN(sum);
    }
};
struct Tab { CM sf, si, ff, fi; u32x4 mt[2], xf, xb; f32x2 ti[2]; };
DEV void mid_tables(lptr smem, int gb, int w, int lane, Tab& t) {
    int mp, j1; slot_of(gb, mp, j1);
    const int c = lane & 15, q = lane >> 4;
    lptr mb = smem + A_MAT + lane * 16;
#pragma unroll
    for (int i = 0; i < 2; ++i) { t.ti[i] = lld<f32x2>(smem + A_MAT + 9216 + (2 * w + i) * 128 + c * 8); t.mt[i] = lld<u32x4>(mb + (6 + i) * 1024); }
    u32x4 sf0 = lld<u32x4>(mb + 4096), si0 = lld<u32x4>(mb + 5120);
    const u32x4 f0 = lld<u32x4>(mb + 8192);
    SCHED();
    asm volatile("" : "=v"(t.xf), "=v"(t.xb));
    if (w >= 4 && mp == 0 && !(j1 & 1)) {
        sf0 = mat_sf(64, c, q); si0 = mat_si(64, c, q);
        const float dt = -(float)(32 + 64 * c) * (1.0f / 1024.0f);
        t.xf = cmat1(dt * (float)(4 * q), dt); t.xb = cmat1((float)(c * (32 + 256 * q)) * (1.0f / 1024.0f), (float)c * (1.0f / 16.0f));
    }
    PIN(t.xf); PIN(t.xb);
    t.sf = cexp(sf0); t.si = cexp(si0); t.ff = cexp(f0); t.fi = cexp((u32x4){f0.x ^ SG, f0.y ^ SG, f0.z, f0.w});
    PIN(t.mt[0]); PIN(t.mt[1]); PIN(t.ti[0]); PIN(t.ti[1]);
}
DEV int u_exp(unsigned em) { return min(max((int)(em & 0xffu), 4), 254); }

struct Ld {
    u32x4 rx, rb, vA, vB; unsigned lb, sA, sB; int rot;
    DEV Ld(const u16* x, const u16* Bre, const u16* Bim, lptr smem, int gb, int lw, int lane) {
        int lt, pt; tile_of(gb, lt, pt);
        const int srow = lane >> 3, schunk = (lane & 7) ^ srow;
        rot = ROT;
        lb = ldsa(smem) + lw * 4096;
        const unsigned a = (16 * (srow >> 2) + (srow & 3)) * 2048 + schunk * 16, b = 2 * srow * 2048 + schunk * 16;
        vA = (u32x4){a, a + 65536 - 1024, a + 8192 - 2048, a + 73728 - 3072}; vB = (u32x4){b, b + 32768 - 1024, b, b + 34816 - 3072};
        sA = (lt * 128 + 64 * (lw >> 1) + 8 * (lw & 1)) * 2048; sB = (pt * 64 + 32 * (lw >> 1)) * 2048;
        PIN(vA.x); PIN(vA.y); PIN(vA.z); PIN(vA.w); PIN(vB.x); PIN(vB.y); vB.z = vB.x; vB.w = vB.y;
        SPIN(sA); SPIN(sB); SPIN(lb); SPIN(rot);
        SCHED();
        rx = mkdesc(x, S_ * D_ * 2); rb = mkdesc((lw & 1) ? Bim : Bre, D_ * D_ * 2);
    }
    DEV void st(int kk) const {
        dma64(rx, lb + (kk & 3) * A_STAGE, vA, sA + (kk ^ rot) * 128);
        dma64(rb, lb + (kk & 3) * A_STAGE + 16384, vB, sB + (kk ^ rot) * 128);
    }
};
DEV void first_loads(const u16* __restrict__ x, const u16* __restrict__ Bre, const u16* __restrict__ Bim, const u16* __restrict__ gate_w, lptr smem, int gb, int lw, int lane) {
    __builtin_amdgcn_s_setprio(PRIOL);
    const Ld ld(x, Bre, Bim, smem, gb, lw, lane);
    ld.st(0);
    SCHED();
    int lt, pt; tile_of(gb, lt, pt);
    dma16(mkdesc(gate_w, D_ * 2), ldsa(smem) + A_GW + (lw & 1) * 1024, lane * 16, (lw & 1) * 1024);
    const unsigned t = (lane >> 1) * 32768 + (lane & 1) * 16;
    dma64(ld.rx, ld.lb + A_XT, (u32x4){t, t + 1047552, t + 2 * 1047552, t + 3 * 1047552}, lt * 2048 + lw * 512 + pt * 32);
    if (FSL) __builtin_amdgcn_s_sleep(FSL);
    if (FLN > 1) ld.st(1);
    if (FLN > 2) ld.st(2);
}
DEV void role_load(const u16* __restrict__ x, const u16* __restrict__ Bre, const u16* __restrict__ Bim, const u16* __restrict__ Cre, const u16* __restrict__ Cim,
                   const u16* __restrict__ gate_w, char* ws, lptr smem, int gb, int lw, int lane, Tab& tab TSARG) {
    if (!ENT) __builtin_amdgcn_s_waitcnt(0x0F70);
    Gate g;
    int lt, pt; tile_of(gb, lt, pt);
    TS(0);
    if (!ENT) first_loads(x, Bre, Bim, gate_w, smem, gb, lw, lane);
    const Ld ld(x, Bre, Bim, smem, gb, lw, lane);
    if (FLN < 2) ld.st(1);
    if (FLN < 3) ld.st(2);
    const unsigned lb = ldsa(smem);
    const int gi = 8 * pt + 2 * lw + (lane >> 5);
    g.xa = lb + gi * 128 + ((((lane & 31) >> 2) ^ (gi & 7)) << 4) + (lane & 3) * 4;
    g.wa = lb + A_GW + (lane & 31) * 4; g.sum = 0.f; g.rot = ld.rot;
    TS(6);
    const int c = lane & 15, q = lane >> 4;
    const unsigned fb = lb + A_FLG;
    lptr ub = smem + A_XT + lw * 4096 + (lane >> 2) * 64 + (lane & 3) * 8, ea = smem + A_E1 + c * E1P + q * 256 + lw * 64;
    lptr mb = smem + A_MAT + lane * 16, tb = smem + A_MAT + 9216 + c * 128 + q * 32;
    PIN(ub); PIN(ea); PIN(mb); PIN(tb);
#pragma unroll 1
    for (int kt = 0; kt < 13; ++kt) { wait_vm<16>(); TL(8 + kt); BAR(); TL(28 + kt); if (kt == 0) { TS(28); TC(60); } g.rd(kt); ld.st(kt + 3); g.acc(); }
    u32x4 mt[4], ut[4];
    f32x4 tf[2];
    wait_vm<16>(); TL(21); BAR(); TL(41); g.rd(13);
#pragma unroll
    for (int i = 0; i < 4; ++i) mt[i] = lld<u32x4>(mb + i * 1024);
#pragma unroll
    for (int i = 0; i < 2; ++i) tf[i] = lld<f32x4>(tb + i * 16);
    g.acc();
#pragma unroll
    for (int i = 0; i < 4; ++i) PIN(mt[i]);
    PIN(tf[0]); PIN(tf[1]);
    wait_vm<8>(); sig(fb + F_ST, 1u); TL(42);
    wait_vm<0>(); sig(fb + F_ST, 0x10u);
    __builtin_amdgcn_s_setprio(PRIOD);
    await<0>(fb + F_ST, 0x40u);
    TS(43); TC(61);
    g.rd(14); g.acc(); g.rd(15); g.acc();
    sig(fb + F_FREE, 1u << 20);
    TS(2);
    __builtin_amdgcn_s_setprio(PRIOM);
    mid_tables(smem, gb, lw + 4, lane, tab);
    float gg = bfr(1.0f / (1.0f + expf(-bfr(half_sum(g.sum))))), og = bfr(1.0f - gg);
    const int tau = (gi & 64) | ((gi & 12) << 2) | ((gi & 48) >> 2) | (gi & 3);
    unsigned go = (lane & 31) ? ~0u : OFF_G + (tau & 15) * 1024 + (lt * 8 + (tau >> 4)) * 8;
    PIN(gg); PIN(og); PIN(go);
    __builtin_amdgcn_s_setprio(PRIOD);
    TS(53);
    auto c01 = [&]() INL {
        await<1>(fb + F_FREE, 8u << 20);
        c_chunk<C0S>(Cre, Cim, lb, gb, da_of(pt), lw, lane, 0);
        if (C0E != 2) c_chunk<C0S>(Cre, Cim, lb, gb, da_of(pt), lw, lane, 1);
    };
    if (C0E) c01();
    const unsigned em = await<1>(fb + F_EM, 0x10000u << lw);
    TS(3);
#pragma unroll
    for (int i = 0; i < 4; ++i) ut[i] = trfrag(ub + i * 1024, 32);
    const float su = __uint_as_float((unsigned)(258 - u_exp(em)) << 23);
    tf[0] *= su; tf[1] *= su; PIN(tf[0]); PIN(tf[1]);
#pragma unroll
    for (int i = 0; i < 4; ++i) {
        f32x4 r = mfb(ut[i], mt[2], Z4), m = mfb(ut[i], mt[3], Z4);
        r = mfb(ut[i], mt[0], r); m = mfb(ut[i], mt[1], m);
        f32x4 yr, yi;
#pragma unroll
        for (int e = 0; e < 4; ++e) {
            const float tc = tf[e >> 1][2 * (e & 1)], ts = tf[e >> 1][2 * (e & 1) + 1];
            yr[e] = fmaf(r[e], tc, -m[e] * ts); yi[e] = fmaf(r[e], ts, m[e] * tc);
        }
        lst<u32x4>(ea + i * 16, pk8(yr, yi));
    }
    if (gb == XG) {
        const unsigned sg = 0xbf803f80u;
#pragma unroll
        for (int i = 0; i < 4; ++i) {
            const f32x4 e = mfb(ut[i], c == 0 ? (u32x4){sg, sg, 0, 0} : (u32x4){0, 0, sg, sg}, Z4);
            if (c < 2) lst<u32x4>(ea + 16 * E1P + i * 16, pk8(e * su, Z4));
        }
    }
    sig(fb + F_MID, 1u);
    TS(54);
    __builtin_amdgcn_s_setprio(PRIOM);
    if (C0L < 2 && !C0E) await<1>(fb + F_FREE, 8u << 20);
    if (CGW) await<1>(fb + F_ST, 0x4000u);
    if (C0L < 2 && !C0E) c_chunk<C0S>(Cre, Cim, lb, gb, da_of(pt), lw, lane, 0);
    if (C0L < 1 && C0E != 1) c_chunk<C0S>(Cre, Cim, lb, gb, da_of(pt), lw, lane, 1);
    __builtin_amdgcn_raw_buffer_store_b64((u32x2){__float_as_uint(gg), __float_as_uint(og)}, mkrsrc(ws, WS_END), go, 0, SC1);
    TS(52);
}

DEV unsigned ldw(rsrc_t r, unsigned vo, unsigned so) { return __builtin_amdgcn_raw_buffer_load_b32(r, vo, so, 0); }
DEV unsigned amax4(u32x4 p) { return pkmax(pkmax(p.x & 0x7fff7fffu, p.y & 0x7fff7fffu), pkmax(p.z & 0x7fff7fffu, p.w & 0x7fff7fffu)); }

DEV void role_aux(const u16* __restrict__ Cre, const u16* __restrict__ Cim, const u16* __restrict__ w_r, const u16* __restrict__ w_i, const u16* __restrict__ lam_re, const u16* __restrict__ lam_im,
                  const u16* __restrict__ log_dt, char* ws, lptr smem, int gb, int aw, int lane, Tab& tab TSARG) {
    int lt, pt; tile_of(gb, lt, pt);
    int mp, j1; slot_of(gb, mp, j1);
    const int c = lane & 15, q = lane >> 4;
    const bool spc = mp == 0 && !(j1 & 1);
    const rsrc_t rs = mkrsrc(ws, WS_END), rr = mkrsrc(w_r, S_ * KF * 2), ri = mkrsrc(w_i, S_ * KF * 2);
    TS(0);
    __builtin_amdgcn_s_setprio(PRIOA);
    u32x4 fch, fsh;
    const bool m0 = 16 * aw + c == 0;
    {
        const float th = (float)(16 * aw + c) * (1.0f / 128.0f);
#pragma unroll
        for (int i = 0; i < 4; ++i) {
            const float t0 = __builtin_amdgcn_fractf(th * (float)(16 * (i >> 1) + 2 * (i & 1) + 4 * q)), t1 = t0 + th;
            const float c0 = __builtin_amdgcn_cosf(t0), c1 = __builtin_amdgcn_cosf(t1);
            const float s0 = m0 ? 1.f : -__builtin_amdgcn_sinf(t0), s1 = m0 ? -1.f : -__builtin_amdgcn_sinf(t1);
            fch[i] = pkb(c0, c1); fsh[i] = pkb(s0, s1);
        }
        PIN(fch); PIN(fsh);
    }
    unsigned sg64 = (c & 1) ? SG : 0u, sgA = (c & 2) ? SG : 0u, sgB = ((c + 1) & 2) ? SG : 0u;
    constexpr unsigned WR = 128 * KF * 2;
    const bool xh = gb == XG && aw >= 2;
    const int c0 = 2 * (lane & 7) + xh, jb = (lane >> 3) + 4 * (aw & 2);
    const int k1 = mp ? mp : spc ? (lane & 4) * 16 : (j1 == 1 ? 0 : 64);
    const int kd = j1 + 4 * (spc ? c0 & 7 : c0) + 64 * jb;
    unsigned wo = (aw < 2 ? (k1 * KF + kd) * 2 + aw * 8 * WR : ((128 - k1) * KF + 1020 - kd) * 2 + (3 - aw) * 8 * WR) & ~3u;
    unsigned wo0 = aw < 2 ? wo : k1 || aw == 3 ? wo + 7 * WR : wo - 9 * WR;
    unsigned uoff = (lane >> 3) * 131072 + (lane & 7) * 16 + OFF_U + aw * 64 * 16384 + lt * 2048 + pt * 128;
    PIN(sg64); PIN(sgA); PIN(sgB); PIN(wo); PIN(wo0); PIN(uoff);
    unsigned lru, liu, ldu, um = 0;
    u32x2 tag;
    u32x4 fl[2];
    asm volatile("" : "=v"(fl[0]), "=v"(fl[1]));
    u32x3 wre[8], wim[8];
    u32x4 wsc = {0, 0, 0, 0};
    f32x4 vr[4], vi[4];
    float dt = 0.f, er = 0.f; float2 lam = make_float2(0.f, 0.f);
#pragma unroll
    for (int d1 = 0; d1 < 4; ++d1) { vr[d1] = Z4; vi[d1] = Z4; }
    auto wx = [&]() INL {
        if (j1 == 0 && aw == 3) {
            const int ks = (spc ? (lane >> 5) * 64 : mp) + 128 * c, ng = (S_ - ks) & (S_ - 1);
            unsigned o0 = (q & 1) ? (ng * KF + 511) * 2u : ks * KF * 2u, o1 = (q & 1) ? (ks * KF + 511) * 2u : ng * KF * 2u;
            PIN(o0); PIN(o1);
            wsc = (u32x4){ldw(rr, o0, 0), ldw(rr, o1, 0), ldw(ri, o0, 0), ldw(ri, o1, 0)};
        }
    };
    auto prm = [&]() INL {
        lru = ldw(mkrsrc(lam_re, 2048), (lane >> 1) * 4, pt * 128); liu = ldw(mkrsrc(lam_im, 2048), (lane >> 1) * 4, pt * 128);
        ldu = ldw(mkrsrc(log_dt, 2048), (lane >> 1) * 4, pt * 128);
    };
    static_assert(UFS == 6 && US + 3 / UPS < 6 && CTS + 3 * CTD < UCK, "");
    if (PRS < 0) prm();
    const unsigned fb = ldsa(smem + A_FLG);
    bool uok = false;
    auto upub = [&]() INL { sig(fb + F_EM, (wave_pkmax(pkmax(pkmax(fl[0].x, fl[0].z), pkmax(fl[1].x, fl[1].z))) & 0xffu) | 0x8000u); };
    u32x4 uo[4];
    f32x4 pq[4];
    asm volatile("" : "=v"(pq[0]), "=v"(pq[1]), "=v"(pq[2]), "=v"(pq[3]));
    sfor<0, 14>([&](auto ic) INL {
        constexpr int kt = decltype(ic)::v;
        constexpr int GS[4] = GSTEP, KSO[4] = {0, 2, 3, 1}, JO[4] = {0, 2, 1, 3}, WRW[5] = WROW;
        auto ust = [&](int k) INL {
#pragma unroll
            for (int u = 0; u < 4; ++u) if (US + u / UPS == k && !(XM & 2)) st128<SC1>(uo[u], rs, uoff + JO[u] * 32768);
        };
        auto mem = [&]() INL {
            if (kt >= 10) {
#pragma unroll
                for (int r = 0; r < 8; ++r) {
                    if (r < WRW[kt < 10 ? 0 : kt - 10] || r >= WRW[kt < 10 ? 0 : kt - 9]) continue;
                    const unsigned so = r ? (aw < 2 ? r : 7 - r) * WR : 0u;
                    wre[r] = __builtin_amdgcn_raw_buffer_load_b96(rr, r ? wo : wo0, so, 0); wim[r] = __builtin_amdgcn_raw_buffer_load_b96(ri, r ? wo : wo0, so, 0);
                    if (r + 1 == URS) { SCHED(); wait_vm<2 * (URS - WRW[UCK - 10])>(); sig(fb + F_EM, 0x10000u << aw); TS(6); SCHED(); }
                }
            }
            if (kt == PRS) prm();
            if (kt == FTS && aw == 3)
                asm volatile("s_nop 0\n\tbuffer_load_dword %0, %1, 0 offen sc1 lds" :: "v"(lane * 64), "s"(mkdesc(ws + OFF_G + gb * FTB, FTB)), "{m0}"(ldsa(smem + A_AGG + 256)) : "memory");
            if (kt >= CTS && kt - CTS == CTD * aw)
                asm volatile("s_nop 0\n\tbuffer_load_dword %0, %1, %2 offen lds" :: "v"(lane * 64), "s"(mkdesc((aw & 1) ? Cim : Cre, D_ * D_ * 2)), "s"(gb * 8192 + (aw >> 1) * 4096),
                             "{m0}"(ldsa(smem) + (kt < 6 ? A_AGG + 256 : A_XT + aw * 4096)) : "memory");
            if (kt == WXS) wx();
            if (kt == UPL && UPL < 13 && aw == 2) flag_get(rs, OFF_FU, gb, lane, fl);
        };
        if (kt == UFS) wait_vm<0>();
        if (kt == UFS + 1) { uoff = (lane >> 2) * 128 + (lane & 3) * 16; PIN(uoff); }
        if (kt == UCK && UPRE && aw == 2) { uok = flag_ok(fl, tag); if (uok) { upub(); LDS_WAIT(); } }
        TL(8 + kt);
        BAR();
        if (VL == 2) ust(kt - 1);
        if (kt == 13 && aw == 1) {
            lst<f32x4>(smem + A_PW + lane * 16, (f32x4){1.f, 0.f, 1.f, 0.f});
#pragma unroll
            for (int i = 0; i < 4; ++i) lst<f32x4>(smem + A_PW + (i + 1) * 1024 + lane * 16, pq[i]);
            sig(fb + F_ST, 0x100u);
        }
        if (kt == UCK) {
            if (aw == 2 && !uok) {
                int spin;
                if (UPL > 12) { if (UDL) __builtin_amdgcn_s_sleep(UDL); flag_get(rs, OFF_FU, gb, lane, fl); }
#pragma unroll 1
                for (spin = 0; spin < (1 << 20); ++spin) { if (flag_ok(fl, tag)) break; flag_get(rs, OFF_FU, gb, lane, fl); }
                upub();
            }
            await<0>(fb + F_EM, 0x8000u);
            const u32x4 rw = mkdesc(ws, WS_END);
#pragma unroll
            for (int i = 0; i < 4; ++i)
                dma16s(rw, ldsa(smem + A_XT) + aw * 4096 + i * 1024, uoff, OFF_U + ((mp >> 1) * 4 + j1) * 32768 + (mp & 1) * 64 + aw * 8192 + i * 2048);
            TS(2);
        }
        if (kt == ATS) mid_tables(smem, gb, aw, lane, tab);
        u32x4 xa[4][4];
#pragma unroll
        for (int g = 0; g < 4; ++g) {
            if (GS[g] != kt) continue;
#pragma unroll
            for (int i = 0; i < 4; ++i) xa[g][i] = trfrag(smem + A_XT + i * 4096 + KSO[g] * 1024 + lane * 8, 512);
        }
        if (VL != 1) mem();
        SCHED();
#pragma unroll
        for (int g = 0; g < 4; ++g) {
            if (GS[g] != kt) continue;
#pragma unroll
            for (int i = 0; i < 4; ++i) { vr[i] = mfb(xa[g][i], fch, vr[i]); vi[i] = mfb(xa[g][i], fsh, vi[i]); PIN(vr[i]); PIN(vi[i]); }
            if (g == 0 || g == 2) { fch ^= sg64; fsh ^= sg64; PIN(fch); PIN(fsh); }
            if (g == 1) {
                const bool od = c & 1;
                const u32x4 a = (od ? fsh : fch) ^ sgA, b = (od ? fch : fsh) ^ sgB;
                fch = a; fsh = b; PIN(fch); PIN(fsh);
            }
        }
        if (kt == B1S) {
            const f32x4 t0r = vr[0] + vr[2], t1r = vr[0] - vr[2], t2r = vr[1] + vr[3], t3r = vr[1] - vr[3];
            const f32x4 t0i = vi[0] + vi[2], t1i = vi[0] - vi[2], t2i = vi[1] + vi[3], t3i = vi[1] - vi[3];
            vr[0] = t0r; vr[1] = t1r; vr[2] = t2r; vr[3] = t3r; vi[0] = t0i; vi[1] = t1i; vi[2] = t2i; vi[3] = t3i;
#pragma unroll
            for (int i = 0; i < 4; ++i) { PIN(vr[i]); PIN(vi[i]); }
        }
#pragma unroll
        for (int u = 0; u < 4; ++u) {
            if (US + u / UPS != kt) continue;
            const int j = JO[u];
            f32x4 a, b;
            if (j == 0) { a = vr[0] + vr[2]; b = vi[0] + vi[2]; }
            if (j == 2) { a = vr[0] - vr[2]; b = vi[0] - vi[2]; }
            if (j == 1) { a = vr[1] + vi[3]; b = vi[1] - vr[3]; if (aw == 0) { if (m0) { a = vr[1]; b = -vr[3]; } } }
            if (j == 3) { a = vr[1] - vi[3]; b = vi[1] + vr[3]; if (aw == 0) { if (m0) { a = vi[1]; b = vi[3]; } } }
            uo[u] = quads(smem + A_XT + aw * 4096 + j * 1024, lane, (u32x2){pkb(a[0], a[1]), pkb(a[2], a[3])}, (u32x2){pkb(b[0], b[1]), pkb(b[2], b[3])});
            uo[u].x = (uo[u].x + ((uo[u].x >> 1) & 1u)) & ~1u;
            um = pkmax(um, amax4(uo[u]));
            PIN(um); PIN(uo[u]);
#pragma unroll
            for (int i = 0; i < 4; ++i) if (u < 3 && ((i & 1) || u == 0)) { PIN(vr[i]); PIN(vi[i]); }
        }
        if (kt == UFS - 1) {
            um = wave_pkmax(pkmax(um, um >> 16));
            if (lane == 0) lst<unsigned>(smem + A_FLG + F_MAX + aw * 4, um);
            tag = lld<u32x2>(smem + A_FLG + F_TAG); PIN(tag);
        }
        if (kt == P0S) {
            PIN(lru); PIN(liu); PIN(ldu);
            if (lane & 1) { lru >>= 16; liu >>= 16; ldu >>= 16; }
            PIN(lru); PIN(liu); PIN(ldu);
        }
        if (kt >= P0S && kt < P0S + 5 && aw == 1) {
            const float lr = lo2f(lru), li = lo2f(liu);
            if (kt == P0S) { dt = bfr(expf(lo2f(ldu))); er = expf(dt * lr); PIN(dt); PIN(er); }
            if (kt == P0S + 1) { float sn, cs; sincosf(dt * li, &sn, &cs); lam = make_float2(er * cs, er * sn); PIN(lam.x); PIN(lam.y); }
            if (kt == P0S + 2) {
                const float nr = lam.x - 1.0f, ni = lam.y, den = lr * lr + li * li;
                pq[3][0] = (nr * lr + ni * li) / den; pq[3][1] = (ni * lr - nr * li) / den;
                PIN(pq[3]);
            }
            if (kt == P0S + 3) {
                float2 l16 = lam;
#pragma unroll
                for (int i = 0; i < 4; ++i) l16 = cmul(l16, l16);
                const float2 l32 = cmul(l16, l16), l48 = cmul(l32, l16);
                lst<f32x4>(smem + A_PRM + lane * 32, (f32x4){lam.x, lam.y, l16.x, l16.y});
                lam = cmul(l32, l32);
                pq[0][0] = l16.x; pq[0][1] = l16.y; pq[1][0] = l32.x; pq[1][1] = l32.y; pq[2][0] = l48.x; pq[2][1] = l48.y;
                PIN(pq[0]); PIN(pq[1]); PIN(pq[2]); PIN(lam.x); PIN(lam.y);
            }
            if (kt == P0S + 4) {
                const float2 l128 = cmul(lam, lam), l256 = cmul(l128, l128), l384 = cmul(l256, l128), l512 = cmul(l256, l256), cl = cmul(make_float2(pq[3][0], pq[3][1]), lam);
                lst<f32x4>(smem + A_PRM + lane * 32 + 16, (f32x4){lam.x, lam.y, l512.x, l512.y});
                pq[0][2] = l128.x; pq[0][3] = l128.y; pq[1][2] = l256.x; pq[1][3] = l256.y; pq[2][2] = l384.x; pq[2][3] = l384.y; pq[3][2] = cl.x; pq[3][3] = cl.y;
                PIN(pq[0]); PIN(pq[1]); PIN(pq[2]); PIN(pq[3]);
            }
        }
        SCHED();
        if (kt == UFS && aw == 0) {
            const u32x4 m4 = lld<u32x4>(smem + A_FLG + F_MAX);
            const unsigned m = pkmax(pkmax(m4.x, m4.y), pkmax(m4.z, m4.w));
            flag_put(rs, OFF_FU, gb, (u32x2){(tag.x & 0xffff0000u) | ((m & 0xffffu) >> 7), tag.y}, lane);
        }
        if (VL != 2) ust(kt);
        if (VL == 1) mem();
        LDS_WAIT();
        SCHED();
    });
    TS(54);
    if (gb == XG && aw == 0 && lane == 0) __builtin_amdgcn_raw_buffer_store_b64(tag, rs, 0, OFF_E, SC1);
    __builtin_amdgcn_s_setprio(PRIOM);
    if (ATS == 14) mid_tables(smem, gb, aw, lane, tab);
    await<1>(fb + F_FREE, 8u << 20);
    TS(3);
    unsigned mx = 0;
    const bool skip = j1 == 0 && (lane & (spc ? 59 : 63)) == 0 && !xh, x8 = xh && (lane & 3) == 3;
    lptr wd[2];
    wd[0] = smem + A_W + (aw & 1) * 512 + c0 * 1024 + ((jb ^ c0) << 4);
    wd[1] = smem + A_W + (aw & 1) * 512 + (x8 ? 16384 + (lane & 4) * 256 + (jb << 4) : (c0 + 1) * 1024 + ((jb ^ (c0 + 1)) << 4));
    if (skip) wd[0] = smem + A_W + 18432;
    if (aw >= 2) { lptr t = wd[0]; wd[0] = wd[1]; wd[1] = t; }
    const unsigned wsel = (mp + j1) & 1 ? 0x07060302u : 0x05040100u;
    PIN(wd[0]); PIN(wd[1]);
    u32x4 wp[2][2];
#pragma unroll
    for (int i = 0; i < 2; ++i) {
#pragma unroll
        for (int e = 0; e < 4; ++e) {
            wp[0][i][e] = __builtin_amdgcn_perm(wim[4 * i + e].x, wre[4 * i + e].x, wsel);
            wp[1][i][e] = __builtin_amdgcn_perm(wim[4 * i + e].z, wre[4 * i + e].z, wsel);
        }
        lst<u32x4>(wd[0] + i * 256, wp[0][i]); lst<u32x4>(wd[1] + i * 256, wp[1][i]);
        mx = pkmax(mx, pkmax(amax4(wp[0][i]), amax4(wp[1][i])));
    }
    if (j1 == 0) {
        if (aw == 3) {
            PIN(wsc);
            const unsigned sel = (q & 1) ? 0x07060302u : 0x05040100u, p0 = __builtin_amdgcn_perm(wsc.z, wsc.x, sel), p1 = __builtin_amdgcn_perm(wsc.w, wsc.y, sel);
            mx = pkmax(mx, pkmax(p0 & 0x7fff7fffu, p1 & 0x7fff7fffu));
            const int it = spc ? (lane >> 5) * 8 : 0;
            lst<unsigned>(smem + A_W + it * 1024 + (c >> 2) * 256 + ((((q & 1) * 8) ^ it) << 4) + (c & 3) * 4,
                          pkb(0.5f * (lo2f(p0) + lo2f(p1)), 0.5f * (hi2f(p0) - hi2f(p1))));
        }
        if (gb == XG && (xh ? x8 : !(lane & 3) && !skip)) {
            lptr wm = smem + A_W + (xh ? 16384 + (lane & 4) * 256 + ((15 - jb) << 4) : c0 * 1024 + (((16 - jb) ^ c0) << 4));
#pragma unroll
            for (int r = 0; r < 8; ++r) {
                const int km = (16 - ((lane >> 2) & 1) - 8 * (aw & 1) - r) & 15;
                lst<unsigned>(wm + (km >> 2) * 256 + (km & 3) * 4, wp[0][r >> 2][r & 3]);
            }
        }
    }
    mx = wave_pkmax(pkmax(mx, mx >> 16));
    if (lane == 0) lst<unsigned>(smem + A_FLG + F_WM + aw * 4, mx);
    sig(ldsa(smem + A_FLG + F_MID), 1u);
    wait_vm<0>();
    SCHED();
}

DEV int role_mid(const u16* __restrict__ Cre, const u16* __restrict__ Cim, char* ws, lptr smem, int gb, int w, int lane, u32x2& tag, const Tab& tab TSARG) {
    int mp, j1; slot_of(gb, mp, j1);
    const int c = lane & 15, q = lane >> 4;
    const bool xtr = gb == XG, ld = w >= 4;
    const rsrc_t rs = mkrsrc(ws, WS_END);
    const unsigned lb = ldsa(smem), fb = lb + A_FLG;
    const CM &sf = tab.sf, &si = tab.si, &ff = tab.ff, &fi = tab.fi;
    const f32x2 (&ti)[2] = tab.ti;
    lptr mb = smem + A_MAT + lane * 16;
    u32x4 wv[2];
    TS(4);
    await<MSL>(fb + F_MID, 24u);
    u32x4 dv[2];
#pragma unroll
    for (int i = 0; i < 2; ++i) dv[i] = lld<u32x4>(smem + A_E1 + (2 * w + i) * E1P + lane * 16);
    const u32x4 m4 = lld<u32x4>(smem + A_FLG + F_WM);
    tag = lld<u32x2>(smem + A_FLG + F_TAG);
    const unsigned em = lld<unsigned>(smem + A_FLG + F_EM);
#pragma unroll
    for (int i = 0; i < 2; ++i) wv[i] = lld<u32x4>(smem + A_W + (2 * w + i) * 1024 + ((lane ^ (2 * w + i)) << 4));
#pragma unroll
    for (int i = 0; i < 2; ++i) dv[i] = pk8(mfh(dv[i], ff.re, Z4), mfh(dv[i], ff.im, Z4));
    const unsigned ew = min((pkmax(pkmax(m4.x, m4.y), pkmax(m4.z, m4.w)) & 0xffffu) >> 7, 245u);
    const float sw = __uint_as_float((246u - ew) << 23), swi = c < 8 ? sw : -sw;
    wv[0] = wcvt(wv[0], sw, swi); wv[1] = wcvt(wv[1], sw, swi);
#pragma unroll
    for (int i = 0; i < 2; ++i) dv[i] = wmul(mfh(sf.re, dv[i], Z4), mfh(sf.im, dv[i], Z4), wv[i]);
#pragma unroll
    for (int i = 0; i < 2; ++i) dv[i] = pk8(mfh(dv[i], si.re, Z4), mfh(dv[i], si.im, Z4));
    {
        f32x4 r[2], m[2];
#pragma unroll
        for (int i = 0; i < 2; ++i) {
            const f32x4 a = mfh(dv[i], fi.re, Z4), b = mfh(dv[i], fi.im, Z4);
#pragma unroll
            for (int e = 0; e < 4; ++e) { r[i][e] = fmaf(a[e], ti[i][0], b[e] * ti[i][1]); m[i][e] = fmaf(b[e], ti[i][0], -a[e] * ti[i][1]); }
        }
#pragma unroll
        for (int e = 0; e < 4; ++e)
            lst<u32x2>(smem + A_XT + (4 * q + e) * 1024 + (w >> 1) * 256 + c * 16 + (w & 1) * 8, (u32x2){pkh(r[0][e], r[1][e]), pkh(m[0][e], m[1][e])});
    }
    if (xtr && (w & 6) == 4) {
        const int it = 5 - w;
        const CM f = cexp(tab.xf), sf2 = cexp(it ? sf.im : lld<u32x4>(mb + 4096)), si2 = cexp(it ? si.im : lld<u32x4>(mb + 5120));
        u32x4 d = lld<u32x4>(smem + A_E1 + (16 + it) * E1P + lane * 16);
        d = pk8(mfh(d, f.re, Z4), mfh(d, f.im, Z4));
        d = wmul(mfh(sf2.re, d, Z4), mfh(sf2.im, d, Z4), wcvt(lld<u32x4>(smem + A_W + 16384 + it * 1024 + lane * 16), sw, swi));
        d = pk8(mfh(d, si2.re, Z4), mfh(d, si2.im, Z4));
        const f32x4 r = mfh(d, cexp(tab.xb).re, Z4);
#pragma unroll
        for (int e = 0; e < 4; ++e) lst<float>(smem + A_EX + (it * 256 + (4 * q + e) * 16 + c) * 4, r[e]);
    }
    TS(5);
    sig(fb + F_MID, 1u);
    auto clate = [&]() INL {
        if (ld) {
            int lt, pt; tile_of(gb, lt, pt);
            if (C0L > 1) c_chunk<C0S>(Cre, Cim, lb, gb, da_of(pt), w - 4, lane, 0);
            c_chunk<C0S>(Cre, Cim, lb, gb, da_of(pt), w - 4, lane, 1);
        }
    };
    if (C0L && C0P == 0) clate();
    await<0>(fb + F_MID, 16u);
    auto cearly = [&]() INL {
        if (ld) {
            int lt, pt; tile_of(gb, lt, pt);
            await<0>(fb + F_ST, 0x4000u);
            c_chunk<CES>(Cre, Cim, lb, gb, da_of(pt), w - 4, lane, 2);
            if (CE > 1 && w < 7) c_chunk<CES>(Cre, Cim, lb, gb, da_of(pt), w - 4, lane, 3);
        }
    };
    if (CE && !CEL) cearly();
    {
        f32x4 xa[2], xe[2];
#pragma unroll
        for (int i = 0; i < 2; ++i) { xa[i] = Z4; xe[i] = Z4; }
        if (xtr) {
            const float sg = (c & 1) ? -0.125f : 0.125f;
#pragma unroll
            for (int i = 0; i < 2; ++i) {
                xa[i] = lld<f32x4>(smem + A_EX + (2 * w + i) * 64 + q * 16) * sg;
                xe[i] = lld<f32x4>(smem + A_EX + 1024 + (2 * w + i) * 64 + q * 16) * -sg;
            }
        }
#pragma unroll
        for (int i = 0; i < 2; ++i) {
            const int s2 = 2 * w + i;
            const u32x4 ea = lld<u32x4>(smem + A_XT + s2 * 1024 + lane * 16);
            const f32x4 ta = mfh(ea, tab.mt[0], xa[i]), tb = mfh(ea, tab.mt[1], xe[i]);
            u32x4 tq = quads(smem + A_XT + s2 * 1024, lane, (u32x2){pkh(ta[0], ta[1]), pkh(ta[2], ta[3])}, (u32x2){pkh(tb[0], tb[1]), pkh(tb[2], tb[3])});
            PIN(tq);
            if (C0L && C0P == 1 && i == 0) clate();
            if (CE && CEL && i == 0) cearly();
            if (TWH && i == 0) await<1>(fb + F_HC, TWH);
            if (CB >= 2 && i == 0 && ld && !late(gb)) {
                int lt, pt; tile_of(gb, lt, pt);
                c_chunk(Cre, Cim, lb, gb, da_of(pt), w - 4, lane, 2);
                if (CB == 2) c_chunk(Cre, Cim, lb, gb, da_of(pt), w - 4, lane, 3);
            }
            st128<SC1>(tq, rs, (lane >> 3) * 32768 + (lane & 7) * 16 + OFF_T + s2 * 262144 + j1 * 8192 + mp * 128);
        }
    }
    TS(7);
    tag.x = (tag.x & 0xffff0000u) | ew;
    return u_exp(em) - 267;
}

DEV void t_done(rsrc_t rs, unsigned fb, int gb, u32x2 tag, int lane) {
    int mp, j1; slot_of(gb, mp, j1);
    if (sig_rtn(fb + F_TC) == 7u) flag_put(rs, OFF_FT, (mp >> 3) * 32 + j1 * 8 + (mp & 7), tag, lane);
}

DEV void role_cload(const u16* __restrict__ Cre, const u16* __restrict__ Cim, char* ws, lptr smem, int gb, int lw, int lane, u32x2 tag TSARG) {
    int lt, pt; tile_of(gb, lt, pt);
    pt = da_of(pt);
    const unsigned lb = ldsa(smem), fb = lb + A_FLG;
    __builtin_amdgcn_s_setprio(3);
    static_assert(!CB || (TWH == 4 && !CE), "");
    if (CB == 1) c_chunk(Cre, Cim, lb, gb, pt, lw, lane, 2);
    if (CB & 1) c_chunk(Cre, Cim, lb, gb, pt, lw, lane, 3);
    wait_vm<CB == 1 ? 16 : CB == 3 ? 8 : 0>();
    t_done(mkrsrc(ws, WS_END), fb, gb, tag, lane);
    TS(44);
    sig(fb + F_FULL, CE > 1 && lw < 3 ? 0x1111u : CE ? 0x111u : 0x11u);
    auto hpoll = [&](unsigned m, unsigned end) INL {
        int spin;
#pragma unroll 1
        for (spin = 0; spin < (1 << 20) && (m & end) != end; ++spin) {
            m = h_poll<2>(mkrsrc(ws, WS_END), gb, pt & 1, lane, tag);
            sig_or(fb + F_FULL, m);
            if (spin == 0) TS(16);
        }
    };
    if (TWH != 4) await<2>(fb + F_HC, 4u);
    if (HPL == 1 && lw == HPW) { if (PDL) __builtin_amdgcn_s_sleep(PDL); TS(19); hpoll(0, CE > 1 ? 0x88888888u : 8u); }
    if (HPL == 1 && CW) await<1>(fb + F_FULL, 8u);
    if (!CE && !CB) c_chunk(Cre, Cim, lb, gb, pt, lw, lane, 2);
    if (CE < 2 || lw == 3) {
        if (CB == 4) {
            if (!late(gb)) sig(fb + F_FULL, 0x100u);
            await<1>(fb + F_FULL, 8u);
            if (late(gb)) { __builtin_amdgcn_s_sleep(C2D); c_chunk(Cre, Cim, lb, gb, pt, lw, lane, 2); }
            else if (C3D) __builtin_amdgcn_s_sleep(C3D);
        }
        if (!(CB & 3)) c_chunk<C3S>(Cre, Cim, lb, gb, pt, lw, lane, 3);
        if (late(gb)) { wait_vm<8>(); sig(fb + F_FULL, 0x100u); }
        wait_vm<0>();
        sig(fb + F_FULL, CE || CB == 4 ? 0x1000u : 0x1100u);
    }
    TS(47);
    if (lw == HPW && !(HPL == 1 && CE > 1)) hpoll(await<2>(fb + F_FULL, 8u) & 0x88888888u, 0x88888888u);
    const unsigned sa = fb + ((lw & 1) ? F_FB : F_FULL);
    unsigned sv = 0;
#pragma unroll 1
    for (int i = 4; i < 8; ++i) {
        const int j = (lw & 1) ? 11 - i : i;
        await_all<2>(fb + F_RD + 4 * (i - 4) + 16 * (lw & 1), 0x40404040u);
        if (!(XE & 1)) c_chunk<CSL>(Cre, Cim, lb, gb, pt, lw, lane, j, ((i + 2) & 3) * A_STAGE + (lw & 2) * 8192);
        wait_vm<8>();
        sig(sa, sv);
        sv = 2u << (4 * j);
        TS(44 + i);
    }
    wait_vm<0>();
    sig(sa, sv);
    if (HD && !(lw & 1)) {
        const u32x4 rh = mkdesc(ws, WS_END);
        unsigned vo = ((lane & 15) >> 2) * 256 + (lane >> 4) * 64 + (lane & 3) * 16; PIN(vo);
        await_all<2>(fb + F_RD + 4 * HDT, 0x40404040u);
        if (HD0) __builtin_amdgcn_s_sleep(HD0);
#pragma unroll
        for (int i = 0; i < 8; ++i) {
            const int k2 = 11 - (i >> 1), k1 = 1 - (lw >> 1);
            const unsigned so = OFF_H + lt * 524288 + (pt & 1) * 131072 + ((k2 >> 1) ^ CX(gb)) * 16384 + (k1 + (k2 & 1) * 32) * 8192 + (i & 1) * 4096;
            asm volatile("s_nop 0\n\tbuffer_load_dwordx4 %0, %1, %2 offen sc1 lds\n\tbuffer_load_dwordx4 %0, %1, %2 offen offset:1024 sc1 lds\n\t"
                         "buffer_load_dwordx4 %0, %1, %2 offen offset:2048 sc1 lds\n\tbuffer_load_dwordx4 %0, %1, %2 offen offset:3072 sc1 lds"
                         :: "v"(vo), "s"(rh), "s"(so), "{m0}"(lb + ((k2 + 2) & 3) * A_STAGE + 8192 + k1 * 16384 + (i & 1) * 4096) : "memory");
            if (HDS) __builtin_amdgcn_s_sleep(HDS);
            if (i == 5) { wait_vm<8>(); sig(fb + F_FB, 0x200u); TS(52); }
        }
        wait_vm<0>();
        sig(fb + F_FB, 0x2000u);
    }
    TSE(1, lw == 0);
}

DEV void role_last(char* ws, lptr smem, int gb, int w, int lane, u32x2 tag, int fe TSARG) {
    int s2t, dat; tile_of(gb, s2t, dat);
    dat = da_of(dat);
    const int c = lane & 15, q = lane >> 4, kh = dat & 1;
    const rsrc_t rs = mkrsrc(ws, WS_END);
    const unsigned fb = ldsa(smem + A_FLG);
    SCHED();
    u32x4 fc[2][2], fs[2][2];
    unsigned ce[2], se[2];
#pragma unroll
    for (int r = 0; r < 2; ++r) {
        float dt = (float)(32 * w + 16 * r + c) * (1.0f / 128.0f); PIN(dt);
        float cc[8], ss[8];
        const float t0 = __builtin_amdgcn_fractf(dt * (float)(4 * q)), t1 = __builtin_amdgcn_fractf(dt * (float)(16 + 4 * q));
#pragma unroll
        for (int e = 0; e < 8; ++e) { const float f = fmaf((float)(e & 3), dt, e < 4 ? t0 : t1); cc[e] = __builtin_amdgcn_cosf(f); ss[e] = __builtin_amdgcn_sinf(f); }
        fc[0][r] = (u32x4){pkh(cc[0], cc[1]), pkh(cc[2], cc[3]), pkh(cc[4], cc[5]), pkh(cc[6], cc[7])};
        fs[0][r] = (u32x4){pkh(ss[0], ss[1]), pkh(ss[2], ss[3]), pkh(ss[4], ss[5]), pkh(ss[6], ss[7])};
        PIN(fc[0][r]); PIN(fs[0][r]);
        if (r == TDR) { wait_vm<0>(); t_done(rs, fb, gb, tag, lane); TS(44); }
    }
#pragma unroll
    for (int r = 0; r < 2; ++r) {
        fc[1][r] = ((c & 1) ? fs[0][r] : fc[0][r]) ^ (((c + 1) & 2) ? SG : 0u);
        fs[1][r] = ((c & 1) ? fc[0][r] : fs[0][r]) ^ ((c & 2) ? SG : 0u);
        ce[r] = fc[0][r].x; se[r] = fs[0][r].x;
        if (q == 0) {
            ce[r] = (ce[r] & 0xffff0000u) | 0x3800u; se[r] = (se[r] & 0xffff0000u) | ((c & 1) ? 0x3800u : 0xb800u);
            fs[0][r].x = (se[r] & 0xffff0000u) | ((c & 1) ? 0xbc00u : 0x3c00u);
        }
        PIN(fc[1][r]); PIN(fs[1][r]); PIN(fs[0][r]); PIN(ce[r]); PIN(se[r]);
    }
    const int ls = lane ^ (2 * (kh & w));
    const bool sp = w == 1 && lane < 8;
    lptr tw = smem + A_XT + ((lane >> 2) & 1) * 8192 + w * 2048 + ((ls >> 1) & 1) * 1024 + (((lane >> 3) ^ (ls & 6)) << 5) + (lane & 1) * 16;
    lptr tsc = smem + A_MAT + (w >> 1) * 512 + (w & 1) * 32 + (lane >> 3) * 4, tr[4];
#pragma unroll
    for (int i = 0; i < 4; ++i) {
        tr[i] = smem + A_XT + (i >> 1) * 8192 + (i & 1) * 1024 + ((lane * 8) ^ (i * 64));
        PIN(tr[i]);
    }
    lptr aw_ = smem + A_OG + (c >> 2) * 64 + 256 * w + (c & 3) * 4, rw[4];
#pragma unroll
    for (int i = 0; i < 4; ++i) { rw[i] = smem + A_XT + w * 4096 + (c & 2) * 512 + (c >> 2) * 256 + (((4 * i + q) ^ (c & 14)) << 4) + (c & 1) * 8; PIN(rw[i]); }
    {
        const unsigned v = q == (c >> 2) ? 0x3f80u << (16 * (c & 1)) : 0u, x = (c & 2) ? 0u : v, y = v ^ x, o = q * 64 * D_ * 4 + (c >> 3) * 2048 + (c & 7) * 16;
        lst<u32x4>(smem + A_CN + w * 1024 + lane * 16, w == 0 ? (u32x4){x, y, 0, 0} : w == 1 ? (u32x4){0, 0, x, y} : (u32x4){o, o + 16 * D_ * 4, o + 32 * D_ * 4, o + 48 * D_ * 4});
    }
    unsigned o = lane * 16, o4 = (sp ? lane ^ (ls & 2) : lane) * 16, ft = fb + F_TT;
    unsigned n0 = kh && (((w & 1) + 1 - ((lane >> 1) & 1)) & 2) ? SG : 0u, n1 = kh ? n0 ^ SG : 0u, n4 = kh && (((w & 1) + 3 - ((o4 >> 5) & 1)) & 2) ? SG : 0u;
    unsigned m0 = sp && (ls & 2) ? 0u : ~0u, m4 = sp && !(ls & 2) ? 0u : ~0u;
    PIN(tw); PIN(tsc); PIN(aw_); PIN(o); PIN(o4); PIN(ft); PIN(n0); PIN(n1); PIN(n4); PIN(m0); PIN(m4);
    TS(45);
    __builtin_amdgcn_s_setprio(3);
    if (w == 0) {
        u32x4 fl[2];
        if (TP0 && !(XTP && gb == XG)) __builtin_amdgcn_s_sleep(TP0);
        TS(50);
#pragma unroll 1
        for (int spin = 0; spin < (1 << 20); ++spin) { flag_get(rs, OFF_FT, gb, lane, fl); if (flag_ok(fl, tag)) break; if (spin == 0) TS(49); if (TSL) __builtin_amdgcn_s_sleep(TSL); }
        const unsigned em = wave_pkmax(pkmax(pkmax(fl[0].x, fl[0].z), pkmax(fl[1].x, fl[1].z))) & 0xffu;
#pragma unroll
        for (int i = 0; i < 2; ++i) {
            const float a = ldexpf(1.f, (int)(fl[i].x & 0xffu) - (int)em - 1), b = ldexpf(1.f, (int)(fl[i].z & 0xffu) - (int)em - 1);
            lst<u32x2>(smem + A_MAT + i * 512 + lane * 8, (u32x2){pkh(a, a), pkh(b, b)});
        }
        sig(fb + F_TR, em | 0x8000u);
        sig(fb + F_FB, 4u);
    }
    fe += 1 + (await<1>(fb + F_TR, 0x8000u) & 0xffu);
    TS(46);
    if (XE & 4) { sig(fb + F_FB, 0x10u); TSE(w ? 4 : 5, w == 0 || w == 2); asm volatile("s_endpgm"); }
    u32x4 tv[8];
#pragma unroll
    for (int i = 0; i < 8; ++i) {
        tv[i] = __builtin_amdgcn_raw_buffer_load_b128(rs, (i == 1 ? o4 : o) + (i >> 1) * 1024, OFF_T + (s2t * 8 + (dat >> 1)) * 32768 + ((w & 1) + (i & 1) * 2) * 8192 + (w >> 1) * 4096, SC1);
        if (XE & 64) { tv[i] = fc[0][0]; PIN(tv[i]); }
        if (TLS) __builtin_amdgcn_s_sleep(TLS);
        SCHED();
    }
    if (XE & 64) __builtin_amdgcn_s_sleep(32);
    if (XE & 128) { for (int i = 0; i < 8; ++i) PIN(tv[i]); sig(fb + F_FB, 0x10u); TSE(w ? 4 : 5, w == 0 || w == 2); asm volatile("s_endpgm"); }
    u32x2 gg[2];
#pragma unroll
    for (int r = 0; r < 2; ++r) gg[r] = __builtin_amdgcn_raw_buffer_load_b64(rs, c * 8 + r * 128, OFF_G + s2t * 1024 + w * 256, SC1);
    __builtin_amdgcn_s_setprio(PRIOR);
    unsigned ts[8];
#pragma unroll
    for (int i = 0; i < 8; ++i) {
        ts[i] = lld<unsigned>(tsc + (i >> 1) * 128 + (i & 1) * 64) ^ (i == 1 ? n4 : (i & 1) ? n1 : n0);
        if (i < 2) ts[i] &= i ? m4 : m0;
        PIN(ts[i]);
    }
    auto h8 = [&](u32x4 v) INL { return __builtin_bit_cast(f16x8, v); };
#pragma unroll
    for (int i = 0; i < 8; i += 2) {
        const f16x8 t = h8(tv[i]) * h8((u32x4){ts[i], ts[i], ts[i], ts[i]});
        lst<f16x8>(tw + i * 128, __builtin_elementwise_fma(h8(tv[i + 1]), h8((u32x4){ts[i + 1], ts[i + 1], ts[i + 1], ts[i + 1]}), t));
        SCHED();
    }
    sig(ft, 1u);
    TS(47);
    float g[2];
#pragma unroll
    for (int r = 0; r < 2; ++r) { g[r] = ldexpf(__uint_as_float(gg[r].x), fe); PIN(g[r]); lst<float>(aw_ + 16 * r, 2.0f * __uint_as_float(gg[r].y)); }
    __builtin_amdgcn_s_setprio(PRT);
    await<AWS>(ft, 4u);
    TS(51);
    if (XE & 256) { sig(fb + F_FB, 0x10u); TSE(w ? 4 : 5, w == 0 || w == 2); asm volatile("s_endpgm"); }
    u32x4 tf[16];
#pragma unroll
    for (int i = 0; i < 16; ++i) { tf[i] = trfrag(tr[(i & 1) * 2 + ((i >> 1) & 1)] + ((i >> 2) & 1) * 4096 + (1 - (i >> 3)) * 2048, 512); SCHED(); }
    sig(ft, 0x10u);
    f32x4 P[2][2], O[2][2];
#pragma unroll
    for (int i = 0; i < 4; ++i) O[i >> 1][i & 1] = Z4;
    if (XE & 512) { for (int i = 0; i < 16; ++i) PIN(tf[i]); sig(fb + F_FB, 0x10u); TSE(w ? 4 : 5, w == 0 || w == 2); asm volatile("s_endpgm"); }
    if (MWT) wait_lds(MWT - 1);
    __builtin_amdgcn_s_setprio(PRX);
    SCHED();
#pragma unroll
    for (int i = 0; i < 8; ++i) {
#pragma unroll
        for (int r = 0; r < 2; ++r) O[r][i & 1] = mfh(tf[i], (i & 2) ? fs[i >> 2][r] : fc[i >> 2][r], O[r][i & 1]);
        SCHED();
        if (i == 5) {
#pragma unroll
            for (int r = 0; r < 2; ++r) { fc[0][r].x = ce[r]; fs[0][r].x = se[r]; PIN(fc[0][r]); PIN(fs[0][r]); }
            SCHED();
        }
    }
#pragma unroll
    for (int i = 0; i < 8; ++i) {
#pragma unroll
        for (int r = 0; r < 2; ++r) P[r][i & 1] = mfh(tf[8 + i], (i & 2) ? fs[i >> 2][r] : fc[i >> 2][r], i < 2 ? O[r][i] : P[r][i & 1]);
        SCHED();
    }
    __builtin_amdgcn_s_setprio(PRV);
    TS(53);
    if (XE & 1024) { for (int i = 0; i < 4; ++i) { PIN(P[i >> 1][i & 1]); PIN(O[i >> 1][i & 1]); } sig(fb + F_FB, 0x10u); TSE(w ? 4 : 5, w == 0 || w == 2); asm volatile("s_endpgm"); }
    await<0>(ft, 0x40u);
#pragma unroll
    for (int i = 0; i < 8; ++i) {
        const int r = i >> 2;
        float av[4];
#pragma unroll
        for (int e = 0; e < 4; ++e) av[e] = g[r] * __uint_as_float(pkb(0.f, (i & 2) ? fmaf(O[r][i & 1][e], -2.0f, P[r][i & 1][e]) : P[r][i & 1][e]));
        lst<u32x2>(rw[i & 3] + r * 2048, (u32x2){pkb(av[0], av[1]), pkb(av[2], av[3])});
    }
    sig(fb + F_FB, 0x10u);
    TS(48);
    asm volatile(".subsection 2\ngm_end:\n\t.fill 64, 4, 0xbf800000\n\t.subsection 0");
    TSE(w ? 4 : 5, w == 0 || w == 2);
}

NOPK __global__ __launch_bounds__(768, 3) void gm_kernel(const u16* __restrict__ x, const u16* __restrict__ Bre, const u16* __restrict__ Bim,
                                                         const u16* __restrict__ gate_w, char* ws, const u16* __restrict__ lam_re,
                                                         const u16* __restrict__ lam_im, const u16* __restrict__ log_dt,
                                                         const u16* __restrict__ w_r, const u16* __restrict__ w_i, const u16* __restrict__ Cre,
                                                         const u16* __restrict__ Cim, const u16* __restrict__ Dskip, float* __restrict__ out) {
    extern __shared__ __attribute__((aligned(16))) char smem_[];
    lptr smem = (lptr)smem_;
    unsigned t00 = 0;
    const int wid = __builtin_amdgcn_readfirstlane(threadIdx.x >> 6), lane = threadIdx.x & 63;
    u32x4 cd; unsigned cpf = 0, ct;
    asm volatile("s_getpc_b64 s[40:41]\ngm_beg:\n\ts_mov_b32 s42, gm_end - gm_beg\n\ts_mov_b32 s43, 0x20000\n\ts_and_b32 s41, s41, 0xffff\n\t"
                 "s_cmp_lg_u32 %3, %6\n\ts_cbranch_scc1 1f\n\t"
                 "s_bfe_u32 %2, %4, 0x30003\n\ts_mov_b32 m0, %7\n\ts_lshl_b32 %2, %2, 13\n\tv_lshlrev_b32 %1, 7, %5\n\tbuffer_load_dword %1, s[40:43], %2 offen lds\n1:"
                 : "=&{s[40:43]}"(cd), "=&v"(cpf), "=&s"(ct) : "s"(wid), "s"(blockIdx.x), "v"(lane), "i"(ENT ? 3 : 99), "i"(A_AGG) : "scc", "m0", "memory");
    if (ENT && (wid & 12) == 4) first_loads(x, Bre, Bim, gate_w, smem, blockIdx.x, wid - 4, lane);
    int w = wid & 7, gb = blockIdx.x;
    asm volatile("" : "+s"(w), "+s"(gb));
    if (wid < 4) role_mma(x, Dskip, ws, out, smem, blockIdx.x, wid, lane, cd, t00);
    else {
        TSD();
        u32x2 tag;
        Tab tab;
        if (wid < 8) role_load(x, Bre, Bim, Cre, Cim, gate_w, ws, smem, blockIdx.x, wid - 4, lane, tab TSPASS);
        else role_aux(Cre, Cim, w_r, w_i, lam_re, lam_im, log_dt, ws, smem, blockIdx.x, wid - 8, lane, tab TSPASS);
        const int fe = role_mid(Cre, Cim, ws, smem, gb, w, lane, tag, tab TSPASS);
        if (w >= 4) role_cload(Cre, Cim, ws, smem, gb, w - 4, lane, tag TSPASS);
        else role_last(ws, smem, gb, w, lane, tag, fe TSPASS);
    }
}

extern "C" void kernel_launch(void* const* d_in, const int* in_sizes, int n_in, void* d_out, int out_size, void* d_ws, size_t ws_size,
                              hipStream_t stream) {
    (void)hipFuncSetAttribute((const void*)gm_kernel, hipFuncAttributeMaxDynamicSharedMemorySize, SMEM);
    gm_kernel<<<256, 768, SMEM, stream>>>((const u16*)d_in[0], (const u16*)d_in[6], (const u16*)d_in[7], (const u16*)d_in[12], (char*)d_ws,
                                          (const u16*)d_in[4], (const u16*)d_in[5], (const u16*)d_in[10], (const u16*)d_in[1], (const u16*)d_in[2],
                                          (const u16*)d_in[8], (const u16*)d_in[9], (const u16*)d_in[11], (float*)d_out);
}
```

```cpp
#include <hip/hip_runtime.h>
#include <stdint.h>

typedef __attribute__((ext_vector_type(8))) __bf16 bf16x8;
typedef __attribute__((ext_vector_type(2))) __bf16 bf16x2;
typedef __attribute__((ext_vector_type(8))) _Float16 f16x8;
typedef __attribute__((ext_vector_type(2))) _Float16 f16x2;
typedef __attribute__((ext_vector_type(4))) float f32x4;
typedef __attribute__((ext_vector_type(2))) float f32x2;
typedef __attribute__((ext_vector_type(4))) unsigned u32x4;
typedef __attribute__((ext_vector_type(3))) unsigned u32x3;
typedef __attribute__((ext_vector_type(2))) unsigned u32x2;
typedef __attribute__((ext_vector_type(4))) short s16x4;
typedef __attribute__((ext_vector_type(2))) unsigned short u16x2;
typedef unsigned short u16;
typedef unsigned long long u64;
#define GAS __attribute__((address_space(1)))
#define LAS __attribute__((address_space(3)))
#define NOPK __attribute__((target("no-packed-fp32-ops")))
#define DEV __device__ __forceinline__

constexpr int S_ = 2048, D_ = 1024, KF = 513;

constexpr unsigned OFF_H = 0;
constexpr unsigned OFF_U = OFF_H + (8u << 20);
constexpr unsigned OFF_T = OFF_U + (4u << 20);
constexpr unsigned OFF_G = OFF_T + (4u << 20);
constexpr unsigned OFF_FU = OFF_G + 16384u;
constexpr unsigned OFF_FT = OFF_FU + 16384u;
constexpr unsigned OFF_FH = OFF_FT + 16384u;
constexpr unsigned OFF_GRAN = OFF_FH + 16384u;
constexpr unsigned OFF_E = OFF_GRAN + 16u * 512u * 16u;
constexpr unsigned WS_END = OFF_E + 128u;

#define TL(i)
#define TSD()
#define TS(i)
#define TS0()
#define TC(i)
#define TSE(slot, on)

DEV unsigned pkb(float a, float b) { f32x2 v = {a, b}; return __builtin_bit_cast(unsigned, __builtin_convertvector(v, bf16x2)); }
DEV unsigned pkh(float a, float b) { f32x2 v = {a, b}; return __builtin_bit_cast(unsigned, __builtin_convertvector(v, f16x2)); }
DEV float lo2f(unsigned p) { return __uint_as_float(p << 16); }
DEV float hi2f(unsigned p) { return __uint_as_float(p & 0xffff0000u); }
DEV float bfr(float f) { return lo2f(pkb(f, f)); }
DEV float2 cmul(float2 a, float2 b) { return make_float2(a.x * b.x - a.y * b.y, a.x * b.y + a.y * b.x); }
DEV float2 cfma(float2 a, float2 b, float2 c) { return make_float2(fmaf(a.x, b.x, fmaf(-a.y, b.y, c.x)), fmaf(a.x, b.y, fmaf(a.y, b.x, c.y))); }
DEV f32x4 mfb(u32x4 a, u32x4 b, f32x4 c) { return __builtin_amdgcn_mfma_f32_16x16x32_bf16(__builtin_bit_cast(bf16x8, a), __builtin_bit_cast(bf16x8, b), c, 0, 0, 0); }
DEV f32x4 mfh(u32x4 a, u32x4 b, f32x4 c) { return __builtin_amdgcn_mfma_f32_16x16x32_f16(__builtin_bit_cast(f16x8, a), __builtin_bit_cast(f16x8, b), c, 0, 0, 0); }
DEV unsigned pkmax(unsigned a, unsigned b) { return __builtin_bit_cast(unsigned, __builtin_elementwise_max(__builtin_bit_cast(u16x2, a), __builtin_bit_cast(u16x2, b))); }
typedef LAS char* lptr;
DEV unsigned ldsa(lptr p) { return (unsigned)(uintptr_t)p; }
template <class T> DEV T lld(lptr p) { return *(LAS const T*)p; }
template <class T> DEV void lst(lptr p, T v) { *(LAS T*)p = v; }
DEV u32x2 trread(lptr p) { return __builtin_bit_cast(u32x2, __builtin_amdgcn_ds_read_tr16_b64_v4i16((LAS s16x4*)p)); }
DEV u32x4 trfrag(lptr p, int second) { const u32x2 a = trread(p), b = trread(p + second); return (u32x4){a.x, a.y, b.x, b.y}; }
typedef __amdgpu_buffer_rsrc_t rsrc_t;
DEV u32x4 ldfrag(lptr tile, int row, int chunk) { return lld<u32x4>(tile + row * 128 + ((chunk ^ (row & 7)) << 4)); }
template <int N> DEV void wait_vm() { asm volatile("s_waitcnt vmcnt(%0)" :: "i"(N) : "memory"); }
#define LDS_WAIT() asm volatile("s_waitcnt lgkmcnt(0)" ::: "memory")
#define SCHED() __builtin_amdgcn_sched_barrier(0)
#define BAR() do { __builtin_amdgcn_s_barrier(); asm volatile("" ::: "memory"); } while (0)
#define PIN(x) asm volatile("" : "+v"(x))
template <int I> struct IC { static constexpr int v = I; };
template <int I, int N, class F> DEV void sfor(F&& f) { if constexpr (I < N) { f(IC<I>{}); sfor<I + 1, N>(f); } }
#define INL __attribute__((always_inline))
DEV rsrc_t mkrsrc(const void* p, unsigned n) { return __builtin_amdgcn_make_buffer_rsrc((void*)p, 0, n, 0x00020000); }
constexpr int SC1 = 16;
#define WTO 16
template <int AUX> DEV void st128(u32x4 v, rsrc_t rs, unsigned off) { __builtin_amdgcn_raw_buffer_store_b128(v, rs, off, 0, AUX); }
DEV void lds_add(unsigned a) { asm volatile("ds_add_u32 %0, %1" :: "v"(a), "v"(1u) : "memory"); }
DEV unsigned lds_peek(unsigned a) {
    unsigned v;
    asm volatile("ds_read_b32 %0, %1\n\ts_waitcnt lgkmcnt(0)" : "=v"(v) : "v"(a) : "memory");
    return __builtin_amdgcn_readfirstlane(v);
}
DEV void tile_of(int gb, int& ta, int& tb) {
    const int xcd = gb & 7, loc = gb >> 3;
    ta = (xcd >> 1) * 4 + (loc >> 3);
    tb = (xcd & 1) * 8 + (loc & 7);
}
#define DMAP 1
DEV int da_of(int pt) { return DMAP ? ((pt & 7) << 1) | (pt >> 3) : pt; }
DEV void rows4(float a, float& a0, float& a1, float& a2, float& a3) {
    const unsigned u = __float_as_uint(a);
    const auto r = __builtin_amdgcn_permlane32_swap(u, u, false, false);
    const auto p = __builtin_amdgcn_permlane16_swap(r[0], r[0], false, false);
    const auto s = __builtin_amdgcn_permlane16_swap(r[1], r[1], false, false);
    a0 = __uint_as_float(p[0]); a1 = __uint_as_float(p[1]); a2 = __uint_as_float(s[0]); a3 = __uint_as_float(s[1]);
}
DEV void rowsum4(float& a, float& b, float& c, float& d) {
    const auto r = __builtin_amdgcn_permlane32_swap(__float_as_uint(a), __float_as_uint(b), false, false);
    const auto t = __builtin_amdgcn_permlane32_swap(__float_as_uint(c), __float_as_uint(d), false, false);
    const auto p = __builtin_amdgcn_permlane16_swap(__float_as_uint(__uint_as_float(r[0]) + __uint_as_float(r[1])), __float_as_uint(__uint_as_float(t[0]) + __uint_as_float(t[1])), false, false);
    rows4(__uint_as_float(p[0]) + __uint_as_float(p[1]), a, c, b, d);
}
DEV float2 sel4(int q, float2 a, float2 b, float2 c, float2 d) {
    PIN(d.x); PIN(d.y);
    const float2 e = (q & 1) ? b : a, f = (q & 1) ? d : c;
    return (q & 2) ? f : e;
}
template <int R> DEV unsigned ror16(unsigned v) { return __builtin_amdgcn_update_dpp(0u, v, 0x120 + R, 0xf, 0xf, false); }
DEV unsigned wave_pkmax(unsigned m) {
    m = pkmax(m, ror16<8>(m)); m = pkmax(m, ror16<4>(m)); m = pkmax(m, ror16<2>(m)); m = pkmax(m, ror16<1>(m));
    const auto r = __builtin_amdgcn_permlane32_swap(m, m, false, false);
    m = pkmax(r[0], r[1]);
    const auto p = __builtin_amdgcn_permlane16_swap(m, m, false, false);
    return pkmax(p[0], p[1]);
}
DEV float half_sum(float v) {
    v += __uint_as_float(ror16<8>(__float_as_uint(v))); v += __uint_as_float(ror16<4>(__float_as_uint(v)));
    v += __uint_as_float(ror16<2>(__float_as_uint(v))); v += __uint_as_float(ror16<1>(__float_as_uint(v)));
    const auto p = __builtin_amdgcn_permlane16_swap(__float_as_uint(v), __float_as_uint(v), false, false);
    return __uint_as_float(p[0]) + __uint_as_float(p[1]);
}
DEV u32x4 quads(lptr buf, int lane, u32x2 p, u32x2 n) {
    lptr wp = buf + (lane & 15) * 64 + ((lane >> 4) & 1) * 8;
    const int v = (lane >> 5) ^ ((lane >> 2) & 3);
    lst<u32x2>(wp + (v << 4), p); lst<u32x2>(wp + ((v ^ 2) << 4), n);
    asm volatile("" ::: "memory");
    return lld<u32x4>(buf + ((lane ^ (lane >> 4)) << 4));
}

#define PRIOA 2
#define NREP 8
#define PRIOM 1
#define PRIOT 2
#define PRIOG 2
#define AD 8
#define NB 12
#define AD2 4
#define AG2 168
#define AD3 8
#define AG3 200
#define AD4 6
#define AG4 244
#define ENT 1
#define STG 1
#define FTS 1
#define FLN 2
#define FSL 0
#define PKR 1
#define GS0 474
#define US0 10
#define WGL 2
#define RS0 (HD ? 50 : 20)
#define RSP (HD ? 4 : 6)
#define WS0 0
#define WS1 4
#define RSD 88
#define CPD 4
#define CB 4
#define AWS 0
#define HD 0
#define HDT 5
#define HDS 1
#define HD0 0
#define C3D 14
#define C3S 1
#define POW 0
#define AW0 0
#define C0E 1
#define HST 1
#define PRT 0
#define LX 0
#define LXG 1
#define PDX 12
#define C2D 6
#define CTS 7
#define CTD 0
#define TLT -1
#define TLK 12
#define TLM 12
#define MWT 0
#define TDR 0
#define SA0 24
#define AM 1
#define CP 0
#define HP 0
#define XE 0
#define TSL 8
#define CSL 1
#define WG2 2
#define WG1 1
#define SPC 1
#define PRV 0
#define PRX 3
#define TLS 4
#define RDL 0
#define WROW {0, 0, 0, 0, 8}
#define ROT (lt & 12)
#define PRIOL 3
#define XM 0
#define UFS 6
#define PRIOR 0
#define PRS 2
#define WXS 13
#define CPF 1
#define FST 8
#define HREP 8
#define NP1 1
#define PDL 16
#define PDE 0
#define PDM 0x80
#define NB2 NB
#define XP 4
#define PU 0
#define GDL 16
#define BAL -1
#define BA2 0
#define FTC 1
#define LBS 0
#define PT 0
#define PTS 4
#define FTB 768
#define XTP 1
#define TWH 4
#define ACI 0
#define CE 0
#define CES 0
#define CEL 1
#define HPL 0
#define HPW 0
#define CW 0
#define GSP 1
#define PV0 "144"
#define PV0E "147"
#define PV1 "148"
#define PV1E "151"
#define PV2 "152"
#define PV2E "155"
#define PV3 "156"
#define PV3E "159"
#define UDL 0
#define CGW 0
#define C0S 0
#define GSL 0
#define C0L 0
#define C0P 0
#define BSL 0
#define BS2 0
#define PZ1 0
#define PZ2 0
#define PZ3 0
#define PZA 0
#define PZB 48
#define XLP 0
#define PRIOI 0
#define RST (NST - 5)
#define SIL 1
#define CPM 7
#define PRIOE 2
#define MSL 1
#define URS 6
#define UPRE 1
#define UCK 13
#define PRIOD 1
#define PRIOW 1
#define CDL 0
#define ATS 14
#define UPL 10
#define C1E 1
#define TP0 40
#define PD0 0
#define CXM 6
#define CX(g) ((g) & CXM)
#define GSTEP {0, 0, 0, 0}
#define XS 1
#define B1S 0
#define US 1
#define UPS 1
#define DS 8
#define P0S 8
#define VL 1
#define PN -1
#define PG -1
#define SNK 1
#define HFIRST 8u
#define RDPAT {0, 1, 2, -1, 3, 4, -1, 5, 6, -1, 7, -1, -1, -1, -1, -1}
#define Z4 ((f32x4){0.f, 0.f, 0.f, 0.f})
constexpr unsigned SG = 0x80008000u;
struct CM { u32x4 re, im; };
DEV u32x4 cmat1(float t0, float dt) {
    float cc[4], ss[4];
    t0 = __builtin_amdgcn_fractf(t0);
#pragma unroll
    for (int e = 0; e < 4; ++e) { const float f = fmaf((float)e, dt, t0); cc[e] = __builtin_amdgcn_cosf(f); ss[e] = __builtin_amdgcn_sinf(f); }
    return (u32x4){pkh(ss[0], ss[1]), pkh(ss[2], ss[3]), pkh(cc[0], cc[1]), pkh(cc[2], cc[3])};
}
DEV CM cexp(u32x4 v) { CM m; m.re = (u32x4){v.z, v.w, v.x ^ SG, v.y ^ SG}; m.im = v; PIN(m.re); PIN(m.im); return m; }
DEV u32x4 pk8(f32x4 r, f32x4 i) { return (u32x4){pkh(r[0], r[1]), pkh(r[2], r[3]), pkh(i[0], i[1]), pkh(i[2], i[3])}; }
DEV u32x4 wmul(f32x4 r, f32x4 i, u32x4 w) {
    float yr[4], yi[4];
#pragma unroll
    for (int e = 0; e < 4; ++e) {
        const unsigned wu = w[e];
        const f16x2 h = __builtin_bit_cast(f16x2, wu);
        yr[e] = fmaf(r[e], (float)h[0], -i[e] * (float)h[1]); yi[e] = fmaf(r[e], (float)h[1], i[e] * (float)h[0]);
    }
    return (u32x4){pkh(yr[0], yr[1]), pkh(yr[2], yr[3]), pkh(yi[0], yi[1]), pkh(yi[2], yi[3])};
}
DEV u32x4 wcvt(u32x4 w, float sr, float si) {
#pragma unroll
    for (int e = 0; e < 4; ++e) w[e] = pkh(lo2f(w[e]) * sr, hi2f(w[e]) * si);
    PIN(w);
    return w;
}
DEV u32x4 mkdesc(const void* p, unsigned n) { const u64 a = (u64)p; return (u32x4){(unsigned)a, (unsigned)(a >> 32) & 0xffffu, n, 0x00020000u}; }
DEV void dma16(u32x4 d, unsigned l, unsigned vo, unsigned so) {
    asm volatile("s_nop 0\n\tbuffer_load_dwordx4 %0, %1, %2 offen lds" :: "v"(vo), "s"(d), "s"(so), "{m0}"(l) : "memory");
}
DEV void dma64(u32x4 d, unsigned l, u32x4 vo, unsigned so) {
    asm volatile("s_nop 0\n\tbuffer_load_dwordx4 %0, %4, %5 offen lds\n\tbuffer_load_dwordx4 %1, %4, %5 offen offset:1024 lds\n\t"
                 "buffer_load_dwordx4 %2, %4, %5 offen offset:2048 lds\n\tbuffer_load_dwordx4 %3, %4, %5 offen offset:3072 lds"
                 :: "v"(vo.x), "v"(vo.y), "v"(vo.z), "v"(vo.w), "s"(d), "s"(so), "{m0}"(l) : "memory");
}
#define SPIN(x) asm volatile("" : "+s"(x))
DEV void dma16s(u32x4 d, unsigned l, unsigned vo, unsigned so) {
    asm volatile("s_nop 0\n\tbuffer_load_dwordx4 %0, %1, %2 offen sc1 lds" :: "v"(vo), "s"(d), "s"(so), "{m0}"(l) : "memory");
}
DEV void sig(unsigned a, unsigned v) { asm volatile("s_mov_b64 exec, 1\n\tds_add_u32 %0, %1\n\ts_mov_b64 exec, -1" :: "v"(a), "v"(v) : "memory"); }
template <int O> DEV void sig_o(unsigned a, unsigned v) { asm volatile("s_mov_b64 exec, 1\n\tds_add_u32 %0, %1 offset:%2\n\ts_mov_b64 exec, -1" :: "v"(a), "v"(v), "i"(O) : "memory"); }
DEV unsigned sig_rtn(unsigned a) {
    unsigned v;
    asm volatile("s_mov_b64 exec, 1\n\tds_add_rtn_u32 %0, %1, %2\n\ts_mov_b64 exec, -1\n\ts_waitcnt lgkmcnt(0)" : "=&v"(v) : "v"(a), "v"(1u) : "memory");
    return __builtin_amdgcn_readfirstlane(v);
}
template <int SLP> DEV unsigned await(unsigned a, unsigned mask) {
    unsigned v, n, r, t;
    asm volatile("s_mov_b32 %1, 0x400000\n"
                 "1:\tds_read_b32 %0, %4\n\ts_waitcnt lgkmcnt(0)\n\tv_readfirstlane_b32 %2, %0\n\ts_and_b32 %3, %2, %5\n\ts_cbranch_scc1 2f\n\t"
                 "s_sleep %6\n\ts_sub_u32 %1, %1, 1\n\ts_cbranch_scc0 1b\n"
                 "2:" : "=&v"(v), "=&s"(n), "=&s"(r), "=&s"(t) : "v"(a), "s"(mask), "i"(SLP) : "memory", "scc");
    return r;
}
#define BMF "v_mfma_f32_16x16x32_bf16 %0, %2, %3, %0\n\tv_mfma_f32_16x16x32_bf16 %1, %3, %2, %1\n\t"
template <int SLP> DEV void burn(int n, u32x4& a, u32x4& b) {
    f32x4 d0, d1;
    asm volatile("1:\t" BMF BMF "s_nop %5\n\ts_sub_u32 %4, %4, 1\n\ts_cbranch_scc0 1b\n\ts_nop 7\n\ts_nop 3"
                 : "=&v"(d0), "=&v"(d1), "+v"(a), "+v"(b), "+s"(n) : "i"(SLP) : "scc");
}
template <int SLP> DEV unsigned await_burn(unsigned ad, unsigned mask, u32x4 a, u32x4 b) {
    unsigned v, n, r, t; f32x4 d0, d1;
    asm volatile("s_mov_b32 %5, 0x400000\n"
                 "1:\tds_read_b32 %4, %8\n\t" BMF BMF "s_waitcnt lgkmcnt(0)\n\tv_readfirstlane_b32 %6, %4\n\ts_and_b32 %7, %6, %9\n\ts_cbranch_scc1 2f\n\t"
                 ".rept %10\n\t" BMF ".endr\n\ts_sub_u32 %5, %5, 1\n\ts_cbranch_scc0 1b\n"
                 "2:\ts_nop 7\n\ts_nop 3" : "=&v"(d0), "=&v"(d1), "+v"(a), "+v"(b), "=&v"(v), "=&s"(n), "=&s"(r), "=&s"(t) : "v"(ad), "s"(mask), "i"(SLP) : "memory", "scc");
    return r;
}
DEV void need_bit(unsigned a, unsigned& r, unsigned mask, int off = 0) {
    unsigned v, n, t;
    asm volatile("s_andn2_b32 %3, %5, %2\n\ts_cbranch_scc1 1f\n2:\n\t.subsection 1\n"
                 "1:\ts_mov_b32 %1, 0x400000\n"
                 "3:\tds_read_b32 %0, %4 offset:%6\n\ts_waitcnt lgkmcnt(0)\n\tv_readfirstlane_b32 %2, %0\n\ts_andn2_b32 %3, %5, %2\n\ts_cbranch_scc0 2b\n\t"
                 "s_sub_u32 %1, %1, 1\n\ts_cbranch_scc0 3b\n\ts_branch 2b\n\t.subsection 0"
                 : "=&v"(v), "=&s"(n), "+s"(r), "=&s"(t) : "v"(a), "i"(mask), "i"(off) : "memory", "scc");
}
template <int SLP> DEV void await_all(unsigned a, unsigned mask) {
    unsigned v, n, r;
    asm volatile("s_mov_b32 %1, 0x400000\n"
                 "1:\tds_read_b32 %0, %3\n\ts_waitcnt lgkmcnt(0)\n\tv_readfirstlane_b32 %2, %0\n\ts_andn2_b32 %2, %4, %2\n\ts_cbranch_scc0 2f\n\t"
                 "s_sleep %5\n\ts_sub_u32 %1, %1, 1\n\ts_cbranch_scc0 1b\n"
                 "2:" : "=&v"(v), "=&s"(n), "=&s"(r) : "v"(a), "s"(mask), "i"(SLP) : "memory", "scc");
}
#define MF2(a, b, c) asm("v_mfma_f32_16x16x32_bf16 %0, %1, %2, %0" : "+v"(c) : "v"(a), "v"(b))
DEV void flag_put(rsrc_t rs, unsigned off, int gb, u32x2 v, int lane) {
    if (lane < NREP) __builtin_amdgcn_raw_buffer_store_b64(v, rs, lane * 2048, off + gb * 8, SC1);
}
DEV void flag_get(rsrc_t rs, unsigned off, int gb, int lane, u32x4 (&fl)[2]) {
    unsigned o = lane * 16; PIN(o);
#pragma unroll
    for (int i = 0; i < 2; ++i) fl[i] = __builtin_amdgcn_raw_buffer_load_b128(rs, o + i * 1024, off + (gb & (NREP - 1)) * 2048, SC1);
}
DEV bool flag_ok(const u32x4 (&fl)[2], u32x2 tag) {
    bool ok = true;
#pragma unroll
    for (int i = 0; i < 2; ++i) ok = ok & ((fl[i].x ^ tag.x) < 0x10000u) & (fl[i].y == tag.y) & ((fl[i].z ^ tag.x) < 0x10000u) & (fl[i].w == tag.y);
    return __all(ok);
}
template <int N> DEV unsigned h_poll(rsrc_t rs, int gb, int kh, int lane, u32x2 tag) {
    unsigned o = (lane ^ ((CX(gb) & 3) << 4)) * FST, m = 0; PIN(o);
    u32x2 fl[N];
#pragma unroll
    for (int i = 0; i < N; ++i) fl[i] = __builtin_amdgcn_raw_buffer_load_b64(rs, o, OFF_FH + ((gb & (HREP - 1)) * 256 + kh * 128 + (i ^ (CX(gb) >> 2)) * 64) * FST, SC1);
#pragma unroll
    for (int i = 0; i < N; ++i) {
        const u64 b = __ballot(((fl[i].x ^ tag.x) < 0x10000u) & (fl[i].y == tag.y));
#pragma unroll
        for (int j = 0; j < 4; ++j) m |= ((unsigned)(b >> (16 * j)) & 0xffffu) == 0xffffu ? 8u << (16 * i + 4 * j) : 0u;
    }
    return m;
}
DEV void sig_or(unsigned a, unsigned v) { asm volatile("s_mov_b64 exec, 1\n\tds_or_b32 %0, %1\n\ts_mov_b64 exec, -1" :: "v"(a), "v"(v) : "memory"); }

constexpr int A_STAGE = 32768, A_XT = 4 * A_STAGE, A_PRM = A_XT + 16384, A_AGG = A_PRM + 2048, A_FLG = A_AGG + 512, A_GW = A_FLG + 128,
              A_MAT = A_GW + 2048, SMEM = A_MAT + 11264;
constexpr int E1P = 1040, A_EX = A_GW, A_E1 = 0, A_PW = 18 * E1P + 224, A_W = A_STAGE, A_HS = 2 * A_STAGE - 8192, A_OG = A_MAT + 9216, A_CN = A_MAT + 1024;
static_assert(A_PW % 256 == 0 && A_W + 18432 + 512 <= A_HS, "");
constexpr int F_MID = 8, F_EM = 12, F_MAX = 16, F_TAG = 32, F_FULL = 40, F_FREE = 44, F_HC = 48, F_FB = 52, F_TC = 56, F_TR = 60, F_TT = 64, F_ST = 76, F_WM = 80, F_RD = 96;
static_assert((A_FLG + F_RD & 0xff) == 0x60, "");

#define XGV 0xFF
constexpr int XG = XGV;
DEV bool late(int gb) { return (LX >> (gb & 7) & 1) || (LXG && (gb | 16) == (XG | 16)); }
DEV void slot_of(int gb, int& mp, int& j1) { gb ^= XG; mp = (gb & 7) * 8 + (gb >> 5); j1 = (gb >> 3) & 3; }
DEV u32x4 mat_sf(int k1, int c, int q) { const float dt = -(float)(k1 + 128 * c) * (1.0f / 2048.0f); return cmat1(dt * (float)(4 * q), dt); }
DEV u32x4 mat_si(int k1, int c, int q) { return cmat1((float)((k1 + 512 * q) * c) * (1.0f / 2048.0f), (float)c * (1.0f / 16.0f)); }

DEV void mk_tables(lptr smem, int gb, int wid, int lane) {
    int mp, j1; slot_of(gb, mp, j1);
    const int c = lane & 15, q = lane >> 4;
    const bool spc = mp == 0 && !(j1 & 1);
    const int k1 = mp ? mp : (j1 == 3 ? 64 : 0);
    lptr mt = smem + A_MAT + lane * 16;
    if (wid == 0) {
        const float dt = -(float)(j1 + 4 * (spc ? c & 7 : c)) * (1.0f / 64.0f), t0 = __builtin_amdgcn_fractf(dt * (float)(4 * q));
        float cc[4], ss[4];
#pragma unroll
        for (int e = 0; e < 4; ++e) { const float f = fmaf((float)e, dt, t0); cc[e] = __builtin_amdgcn_cosf(f); ss[e] = __builtin_amdgcn_sinf(f); }
        const unsigned c0 = pkb(cc[0], cc[1]), c1 = pkb(cc[2], cc[3]), s0 = pkb(ss[0], ss[1]), s1 = pkb(ss[2], ss[3]);
        const unsigned c0l = pkb(cc[0] - lo2f(c0), cc[1] - hi2f(c0)), c1l = pkb(cc[2] - lo2f(c1), cc[3] - hi2f(c1));
        const unsigned s0l = pkb(ss[0] - lo2f(s0), ss[1] - hi2f(s0)), s1l = pkb(ss[2] - lo2f(s1), ss[3] - hi2f(s1));
        u32x4 m[4] = {(u32x4){c0, c1, s0 ^ SG, s1 ^ SG}, (u32x4){s0, s1, c0, c1}, (u32x4){c0l, c1l, s0l ^ SG, s1l ^ SG}, (u32x4){s0l, s1l, c0l, c1l}};
        if (spc) {
            m[0] = c < 8 ? (u32x4){c0, c1, 0, 0} : (u32x4){0, 0, c0, c1}; m[1] = c < 8 ? (u32x4){s0, s1, 0, 0} : (u32x4){0, 0, s0, s1};
            m[2] = c < 8 ? (u32x4){c0l, c1l, 0, 0} : (u32x4){0, 0, c0l, c1l}; m[3] = c < 8 ? (u32x4){s0l, s1l, 0, 0} : (u32x4){0, 0, s0l, s1l};
        }
#pragma unroll
        for (int i = 0; i < 4; ++i) lst<u32x4>(mt + i * 1024, m[i]);
    } else if (wid == 1) {
        const int jq = spc ? (4 * q) & 7 : 4 * q;
        float cc[4], ss[4];
        const float t0 = __builtin_amdgcn_fractf((float)(c * (j1 + 4 * jq)) * (1.0f / 64.0f));
#pragma unroll
        for (int e = 0; e < 4; ++e) {
            const float f = fmaf((float)c, (float)e * (1.0f / 16.0f), t0);
            const float wg = !spc || (gb == XG && jq + e == 0) ? 0.125f : 0.25f;
            cc[e] = __builtin_amdgcn_cosf(f) * wg; ss[e] = -__builtin_amdgcn_sinf(f) * wg;
        }
        const unsigned c0 = pkh(cc[0], cc[1]), c1 = pkh(cc[2], cc[3]), n0 = pkh(ss[0], ss[1]), n1 = pkh(ss[2], ss[3]);
        u32x4 mia = (u32x4){c0, n0, c1, n1}, mib = (u32x4){n0, c0 ^ SG, n1, c1 ^ SG};
        if (spc) { mib = q < 2 ? (u32x4){0, 0, 0, 0} : mia ^ SG; if (q >= 2) mia = (u32x4){0, 0, 0, 0}; }
        lst<u32x4>(mt + 6 * 1024, mia); lst<u32x4>(mt + 7 * 1024, mib);
        lst<u32x4>(mt + 8 * 1024, cmat1((float)(c * q) * -0.25f, (float)c * (-1.0f / 16.0f)));
    } else {
        lst<u32x4>(mt + (2 + wid) * 1024, wid == 2 ? mat_sf(k1, c, q) : mat_si(k1, c, q));
        const int ja = (spc ? 0 : 8 * (wid - 2)) + (lane >> 3);
        const float dt = -(float)(j1 + 4 * ja) * (1.0f / 1024.0f), t0 = dt * (float)(2 * (lane & 7)), t1 = t0 + dt;
        lst<f32x4>(mt + (7 + wid) * 1024, (f32x4){__builtin_amdgcn_cosf(t0), __builtin_amdgcn_sinf(t0), __builtin_amdgcn_cosf(t1), __builtin_amdgcn_sinf(t1)});
    }
}

template <int SLP = 0> DEV void c_chunk(const u16* __restrict__ Cre, const u16* __restrict__ Cim, unsigned lb, int gb, int da, int lw, int lane, int j, int at = -1) {
    if (at < 0) at = ((j + 2) & 3) * A_STAGE + lw * 8192;
    const u32x4 rc = mkdesc(lw < 2 ? Cre : Cim, D_ * D_ * 2);
    const unsigned vo = (lane >> 3) * 8192 + (((lane & 7) ^ (lane >> 3)) << 4);
#pragma unroll
    for (int i = 0; i < 8; ++i) {
        if (SLP && i) __builtin_amdgcn_s_sleep(SLP);
        dma16(rc, lb + at + i * 1024, vo, (512 * (i & 1) + 256 * ((lw ^ da ^ 1) & 1) + 32 * (da >> 1) + (i >> 1)) * 2048 + (8 * (da & 1) + (j ^ CX(gb))) * 128);
    }
}
constexpr int KS2 = 16, KS1 = 16, F1 = 8 * KS1, FT = F1 + 4 * KS2, RT = 8, NRL = 16;
static_assert(!(NB & 1) && NB <= 16, "");
constexpr int f_k(int f) { return f < F1 ? f >> 3 : f < FT ? KS1 + ((f - F1) >> 2) : 31 - ((f - FT) >> 2); }
constexpr int f_m(int f) { return f < F1 ? f & 7 : f < FT ? (f - F1) & 3 : 4 + ((f - FT) & 3); }
constexpr int f_i(int f) { return f < F1 ? f & 7 : (f - F1) & 3; }
constexpr bool f_hi(int f) { return f_i(f) == (f < F1 ? 7 : 3); }
constexpr bool c_new(int f) { return f < FT && !f_i(f) && !(f_k(f) & 3); }
constexpr bool c_new3(int f) { return f >= FT && f < 256 && !f_i(f) && (f_k(f) & 3) == 3; }
constexpr int k_f(int k) { return k < KS1 ? 8 * k : F1 + 4 * (k - KS1); }
struct SCH {
    int a[512], p[512], w[512]; unsigned m[512];
    constexpr void chk(int s, unsigned mask, int word) { m[s] |= mask; p[s - 2] = 3; p[s - 2 - 2 * CPD] = word; }
    constexpr SCH() : a{}, p{}, w{}, m{} {
        int s = -1, as[256] = {}, cs[9] = {};
        for (int i = AD; i < 256; ++i) { const int t = 2 * (i - (i < AG2 ? AD : i < AG3 ? AD2 : i < AG4 ? AD3 : AD4)) + 1; s = t > s ? t : s + 1; a[s] = i; as[i] = s; }
        for (int c = 1; c < 8; ++c) {
            cs[c] = (as[k_f(4 * c)] - (c < 5 ? 5 : 1)) & ~1;
            if (!HD && c > 4 && c < 7 && cs[c] > 2 * k_f(2 * c + 10)) cs[c] = 2 * k_f(2 * c + 10);
            chk(cs[c], 4u << (4 * c), 1);
        }
        for (int j = (NB + 7) / 8; j < 8; ++j) {
            int c = 7;
            while (cs[c] > 2 * k_f((8 * j - NB) / 2 + 1) - 2) --c;
            chk(cs[c], 8u << (4 * j), 1);
        }
        for (int j = 7; j > 3; --j) {
            const int t = (as[FT + 16 * (7 - j)] - 1) & ~1;
            chk(t, 4u << (4 * j) | 4u | (HD && j < 6 ? 0x400u << (4 * (5 - j)) : 0u), 2);
            if (HD && j < 6 && t > 2 * FT + RS0 + RSP * 8 * (5 - j)) p[-1] = 0;
        }
        chk(GS0, 0x40u, 2);
        for (int f = 0; f < 252; f += WGL) {
            int n = 0;
            for (int i = f + WGL; i < 256; ++i) n += i < AD || as[i] < 2 * f;
            if (f && WGL > 1) w[2 * f - 1] = n + 1;
        }
    }
    constexpr bool ok() const {
        int e = 0, r = 0;
        for (int s = 0; s < 512; ++s) {
            if (p[s] == 3) { r = e; e = 0; } else if (p[s]) { if (e) return false; e = p[s]; }
            if (m[s]) { if (r != ((m[s] & 0x44u) && s > 128 ? 2 : 1)) return false; r = 0; }
        }
        return true;
    }
};
constexpr SCH ST;
static_assert(ST.ok(), "");
DEV void wait_lds(int n) { sfor<0, 16>([&](auto ic) INL { if (n == decltype(ic)::v) __builtin_amdgcn_s_waitcnt(0xC07F | decltype(ic)::v << 8); }); }
constexpr int s_f(int s) { return s < 504 ? s >> 1 : 252 + (s & 3); }
constexpr int s_n(int s) { return s < 504 ? s & 1 : (s >> 2) & 1; }
constexpr int s_b(int s) { return s >= 2 * FT && f_k(s_f(s)) < 32 - RT ? 64 + 2 * (31 - RT - f_k(s_f(s))) + s_n(s) : 2 * f_k(s_f(s)) + s_n(s); }
constexpr int h_off(int k, int n) { return ((k >> 1) & 1) * A_STAGE + (k & 1) * 16384 + n * 1024; }

DEV void role_mma(const u16* __restrict__ x, const u16* __restrict__ Dskip, char* ws, float* __restrict__ out, lptr smem, int gb, int wid, int lane, u32x4 cd, unsigned t00) {
    int lt, pt; tile_of(gb, lt, pt);
    const int wr = wid >> 1, wc = wid & 1, c = lane & 15, q = lane >> 4;
    const rsrc_t rs = mkrsrc(ws, WS_END);
    const unsigned fb = ldsa(smem + A_FLG);
    TSD(); TS0();
    u64 tag;
    if (STG) tag = *(const __attribute__((address_space(4))) u64*)(uintptr_t)(ws + OFF_E);
    else tag = __hip_atomic_load((GAS u64*)(ws + OFF_E), __ATOMIC_RELAXED, __HIP_MEMORY_SCOPE_WORKGROUP);
    static_assert(ENT && STG, "");
    if (FTS < 0 && wid == 2) asm volatile("s_nop 0\n\tbuffer_load_dword %0, %1, 0 offen sc1 lds" :: "v"(lane * 64), "s"(mkdesc(ws + OFF_G + gb * FTB, FTB)), "{m0}"(ldsa(smem + A_AGG + 256)) : "memory");
    SCHED();
    if (wid == 0 && lane < 32) lst<unsigned>(smem + A_FLG + lane * 4, 0u);
    mk_tables(smem, gb, wid, lane);
    f32x4 acc[4][4];
#pragma unroll
    for (int m = 0; m < 4; ++m)
#pragma unroll
        for (int n = 0; n < 4; ++n) acc[m][n] = Z4;
    u32x4 af[2][4], bf_[2][4];
#pragma unroll
    for (int m = 0; m < 4; ++m) { af[1][m] = (u32x4){0, 0, 0, 0}; bf_[1][m] = af[1][m]; }
    lptr fp[8];
#pragma unroll
    for (int i = 0; i < 8; ++i) { fp[i] = smem + (i & 2) * A_STAGE + ((i & 4 ? 128 + wc * 64 : wr * 64) + c) * 128 + (((4 * (i & 1) + q) ^ (c & 7)) << 4); PIN(fp[i]); }
    tag += 0x9E3779B97F4A7C15ull;
    PIN(tag);
    if (wid == 0 && lane == 0) lst<u64>(smem + A_FLG + F_TAG, tag);
    tag = (u64)(unsigned)__builtin_amdgcn_readfirstlane((unsigned)tag) | (u64)(unsigned)__builtin_amdgcn_readfirstlane((unsigned)(tag >> 32)) << 32;
    __builtin_amdgcn_s_waitcnt(0xC07F);
    TS(13);
    unsigned fst = fb + F_ST, sv = 0;
    auto step = [&](int sl, bool pf) INL {
#pragma unroll
        for (int hf = 0; hf < 2; ++hf)
#pragma unroll
            for (int i = 0; i < 16; ++i) {
                const int nn = SNK && (i & 4) ? 3 - (i & 3) : i & 3;
                acc[i >> 2][nn] = mfb(af[1 - hf][i >> 2], bf_[1 - hf][nn], acc[i >> 2][nn]);
                constexpr int RD[16] = RDPAT;
                const int r = RD[i];
                if (r == 0) af[hf][0] = lld<u32x4>(fp[(sl & 2) + hf] + (sl & 1) * A_STAGE);
                else if (r > 0 && r < 5) bf_[hf][r - 1] = lld<u32x4>(fp[4 + (sl & 2) + hf] + (sl & 1) * A_STAGE + (r - 1) * 2048);
                else if (r >= 5) af[hf][r - 4] = lld<u32x4>(fp[(sl & 2) + hf] + (sl & 1) * A_STAGE + (r - 4) * 2048);
                if (pf && hf && i == 11) sv = *(volatile LAS unsigned*)(lptr)(uintptr_t)fst;
                SCHED();
            }
        LDS_WAIT();
    };
#pragma unroll 1
    for (int kt = 0; kt < 3; ++kt) {
#pragma unroll
        for (int i = 0; i < 4; ++i) { BAR(); step(i, false); }
    }
    PIN(fst);
    unsigned agp = ldsa(smem + A_AGG) + (wc * 16 + c) * 16, tgv = (unsigned)tag, sel = 0x07060500u;
    unsigned gro = lt < 15 && q == 0 ? c * 16 + OFF_GRAN + ((15 - lt) * 512 + pt * 32 + wc * 16) * 16 : ~15u;
    PIN(agp); PIN(tgv); PIN(gro);
    wait_vm<0>();
    BAR();
    if (wr) lst<u32x4>((lptr)(uintptr_t)agp, (u32x4){0, 0, 0, 0});
    step(0, false);
    BAR(); __builtin_amdgcn_s_setprio(PRIOE); step(1, true);
#pragma unroll
    for (int i = 0; i < 2; ++i) {
        unsigned ss = __builtin_amdgcn_readfirstlane(sv);
        need_bit(fst, ss, 4u << (4 * i));
        TL(30 + i);
        if (i) need_bit(fst, ss, 0x100u);
        step(2 + i, i == 0);
    }
    __builtin_amdgcn_s_setprio(PRIOT);
    sig(fb + F_FREE, 1u << 20);
    float2 lam[2], coef[2], l16[2], l64[2], l512[2], cw[2], wq[2], lq[2];
#pragma unroll
    for (int n = 0; n < 2; ++n) {
        const f32x4 a = lld<f32x4>(smem + A_PRM + (wc * 32 + 2 * c + n) * 32), b = lld<f32x4>(smem + A_PRM + (wc * 32 + 2 * c + n) * 32 + 16);
        lam[n] = make_float2(a[0], a[1]); l16[n] = make_float2(a[2], a[3]); l64[n] = make_float2(b[0], b[1]); l512[n] = make_float2(b[2], b[3]);
    }
    float2 h[2];
#pragma unroll
    for (int m = 0; m < 5; ++m) {
#pragma unroll
        for (int n = 0; n < 4; ++n) {
            if (m < 4) acc[m][n] = mfb(af[1][m], bf_[1][n], acc[m][n]);
            if (m == 1 && n == 0) { h[0] = make_float2(acc[0][0][0], acc[0][2][0]); h[1] = make_float2(acc[0][1][0], acc[0][3][0]); }
            else if (m) {
#pragma unroll
                for (int i = 0; i < 2; ++i) h[i] = cfma(lam[i], h[i], make_float2(acc[m - 1][i][n], acc[m - 1][2 + i][n]));
            }
            if (SIL) SCHED();
        }
        if (m == 3) {
#pragma unroll
            for (int n = 0; n < 2; ++n) {
                const f32x4 a = lld<f32x4>(smem + A_PW + 4096 + (wc * 32 + 2 * c + n) * 16), b = lld<f32x4>(smem + A_PW + q * 1024 + (wc * 32 + 2 * c + n) * 16);
                coef[n] = make_float2(a[0], a[1]); cw[n] = wr ? make_float2(a[2], a[3]) : coef[n]; wq[n] = make_float2(b[0], b[1]); lq[n] = make_float2(b[2], b[3]);
            }
        }
    }
    TS(1);
    float2 loc[2], wag[2];
#pragma unroll
    for (int n = 0; n < 2; ++n) {
        float2 a0, a1, a2, a3;
        rows4(h[n].x, a0.x, a1.x, a2.x, a3.x);
        rows4(h[n].y, a0.y, a1.y, a2.y, a3.y);
        const float2 s2 = cfma(l16[n], a0, a1), s3 = cfma(l16[n], s2, a2);
        wag[n] = cfma(l16[n], s3, a3);
        loc[n] = sel4(q, make_float2(0.f, 0.f), a0, s2, s3);
    }
    float2 W0[2] = {make_float2(0.f, 0.f), make_float2(0.f, 0.f)};
    if (wr == 0) lst<u32x4>((lptr)(uintptr_t)agp, (u32x4){__float_as_uint(wag[0].x) | 1u, __float_as_uint(wag[0].y), __float_as_uint(wag[1].x), __float_as_uint(wag[1].y) | 1u});
    else {
        f32x4 w0; unsigned t, n;
        asm volatile("s_mov_b32 %2, 0x400000\n"
                     "1:\tds_read_b128 %0, %3\n\ts_waitcnt lgkmcnt(0)\n\tv_and_b32 %1, v" PV0 ", v" PV0E "\n\tv_and_b32 %1, 1, %1\n\tv_cmp_eq_u32 vcc, 0, %1\n\ts_cbranch_vccz 2f\n\t"
                     "s_sub_u32 %2, %2, 1\n\ts_cbranch_scc0 1b\n"
                     "2:" : "=&{v[" PV0 ":" PV0E "]}"(w0), "=&v"(t), "=&s"(n) : "v"(agp) : "memory", "scc", "vcc");
        W0[0] = make_float2(w0[0], w0[1]); W0[1] = make_float2(w0[2], w0[3]);
        const float2 g0 = cfma(l64[0], W0[0], wag[0]), g1 = cfma(l64[1], W0[1], wag[1]);
        auto tb_ = [&](float v, unsigned i) INL { unsigned k = sel + i; SPIN(k); return __builtin_amdgcn_perm(__float_as_uint(v), tgv, k); };
        st128<SC1>((u32x4){tb_(g0.x, 0), tb_(g0.y, 1), tb_(g1.x, 2), tb_(g1.y, 3)}, rs, gro);
    }
    TS(2);
    if (CE || CGW) sig(fst, 0x1000u);
    __builtin_amdgcn_s_setprio(PRIOW);
    const int da = da_of(pt), kh = da & 1;
    u32x2 xi[8], dk;
    auto xload = [&]() INL {
        const unsigned co = (512 * ((lane >> 3) & 1) + 4 * (lane & 7)) * 2;
        SCHED();
#pragma unroll
        for (int i = 0; i < 8; ++i)
            xi[i] = __builtin_amdgcn_raw_buffer_load_b64(mkrsrc(x, S_ * D_ * 2), (lane >> 4) * 64 * D_ * 2 + co, ((512 * wid + 256 * (i >> 2) + 16 * (i & 3) + lt) * D_ + 256 * kh + 32 * (da >> 1)) * 2, 0);
        dk = __builtin_amdgcn_raw_buffer_load_b64(mkrsrc(Dskip, D_ * 2), co, 512 * kh + 64 * (da >> 1), 0);
        SCHED();
    };
    if (XP == 0) xload();
#pragma unroll
    for (int n = 0; n < 2; ++n) {
        PIN(coef[n].x); PIN(coef[n].y);
        loc[n] = cmul(coef[n], cfma(wq[n], W0[n], loc[n]));
        wq[n] = cmul(wq[n], cw[n]);
        loc[n].y = -loc[n].y;
        PIN(wq[n].x); PIN(wq[n].y); PIN(loc[n].x); PIN(loc[n].y);
    }
    float2 u[2][16];
#pragma unroll
    for (int t = 0; t < 16; ++t) {
#pragma unroll
        for (int n = 0; n < 2; ++n) { u[n][t] = cmul(make_float2(coef[n].x, -coef[n].y), make_float2(acc[t >> 2][n][t & 3], -acc[t >> 2][2 + n][t & 3])); PIN(u[n][t].x); PIN(u[n][t].y); }
        SCHED();
    }
    lptr tb = smem + A_HS + wid * 2048 + lane * 4, rb = smem + A_HS + wid * 2048 + (lane >> 4) * 256 + (lane & 3) * 64 + ((lane >> 2) & 3) * 16;
    unsigned hoff = OFF_H + ((lane >> 4) * 64 + pt * 2 + wc) * 8192 + lt * 512 + wr * 256 + (lane & 15) * 16;
    PIN(tb); PIN(rb); PIN(hoff);
    unsigned ex[4], s0 = 0x04000400u, s1 = 0x05040100u, km = 0xffffff00u;
#pragma unroll
    for (int j = 0; j < 4; ++j) { ex[j] = 4 * j + q < lt ? (unsigned)tag : 0u; PIN(ex[j]); }
    SPIN(s0); SPIN(s1); SPIN(km);
    __builtin_amdgcn_s_setprio(PRIOT);
    await<0>(fb + F_FREE, 8u << 20);
    if (XP == 1) xload();
    if (GDL) __builtin_amdgcn_s_sleep(GDL);
    TS(3);
    {
        u32x4 gr[4];
        unsigned off = ((16 - lt + q) * 512 + pt * 32 + wc * 16 + c) * 16;
        auto gload = [&]() INL {
            PIN(off);
#pragma unroll
            for (int j = 0; j < 4; ++j) gr[j] = __builtin_amdgcn_raw_buffer_load_b128(mkrsrc(ws + OFF_GRAN + j * 32768, (4 - j) * 32768), off, 0, SC1);
        };
        auto gok = [&]() INL {
            unsigned d = 0;
#pragma unroll
            for (int j = 0; j < 4; ++j) d |= __builtin_amdgcn_perm(__builtin_amdgcn_perm(gr[j].w, gr[j].z, s0), __builtin_amdgcn_perm(gr[j].y, gr[j].x, s0), s1) ^ ex[j];
            return __builtin_amdgcn_uicmp(d, 0u, 33) == 0ull;
        };
        gload();
        if (XP == 4) xload();
        if (__builtin_expect(!gok(), 0)) {
            int spin;
#pragma unroll 1
            for (spin = 1; spin < (1 << 20); ++spin) {
                asm volatile("" ::: "memory");
                if (GSL) __builtin_amdgcn_s_sleep(GSL);
                gload();
                if (gok()) break;
            }
        }
        TS(25);
        if (XP == 5) xload();
#pragma unroll
        for (int j = 0; j < 4; ++j) gr[j] &= km;
        float2 cs[2];
#pragma unroll
        for (int j = 3; j >= -1; --j) {
#pragma unroll
            for (int n = 0; n < 2; ++n) {
                const float2 g = make_float2(__uint_as_float(gr[j & 3][2 * n]), __uint_as_float(gr[j & 3][2 * n + 1]));
                cs[n] = j == 3 ? g : j < 0 ? cmul(cs[n], lq[n]) : cfma(l512[n], cs[n], g);
            }
        }
        rowsum4(cs[0].x, cs[0].y, cs[1].x, cs[1].y);
#pragma unroll
        for (int n = 0; n < 2; ++n) loc[n] = cfma(make_float2(wq[n].x, -wq[n].y), make_float2(cs[n].x, -cs[n].y), loc[n]);
    }
    TS(4);
    u32x4 hv[2];
#pragma unroll
    for (int r = 0; r < 5; ++r) {
#pragma unroll
        for (int t = 0; t < 4; ++t) {
            if (r < 4) {
#pragma unroll
                for (int n = 0; n < 2; ++n) loc[n] = cfma(make_float2(lam[n].x, -lam[n].y), loc[n], u[n][4 * r + t]);
                lst<unsigned>(tb + t * 256, pkb(loc[0].x, loc[1].x));
                lst<unsigned>(tb + 1024 + t * 256, pkb(loc[0].y, loc[1].y));
            }
            if (r && t >= HST && t < HST + 2) { SCHED(); st128<SC1>(hv[t - HST], rs, hoff + ((r - 1) * 256 + (t - HST) * 32) * 8192); SCHED(); }
        }
        asm volatile("" ::: "memory");
        if (r < 4) { hv[0] = lld<u32x4>(rb); hv[1] = lld<u32x4>(rb + 1024); }
        asm volatile("" ::: "memory");
        SCHED();
    }
    TS(5);
    PIN(lane);
    auto cp2 = [&]() INL {
        if (wid == 3) asm volatile("s_nop 0\n\tbuffer_load_dword %0, %1, %2 offen lds" :: "v"(lane * 128), "s"(cd), "s"(((gb >> 3) & 7) * 8192), "{m0}"(ldsa(smem + A_AGG + 256)) : "memory");
    };
    if (XLP == 0) cp2();
    f32x4 ac[8][2];
    auto acin = [&]() INL {
#pragma unroll
        for (int i = 0; i < 8; ++i) {
            PIN(xi[i]);
            const u32x2 d = dk, v = xi[i];
            const unsigned p0 = pkb(lo2f(v.x) * (0.5f * lo2f(d.x)), hi2f(v.x) * (0.5f * hi2f(d.x))), p1 = pkb(lo2f(v.y) * (0.5f * lo2f(d.y)), hi2f(v.y) * (0.5f * hi2f(d.y)));
            ac[4][i >> 2][i & 3] = lo2f(p0); ac[5][i >> 2][i & 3] = hi2f(p0); ac[6][i >> 2][i & 3] = lo2f(p1); ac[7][i >> 2][i & 3] = hi2f(p1);
            ac[i >> 1][i & 1] = Z4;
        }
#pragma unroll
        for (int i = 0; i < 16; ++i) PIN(ac[i >> 1][i & 1]);
    };
    if (XP == 2) xload();
    if (ACI == 2) { SCHED(); acin(); SCHED(); }
    const u32x2 tg = {(unsigned)tag, (unsigned)(tag >> 32)};
    unsigned ord, tv;
    asm volatile("s_mov_b64 exec, 1\n\ts_waitcnt vmcnt(%8)\n\tds_add_rtn_u32 %1, %2, %3\n\ts_mov_b64 exec, %9\n\ts_waitcnt lgkmcnt(0)\n\tv_readfirstlane_b32 %0, %1\n\t"
                 "s_cmp_lg_u32 %0, 3\n\ts_cbranch_scc1 1f\n\tbuffer_store_dwordx2 %4, %5, %6, %7 offen sc1\n1:\ts_mov_b64 exec, -1"
                 : "=&s"(ord), "=&v"(tv) : "v"(fb + F_HC), "v"(1u), "v"(tg), "v"(lane * 256 * FST), "s"(mkdesc(ws, WS_END)), "s"(OFF_FH + (pt * 16 + lt) * FST),
                   "i"(XP == 2 ? 9 : 0), "i"((1 << HREP) - 1) : "memory", "scc");
    ord = __builtin_amdgcn_readfirstlane(ord);
    const bool pub = ord == 3u;
    TS(15);
    if (XLP == 1) cp2();
    if (XP == 3 || XP == 6) xload();
    __builtin_amdgcn_s_setprio(PRIOI);
    if (ACI == 0) acin();
    unsigned vo = ((lane & 15) >> 2) * 256 + (lane >> 4) * 64 + (lane & 3) * 16, so[32];
    PIN(vo);
#pragma unroll
    for (int k = 0; k < 32; ++k) {
        so[k] = OFF_H + lt * 524288 + kh * 131072 + wid * 2048 + (((XE & 2) ? 0 : k >> 2) ^ CX(gb)) * 16384 + ((k & 1) + (k & 2) * 16) * 8192;
        if (SPC || k < 8) asm volatile("" : "+s"(so[k]));
    }
    auto ldB = [&](int b) { return __builtin_amdgcn_raw_buffer_load_b128(rs, vo + (b & 1) * 1024, so[b >> 1], SC1); };
    lptr ab[2];
#pragma unroll
    for (int i = 0; i < 2; ++i) { ab[i] = smem + 2 * A_STAGE + (lane & 15) * 128 + (((4 * i + (lane >> 4)) ^ (lane & 7)) << 4); PIN(ab[i]); }
    auto ldA = [&](int f) INL {
        const int k = f < FT ? f_k(f) : f_k(f) ^ 12;
        if (f > 8 && !f_i(f) && (k & 6) == (f < FT ? 0 : 2)) { ab[k & 1] += (k & 8) ? -2 * A_STAGE : 2 * A_STAGE; PIN(ab[k & 1]); }
        return lld<u32x4>(ab[k & 1] + ((k >> 1) & 3) * 16384 + f_i(f) * 2048);
    };
    const unsigned hb = ldsa(smem) + 8192 + wid * 2048;
    unsigned fv = fb + F_FULL, fw = fb + F_RD + wid, pa = hb + (HD ? 0 : 2 * A_STAGE) + lane * 16; PIN(fv); PIN(fw); PIN(pa);
    u64 ob = (u64)(out + (512 * wid + lt) * D_ + 256 * kh + 32 * (da >> 1)); SPIN(ob);
    __builtin_amdgcn_s_setprio(PRIOG);
    TS(27);
    unsigned fe = 0, fs;
    static_assert(!HPL && NP1 == 1, "");
    if (ord == POW) {
        int spin = 0;
        unsigned o = (lane ^ ((CX(gb) & 3) << 4)) * FST, all = 0x8888u; PIN(o); PIN(all);
        if (POW != 3) await<1>(fb + F_HC, 4u);
        if (late(gb)) __builtin_amdgcn_s_sleep(PDX); else __builtin_amdgcn_s_sleep(PDL);
        TS(19);
        const u32x2 fl = __builtin_amdgcn_raw_buffer_load_b64(rs, o, OFF_FH + ((gb & (HREP - 1)) * 256 + kh * 128 + (CX(gb) >> 2) * 64) * FST, SC1);
        fs = lds_peek(fv) | 0x8888u;
        const u64 b = __builtin_amdgcn_uicmpl(__builtin_bit_cast(u64, fl), tag, 32);
        if (__builtin_expect(b == ~0ull, 1)) sig_or(fv, all);
        else {
            fs = 0;
#pragma unroll
            for (int j = 0; j < 4; ++j) fs |= ((unsigned)(b >> (16 * j)) & 0xffffu) == 0xffffu ? 8u << (4 * j) : 0u;
#pragma unroll 1
            for (; spin < (1 << 20) && !(fs & 8u); ++spin) fs = h_poll<1>(rs, gb, kh, lane, tg);
            sig_or(fv, fs);
        }
    } else fs = await<AW0>(fv, 8u);
    TS(7);
    auto need = [&](unsigned bit) INL { need_bit(fv, fs, bit); };
    u32x4 A[256], B[64 + NRL];
#pragma unroll
    for (int b = 0; b < NB; ++b) { if (b && !(b & 7)) need(8u << (b >> 1)); B[b] = ldB(b); SCHED(); }
    SCHED();
    if (ACI == 1) { acin(); SCHED(); }
    need(4u);
#pragma unroll
    for (int f = 0; f < AD; ++f) A[f] = ldA(f);
    SCHED();
    if (BSL) { for (int i = 0; i < BSL; ++i) __builtin_amdgcn_s_sleep(16); }
    if (BA2) burn<BS2>(BA2, A[0], A[1]);
    const unsigned po = OFF_U + ((lt * 16 + da) * 4 + wid) * 4096;
    unsigned pk[16], t0, pq = (po ^ 16384) - hb, pr = po - hb, gc = ldsa(smem) + A_XT + wid * 4096 - hb, cc = ldsa(smem) + A_CN - hb, oc = ldsa(smem) + A_OG + 256 * wid, ks = 0x07060302u;
    SPIN(pq); SPIN(pr); SPIN(gc); SPIN(cc); SPIN(oc); SPIN(ks);
    lptr og, cp;
    unsigned gp[2], ph;
    u32x4 pv[4], sl[2], ga[4], oo; f32x4 o2[2], r0[8];
    auto put = [&](int i) INL {
        const int e = i & 3, n = i >> 2;
#pragma unroll
        for (int j = 0; j < 4; ++j) r0[i][j] = fmaf(o2[n][e], ac[4 + j][n][e], r0[i][j]);
        st128<WTO>(__builtin_bit_cast(u32x4, r0[i]), mkrsrc((const char*)ob + n * 256 * D_ * 4, 241 * D_ * 4), oo[e]);
    };
    auto nextB = [&](int b) INL {
        if (b + NB >= 64) return;
        if (XE & 16) { B[b + NB] = B[b]; PIN(B[b + NB]); } else B[b + NB] = ldB(b + NB);
    };
    auto unp = [&](int j) INL {
        if (j < 0 || j > 31) return;
        const unsigned v = ga[j >> 3][(j >> 1) & 3];
        float t = (j & 1) ? hi2f(v) : lo2f(v);
        PIN(t);
        r0[j >> 2][j & 3] = t;
    };
    static_assert(GS0 + US0 + 32 <= 516 && US0 >= 8, "");
    sfor<0, 64>([&](auto ic) INL {
#define T8(x) {ST.x[s0], ST.x[s0 + 1], ST.x[s0 + 2], ST.x[s0 + 3], ST.x[s0 + 4], ST.x[s0 + 5], ST.x[s0 + 6], ST.x[s0 + 7]}
        constexpr int s0 = 8 * decltype(ic)::v, TA_[8] = T8(a), TP_[8] = T8(p), TW_[8] = T8(w);
        constexpr unsigned TM_[8] = T8(m);
#pragma unroll
        for (int u = 0; u < 8; ++u) {
            const int s = s0 + u, f = s_f(s), n = s_n(s), k = f_k(f), z = s - 2 * FT;
            MF2(B[s_b(s)], A[f], ac[f_m(f)][n]);
            SCHED();
            if (TP_[u] == 3) { fs = __builtin_amdgcn_readfirstlane(fe); SPIN(fs); }
            if (TP_[u] == 1 || TP_[u] == 2) fe = *(volatile LAS unsigned*)(lptr)(uintptr_t)(fv + (TP_[u] == 2 ? F_FB - F_FULL : 0));
            if (TM_[u]) { need_bit(fv, fs, TM_[u], (TM_[u] & 0x44u) && s > 128 ? F_FB - F_FULL : 0); if (s == GS0 && !(XE & 32)) TS(26); }
            if (z < 0 && !n) {
                if (f_hi(f)) nextB(2 * k);
                if (f_i(f) == 1 && k) nextB(2 * k - 1);
            }
            if (!HD && z < 0 && k >= 16 && k < 24) {
                const int v = s & 7;
                if (v == 6 && k == 19) { pa -= 2 * A_STAGE; PIN(pa); }
                if (v == WS0) lst<u32x4>((lptr)(uintptr_t)pa + h_off(k, 0), B[2 * k]);
                if (v == WS1) lst<u32x4>((lptr)(uintptr_t)pa + h_off(k, 1), B[2 * k + 1]);
            }
            if (z == -6) { t0 = pa + pr; PIN(t0); }
#pragma unroll
            for (int i = 0; i < 24; ++i) {
                const int j = i / 6, o = i % 6;
                if (z != (PKR == 1 ? i - 4 - j : PKR == 2 ? 3 * j + o / 2 - 4 : 2 * j + o / 3 - 4)) continue;
                if (o == 0) pk[4 * j] = __builtin_amdgcn_perm(__float_as_uint(ac[j][0][1]), __float_as_uint(ac[j][0][0]), ks);
                if (o == 1) pk[4 * j + 1] = pkb(ac[j][0][2], ac[j][0][3]);
                if (o == 2) pk[4 * j] |= 0x10001u;
                if (o == 3) pk[4 * j + 2] = pkb(ac[j][1][0], ac[j][1][1]);
                if (o == 4) pk[4 * j + 3] = pkb(ac[j][1][2], ac[j][1][3]);
                if (o < 5) PIN(pk[4 * j + (o == 2 ? 0 : o > 2 ? o - 1 : o)]);
                else { st128<SC1>((u32x4){pk[4 * j], pk[4 * j + 1], pk[4 * j + 2], pk[4 * j + 3]}, rs, t0 + j * 1024); if (j == 3 && !(XE & 32)) TS(29); }
            }
            if (z == 18) { ph = pa + 2 * A_STAGE; PIN(ph); }
#pragma unroll
            for (int r = 0; r < NRL; ++r) if (z == RS0 + RSP * r) B[64 + r] = lld<u32x4>((lptr)(uintptr_t)(r < 8 ? pa : ph) + h_off(23 - (r >> 1), r & 1));
            constexpr int RS = 2 * FT + RSD;
            static_assert(RS + 6 <= 507, "");
            if (s >= RS && s < RS + 8 && !((s - RS) & 1)) pv[(s - RS) >> 1] = __builtin_amdgcn_raw_buffer_load_b128(rs, pa + (s - RS) * 512, pq, SC1);
            if (z == 22) { gp[0] = pa >> 2; PIN(gp[0]); }
            if (z == 24) { gp[0] &= 0xc0u; PIN(gp[0]); }
            if (z == 28) { og = (lptr)(uintptr_t)(gp[0] + oc); PIN(og); }
            if (z == 30) { gp[0] = (gp[0] ^ pa) + gc; PIN(gp[0]); }
            if (z == 34) { gp[1] = gp[0] ^ 32u; PIN(gp[1]); }
            if (z == 36) { cp = (lptr)(uintptr_t)(pa + cc); PIN(cp); }
            if (s > GS0 && s <= GS0 + 4) ga[s - GS0 - 1] = lld<u32x4>((lptr)(uintptr_t)gp[(s - GS0 - 1) & 1] + (s - GS0 - 1) * 1024);
            if (s == 496) sl[0] = lld<u32x4>(cp);
            if (s == 498) sl[1] = lld<u32x4>(cp + 1024);
            if (s == 500) oo = lld<u32x4>(cp + 2048);
            if (s == 501) o2[0] = lld<f32x4>(og);
            if (s == 502) o2[1] = lld<f32x4>(og + 16);
            unp(s + (s > 507 ? 4 : 0) - GS0 - US0);
            if (const int g = TA_[u]; g) {
                A[g] = ldA(g);
                if (g < FT && f_hi(g) && (f_k(g) & 3) == 3) asm volatile("ds_write_b8 %0, %0 offset:%1" :: "v"(fw), "i"(f_k(g) & 28) : "memory");
            }
            if (TW_[u]) wait_lds(TW_[u] - 1);
            SCHED();
            if (s == 507 || s == 511) {
                if (s == 507) {
                    TS(10);
                    unsigned n;
                    asm volatile("s_mov_b32 %5, 0x100000\n"
                                 "1:\ts_waitcnt vmcnt(0)\n\tv_and_b32 %4, v" PV0 ", v" PV1 "\n\tv_and_b32 %4, v" PV2 ", %4\n\tv_and_b32 %4, v" PV3 ", %4\n\tv_and_b32 %4, 1, %4\n\t"
                                 "v_cmp_eq_u32 vcc, 0, %4\n\ts_cbranch_vccz 2f\n\t"
                                 "buffer_load_dwordx4 %0, %6, %7, %8 offen sc1\n\tbuffer_load_dwordx4 %1, %6, %7, %8 offen offset:1024 sc1\n\t"
                                 "buffer_load_dwordx4 %2, %6, %7, %8 offen offset:2048 sc1\n\tbuffer_load_dwordx4 %3, %6, %7, %8 offen offset:3072 sc1\n\t"
                                 "s_sub_u32 %5, %5, 1\n\ts_cbranch_scc0 1b\n\ts_waitcnt vmcnt(0)\n"
                                 "2:" : "+{v[" PV0 ":" PV0E "]}"(pv[0]), "+{v[" PV1 ":" PV1E "]}"(pv[1]), "+{v[" PV2 ":" PV2E "]}"(pv[2]), "+{v[" PV3 ":" PV3E "]}"(pv[3]), "=&v"(t0), "=&s"(n)
                                 : "v"(pa), "s"(mkdesc(ws, WS_END)), "s"(pq) : "memory", "scc", "vcc");
                    TS(11);
                    SCHED();
                }
#pragma unroll
                for (int m = 0; m < 4; ++m) { MF2(sl[n], pv[m], ac[4 + m][n]); SCHED(); if (n) put(m); else unp(508 + m - GS0 - US0); SCHED(); }
            }
            if (s == 15) { TS(8); TC(60); }
            if (s == 2 * F1 - 1) { if (!(XE & 32)) TS(9); TC(55); }
            if (z == -1) { if (!(XE & 32)) TS(28); TC(56); }
            if (s == 495) { TC(61); TS(14); }
        }
    });
    asm volatile("s_nop 7");
    SCHED();
#pragma unroll
    for (int i = 4; i < 8; ++i) { put(i); SCHED(); }
    TS(12);
    TSE(0, wid == 0);
    TSE(2, pub);
    TSE(3, ord == POW);
}

#define TSARG
#define TSPASS
struct Gate {
    unsigned xa, wa, xv, gw; float sum; int rot;
    DEV void rd(int kt) { asm volatile("ds_read_b32 %0, %2\n\tds_read_b32 %1, %3" : "=&v"(xv), "=&v"(gw) : "v"(xa + (kt & 3) * A_STAGE), "v"(wa + (kt ^ rot) * 128) : "memory"); }
    DEV void acc() {
        asm volatile("s_waitcnt lgkmcnt(0)" : "+v"(xv), "+v"(gw) :: "memory");
        const unsigned pr = pkb(lo2f(xv) * lo2f(gw), hi2f(xv) * hi2f(gw));
        sum += lo2f(pr); sum += hi2f(pr); PIN(sum);
    }
};
struct Tab { CM sf, si, ff, fi; u32x4 mt[2], xf, xb; f32x2 ti[2]; };
DEV void mid_tables(lptr smem, int gb, int w, int lane, Tab& t) {
    int mp, j1; slot_of(gb, mp, j1);
    const int c = lane & 15, q = lane >> 4;
    lptr mb = smem + A_MAT + lane * 16;
#pragma unroll
    for (int i = 0; i < 2; ++i) { t.ti[i] = lld<f32x2>(smem + A_MAT + 9216 + (2 * w + i) * 128 + c * 8); t.mt[i] = lld<u32x4>(mb + (6 + i) * 1024); }
    u32x4 sf0 = lld<u32x4>(mb + 4096), si0 = lld<u32x4>(mb + 5120);
    const u32x4 f0 = lld<u32x4>(mb + 8192);
    SCHED();
    asm volatile("" : "=v"(t.xf), "=v"(t.xb));
    if (w >= 4 && mp == 0 && !(j1 & 1)) {
        sf0 = mat_sf(64, c, q); si0 = mat_si(64, c, q);
        const float dt = -(float)(32 + 64 * c) * (1.0f / 1024.0f);
        t.xf = cmat1(dt * (float)(4 * q), dt); t.xb = cmat1((float)(c * (32 + 256 * q)) * (1.0f / 1024.0f), (float)c * (1.0f / 16.0f));
    }
    PIN(t.xf); PIN(t.xb);
    t.sf = cexp(sf0); t.si = cexp(si0); t.ff = cexp(f0); t.fi = cexp((u32x4){f0.x ^ SG, f0.y ^ SG, f0.z, f0.w});
    PIN(t.mt[0]); PIN(t.mt[1]); PIN(t.ti[0]); PIN(t.ti[1]);
}
DEV int u_exp(unsigned em) { return min(max((int)(em & 0xffu), 4), 254); }

struct Ld {
    u32x4 rx, rb, vA, vB; unsigned lb, sA, sB; int rot;
    DEV Ld(const u16* x, const u16* Bre, const u16* Bim, lptr smem, int gb, int lw, int lane) {
        int lt, pt; tile_of(gb, lt, pt);
        const int srow = lane >> 3, schunk = (lane & 7) ^ srow;
        rot = ROT;
        lb = ldsa(smem) + lw * 4096;
        const unsigned a = (16 * (srow >> 2) + (srow & 3)) * 2048 + schunk * 16, b = 2 * srow * 2048 + schunk * 16;
        vA = (u32x4){a, a + 65536 - 1024, a + 8192 - 2048, a + 73728 - 3072}; vB = (u32x4){b, b + 32768 - 1024, b, b + 34816 - 3072};
        sA = (lt * 128 + 64 * (lw >> 1) + 8 * (lw & 1)) * 2048; sB = (pt * 64 + 32 * (lw >> 1)) * 2048;
        PIN(vA.x); PIN(vA.y); PIN(vA.z); PIN(vA.w); PIN(vB.x); PIN(vB.y); vB.z = vB.x; vB.w = vB.y;
        SPIN(sA); SPIN(sB); SPIN(lb); SPIN(rot);
        SCHED();
        rx = mkdesc(x, S_ * D_ * 2); rb = mkdesc((lw & 1) ? Bim : Bre, D_ * D_ * 2);
    }
    DEV void st(int kk) const {
        dma64(rx, lb + (kk & 3) * A_STAGE, vA, sA + (kk ^ rot) * 128);
        dma64(rb, lb + (kk & 3) * A_STAGE + 16384, vB, sB + (kk ^ rot) * 128);
    }
};
DEV void first_loads(const u16* __restrict__ x, const u16* __restrict__ Bre, const u16* __restrict__ Bim, const u16* __restrict__ gate_w, lptr smem, int gb, int lw, int lane) {
    __builtin_amdgcn_s_setprio(PRIOL);
    const Ld ld(x, Bre, Bim, smem, gb, lw, lane);
    ld.st(0);
    SCHED();
    int lt, pt; tile_of(gb, lt, pt);
    dma16(mkdesc(gate_w, D_ * 2), ldsa(smem) + A_GW + (lw & 1) * 1024, lane * 16, (lw & 1) * 1024);
    const unsigned t = (lane >> 1) * 32768 + (lane & 1) * 16;
    dma64(ld.rx, ld.lb + A_XT, (u32x4){t, t + 1047552, t + 2 * 1047552, t + 3 * 1047552}, lt * 2048 + lw * 512 + pt * 32);
    if (FSL) __builtin_amdgcn_s_sleep(FSL);
    if (FLN > 1) ld.st(1);
    if (FLN > 2) ld.st(2);
}
DEV void role_load(const u16* __restrict__ x, const u16* __restrict__ Bre, const u16* __restrict__ Bim, const u16* __restrict__ Cre, const u16* __restrict__ Cim,
                   const u16* __restrict__ gate_w, char* ws, lptr smem, int gb, int lw, int lane, Tab& tab TSARG) {
    if (!ENT) __builtin_amdgcn_s_waitcnt(0x0F70);
    Gate g;
    int lt, pt; tile_of(gb, lt, pt);
    TS(0);
    if (!ENT) first_loads(x, Bre, Bim, gate_w, smem, gb, lw, lane);
    const Ld ld(x, Bre, Bim, smem, gb, lw, lane);
    if (FLN < 2) ld.st(1);
    if (FLN < 3) ld.st(2);
    const unsigned lb = ldsa(smem);
    const int gi = 8 * pt + 2 * lw + (lane >> 5);
    g.xa = lb + gi * 128 + ((((lane & 31) >> 2) ^ (gi & 7)) << 4) + (lane & 3) * 4;
    g.wa = lb + A_GW + (lane & 31) * 4; g.sum = 0.f; g.rot = ld.rot;
    TS(6);
    const int c = lane & 15, q = lane >> 4;
    const unsigned fb = lb + A_FLG;
    lptr ub = smem + A_XT + lw * 4096 + (lane >> 2) * 64 + (lane & 3) * 8, ea = smem + A_E1 + c * E1P + q * 256 + lw * 64;
    lptr mb = smem + A_MAT + lane * 16, tb = smem + A_MAT + 9216 + c * 128 + q * 32;
    PIN(ub); PIN(ea); PIN(mb); PIN(tb);
#pragma unroll 1
    for (int kt = 0; kt < 13; ++kt) { wait_vm<16>(); TL(8 + kt); BAR(); TL(28 + kt); if (kt == 0) { TS(28); TC(60); } g.rd(kt); ld.st(kt + 3); g.acc(); }
    u32x4 mt[4], ut[4];
    f32x4 tf[2];
    wait_vm<16>(); TL(21); BAR(); TL(41); g.rd(13);
#pragma unroll
    for (int i = 0; i < 4; ++i) mt[i] = lld<u32x4>(mb + i * 1024);
#pragma unroll
    for (int i = 0; i < 2; ++i) tf[i] = lld<f32x4>(tb + i * 16);
    g.acc();
#pragma unroll
    for (int i = 0; i < 4; ++i) PIN(mt[i]);
    PIN(tf[0]); PIN(tf[1]);
    wait_vm<8>(); sig(fb + F_ST, 1u); TL(42);
    wait_vm<0>(); sig(fb + F_ST, 0x10u);
    __builtin_amdgcn_s_setprio(PRIOD);
    await<0>(fb + F_ST, 0x40u);
    TS(43); TC(61);
    g.rd(14); g.acc(); g.rd(15); g.acc();
    sig(fb + F_FREE, 1u << 20);
    TS(2);
    __builtin_amdgcn_s_setprio(PRIOM);
    mid_tables(smem, gb, lw + 4, lane, tab);
    float gg = bfr(1.0f / (1.0f + expf(-bfr(half_sum(g.sum))))), og = bfr(1.0f - gg);
    const int tau = (gi & 64) | ((gi & 12) << 2) | ((gi & 48) >> 2) | (gi & 3);
    unsigned go = (lane & 31) ? ~0u : OFF_G + (tau & 15) * 1024 + (lt * 8 + (tau >> 4)) * 8;
    PIN(gg); PIN(og); PIN(go);
    __builtin_amdgcn_s_setprio(PRIOD);
    TS(53);
    auto c01 = [&]() INL {
        await<1>(fb + F_FREE, 8u << 20);
        c_chunk<C0S>(Cre, Cim, lb, gb, da_of(pt), lw, lane, 0);
        if (C0E != 2) c_chunk<C0S>(Cre, Cim, lb, gb, da_of(pt), lw, lane, 1);
    };
    if (C0E) c01();
    const unsigned em = await<1>(fb + F_EM, 0x10000u << lw);
    TS(3);
#pragma unroll
    for (int i = 0; i < 4; ++i) ut[i] = trfrag(ub + i * 1024, 32);
    const float su = __uint_as_float((unsigned)(258 - u_exp(em)) << 23);
    tf[0] *= su; tf[1] *= su; PIN(tf[0]); PIN(tf[1]);
#pragma unroll
    for (int i = 0; i < 4; ++i) {
        f32x4 r = mfb(ut[i], mt[2], Z4), m = mfb(ut[i], mt[3], Z4);
        r = mfb(ut[i], mt[0], r); m = mfb(ut[i], mt[1], m);
        f32x4 yr, yi;
#pragma unroll
        for (int e = 0; e < 4; ++e) {
            const float tc = tf[e >> 1][2 * (e & 1)], ts = tf[e >> 1][2 * (e & 1) + 1];
            yr[e] = fmaf(r[e], tc, -m[e] * ts); yi[e] = fmaf(r[e], ts, m[e] * tc);
        }
        lst<u32x4>(ea + i * 16, pk8(yr, yi));
    }
    if (gb == XG) {
        const unsigned sg = 0xbf803f80u;
#pragma unroll
        for (int i = 0; i < 4; ++i) {
            const f32x4 e = mfb(ut[i], c == 0 ? (u32x4){sg, sg, 0, 0} : (u32x4){0, 0, sg, sg}, Z4);
            if (c < 2) lst<u32x4>(ea + 16 * E1P + i * 16, pk8(e * su, Z4));
        }
    }
    sig(fb + F_MID, 1u);
    TS(54);
    __builtin_amdgcn_s_setprio(PRIOM);
    if (C0L < 2 && !C0E) await<1>(fb + F_FREE, 8u << 20);
    if (CGW) await<1>(fb + F_ST, 0x4000u);
    if (C0L < 2 && !C0E) c_chunk<C0S>(Cre, Cim, lb, gb, da_of(pt), lw, lane, 0);
    if (C0L < 1 && C0E != 1) c_chunk<C0S>(Cre, Cim, lb, gb, da_of(pt), lw, lane, 1);
    __builtin_amdgcn_raw_buffer_store_b64((u32x2){__float_as_uint(gg), __float_as_uint(og)}, mkrsrc(ws, WS_END), go, 0, SC1);
    TS(52);
}

DEV unsigned ldw(rsrc_t r, unsigned vo, unsigned so) { return __builtin_amdgcn_raw_buffer_load_b32(r, vo, so, 0); }
DEV unsigned amax4(u32x4 p) { return pkmax(pkmax(p.x & 0x7fff7fffu, p.y & 0x7fff7fffu), pkmax(p.z & 0x7fff7fffu, p.w & 0x7fff7fffu)); }

DEV void role_aux(const u16* __restrict__ Cre, const u16* __restrict__ Cim, const u16* __restrict__ w_r, const u16* __restrict__ w_i, const u16* __restrict__ lam_re, const u16* __restrict__ lam_im,
                  const u16* __restrict__ log_dt, char* ws, const float* out, const u16* Dskip, lptr smem, int gb, int aw, int lane, Tab& tab TSARG) {
    int lt, pt; tile_of(gb, lt, pt);
    int mp, j1; slot_of(gb, mp, j1);
    const int c = lane & 15, q = lane >> 4;
    const bool spc = mp == 0 && !(j1 & 1);
    const rsrc_t rs = mkrsrc(ws, WS_END), rr = mkrsrc(w_r, S_ * KF * 2), ri = mkrsrc(w_i, S_ * KF * 2);
    TS(0);
    __builtin_amdgcn_s_setprio(PRIOA);
    u32x4 fch, fsh;
    const bool m0 = 16 * aw + c == 0;
    {
        const float th = (float)(16 * aw + c) * (1.0f / 128.0f);
#pragma unroll
        for (int i = 0; i < 4; ++i) {
            const float t0 = __builtin_amdgcn_fractf(th * (float)(16 * (i >> 1) + 2 * (i & 1) + 4 * q)), t1 = t0 + th;
            const float c0 = __builtin_amdgcn_cosf(t0), c1 = __builtin_amdgcn_cosf(t1);
            const float s0 = m0 ? 1.f : -__builtin_amdgcn_sinf(t0), s1 = m0 ? -1.f : -__builtin_amdgcn_sinf(t1);
            fch[i] = pkb(c0, c1); fsh[i] = pkb(s0, s1);
        }
        PIN(fch); PIN(fsh);
    }
    unsigned sg64 = (c & 1) ? SG : 0u, sgA = (c & 2) ? SG : 0u, sgB = ((c + 1) & 2) ? SG : 0u;
    constexpr unsigned WR = 128 * KF * 2;
    const bool xh = gb == XG && aw >= 2;
    const int c0 = 2 * (lane & 7) + xh, jb = (lane >> 3) + 4 * (aw & 2);
    const int k1 = mp ? mp : spc ? (lane & 4) * 16 : (j1 == 1 ? 0 : 64);
    const int kd = j1 + 4 * (spc ? c0 & 7 : c0) + 64 * jb;
    unsigned wo = (aw < 2 ? (k1 * KF + kd) * 2 + aw * 8 * WR : ((128 - k1) * KF + 1020 - kd) * 2 + (3 - aw) * 8 * WR) & ~3u;
    unsigned wo0 = aw < 2 ? wo : k1 || aw == 3 ? wo + 7 * WR : wo - 9 * WR;
    unsigned uoff = (lane >> 3) * 131072 + (lane & 7) * 16 + OFF_U + aw * 64 * 16384 + lt * 2048 + pt * 128;
    PIN(sg64); PIN(sgA); PIN(sgB); PIN(wo); PIN(wo0); PIN(uoff);
    unsigned lru, liu, ldu, um = 0;
    u32x2 tag;
    u32x4 fl[2];
    asm volatile("" : "=v"(fl[0]), "=v"(fl[1]));
    u32x3 wre[8], wim[8];
    u32x4 wsc = {0, 0, 0, 0};
    f32x4 vr[4], vi[4];
    float dt = 0.f, er = 0.f; float2 lam = make_float2(0.f, 0.f);
#pragma unroll
    for (int d1 = 0; d1 < 4; ++d1) { vr[d1] = Z4; vi[d1] = Z4; }
    auto wx = [&]() INL {
        if (j1 == 0 && aw == 3) {
            const int ks = (spc ? (lane >> 5) * 64 : mp) + 128 * c, ng = (S_ - ks) & (S_ - 1);
            unsigned o0 = (q & 1) ? (ng * KF + 511) * 2u : ks * KF * 2u, o1 = (q & 1) ? (ks * KF + 511) * 2u : ng * KF * 2u;
            PIN(o0); PIN(o1);
            wsc = (u32x4){ldw(rr, o0, 0), ldw(rr, o1, 0), ldw(ri, o0, 0), ldw(ri, o1, 0)};
        }
    };
    auto prm = [&]() INL {
        lru = ldw(mkrsrc(lam_re, 2048), (lane >> 1) * 4, pt * 128); liu = ldw(mkrsrc(lam_im, 2048), (lane >> 1) * 4, pt * 128);
        ldu = ldw(mkrsrc(log_dt, 2048), (lane >> 1) * 4, pt * 128);
    };
    static_assert(UFS == 6 && US + 3 / UPS < 6 && CTS + 3 * CTD < UCK && TLT < 6, "");
    auto tlt = [&]() INL {
        auto tch = [&](const void* p, unsigned n, unsigned vo, unsigned so) INL {
            asm volatile("s_nop 0\n\tbuffer_load_dword %0, %1, %2 offen lds" :: "v"(vo), "s"(mkdesc(p, n)), "s"(so), "{m0}"(ldsa(smem) + A_AGG + 256) : "memory");
        };
        if ((TLM & 4) && aw == 2) tch(out, S_ * D_ * 4, (c & 12) << 19, lt * 4096 + pt * 128);
        if ((TLM & 8) && aw == 3) tch(Dskip, D_ * 2, 0, (gb & 15) * 128);
        if ((TLM & 1) && aw < 2) tch(aw ? Cim : Cre, D_ * D_ * 2, (c & TLK) << 17, gb * 128);
        if ((TLM & 2) && aw < 2) tch(aw ? w_i : w_r, S_ * KF * 2, q ? (c & TLK) << 17 : 2u << 20, (gb & 31) * 128);
    };
    if (TLT == -2) tlt();
    if (PRS < 0) prm();
    const unsigned fb = ldsa(smem + A_FLG);
    bool uok = false;
    auto upub = [&]() INL { sig(fb + F_EM, (wave_pkmax(pkmax(pkmax(fl[0].x, fl[0].z), pkmax(fl[1].x, fl[1].z))) & 0xffu) | 0x8000u); };
    u32x4 uo[4];
    f32x4 pq[4];
    asm volatile("" : "=v"(pq[0]), "=v"(pq[1]), "=v"(pq[2]), "=v"(pq[3]));
    sfor<0, 14>([&](auto ic) INL {
        constexpr int kt = decltype(ic)::v;
        constexpr int GS[4] = GSTEP, KSO[4] = {0, 2, 3, 1}, JO[4] = {0, 2, 1, 3}, WRW[5] = WROW;
        auto ust = [&](int k) INL {
#pragma unroll
            for (int u = 0; u < 4; ++u) if (US + u / UPS == k && !(XM & 2)) st128<SC1>(uo[u], rs, uoff + JO[u] * 32768);
        };
        auto mem = [&]() INL {
            if (kt >= 10) {
#pragma unroll
                for (int r = 0; r < 8; ++r) {
                    if (r < WRW[kt < 10 ? 0 : kt - 10] || r >= WRW[kt < 10 ? 0 : kt - 9]) continue;
                    const unsigned so = r ? (aw < 2 ? r : 7 - r) * WR : 0u;
                    wre[r] = __builtin_amdgcn_raw_buffer_load_b96(rr, r ? wo : wo0, so, 0); wim[r] = __builtin_amdgcn_raw_buffer_load_b96(ri, r ? wo : wo0, so, 0);
                    if (r + 1 == URS) { SCHED(); wait_vm<2 * (URS - WRW[UCK - 10])>(); sig(fb + F_EM, 0x10000u << aw); TS(6); SCHED(); }
                }
            }
            if (kt == PRS) prm();
            if (kt == FTS && aw == 3)
                asm volatile("s_nop 0\n\tbuffer_load_dword %0, %1, 0 offen sc1 lds" :: "v"(lane * 64), "s"(mkdesc(ws + OFF_G + gb * FTB, FTB)), "{m0}"(ldsa(smem + A_AGG + 256)) : "memory");
            if (kt >= CTS && kt - CTS == CTD * aw)
                asm volatile("s_nop 0\n\tbuffer_load_dword %0, %1, %2 offen lds" :: "v"(lane * 64), "s"(mkdesc((aw & 1) ? Cim : Cre, D_ * D_ * 2)), "s"(gb * 8192 + (aw >> 1) * 4096),
                             "{m0}"(ldsa(smem) + (kt < 6 ? A_AGG + 256 : A_XT + aw * 4096)) : "memory");
            if (kt == TLT) tlt();
            if (kt == WXS) wx();
            if (kt == UPL && UPL < 13 && aw == 2) flag_get(rs, OFF_FU, gb, lane, fl);
        };
        if (kt == UFS) wait_vm<0>();
        if (kt == UFS + 1) { uoff = (lane >> 2) * 128 + (lane & 3) * 16; PIN(uoff); }
        if (kt == UCK && UPRE && aw == 2) { uok = flag_ok(fl, tag); if (uok) { upub(); LDS_WAIT(); } }
        TL(8 + kt);
        BAR();
        if (VL == 2) ust(kt - 1);
        if (kt == 13 && aw == 1) {
            lst<f32x4>(smem + A_PW + lane * 16, (f32x4){1.f, 0.f, 1.f, 0.f});
#pragma unroll
            for (int i = 0; i < 4; ++i) lst<f32x4>(smem + A_PW + (i + 1) * 1024 + lane * 16, pq[i]);
            sig(fb + F_ST, 0x100u);
        }
        if (kt == UCK) {
            if (aw == 2 && !uok) {
                int spin;
                if (UPL > 12) { if (UDL) __builtin_amdgcn_s_sleep(UDL); flag_get(rs, OFF_FU, gb, lane, fl); }
#pragma unroll 1
                for (spin = 0; spin < (1 << 20); ++spin) { if (flag_ok(fl, tag)) break; flag_get(rs, OFF_FU, gb, lane, fl); }
                upub();
            }
            await<0>(fb + F_EM, 0x8000u);
            const u32x4 rw = mkdesc(ws, WS_END);
#pragma unroll
            for (int i = 0; i < 4; ++i)
                dma16s(rw, ldsa(smem + A_XT) + aw * 4096 + i * 1024, uoff, OFF_U + ((mp >> 1) * 4 + j1) * 32768 + (mp & 1) * 64 + aw * 8192 + i * 2048);
            TS(2);
        }
        if (kt == ATS) mid_tables(smem, gb, aw, lane, tab);
        u32x4 xa[4][4];
#pragma unroll
        for (int g = 0; g < 4; ++g) {
            if (GS[g] != kt) continue;
#pragma unroll
            for (int i = 0; i < 4; ++i) xa[g][i] = trfrag(smem + A_XT + i * 4096 + KSO[g] * 1024 + lane * 8, 512);
        }
        if (VL != 1) mem();
        SCHED();
#pragma unroll
        for (int g = 0; g < 4; ++g) {
            if (GS[g] != kt) continue;
#pragma unroll
            for (int i = 0; i < 4; ++i) { vr[i] = mfb(xa[g][i], fch, vr[i]); vi[i] = mfb(xa[g][i], fsh, vi[i]); PIN(vr[i]); PIN(vi[i]); }
            if (g == 0 || g == 2) { fch ^= sg64; fsh ^= sg64; PIN(fch); PIN(fsh); }
            if (g == 1) {
                const bool od = c & 1;
                const u32x4 a = (od ? fsh : fch) ^ sgA, b = (od ? fch : fsh) ^ sgB;
                fch = a; fsh = b; PIN(fch); PIN(fsh);
            }
        }
        if (kt == B1S) {
            const f32x4 t0r = vr[0] + vr[2], t1r = vr[0] - vr[2], t2r = vr[1] + vr[3], t3r = vr[1] - vr[3];
            const f32x4 t0i = vi[0] + vi[2], t1i = vi[0] - vi[2], t2i = vi[1] + vi[3], t3i = vi[1] - vi[3];
            vr[0] = t0r; vr[1] = t1r; vr[2] = t2r; vr[3] = t3r; vi[0] = t0i; vi[1] = t1i; vi[2] = t2i; vi[3] = t3i;
#pragma unroll
            for (int i = 0; i < 4; ++i) { PIN(vr[i]); PIN(vi[i]); }
        }
#pragma unroll
        for (int u = 0; u < 4; ++u) {
            if (US + u / UPS != kt) continue;
            const int j = JO[u];
            f32x4 a, b;
            if (j == 0) { a = vr[0] + vr[2]; b = vi[0] + vi[2]; }
            if (j == 2) { a = vr[0] - vr[2]; b = vi[0] - vi[2]; }
            if (j == 1) { a = vr[1] + vi[3]; b = vi[1] - vr[3]; if (aw == 0) { if (m0) { a = vr[1]; b = -vr[3]; } } }
            if (j == 3) { a = vr[1] - vi[3]; b = vi[1] + vr[3]; if (aw == 0) { if (m0) { a = vi[1]; b = vi[3]; } } }
            uo[u] = quads(smem + A_XT + aw * 4096 + j * 1024, lane, (u32x2){pkb(a[0], a[1]), pkb(a[2], a[3])}, (u32x2){pkb(b[0], b[1]), pkb(b[2], b[3])});
            uo[u].x = (uo[u].x + ((uo[u].x >> 1) & 1u)) & ~1u;
            um = pkmax(um, amax4(uo[u]));
            PIN(um); PIN(uo[u]);
#pragma unroll
            for (int i = 0; i < 4; ++i) if (u < 3 && ((i & 1) || u == 0)) { PIN(vr[i]); PIN(vi[i]); }
        }
        if (kt == UFS - 1) {
            um = wave_pkmax(pkmax(um, um >> 16));
            if (lane == 0) lst<unsigned>(smem + A_FLG + F_MAX + aw * 4, um);
            tag = lld<u32x2>(smem + A_FLG + F_TAG); PIN(tag);
        }
        if (kt == P0S) {
            PIN(lru); PIN(liu); PIN(ldu);
            if (lane & 1) { lru >>= 16; liu >>= 16; ldu >>= 16; }
            PIN(lru); PIN(liu); PIN(ldu);
        }
        if (kt >= P0S && kt < P0S + 5 && aw == 1) {
            const float lr = lo2f(lru), li = lo2f(liu);
            if (kt == P0S) { dt = bfr(expf(lo2f(ldu))); er = expf(dt * lr); PIN(dt); PIN(er); }
            if (kt == P0S + 1) { float sn, cs; sincosf(dt * li, &sn, &cs); lam = make_float2(er * cs, er * sn); PIN(lam.x); PIN(lam.y); }
            if (kt == P0S + 2) {
                const float nr = lam.x - 1.0f, ni = lam.y, den = lr * lr + li * li;
                pq[3][0] = (nr * lr + ni * li) / den; pq[3][1] = (ni * lr - nr * li) / den;
                PIN(pq[3]);
            }
            if (kt == P0S + 3) {
                float2 l16 = lam;
#pragma unroll
                for (int i = 0; i < 4; ++i) l16 = cmul(l16, l16);
                const float2 l32 = cmul(l16, l16), l48 = cmul(l32, l16);
                lst<f32x4>(smem + A_PRM + lane * 32, (f32x4){lam.x, lam.y, l16.x, l16.y});
                lam = cmul(l32, l32);
                pq[0][0] = l16.x; pq[0][1] = l16.y; pq[1][0] = l32.x; pq[1][1] = l32.y; pq[2][0] = l48.x; pq[2][1] = l48.y;
                PIN(pq[0]); PIN(pq[1]); PIN(pq[2]); PIN(lam.x); PIN(lam.y);
            }
            if (kt == P0S + 4) {
                const float2 l128 = cmul(lam, lam), l256 = cmul(l128, l128), l384 = cmul(l256, l128), l512 = cmul(l256, l256), cl = cmul(make_float2(pq[3][0], pq[3][1]), lam);
                lst<f32x4>(smem + A_PRM + lane * 32 + 16, (f32x4){lam.x, lam.y, l512.x, l512.y});
                pq[0][2] = l128.x; pq[0][3] = l128.y; pq[1][2] = l256.x; pq[1][3] = l256.y; pq[2][2] = l384.x; pq[2][3] = l384.y; pq[3][2] = cl.x; pq[3][3] = cl.y;
                PIN(pq[0]); PIN(pq[1]); PIN(pq[2]); PIN(pq[3]);
            }
        }
        SCHED();
        if (kt == UFS && aw == 0) {
            const u32x4 m4 = lld<u32x4>(smem + A_FLG + F_MAX);
            const unsigned m = pkmax(pkmax(m4.x, m4.y), pkmax(m4.z, m4.w));
            flag_put(rs, OFF_FU, gb, (u32x2){(tag.x & 0xffff0000u) | ((m & 0xffffu) >> 7), tag.y}, lane);
        }
        if (VL != 2) ust(kt);
        if (VL == 1) mem();
        LDS_WAIT();
        SCHED();
    });
    TS(54);
    if (gb == XG && aw == 0 && lane == 0) __builtin_amdgcn_raw_buffer_store_b64(tag, rs, 0, OFF_E, SC1);
    __builtin_amdgcn_s_setprio(PRIOM);
    if (ATS == 14) mid_tables(smem, gb, aw, lane, tab);
    await<1>(fb + F_FREE, 8u << 20);
    TS(3);
    unsigned mx = 0;
    const bool skip = j1 == 0 && (lane & (spc ? 59 : 63)) == 0 && !xh, x8 = xh && (lane & 3) == 3;
    lptr wd[2];
    wd[0] = smem + A_W + (aw & 1) * 512 + c0 * 1024 + ((jb ^ c0) << 4);
    wd[1] = smem + A_W + (aw & 1) * 512 + (x8 ? 16384 + (lane & 4) * 256 + (jb << 4) : (c0 + 1) * 1024 + ((jb ^ (c0 + 1)) << 4));
    if (skip) wd[0] = smem + A_W + 18432;
    if (aw >= 2) { lptr t = wd[0]; wd[0] = wd[1]; wd[1] = t; }
    const unsigned wsel = (mp + j1) & 1 ? 0x07060302u : 0x05040100u;
    PIN(wd[0]); PIN(wd[1]);
    u32x4 wp[2][2];
#pragma unroll
    for (int i = 0; i < 2; ++i) {
#pragma unroll
        for (int e = 0; e < 4; ++e) {
            wp[0][i][e] = __builtin_amdgcn_perm(wim[4 * i + e].x, wre[4 * i + e].x, wsel);
            wp[1][i][e] = __builtin_amdgcn_perm(wim[4 * i + e].z, wre[4 * i + e].z, wsel);
        }
        lst<u32x4>(wd[0] + i * 256, wp[0][i]); lst<u32x4>(wd[1] + i * 256, wp[1][i]);
        mx = pkmax(mx, pkmax(amax4(wp[0][i]), amax4(wp[1][i])));
    }
    if (j1 == 0) {
        if (aw == 3) {
            PIN(wsc);
            const unsigned sel = (q & 1) ? 0x07060302u : 0x05040100u, p0 = __builtin_amdgcn_perm(wsc.z, wsc.x, sel), p1 = __builtin_amdgcn_perm(wsc.w, wsc.y, sel);
            mx = pkmax(mx, pkmax(p0 & 0x7fff7fffu, p1 & 0x7fff7fffu));
            const int it = spc ? (lane >> 5) * 8 : 0;
            lst<unsigned>(smem + A_W + it * 1024 + (c >> 2) * 256 + ((((q & 1) * 8) ^ it) << 4) + (c & 3) * 4,
                          pkb(0.5f * (lo2f(p0) + lo2f(p1)), 0.5f * (hi2f(p0) - hi2f(p1))));
        }
        if (gb == XG && (xh ? x8 : !(lane & 3) && !skip)) {
            lptr wm = smem + A_W + (xh ? 16384 + (lane & 4) * 256 + ((15 - jb) << 4) : c0 * 1024 + (((16 - jb) ^ c0) << 4));
#pragma unroll
            for (int r = 0; r < 8; ++r) {
                const int km = (16 - ((lane >> 2) & 1) - 8 * (aw & 1) - r) & 15;
                lst<unsigned>(wm + (km >> 2) * 256 + (km & 3) * 4, wp[0][r >> 2][r & 3]);
            }
        }
    }
    mx = wave_pkmax(pkmax(mx, mx >> 16));
    if (lane == 0) lst<unsigned>(smem + A_FLG + F_WM + aw * 4, mx);
    sig(ldsa(smem + A_FLG + F_MID), 1u);
    wait_vm<0>();
    SCHED();
}

DEV int role_mid(const u16* __restrict__ Cre, const u16* __restrict__ Cim, char* ws, lptr smem, int gb, int w, int lane, u32x2& tag, const Tab& tab TSARG) {
    int mp, j1; slot_of(gb, mp, j1);
    const int c = lane & 15, q = lane >> 4;
    const bool xtr = gb == XG, ld = w >= 4;
    const rsrc_t rs = mkrsrc(ws, WS_END);
    const unsigned lb = ldsa(smem), fb = lb + A_FLG;
    const CM &sf = tab.sf, &si = tab.si, &ff = tab.ff, &fi = tab.fi;
    const f32x2 (&ti)[2] = tab.ti;
    lptr mb = smem + A_MAT + lane * 16;
    u32x4 wv[2];
    TS(4);
    await<MSL>(fb + F_MID, 24u);
    u32x4 dv[2];
#pragma unroll
    for (int i = 0; i < 2; ++i) dv[i] = lld<u32x4>(smem + A_E1 + (2 * w + i) * E1P + lane * 16);
    const u32x4 m4 = lld<u32x4>(smem + A_FLG + F_WM);
    tag = lld<u32x2>(smem + A_FLG + F_TAG);
    const unsigned em = lld<unsigned>(smem + A_FLG + F_EM);
#pragma unroll
    for (int i = 0; i < 2; ++i) wv[i] = lld<u32x4>(smem + A_W + (2 * w + i) * 1024 + ((lane ^ (2 * w + i)) << 4));
#pragma unroll
    for (int i = 0; i < 2; ++i) dv[i] = pk8(mfh(dv[i], ff.re, Z4), mfh(dv[i], ff.im, Z4));
    const unsigned ew = min((pkmax(pkmax(m4.x, m4.y), pkmax(m4.z, m4.w)) & 0xffffu) >> 7, 245u);
    const float sw = __uint_as_float((246u - ew) << 23), swi = c < 8 ? sw : -sw;
    wv[0] = wcvt(wv[0], sw, swi); wv[1] = wcvt(wv[1], sw, swi);
#pragma unroll
    for (int i = 0; i < 2; ++i) dv[i] = wmul(mfh(sf.re, dv[i], Z4), mfh(sf.im, dv[i], Z4), wv[i]);
#pragma unroll
    for (int i = 0; i < 2; ++i) dv[i] = pk8(mfh(dv[i], si.re, Z4), mfh(dv[i], si.im, Z4));
    {
        f32x4 r[2], m[2];
#pragma unroll
        for (int i = 0; i < 2; ++i) {
            const f32x4 a = mfh(dv[i], fi.re, Z4), b = mfh(dv[i], fi.im, Z4);
#pragma unroll
            for (int e = 0; e < 4; ++e) { r[i][e] = fmaf(a[e], ti[i][0], b[e] * ti[i][1]); m[i][e] = fmaf(b[e], ti[i][0], -a[e] * ti[i][1]); }
        }
#pragma unroll
        for (int e = 0; e < 4; ++e)
            lst<u32x2>(smem + A_XT + (4 * q + e) * 1024 + (w >> 1) * 256 + c * 16 + (w & 1) * 8, (u32x2){pkh(r[0][e], r[1][e]), pkh(m[0][e], m[1][e])});
    }
    if (xtr && (w & 6) == 4) {
        const int it = 5 - w;
        const CM f = cexp(tab.xf), sf2 = cexp(it ? sf.im : lld<u32x4>(mb + 4096)), si2 = cexp(it ? si.im : lld<u32x4>(mb + 5120));
        u32x4 d = lld<u32x4>(smem + A_E1 + (16 + it) * E1P + lane * 16);
        d = pk8(mfh(d, f.re, Z4), mfh(d, f.im, Z4));
        d = wmul(mfh(sf2.re, d, Z4), mfh(sf2.im, d, Z4), wcvt(lld<u32x4>(smem + A_W + 16384 + it * 1024 + lane * 16), sw, swi));
        d = pk8(mfh(d, si2.re, Z4), mfh(d, si2.im, Z4));
        const f32x4 r = mfh(d, cexp(tab.xb).re, Z4);
#pragma unroll
        for (int e = 0; e < 4; ++e) lst<float>(smem + A_EX + (it * 256 + (4 * q + e) * 16 + c) * 4, r[e]);
    }
    TS(5);
    sig(fb + F_MID, 1u);
    auto clate = [&]() INL {
        if (ld) {
            int lt, pt; tile_of(gb, lt, pt);
            if (C0L > 1) c_chunk<C0S>(Cre, Cim, lb, gb, da_of(pt), w - 4, lane, 0);
            c_chunk<C0S>(Cre, Cim, lb, gb, da_of(pt), w - 4, lane, 1);
        }
    };
    if (C0L && C0P == 0) clate();
    await<0>(fb + F_MID, 16u);
    auto cearly = [&]() INL {
        if (ld) {
            int lt, pt; tile_of(gb, lt, pt);
            await<0>(fb + F_ST, 0x4000u);
            c_chunk<CES>(Cre, Cim, lb, gb, da_of(pt), w - 4, lane, 2);
            if (CE > 1 && w < 7) c_chunk<CES>(Cre, Cim, lb, gb, da_of(pt), w - 4, lane, 3);
        }
    };
    if (CE && !CEL) cearly();
    {
        f32x4 xa[2], xe[2];
#pragma unroll
        for (int i = 0; i < 2; ++i) { xa[i] = Z4; xe[i] = Z4; }
        if (xtr) {
            const float sg = (c & 1) ? -0.125f : 0.125f;
#pragma unroll
            for (int i = 0; i < 2; ++i) {
                xa[i] = lld<f32x4>(smem + A_EX + (2 * w + i) * 64 + q * 16) * sg;
                xe[i] = lld<f32x4>(smem + A_EX + 1024 + (2 * w + i) * 64 + q * 16) * -sg;
            }
        }
#pragma unroll
        for (int i = 0; i < 2; ++i) {
            const int s2 = 2 * w + i;
            const u32x4 ea = lld<u32x4>(smem + A_XT + s2 * 1024 + lane * 16);
            const f32x4 ta = mfh(ea, tab.mt[0], xa[i]), tb = mfh(ea, tab.mt[1], xe[i]);
            u32x4 tq = quads(smem + A_XT + s2 * 1024, lane, (u32x2){pkh(ta[0], ta[1]), pkh(ta[2], ta[3])}, (u32x2){pkh(tb[0], tb[1]), pkh(tb[2], tb[3])});
            PIN(tq);
            if (C0L && C0P == 1 && i == 0) clate();
            if (CE && CEL && i == 0) cearly();
            if (TWH && i == 0) await<1>(fb + F_HC, TWH);
            if (CB >= 2 && i == 0 && ld && !late(gb)) {
                int lt, pt; tile_of(gb, lt, pt);
                c_chunk(Cre, Cim, lb, gb, da_of(pt), w - 4, lane, 2);
                if (CB == 2) c_chunk(Cre, Cim, lb, gb, da_of(pt), w - 4, lane, 3);
            }
            st128<SC1>(tq, rs, (lane >> 3) * 32768 + (lane & 7) * 16 + OFF_T + s2 * 262144 + j1 * 8192 + mp * 128);
        }
    }
    TS(7);
    tag.x = (tag.x & 0xffff0000u) | ew;
    return u_exp(em) - 267;
}

DEV void t_done(rsrc_t rs, unsigned fb, int gb, u32x2 tag, int lane) {
    int mp, j1; slot_of(gb, mp, j1);
    if (sig_rtn(fb + F_TC) == 7u) flag_put(rs, OFF_FT, (mp >> 3) * 32 + j1 * 8 + (mp & 7), tag, lane);
}

DEV void role_cload(const u16* __restrict__ Cre, const u16* __restrict__ Cim, char* ws, lptr smem, int gb, int lw, int lane, u32x2 tag TSARG) {
    int lt, pt; tile_of(gb, lt, pt);
    pt = da_of(pt);
    const unsigned lb = ldsa(smem), fb = lb + A_FLG;
    __builtin_amdgcn_s_setprio(3);
    static_assert(!CB || (TWH == 4 && !CE), "");
    if (CB == 1) c_chunk(Cre, Cim, lb, gb, pt, lw, lane, 2);
    if (CB & 1) c_chunk(Cre, Cim, lb, gb, pt, lw, lane, 3);
    wait_vm<CB == 1 ? 16 : CB == 3 ? 8 : 0>();
    t_done(mkrsrc(ws, WS_END), fb, gb, tag, lane);
    TS(44);
    sig(fb + F_FULL, CE > 1 && lw < 3 ? 0x1111u : CE ? 0x111u : 0x11u);
    auto hpoll = [&](unsigned m, unsigned end) INL {
        int spin;
#pragma unroll 1
        for (spin = 0; spin < (1 << 20) && (m & end) != end; ++spin) {
            m = h_poll<2>(mkrsrc(ws, WS_END), gb, pt & 1, lane, tag);
            sig_or(fb + F_FULL, m);
            if (spin == 0) TS(16);
        }
    };
    if (TWH != 4) await<2>(fb + F_HC, 4u);
    if (HPL == 1 && lw == HPW) { if (PDL) __builtin_amdgcn_s_sleep(PDL); TS(19); hpoll(0, CE > 1 ? 0x88888888u : 8u); }
    if (HPL == 1 && CW) await<1>(fb + F_FULL, 8u);
    if (!CE && !CB) c_chunk(Cre, Cim, lb, gb, pt, lw, lane, 2);
    if (CE < 2 || lw == 3) {
        if (CB == 4) {
            if (!late(gb)) sig(fb + F_FULL, 0x100u);
            await<1>(fb + F_FULL, 8u);
            if (late(gb)) { __builtin_amdgcn_s_sleep(C2D); c_chunk(Cre, Cim, lb, gb, pt, lw, lane, 2); }
            else if (C3D) __builtin_amdgcn_s_sleep(C3D);
        }
        if (!(CB & 3)) c_chunk<C3S>(Cre, Cim, lb, gb, pt, lw, lane, 3);
        if (late(gb)) { wait_vm<8>(); sig(fb + F_FULL, 0x100u); }
        wait_vm<0>();
        sig(fb + F_FULL, CE || CB == 4 ? 0x1000u : 0x1100u);
    }
    TS(47);
    if (lw == HPW && !(HPL == 1 && CE > 1)) hpoll(await<2>(fb + F_FULL, 8u) & 0x88888888u, 0x88888888u);
    const unsigned sa = fb + ((lw & 1) ? F_FB : F_FULL);
    unsigned sv = 0;
#pragma unroll 1
    for (int i = 4; i < 8; ++i) {
        const int j = (lw & 1) ? 11 - i : i;
        await_all<2>(fb + F_RD + 4 * (i - 4) + 16 * (lw & 1), 0x40404040u);
        if (!(XE & 1)) c_chunk<CSL>(Cre, Cim, lb, gb, pt, lw, lane, j, ((i + 2) & 3) * A_STAGE + (lw & 2) * 8192);
        wait_vm<8>();
        sig(sa, sv);
        sv = 2u << (4 * j);
        TS(44 + i);
    }
    wait_vm<0>();
    sig(sa, sv);
    if (HD && !(lw & 1)) {
        const u32x4 rh = mkdesc(ws, WS_END);
        unsigned vo = ((lane & 15) >> 2) * 256 + (lane >> 4) * 64 + (lane & 3) * 16; PIN(vo);
        await_all<2>(fb + F_RD + 4 * HDT, 0x40404040u);
        if (HD0) __builtin_amdgcn_s_sleep(HD0);
#pragma unroll
        for (int i = 0; i < 8; ++i) {
            const int k2 = 11 - (i >> 1), k1 = 1 - (lw >> 1);
            const unsigned so = OFF_H + lt * 524288 + (pt & 1) * 131072 + ((k2 >> 1) ^ CX(gb)) * 16384 + (k1 + (k2 & 1) * 32) * 8192 + (i & 1) * 4096;
            asm volatile("s_nop 0\n\tbuffer_load_dwordx4 %0, %1, %2 offen sc1 lds\n\tbuffer_load_dwordx4 %0, %1, %2 offen offset:1024 sc1 lds\n\t"
                         "buffer_load_dwordx4 %0, %1, %2 offen offset:2048 sc1 lds\n\tbuffer_load_dwordx4 %0, %1, %2 offen offset:3072 sc1 lds"
                         :: "v"(vo), "s"(rh), "s"(so), "{m0}"(lb + ((k2 + 2) & 3) * A_STAGE + 8192 + k1 * 16384 + (i & 1) * 4096) : "memory");
            if (HDS) __builtin_amdgcn_s_sleep(HDS);
            if (i == 5) { wait_vm<8>(); sig(fb + F_FB, 0x200u); TS(52); }
        }
        wait_vm<0>();
        sig(fb + F_FB, 0x2000u);
    }
    TSE(1, lw == 0);
}

DEV void role_last(char* ws, lptr smem, int gb, int w, int lane, u32x2 tag, int fe TSARG) {
    int s2t, dat; tile_of(gb, s2t, dat);
    dat = da_of(dat);
    const int c = lane & 15, q = lane >> 4, kh = dat & 1;
    const rsrc_t rs = mkrsrc(ws, WS_END);
    const unsigned fb = ldsa(smem + A_FLG);
    SCHED();
    u32x4 fc[2][2], fs[2][2];
    unsigned ce[2], se[2];
#pragma unroll
    for (int r = 0; r < 2; ++r) {
        float dt = (float)(32 * w + 16 * r + c) * (1.0f / 128.0f); PIN(dt);
        float cc[8], ss[8];
        const float t0 = __builtin_amdgcn_fractf(dt * (float)(4 * q)), t1 = __builtin_amdgcn_fractf(dt * (float)(16 + 4 * q));
#pragma unroll
        for (int e = 0; e < 8; ++e) { const float f = fmaf((float)(e & 3), dt, e < 4 ? t0 : t1); cc[e] = __builtin_amdgcn_cosf(f); ss[e] = __builtin_amdgcn_sinf(f); }
        fc[0][r] = (u32x4){pkh(cc[0], cc[1]), pkh(cc[2], cc[3]), pkh(cc[4], cc[5]), pkh(cc[6], cc[7])};
        fs[0][r] = (u32x4){pkh(ss[0], ss[1]), pkh(ss[2], ss[3]), pkh(ss[4], ss[5]), pkh(ss[6], ss[7])};
        PIN(fc[0][r]); PIN(fs[0][r]);
        if (r == TDR) { wait_vm<0>(); t_done(rs, fb, gb, tag, lane); TS(44); }
    }
#pragma unroll
    for (int r = 0; r < 2; ++r) {
        fc[1][r] = ((c & 1) ? fs[0][r] : fc[0][r]) ^ (((c + 1) & 2) ? SG : 0u);
        fs[1][r] = ((c & 1) ? fc[0][r] : fs[0][r]) ^ ((c & 2) ? SG : 0u);
        ce[r] = fc[0][r].x; se[r] = fs[0][r].x;
        if (q == 0) {
            ce[r] = (ce[r] & 0xffff0000u) | 0x3800u; se[r] = (se[r] & 0xffff0000u) | ((c & 1) ? 0x3800u : 0xb800u);
            fs[0][r].x = (se[r] & 0xffff0000u) | ((c & 1) ? 0xbc00u : 0x3c00u);
        }
        PIN(fc[1][r]); PIN(fs[1][r]); PIN(fs[0][r]); PIN(ce[r]); PIN(se[r]);
    }
    const int ls = lane ^ (2 * (kh & w));
    const bool sp = w == 1 && lane < 8;
    lptr tw = smem + A_XT + ((lane >> 2) & 1) * 8192 + w * 2048 + ((ls >> 1) & 1) * 1024 + (((lane >> 3) ^ (ls & 6)) << 5) + (lane & 1) * 16;
    lptr tsc = smem + A_MAT + (w >> 1) * 512 + (w & 1) * 32 + (lane >> 3) * 4, tr[4];
#pragma unroll
    for (int i = 0; i < 4; ++i) {
        tr[i] = smem + A_XT + (i >> 1) * 8192 + (i & 1) * 1024 + ((lane * 8) ^ (i * 64));
        PIN(tr[i]);
    }
    lptr aw_ = smem + A_OG + (c >> 2) * 64 + 256 * w + (c & 3) * 4, rw[4];
#pragma unroll
    for (int i = 0; i < 4; ++i) { rw[i] = smem + A_XT + w * 4096 + (c & 2) * 512 + (c >> 2) * 256 + (((4 * i + q) ^ (c & 14)) << 4) + (c & 1) * 8; PIN(rw[i]); }
    {
        const unsigned v = q == (c >> 2) ? 0x3f80u << (16 * (c & 1)) : 0u, x = (c & 2) ? 0u : v, y = v ^ x, o = q * 64 * D_ * 4 + (c >> 3) * 2048 + (c & 7) * 16;
        lst<u32x4>(smem + A_CN + w * 1024 + lane * 16, w == 0 ? (u32x4){x, y, 0, 0} : w == 1 ? (u32x4){0, 0, x, y} : (u32x4){o, o + 16 * D_ * 4, o + 32 * D_ * 4, o + 48 * D_ * 4});
    }
    unsigned o = lane * 16, o4 = (sp ? lane ^ (ls & 2) : lane) * 16, ft = fb + F_TT;
    unsigned n0 = kh && (((w & 1) + 1 - ((lane >> 1) & 1)) & 2) ? SG : 0u, n1 = kh ? n0 ^ SG : 0u, n4 = kh && (((w & 1) + 3 - ((o4 >> 5) & 1)) & 2) ? SG : 0u;
    unsigned m0 = sp && (ls & 2) ? 0u : ~0u, m4 = sp && !(ls & 2) ? 0u : ~0u;
    PIN(tw); PIN(tsc); PIN(aw_); PIN(o); PIN(o4); PIN(ft); PIN(n0); PIN(n1); PIN(n4); PIN(m0); PIN(m4);
    TS(45);
    __builtin_amdgcn_s_setprio(3);
    if (w == 0) {
        u32x4 fl[2];
        if (TP0 && !(XTP && gb == XG)) __builtin_amdgcn_s_sleep(TP0);
        TS(50);
#pragma unroll 1
        for (int spin = 0; spin < (1 << 20); ++spin) { flag_get(rs, OFF_FT, gb, lane, fl); if (flag_ok(fl, tag)) break; if (spin == 0) TS(49); if (TSL) __builtin_amdgcn_s_sleep(TSL); }
        const unsigned em = wave_pkmax(pkmax(pkmax(fl[0].x, fl[0].z), pkmax(fl[1].x, fl[1].z))) & 0xffu;
#pragma unroll
        for (int i = 0; i < 2; ++i) {
            const float a = ldexpf(1.f, (int)(fl[i].x & 0xffu) - (int)em - 1), b = ldexpf(1.f, (int)(fl[i].z & 0xffu) - (int)em - 1);
            lst<u32x2>(smem + A_MAT + i * 512 + lane * 8, (u32x2){pkh(a, a), pkh(b, b)});
        }
        sig(fb + F_TR, em | 0x8000u);
        sig(fb + F_FB, 4u);
    }
    fe += 1 + (await<1>(fb + F_TR, 0x8000u) & 0xffu);
    TS(46);
    if (XE & 4) { sig(fb + F_FB, 0x10u); TSE(w ? 4 : 5, w == 0 || w == 2); asm volatile("s_endpgm"); }
    u32x4 tv[8];
#pragma unroll
    for (int i = 0; i < 8; ++i) {
        tv[i] = __builtin_amdgcn_raw_buffer_load_b128(rs, (i == 1 ? o4 : o) + (i >> 1) * 1024, OFF_T + (s2t * 8 + (dat >> 1)) * 32768 + ((w & 1) + (i & 1) * 2) * 8192 + (w >> 1) * 4096, SC1);
        if (XE & 64) { tv[i] = fc[0][0]; PIN(tv[i]); }
        if (TLS) __builtin_amdgcn_s_sleep(TLS);
        SCHED();
    }
    if (XE & 64) __builtin_amdgcn_s_sleep(32);
    if (XE & 128) { for (int i = 0; i < 8; ++i) PIN(tv[i]); sig(fb + F_FB, 0x10u); TSE(w ? 4 : 5, w == 0 || w == 2); asm volatile("s_endpgm"); }
    u32x2 gg[2];
#pragma unroll
    for (int r = 0; r < 2; ++r) gg[r] = __builtin_amdgcn_raw_buffer_load_b64(rs, c * 8 + r * 128, OFF_G + s2t * 1024 + w * 256, SC1);
    __builtin_amdgcn_s_setprio(PRIOR);
    unsigned ts[8];
#pragma unroll
    for (int i = 0; i < 8; ++i) {
        ts[i] = lld<unsigned>(tsc + (i >> 1) * 128 + (i & 1) * 64) ^ (i == 1 ? n4 : (i & 1) ? n1 : n0);
        if (i < 2) ts[i] &= i ? m4 : m0;
        PIN(ts[i]);
    }
    auto h8 = [&](u32x4 v) INL { return __builtin_bit_cast(f16x8, v); };
#pragma unroll
    for (int i = 0; i < 8; i += 2) {
        const f16x8 t = h8(tv[i]) * h8((u32x4){ts[i], ts[i], ts[i], ts[i]});
        lst<f16x8>(tw + i * 128, __builtin_elementwise_fma(h8(tv[i + 1]), h8((u32x4){ts[i + 1], ts[i + 1], ts[i + 1], ts[i + 1]}), t));
        SCHED();
    }
    sig(ft, 1u);
    TS(47);
    float g[2];
#pragma unroll
    for (int r = 0; r < 2; ++r) { g[r] = ldexpf(__uint_as_float(gg[r].x), fe); PIN(g[r]); lst<float>(aw_ + 16 * r, 2.0f * __uint_as_float(gg[r].y)); }
    __builtin_amdgcn_s_setprio(PRT);
    await<AWS>(ft, 4u);
    TS(51);
    if (XE & 256) { sig(fb + F_FB, 0x10u); TSE(w ? 4 : 5, w == 0 || w == 2); asm volatile("s_endpgm"); }
    u32x4 tf[16];
#pragma unroll
    for (int i = 0; i < 16; ++i) { tf[i] = trfrag(tr[(i & 1) * 2 + ((i >> 1) & 1)] + ((i >> 2) & 1) * 4096 + (1 - (i >> 3)) * 2048, 512); SCHED(); }
    sig(ft, 0x10u);
    f32x4 P[2][2], O[2][2];
#pragma unroll
    for (int i = 0; i < 4; ++i) O[i >> 1][i & 1] = Z4;
    if (XE & 512) { for (int i = 0; i < 16; ++i) PIN(tf[i]); sig(fb + F_FB, 0x10u); TSE(w ? 4 : 5, w == 0 || w == 2); asm volatile("s_endpgm"); }
    if (MWT) wait_lds(MWT - 1);
    __builtin_amdgcn_s_setprio(PRX);
    SCHED();
#pragma unroll
    for (int i = 0; i < 8; ++i) {
#pragma unroll
        for (int r = 0; r < 2; ++r) O[r][i & 1] = mfh(tf[i], (i & 2) ? fs[i >> 2][r] : fc[i >> 2][r], O[r][i & 1]);
        SCHED();
        if (i == 5) {
#pragma unroll
            for (int r = 0; r < 2; ++r) { fc[0][r].x = ce[r]; fs[0][r].x = se[r]; PIN(fc[0][r]); PIN(fs[0][r]); }
            SCHED();
        }
    }
#pragma unroll
    for (int i = 0; i < 8; ++i) {
#pragma unroll
        for (int r = 0; r < 2; ++r) P[r][i & 1] = mfh(tf[8 + i], (i & 2) ? fs[i >> 2][r] : fc[i >> 2][r], i < 2 ? O[r][i] : P[r][i & 1]);
        SCHED();
    }
    __builtin_amdgcn_s_setprio(PRV);
    TS(53);
    if (XE & 1024) { for (int i = 0; i < 4; ++i) { PIN(P[i >> 1][i & 1]); PIN(O[i >> 1][i & 1]); } sig(fb + F_FB, 0x10u); TSE(w ? 4 : 5, w == 0 || w == 2); asm volatile("s_endpgm"); }
    await<0>(ft, 0x40u);
#pragma unroll
    for (int i = 0; i < 8; ++i) {
        const int r = i >> 2;
        float av[4];
#pragma unroll
        for (int e = 0; e < 4; ++e) av[e] = g[r] * __uint_as_float(pkb(0.f, (i & 2) ? fmaf(O[r][i & 1][e], -2.0f, P[r][i & 1][e]) : P[r][i & 1][e]));
        lst<u32x2>(rw[i & 3] + r * 2048, (u32x2){pkb(av[0], av[1]), pkb(av[2], av[3])});
    }
    sig(fb + F_FB, 0x10u);
    TS(48);
    asm volatile(".subsection 2\ngm_end:\n\t.fill 64, 4, 0xbf800000\n\t.subsection 0");
    TSE(w ? 4 : 5, w == 0 || w == 2);
}

NOPK __global__ __launch_bounds__(768, 3) void gm_kernel(const u16* __restrict__ x, const u16* __restrict__ Bre, const u16* __restrict__ Bim,
                                                         const u16* __restrict__ gate_w, char* ws, const u16* __restrict__ lam_re,
                                                         const u16* __restrict__ lam_im, const u16* __restrict__ log_dt,
                                                         const u16* __restrict__ w_r, const u16* __restrict__ w_i, const u16* __restrict__ Cre,
                                                         const u16* __restrict__ Cim, const u16* __restrict__ Dskip, float* __restrict__ out) {
    extern __shared__ __attribute__((aligned(16))) char smem_[];
    lptr smem = (lptr)smem_;
    unsigned t00 = 0;
    const int wid = __builtin_amdgcn_readfirstlane(threadIdx.x >> 6), lane = threadIdx.x & 63;
    u32x4 cd; unsigned cpf = 0, ct;
    asm volatile("s_getpc_b64 s[40:41]\ngm_beg:\n\ts_mov_b32 s42, gm_end - gm_beg\n\ts_mov_b32 s43, 0x20000\n\ts_and_b32 s41, s41, 0xffff\n\t"
                 "s_cmp_lg_u32 %3, %6\n\ts_cbranch_scc1 1f\n\t"
                 "s_bfe_u32 %2, %4, 0x30003\n\ts_mov_b32 m0, %7\n\ts_lshl_b32 %2, %2, 13\n\tv_lshlrev_b32 %1, 7, %5\n\tbuffer_load_dword %1, s[40:43], %2 offen lds\n1:"
                 : "=&{s[40:43]}"(cd), "=&v"(cpf), "=&s"(ct) : "s"(wid), "s"(blockIdx.x), "v"(lane), "i"(ENT ? 3 : 99), "i"(A_AGG) : "scc", "m0", "memory");
    if (ENT && (wid & 12) == 4) first_loads(x, Bre, Bim, gate_w, smem, blockIdx.x, wid - 4, lane);
    int w = wid & 7, gb = blockIdx.x;
    asm volatile("" : "+s"(w), "+s"(gb));
    if (wid < 4) role_mma(x, Dskip, ws, out, smem, blockIdx.x, wid, lane, cd, t00);
    else {
        TSD();
        u32x2 tag;
        Tab tab;
        if (wid < 8) role_load(x, Bre, Bim, Cre, Cim, gate_w, ws, smem, blockIdx.x, wid - 4, lane, tab TSPASS);
        else role_aux(Cre, Cim, w_r, w_i, lam_re, lam_im, log_dt, ws, out, Dskip, smem, blockIdx.x, wid - 8, lane, tab TSPASS);
        const int fe = role_mid(Cre, Cim, ws, smem, gb, w, lane, tag, tab TSPASS);
        if (w >= 4) role_cload(Cre, Cim, ws, smem, gb, w - 4, lane, tag TSPASS);
        else role_last(ws, smem, gb, w, lane, tag, fe TSPASS);
    }
}

extern "C" void kernel_launch(void* const* d_in, const int* in_sizes, int n_in, void* d_out, int out_size, void* d_ws, size_t ws_size,
                              hipStream_t stream) {
    (void)hipFuncSetAttribute((const void*)gm_kernel, hipFuncAttributeMaxDynamicSharedMemorySize, SMEM);
    gm_kernel<<<256, 768, SMEM, stream>>>((const u16*)d_in[0], (const u16*)d_in[6], (const u16*)d_in[7], (const u16*)d_in[12], (char*)d_ws,
                                          (const u16*)d_in[4], (const u16*)d_in[5], (const u16*)d_in[10], (const u16*)d_in[1], (const u16*)d_in[2],
                                          (const u16*)d_in[8], (const u16*)d_in[9], (const u16*)d_in[11], (float*)d_out);
}
```
